```python
import torch

def _softmax(x, axis=-1):
    return torch.softmax(x, dim=axis)
def _gelu(x):
    return torch.nn.functional.gelu(x, approximate="tanh")
def _silu(x):
    return torch.nn.functional.silu(x)
def _sigmoid(x):
    return torch.sigmoid(x)
def _layernorm(x, axis=-1, eps=1e-5):
    m = x.mean(dim=axis, keepdim=True)
    v = x.var(dim=axis, keepdim=True, unbiased=False)
    return (x - m) / torch.sqrt(v + eps)
def _scatter_add(tgt, idx, src, axis=0):
    return tgt.clone().index_add_(axis, idx, src)
def _segment_sum(src, seg, n):
    out = torch.zeros((n,) + src.shape[1:], dtype=src.dtype)
    return out.index_add_(0, seg, src)
def _pad(x, pw):
    flat = tuple(v for pair in reversed(pw) for v in pair)
    return torch.nn.functional.pad(x, flat)

kernel_name = 'gated_mix__ssm_conv_s5__dense_activation_mish__prompt'

def setup_inputs():

    _w = (lambda a: (lambda t: t.to(torch.bfloat16) if t.is_floating_point() else t)(torch.as_tensor(a)))
    def _A_setup():
        B, S, D = 1, 2048, 640
        P = D
        Lambda_re = -0.5 * (lambda _x: torch.exp(torch.as_tensor(_x)))((lambda *s: torch.randn(*s, dtype=torch.bfloat16) * 0.2)(P))
        Lambda_im = (lambda *s: torch.randn(*s, dtype=torch.bfloat16) * 0.2)(P) * 1.0
        B_re = (lambda *s: torch.randn(*s, dtype=torch.bfloat16) * 0.2)(P, D) * (D ** -0.5)
        B_im = (lambda *s: torch.randn(*s, dtype=torch.bfloat16) * 0.2)(P, D) * (D ** -0.5)
        C_re = (lambda *s: torch.randn(*s, dtype=torch.bfloat16) * 0.2)(D, P) * (P ** -0.5)
        C_im = (lambda *s: torch.randn(*s, dtype=torch.bfloat16) * 0.2)(D, P) * (P ** -0.5)
        log_dt = (lambda *s: torch.randn(*s, dtype=torch.bfloat16) * 0.2)(P) * 0.3 - 2.0
        Dskip = (lambda *s: torch.randn(*s, dtype=torch.bfloat16) * 0.05)(D)
        return {
            "x": _w((lambda *s: torch.randn(*s, dtype=torch.bfloat16) * 0.05)(B, S, D)),
            "Lambda_re": _w(Lambda_re),
            "Lambda_im": _w(Lambda_im),
            "B_re": _w(B_re),
            "B_im": _w(B_im),
            "C_re": _w(C_re),
            "C_im": _w(C_im),
            "log_dt": _w(log_dt),
            "Dskip": _w(Dskip),
        }
    _A_in = _A_setup()

    def _B_setup():
        B, S, D, F = 1, 2048, 640, 1536
        return {
            "x": _w((lambda *s: torch.randn(*s, dtype=torch.bfloat16) * 0.05)(B, S, D)),
            "w1": _w((lambda *s: torch.randn(*s, dtype=torch.bfloat16) * 3.2)(D, F) * (D ** -0.5)),
            "b1": _w((lambda *s: torch.randn(*s, dtype=torch.bfloat16) * 0.05)(F)),
            "w2": _w((lambda *s: torch.randn(*s, dtype=torch.bfloat16) * 0.05)(F, D) * (F ** -0.5)),
        }
    _B_in = _B_setup()

    return {
        "A_x": _A_in["x"],
        "A_Lambda_re": _A_in["Lambda_re"],
        "A_Lambda_im": _A_in["Lambda_im"],
        "A_B_re": _A_in["B_re"],
        "A_B_im": _A_in["B_im"],
        "A_C_re": _A_in["C_re"],
        "A_C_im": _A_in["C_im"],
        "A_log_dt": _A_in["log_dt"],
        "A_Dskip": _A_in["Dskip"],
        "B_x": _B_in["x"],
        "B_w1": _B_in["w1"],
        "B_b1": _B_in["b1"],
        "B_w2": _B_in["w2"],
        "gate_w": _w((lambda *s: torch.randn(*s, dtype=torch.bfloat16) * 0.05)(640) * 16.0),
    }

def reference(A_x, A_Lambda_re, A_Lambda_im, A_B_re, A_B_im, A_C_re, A_C_im, A_log_dt, A_Dskip, B_x, B_w1, B_b1, B_w2, gate_w):
    def _A_ref(x, Lambda_re, Lambda_im, B_re, B_im, C_re, C_im, log_dt, Dskip):
        Bsz, L, H = x.shape
        Lam = Lambda_re + 1j * Lambda_im
        Bmat = B_re + 1j * B_im
        Cmat = C_re + 1j * C_im
        dt = torch.exp(log_dt)
        Lam_bar = torch.exp(dt * Lam)
        B_bar = ((Lam_bar - 1.0) / Lam)[:, None] * Bmat
        Bu = torch.einsum("bld,pd->blp", x + 0j, B_bar)
        h = torch.zeros_like(Bu[:, 0, :])
        hs = []
        for t in range(L):
            h = Lam_bar[None, :] * h + Bu[:, t, :]
            hs.append(h)
        hseq = torch.stack(hs, 1)
        y = 2.0 * torch.einsum("blp,dp->bld", hseq, Cmat).real + x * Dskip[None, None, :]
        return y

    def _B_ref(x, w1, b1, w2):
        h = torch.einsum("bsd,df->bsf", x, w1) + b1
        sp = torch.log1p(torch.exp(-torch.abs(h))) + torch.where(h > 0, h, torch.zeros_like(h))
        act = h * torch.tanh(sp)
        return torch.einsum("bsf,fd->bsd", act, w2)

    x = A_x
    g = _sigmoid((x * gate_w).sum(-1, keepdims=True))
    a = _A_ref(x, A_Lambda_re, A_Lambda_im, A_B_re, A_B_im, A_C_re, A_C_im, A_log_dt, A_Dskip)
    b = _B_ref(x, B_w1, B_b1, B_w2)
    return g * a + (1 - g) * b
```

```cpp
#include <hip/hip_runtime.h>
#include <hip/hip_bf16.h>
#include <stdint.h>

constexpr int L = 2048, D = 640, P = 640, F = 1536;

__device__ __forceinline__ float bf2f(unsigned short v) { return __uint_as_float(((unsigned)v) << 16); }

__global__ void k_gemm1(const unsigned short* x, const unsigned short* Bre, const unsigned short* Bim,
                        const unsigned short* w1, const unsigned short* b1, float* Ure, float* Uim, float* act) {
    int l = blockIdx.y;
    int n = blockIdx.x * blockDim.x + threadIdx.x;
    __shared__ float xs[D];
    for (int i = threadIdx.x; i < D; i += blockDim.x) xs[i] = bf2f(x[l * D + i]);
    __syncthreads();
    if (n < P) {
        float a = 0, b = 0;
        for (int d = 0; d < D; ++d) { a += xs[d] * bf2f(Bre[n * D + d]); b += xs[d] * bf2f(Bim[n * D + d]); }
        Ure[l * P + n] = a; Uim[l * P + n] = b;
    } else if (n < P + F) {
        int f = n - P;
        float a = 0;
        for (int d = 0; d < D; ++d) a += xs[d] * bf2f(w1[d * F + f]);
        a += bf2f(b1[f]);
        float sp = log1pf(expf(-fabsf(a))) + (a > 0 ? a : 0.f);
        act[l * F + f] = a * tanhf(sp);
    }
}

__global__ void k_scan(const unsigned short* Lre, const unsigned short* Lim, const unsigned short* logdt,
                       float* Ure, float* Uim, int round_dt) {
    int p = blockIdx.x * blockDim.x + threadIdx.x;
    if (p >= P) return;
    float lr = bf2f(Lre[p]), li = bf2f(Lim[p]);
    float dt = expf(bf2f(logdt[p]));
    if (round_dt) { __hip_bfloat16 t = __float2bfloat16(dt); dt = __bfloat162float(t); }
    float ar = dt * lr, ai = dt * li;
    float m = expf(ar);
    float lbr = m * cosf(ai), lbi = m * sinf(ai);
    float nr = lbr - 1.f, ni = lbi;
    float den = lr * lr + li * li;
    float cr = (nr * lr + ni * li) / den, ci = (ni * lr - nr * li) / den;
    float hr = 0, hi = 0;
    for (int t = 0; t < L; ++t) {
        float ur = Ure[t * P + p], ui = Uim[t * P + p];
        float br = cr * ur - ci * ui, bi = cr * ui + ci * ur;
        float nhr = lbr * hr - lbi * hi + br;
        float nhi = lbr * hi + lbi * hr + bi;
        hr = nhr; hi = nhi;
        Ure[t * P + p] = hr; Uim[t * P + p] = hi;
    }
}

__global__ void k_gemm2(const unsigned short* x, const unsigned short* Cre, const unsigned short* Cim,
                        const unsigned short* Dskip, const unsigned short* w2, const unsigned short* gw,
                        const float* Hre, const float* Him, const float* act, float* out) {
    int l = blockIdx.x;
    __shared__ float hr[P], hi[P], ac[F];
    __shared__ float red[640];
    int tid = threadIdx.x;
    hr[tid] = Hre[l * P + tid]; hi[tid] = Him[l * P + tid];
    for (int i = tid; i < F; i += 640) ac[i] = act[l * F + i];
    float xv = bf2f(x[l * D + tid]);
    red[tid] = xv * bf2f(gw[tid]);
    __syncthreads();
    float s = 0;
    for (int i = 0; i < D; ++i) s += red[i];
    float g = 1.f / (1.f + expf(-s));
    float a = 0;
    for (int p = 0; p < P; ++p) a += hr[p] * bf2f(Cre[tid * P + p]) - hi[p] * bf2f(Cim[tid * P + p]);
    a = 2.f * a + xv * bf2f(Dskip[tid]);
    float b = 0;
    for (int f = 0; f < F; ++f) b += ac[f] * bf2f(w2[f * D + tid]);
    out[l * D + tid] = g * a + (1.f - g) * b;
}

extern "C" void kernel_launch(void* const* d_in, const int* in_sizes, int n_in,
                              void* d_out, int out_size, void* d_ws, size_t ws_size,
                              hipStream_t stream) {
    const unsigned short* x = (const unsigned short*)d_in[0];
    const unsigned short* Lre = (const unsigned short*)d_in[1];
    const unsigned short* Lim = (const unsigned short*)d_in[2];
    const unsigned short* Bre = (const unsigned short*)d_in[3];
    const unsigned short* Bim = (const unsigned short*)d_in[4];
    const unsigned short* Cre = (const unsigned short*)d_in[5];
    const unsigned short* Cim = (const unsigned short*)d_in[6];
    const unsigned short* logdt = (const unsigned short*)d_in[7];
    const unsigned short* Dskip = (const unsigned short*)d_in[8];
    const unsigned short* w1 = (const unsigned short*)d_in[10];
    const unsigned short* b1 = (const unsigned short*)d_in[11];
    const unsigned short* w2 = (const unsigned short*)d_in[12];
    const unsigned short* gw = (const unsigned short*)d_in[13];
    float* Ure = (float*)d_ws;
    float* Uim = Ure + L * P;
    float* act = Uim + L * P;
    k_gemm1<<<dim3((P + F + 255) / 256, L), 256, 0, stream>>>(x, Bre, Bim, w1, b1, Ure, Uim, act);
    k_scan<<<(P + 63) / 64, 64, 0, stream>>>(Lre, Lim, logdt, Ure, Uim, 1);
    k_gemm2<<<L, 640, 0, stream>>>(x, Cre, Cim, Dskip, w2, gw, Ure, Uim, act, (float*)d_out);
}
```

```cpp
#include <hip/hip_runtime.h>
#include <stdint.h>

typedef short s16x4 __attribute__((ext_vector_type(4)));
typedef short s16x8 __attribute__((ext_vector_type(8)));
typedef __bf16 bf16x2 __attribute__((ext_vector_type(2)));
typedef float f32x2 __attribute__((ext_vector_type(2)));
typedef float f32x4 __attribute__((ext_vector_type(4)));
typedef unsigned u32x2 __attribute__((ext_vector_type(2)));
typedef unsigned u32x4 __attribute__((ext_vector_type(4)));
typedef unsigned short u16;
#define LAS __attribute__((address_space(3)))
#define GAS __attribute__((address_space(1)))
#define DI __device__ __forceinline__

constexpr int SEQ = 2048, DM = 640, NP = 640, NF = 1536;
constexpr int NKS = 88;

constexpr size_t WS_A2 = 0;
constexpr size_t WS_W2 = WS_A2 + (size_t)16 * NKS * 8192;
constexpr size_t WS_G = WS_W2 + (size_t)4 * NKS * 10240;
constexpr size_t WS_AGG = WS_G + 2048 * 4;
constexpr size_t WS_PRE = WS_AGG + 16 * 640 * 8;
constexpr size_t WS_CNT = WS_PRE + 16 * 4 * 640 * 8;
constexpr size_t WS_A2C = WS_CNT + 64 * 256;

DI float bflo(unsigned v) { return __uint_as_float(v << 16); }
DI float bfhi(unsigned v) { return __uint_as_float(v & 0xffff0000u); }
DI unsigned pack2(float a, float b) {
    f32x2 p = {a, b};
    return __builtin_bit_cast(unsigned, __builtin_convertvector(p, bf16x2));
}
DI f32x4 mfma16(s16x8 a, s16x8 b, f32x4 c) { return __builtin_amdgcn_mfma_f32_16x16x32_bf16(a, b, c, 0, 0, 0); }
template <int S> DI float shr_zero(float v) {
    return __builtin_bit_cast(float, __builtin_amdgcn_update_dpp(0, __builtin_bit_cast(int, v), 0x110 + S, 0xf, 0xf, true));
}
DI f32x2 cmul(f32x2 a, f32x2 b) { return (f32x2){a.x * b.x - a.y * b.y, a.x * b.y + a.y * b.x}; }
DI f32x2 cfma(f32x2 a, f32x2 b, f32x2 c) {
    return (f32x2){fmaf(-a.y, b.y, fmaf(a.x, b.x, c.x)), fmaf(a.y, b.x, fmaf(a.x, b.y, c.y))};
}
DI f32x2 cmul_pk(f32x2 a, f32x2 b) { return (f32x2){fmaf(-a.y, b.y, a.x * b.x), fmaf(a.y, b.x, a.x * b.y)}; }
DI f32x2 cmul_sc(f32x2 a, f32x2 b) {
    float p = a.x * b.x, r = a.x * b.y;
    asm("" : "+v"(p));
    asm("" : "+v"(r));
    return (f32x2){fmaf(-a.y, b.y, p), fmaf(a.y, b.x, r)};
}
DI float dot2bf(unsigned a, unsigned b, float c) {
    return __builtin_amdgcn_fdot2_f32_bf16(__builtin_bit_cast(bf16x2, a), __builtin_bit_cast(bf16x2, b), c, false);
}
#define WAIT_VM(n) asm volatile("s_waitcnt vmcnt(%0)" ::"n"(n) : "memory")
#define WAIT_LGKM0() asm volatile("s_waitcnt lgkmcnt(0)" ::: "memory")
#define SCHED_FENCE() __builtin_amdgcn_sched_barrier(0)

DI void glds16(const void* gsrc, unsigned lds_dst) {
    unsigned keep;
    asm volatile("s_mov_b32 %0, m0\n\ts_mov_b32 m0, %2\n\ts_nop 0\n\tglobal_load_lds_dwordx4 %1, off\n\ts_mov_b32 m0, %0"
                 : "=&s"(keep) : "v"(gsrc), "s"(lds_dst) : "memory");
}
DI void glds16x4(const void* gsrc, unsigned lds_dst) {
    unsigned keep;
    asm volatile("s_mov_b32 %0, m0\n\ts_mov_b32 m0, %2\n\ts_nop 0\n\tglobal_load_lds_dwordx4 %1, off\n\t"
                 "global_load_lds_dwordx4 %1, off offset:1024\n\tglobal_load_lds_dwordx4 %1, off offset:2048\n\t"
                 "global_load_lds_dwordx4 %1, off offset:3072\n\ts_mov_b32 m0, %0"
                 : "=&s"(keep) : "v"(gsrc), "s"(lds_dst) : "memory");
}
DI unsigned ld32_asm(const void* p) { unsigned r; asm volatile("global_load_dword %0, %1, off" : "=v"(r) : "v"(p) : "memory"); return r; }
DI unsigned ld16_asm(const void* p) { unsigned r; asm volatile("global_load_ushort %0, %1, off" : "=v"(r) : "v"(p) : "memory"); return r; }
DI u32x2 ld64_asm(const void* p) { u32x2 r; asm volatile("global_load_dwordx2 %0, %1, off" : "=v"(r) : "v"(p) : "memory"); return r; }
DI u32x4 ld128_asm(const void* p) { u32x4 r; asm volatile("global_load_dwordx4 %0, %1, off" : "=v"(r) : "v"(p) : "memory"); return r; }
constexpr float LOG2E = 1.44269504088896341f, INV2PI = 0.15915494309189535f;
DI f32x2 lam_bar(float lr, float li, float ld) {
    const float dt = __builtin_amdgcn_exp2f(ld * LOG2E);
    const float m = __builtin_amdgcn_exp2f(dt * lr * LOG2E);
    const float rv = __builtin_amdgcn_fractf(dt * li * INV2PI);
    return (f32x2){m * __builtin_amdgcn_cosf(rv), m * __builtin_amdgcn_sinf(rv)};
}

constexpr int K1_STAGE = 38 * 1024;
constexpr int K1_GW = 4 * K1_STAGE;
constexpr int K1_PLR = K1_GW + 1280, K1_PLI = K1_GW + 1360, K1_PLD = K1_GW + 1440, K1_PB1 = K1_GW + 1536;
constexpr int K1_TAB = K1_GW + 2048;
constexpr int K1_XCH = K1_TAB + 2560;
constexpr int K1_CNT = K1_XCH + 1280;
constexpr int K1_B1 = K1_CNT + 16;
constexpr int K1_LDS = K1_B1 + 384;

__global__ __launch_bounds__(512, 2) void k1(const u16* __restrict__ x, const u16* __restrict__ Lre, const u16* __restrict__ Lim,
                                             const u16* __restrict__ Bre, const u16* __restrict__ Bim, const u16* __restrict__ Cre,
                                             const u16* __restrict__ Cim, const u16* __restrict__ logdt, const u16* __restrict__ w1,
                                             const u16* __restrict__ b1, const u16* __restrict__ w2, const u16* __restrict__ gw,
                                             char* __restrict__ ws) {
    __shared__ __attribute__((aligned(16))) char sm[K1_LDS];
    const int tid = threadIdx.x, lane = tid & 63;
    const int wave = __builtin_amdgcn_readfirstlane(tid >> 6);
    const int bid = blockIdx.x, xcd = bid & 7, idx = bid >> 3;
    const int rb = 8 * (xcd & 1) + (idx & 7);
    const int cb = 4 * (xcd >> 1) + (idx >> 3);
    const int t = lane & 15, q = lane >> 4;
    const unsigned smb = (unsigned)(size_t)(LAS char*)sm;

    const char* src[5];
    unsigned ldo[5];
    int incE, strE;
    {
        const int l8 = lane >> 3;
        auto xsrc = [&](int s) {
            const int p = 8 * s + l8;
            const int tm = 128 * rb + 32 * (p >> 5) + 2 * (p & 15) + ((p >> 4) & 1);
            return (const char*)(x + tm * DM + (((lane & 7) ^ ((p >> 1) & 7)) << 3));
        };
        auto wsrc = [&](int s) {
            const int p = 8 * s + l8;
            const int ch = 40 * cb + 8 * (p >> 4) + ((p & 15) >> 1);
            return (const char*)(((p & 1) ? Bim : Bre) + ch * DM + (((lane & 7) ^ ((p >> 1) & 7)) << 3));
        };
        auto msrc = [&](int s) {
            const int gI = 64 * s + lane, row = gI / 12, cp = gI - 12 * row;
            int j = (cp >> 1) - ((row >> 3) & 1);
            j += j < 0 ? 6 : 0;
            return (const char*)(w1 + row * NF + 96 * cb + 16 * j + 8 * (cp & 1));
        };
        src[0] = xsrc(2 * wave); ldo[0] = smb + 2048 * wave;
        src[1] = xsrc(2 * wave + 1); ldo[1] = smb + 2048 * wave + 1024;
        src[2] = wsrc(wave); ldo[2] = smb + 16384 + 1024 * wave;
        src[3] = msrc(wave); ldo[3] = smb + 26624 + 1024 * wave;
        if (wave < 2) { src[4] = wsrc(8 + wave); ldo[4] = smb + 16384 + 1024 * (8 + wave); incE = 128; strE = K1_STAGE; }
        else if (wave < 6) { src[4] = msrc(6 + wave); ldo[4] = smb + 26624 + 1024 * (6 + wave); incE = 64 * NF * 2; strE = K1_STAGE; }
        else {
            const u16* p = gw + lane * 8;
            if (wave == 7) {
                p = gw + 512 + t * 8;
                if (lane >= 16 && lane < 21) p = Lre + 40 * cb + (lane - 16) * 8;
                if (lane >= 21 && lane < 26) p = Lim + 40 * cb + (lane - 21) * 8;
                if (lane >= 26 && lane < 31) p = logdt + 40 * cb + (lane - 26) * 8;
                if (lane >= 32 && lane < 44) p = b1 + 96 * cb + (lane - 32) * 8;
            }
            src[4] = (const char*)p; ldo[4] = smb + K1_GW + (wave - 6) * 1024; incE = 0; strE = 0;
        }
    }
#define K1_SLOT(i, buf)                                                                  \
    do {                                                                                 \
        glds16(src[i], ldo[i] + (buf) * ((i) == 4 ? strE : K1_STAGE));                   \
        src[i] += (i) < 3 ? 128 : (i) == 3 ? 64 * NF * 2 : incE;                         \
    } while (0)
#pragma unroll
    for (int st = 0; st < 3; ++st) { K1_SLOT(0, st); K1_SLOT(1, st); K1_SLOT(2, st); K1_SLOT(3, st); K1_SLOT(4, st); }
    if (tid == 0) *(unsigned*)(sm + K1_CNT) = 0u;

    const int wc = wave & 3;
    const int xoff = (32 * wc + t) * 128;
    const int co0 = (q ^ ((t >> 1) & 7)) * 16, co1 = co0 ^ 64;
    f32x4 acc[6][2];
#pragma unroll
    for (int a = 0; a < 6; ++a) { acc[a][0] = (f32x4){0.f, 0.f, 0.f, 0.f}; acc[a][1] = (f32x4){0.f, 0.f, 0.f, 0.f}; }
    char* a2 = ws + WS_A2 + (size_t)rb * NKS * 8192 + (2 * wc) * 1024 + lane * 16;

    const int wgid = bid * 8 + wave;
    char* ptl = sm + 2 * K1_STAGE + wave * 2048;
    auto pretile_dma = [&]() {
        const int KS = wgid / 40, cn = wgid - 40 * KS;
        const u16* p0 = (KS < 40) ? ((lane >> 5) ? Cim : Cre) + (16 * cn + ((lane >> 1) & 15)) * NP + 16 * KS + 8 * (lane & 1)
                                  : w2 + (32 * (KS - 40) + (lane >> 1)) * DM + 16 * cn + 8 * (lane & 1);
        glds16(p0, smb + 2 * K1_STAGE + wave * 2048);
        const int f1 = wgid + 2048 < 3520 ? wgid + 2048 : 1600;
        const int KS1 = f1 / 40, cn1 = f1 - 40 * KS1;
        glds16(w2 + (32 * (KS1 - 40) + (lane >> 1)) * DM + 16 * cn1 + 8 * (lane & 1), smb + 2 * K1_STAGE + wave * 2048 + 1024);
    };
    auto pretile_out = [&]() {
        const int tro = (4 * q + (t >> 2)) * 32 + (lane & 3) * 8;
        const int KS = wgid / 40, cn = wgid - 40 * KS;
        u32x4 wf;
        if (KS < 40) {
            const unsigned r0 = *(const unsigned*)(ptl + t * 32 + 4 * q), r1 = *(const unsigned*)(ptl + t * 32 + 16 + 4 * q);
            const unsigned i0 = *(const unsigned*)(ptl + 512 + t * 32 + 4 * q), i1 = *(const unsigned*)(ptl + 512 + t * 32 + 16 + 4 * q);
            wf[0] = (r0 & 0xffffu) | (i0 << 16); wf[1] = (r0 >> 16) | (i0 & 0xffff0000u);
            wf[2] = (r1 & 0xffffu) | (i1 << 16); wf[3] = (r1 >> 16) | (i1 & 0xffff0000u);
        } else {
            const s16x4 lo = __builtin_amdgcn_ds_read_tr16_b64_v4i16((LAS s16x4*)(ptl + tro));
            const s16x4 hi = __builtin_amdgcn_ds_read_tr16_b64_v4i16((LAS s16x4*)(ptl + tro + 512));
            wf = __builtin_bit_cast(u32x4, __builtin_shufflevector(lo, hi, 0, 1, 2, 3, 4, 5, 6, 7));
        }
        *(u32x4*)(ws + WS_W2 + ((size_t)(((cn / 10) * NKS + KS) * 10 + cn % 10) * 64 + lane) * 16) = wf;
        const s16x4 lo = __builtin_amdgcn_ds_read_tr16_b64_v4i16((LAS s16x4*)(ptl + 1024 + tro));
        const s16x4 hi = __builtin_amdgcn_ds_read_tr16_b64_v4i16((LAS s16x4*)(ptl + 1024 + tro + 512));
        if (wgid + 2048 < 3520) {
            const int f = wgid + 2048, KS1 = f / 40, cn1 = f - 40 * KS1;
            *(s16x8*)(ws + WS_W2 + ((size_t)(((cn1 / 10) * NKS + KS1) * 10 + cn1 % 10) * 64 + lane) * 16) = __builtin_shufflevector(lo, hi, 0, 1, 2, 3, 4, 5, 6, 7);
        }
    };
    auto side_tables = [&]() {
        if (wave == 0) {
            if (lane < 40) {
                const float lr = bflo(*(const u16*)(sm + K1_PLR + lane * 2)), li = bflo(*(const u16*)(sm + K1_PLI + lane * 2));
                f32x2 Lp = lam_bar(lr, li, bflo(*(const u16*)(sm + K1_PLD + lane * 2)));
                f32x2* tb = (f32x2*)(sm + K1_TAB + lane * 64);
                const float nr = Lp.x - 1.0f, ni = Lp.y;
                const float rden = 1.0f / (lr * lr + li * li);
                tb[6] = (f32x2){(nr * lr + ni * li) * rden, (ni * lr - nr * li) * rden};
#pragma unroll
                for (int e = 0; e < 6; ++e) { tb[e] = Lp; Lp = cmul(Lp, Lp); }
            }
        } else if (wave == 1) {
            if (lane < 48) {
                const unsigned v = *(const unsigned*)(sm + K1_PB1 + lane * 4);
                *(f32x2*)(sm + K1_B1 + lane * 8) = (f32x2){bflo(v), bfhi(v)};
            }
        }
    };

    if (wave < 4) {
        const bool do_gate = (cb == 0);
        float gs0 = 0.f, gs1 = 0.f;
        s16x8 fx[2][2], fw[2][5];
        const int woff = 16384 + t * 128;
#define S_READ(b, sb, co)                                                                         \
    do {                                                                                          \
        fx[b][0] = *(const s16x8*)((sb) + xoff + (co)); fx[b][1] = *(const s16x8*)((sb) + xoff + 2048 + (co)); \
        _Pragma("unroll") for (int T_ = 0; T_ < 5; ++T_) fw[b][T_] = *(const s16x8*)((sb) + woff + T_ * 2048 + (co)); \
    } while (0)
#define S_GATE(b, kk)                                                                             \
    do {                                                                                          \
        if (do_gate) {                                                                            \
            const u32x4 gv = *(const u32x4*)(sm + K1_GW + ((kk) + 8 * q) * 2);                    \
            const u32x4 xa = __builtin_bit_cast(u32x4, fx[b][0]), xb = __builtin_bit_cast(u32x4, fx[b][1]); \
            _Pragma("unroll") for (int e = 0; e < 4; ++e) { gs0 = dot2bf(xa[e], gv[e], gs0); gs1 = dot2bf(xb[e], gv[e], gs1); } \
        }                                                                                         \
    } while (0)
#define S_BODY(it, MODE)                                                                          \
    do {                                                                                          \
        const char* sb = sm + ((it) & 3) * K1_STAGE;                                              \
        const char* sn = sm + (((it) + 1) & 3) * K1_STAGE;                                        \
        S_READ(1, sb, co1);                                                                       \
        _Pragma("unroll") for (int T = 0; T < 5; ++T) { acc[T][0] = mfma16(fw[0][T], fx[0][0], acc[T][0]); acc[T][1] = mfma16(fw[0][T], fx[0][1], acc[T][1]); } \
        S_GATE(0, 64 * (it));                                                                     \
        if ((MODE) == 1) side_tables();                                                           \
        if ((MODE) < 4) {                                                                         \
            SCHED_FENCE();                                                                        \
            WAIT_VM((MODE) < 3 ? 5 : 2);                                                          \
            WAIT_LGKM0();                                                                         \
            __builtin_amdgcn_s_barrier();                                                         \
            SCHED_FENCE();                                                                        \
            S_READ(0, sn, co0);                                                                   \
        }                                                                                         \
        _Pragma("unroll") for (int T = 0; T < 5; ++T) {                                           \
            acc[T][0] = mfma16(fw[1][T], fx[1][0], acc[T][0]); acc[T][1] = mfma16(fw[1][T], fx[1][1], acc[T][1]); \
            if ((MODE) < 2) { SCHED_FENCE(); K1_SLOT(T, ((it) + 3) & 3); SCHED_FENCE(); }         \
        }                                                                                         \
        if ((MODE) == 2) pretile_dma();                                                           \
        S_GATE(1, 64 * (it) + 32);                                                                \
    } while (0)
        WAIT_VM(10);
        WAIT_LGKM0();
        __builtin_amdgcn_s_barrier();
        S_READ(0, sm, co0);
#pragma unroll 1
        for (int it = 0; it < 6; ++it) S_BODY(it, 0);
        S_BODY(6, 1); S_BODY(7, 2); S_BODY(8, 3); S_BODY(9, 4);
        WAIT_VM(0);
        pretile_out();
        if (do_gate) {
            gs0 += __shfl_xor(gs0, 16); gs0 += __shfl_xor(gs0, 32);
            gs1 += __shfl_xor(gs1, 16); gs1 += __shfl_xor(gs1, 32);
            if (q == 0) {
                f32x2 gg = {1.0f / (1.0f + __expf(-gs0)), 1.0f / (1.0f + __expf(-gs1))};
                *(f32x2*)(ws + WS_G + (128 * rb + 32 * wc + 2 * t) * 4) = gg;
            }
        }
#pragma unroll
        for (int T = 0; T < 5; ++T) {
            u32x2 o0, o1;
#pragma unroll
            for (int cc = 0; cc < 2; ++cc) {
                const int chl = 8 * T + 2 * q + cc;
                const f32x4* tb = (const f32x4*)(sm + K1_TAB + chl * 64);
                const f32x4 t0 = tb[0], t1 = tb[1], t2 = tb[2], t3 = tb[3];
                const f32x2 L1 = {t0[0], t0[1]}, L2 = {t0[2], t0[3]}, L4 = {t1[0], t1[1]}, L8 = {t1[2], t1[3]}, L16 = {t2[0], t2[1]};
                const f32x2 cf = {t3[0], t3[1]};
                const f32x2 u0 = cmul_sc(cf, (f32x2){acc[T][0][2 * cc], acc[T][0][2 * cc + 1]});
                const f32x2 u1 = cmul_sc(cf, (f32x2){acc[T][1][2 * cc], acc[T][1][2 * cc + 1]});
                f32x2 a = cfma(L1, u0, u1);
                a = cfma(L2, (f32x2){shr_zero<1>(a.x), shr_zero<1>(a.y)}, a);
                a = cfma(L4, (f32x2){shr_zero<2>(a.x), shr_zero<2>(a.y)}, a);
                a = cfma(L8, (f32x2){shr_zero<4>(a.x), shr_zero<4>(a.y)}, a);
                a = cfma(L16, (f32x2){shr_zero<8>(a.x), shr_zero<8>(a.y)}, a);
                const f32x2 h0 = cfma(L1, (f32x2){shr_zero<1>(a.x), shr_zero<1>(a.y)}, u0);
                o0[cc] = pack2(h0.x, -h0.y);
                o1[cc] = pack2(a.x, -a.y);
                if (t == 15) *(f32x2*)(sm + K1_XCH + (wc * 40 + chl) * 8) = a;
            }
            const int Tg = 5 * cb + T;
            char* dst = a2 + (size_t)(Tg >> 1) * 8192 + (Tg & 1) * 8;
            *(u32x2*)dst = o0;
            *(u32x2*)(dst + 1024) = o1;
        }
        WAIT_LGKM0();
        unsigned old = 0;
        if (lane == 0) old = __hip_atomic_fetch_add((LAS unsigned*)(sm + K1_CNT), 1u, __ATOMIC_RELAXED, __HIP_MEMORY_SCOPE_WORKGROUP);
        old = __builtin_amdgcn_readfirstlane(old);
        asm volatile("" ::: "memory");
        if (old == 3 && lane < 40) {
            const f32x2 L32 = *(const f32x2*)(sm + K1_TAB + lane * 64 + 40);
            const f32x2* xc = (const f32x2*)(sm + K1_XCH);
            const int cg = cb >> 2, cl = 40 * (cb & 3) + lane;
            f32x2* pre = (f32x2*)(ws + WS_PRE) + (size_t)(rb * 4 + cg) * 640 + cl;
            f32x2 s = {0.f, 0.f};
#pragma unroll
            for (int w = 0; w < 4; ++w) { pre[w * 160] = s; s = cfma(L32, s, xc[w * 40 + lane]); }
            ((f32x2*)(ws + WS_AGG))[(cg * 16 + rb) * 160 + cl] = s;
        }
    } else {
        const int bb = q & 1;
        const int trb = 26624 + (8 * q + (t >> 2)) * 192 + (lane & 3) * 8;
        const int tra = trb + bb * 32;
        const int tr5 = trb + (bb ? 0 : 160);
        s16x8 fx[2][2], fw[2][6];
#define M_READ(b, sb, co, ks)                                                                     \
    do {                                                                                          \
        fx[b][0] = *(const s16x8*)((sb) + xoff + (co)); fx[b][1] = *(const s16x8*)((sb) + xoff + 2048 + (co)); \
        _Pragma("unroll") for (int j_ = 0; j_ < 6; ++j_) {                                        \
            const char* pa_ = (sb) + (j_ < 5 ? tra + j_ * 32 : tr5) + (ks) * 32 * 192;            \
            const s16x4 lo_ = __builtin_amdgcn_ds_read_tr16_b64_v4i16((LAS s16x4*)pa_);           \
            const s16x4 hi_ = __builtin_amdgcn_ds_read_tr16_b64_v4i16((LAS s16x4*)(pa_ + 4 * 192)); \
            fw[b][j_] = __builtin_shufflevector(lo_, hi_, 0, 1, 2, 3, 4, 5, 6, 7);                \
        }                                                                                         \
    } while (0)
#define M_BODY(it, MODE)                                                                          \
    do {                                                                                          \
        const char* sb = sm + ((it) & 3) * K1_STAGE;                                              \
        const char* sn = sm + (((it) + 1) & 3) * K1_STAGE;                                        \
        M_READ(1, sb, co1, 1);                                                                    \
        _Pragma("unroll") for (int j = 0; j < 6; ++j) { acc[j][0] = mfma16(fw[0][j], fx[0][0], acc[j][0]); acc[j][1] = mfma16(fw[0][j], fx[0][1], acc[j][1]); } \
        if ((MODE) < 4) {                                                                         \
            SCHED_FENCE();                                                                        \
            WAIT_VM((MODE) < 3 ? 5 : 2);                                                          \
            WAIT_LGKM0();                                                                         \
            __builtin_amdgcn_s_barrier();                                                         \
            SCHED_FENCE();                                                                        \
            M_READ(0, sn, co0, 0);                                                                \
        }                                                                                         \
        _Pragma("unroll") for (int j = 0; j < 6; ++j) {                                           \
            acc[j][0] = mfma16(fw[1][j], fx[1][0], acc[j][0]); acc[j][1] = mfma16(fw[1][j], fx[1][1], acc[j][1]); \
            if ((MODE) < 2 && j < 5) { SCHED_FENCE(); K1_SLOT(j, ((it) + 3) & 3); SCHED_FENCE(); } \
        }                                                                                         \
        if ((MODE) == 2) pretile_dma();                                                           \
    } while (0)
        WAIT_VM(10);
        __builtin_amdgcn_s_barrier();
        M_READ(0, sm, co0, 0);
#pragma unroll 1
        for (int it = 0; it < 6; ++it) M_BODY(it, 0);
        M_BODY(6, 1); M_BODY(7, 2); M_BODY(8, 3); M_BODY(9, 4);
        WAIT_VM(0);
        pretile_out();
        if (tid == 256 && cb < 2) *(unsigned*)(ws + WS_CNT + (rb * 2 + cb) * 256) = 0u;
#pragma unroll
        for (int v = 0; v < 3; ++v) {
#pragma unroll
            for (int rt = 0; rt < 2; ++rt) {
                u32x4 o;
#pragma unroll
                for (int jj = 0; jj < 2; ++jj) {
                    const int j = 2 * v + jj;
                    const f32x4 bv = *(const f32x4*)(sm + K1_B1 + (16 * j + 4 * q) * 4);
                    float r[4];
#pragma unroll
                    for (int i = 0; i < 4; ++i) {
                        const float h = acc[j][rt][i] + bv[i];
                        const float e = __builtin_amdgcn_exp2f(fminf(h, 20.0f) * LOG2E);
                        const float n = e * (e + 2.0f);
                        r[i] = h * n * __builtin_amdgcn_rcpf(n + 2.0f);
                    }
                    o[2 * jj] = pack2(r[0], r[1]);
                    o[2 * jj + 1] = pack2(r[2], r[3]);
                }
                *(u32x4*)(a2 + (size_t)(40 + 3 * cb + v) * 8192 + rt * 1024) = o;
            }
        }
    }
}

constexpr int K2_RED = 0;
constexpr int K2_WV0 = 80 * 1024;
constexpr int K2_LDS = K2_WV0 + 4 * 6144;

__global__ __launch_bounds__(256, 1) void k2(const u16* __restrict__ x, const u16* __restrict__ Lre, const u16* __restrict__ Lim,
                                             const u16* __restrict__ logdt, const u16* __restrict__ Dskip,
                                             char* __restrict__ ws, float* __restrict__ out) {
    __shared__ __attribute__((aligned(16))) char sm[K2_LDS];
    const int tid = threadIdx.x, lane = tid & 63;
    const int wave = __builtin_amdgcn_readfirstlane(tid >> 6);
    const int bid = blockIdx.x, xcd = bid & 7, idx = bid >> 3;
    const int RG = 8 * (xcd >> 1) + (idx & 7);
    const int CB = 4 * (xcd & 1) + (idx >> 3);
    const int t = lane & 15, q = lane >> 4;
    const int rb = RG >> 1, wc0 = 2 * (RG & 1);
    char* raw = sm + K2_WV0 + wave * 6144;
    char* tab = raw + 5120;
    const unsigned rawb = (unsigned)(size_t)(LAS char*)sm + K2_WV0 + wave * 6144;

    const int ksA = 5 * CB + wave, ksB = 5 * CB + 4;
    const size_t fragA = ((size_t)rb * NKS + ksA) * 8192 + (RG & 1) * 4096 + lane * 16;
    const size_t fragB = ((size_t)rb * NKS + ksB) * 8192 + (RG & 1) * 4096 + wave * 1024 + lane * 16;
    u32x4 fx[5];
    u32x2 agv[8], prv[2];
    unsigned lam[3];
    {
        const char* pf = ws + WS_A2 + fragA;
        asm volatile("global_load_dwordx4 %0, %4, off\n\tglobal_load_dwordx4 %1, %4, off offset:1024\n\t"
                     "global_load_dwordx4 %2, %4, off offset:2048\n\tglobal_load_dwordx4 %3, %4, off offset:3072"
                     : "=&v"(fx[0]), "=&v"(fx[1]), "=&v"(fx[2]), "=&v"(fx[3]) : "v"(pf) : "memory");
        fx[4] = ld128_asm(ws + WS_A2 + fragB);
        const int ch = 16 * ((lane & 16) ? ksB : ksA) + t;
        const int cg = ch / 160, cl = ch - 160 * cg;
        const char* ap = ws + WS_AGG + ((size_t)(cg * 16 + 8 * (lane >> 5)) * 160 + cl) * 8;
#pragma unroll
        for (int j = 0; j < 8; ++j) agv[j] = ld64_asm(ap + j * 1280);
        const char* pp = ws + WS_PRE + ((size_t)((rb * 4 + cg) * 4 + wc0) * 160 + cl) * 8;
        prv[0] = ld64_asm(pp); prv[1] = ld64_asm(pp + 1280);
        lam[0] = ld16_asm(Lre + ch); lam[1] = ld16_asm(Lim + ch); lam[2] = ld16_asm(logdt + ch);
        glds16(Dskip + 80 * CB + (lane < 10 ? lane : 0) * 8, rawb);
        const u16* xr = x + (64 * RG + 32 * (wave >> 1) + 2 * t + (wave & 1)) * DM + 80 * CB;
#pragma unroll
        for (int i = 0; i < 3; ++i) glds16(xr + ((4 * i + q) < 10 ? 4 * i + q : 0) * 8, rawb + 1024 + i * 1024);
        glds16(ws + WS_G + (64 * RG + 4 * t) * 4, rawb + 4096);
    }

    const char* pa = ws + WS_A2 + ((size_t)rb * NKS + 40 + 12 * wave) * 8192 + (RG & 1) * 4096 + lane * 16;
    const char* pb = ws + WS_W2 + ((size_t)(CB >> 1) * NKS + 40 + 12 * wave) * 10240 + (CB & 1) * 5120 + 1024 + lane * 16;
    u32x4 Ab[4][4], Bb[4][5];
#define K2_LOAD(slot, SC)                                                                         \
    do {                                                                                          \
        asm volatile("global_load_dwordx4 %0, %4, off" SC "\n\tglobal_load_dwordx4 %1, %4, off offset:1024" SC "\n\t"       \
                     "global_load_dwordx4 %2, %4, off offset:2048" SC "\n\tglobal_load_dwordx4 %3, %4, off offset:3072" SC \
                     : "=&v"(Ab[slot][0]), "=&v"(Ab[slot][1]), "=&v"(Ab[slot][2]), "=&v"(Ab[slot][3]) : "v"(pa) : "memory"); \
        asm volatile("global_load_dwordx4 %0, %5, off offset:-1024\n\tglobal_load_dwordx4 %1, %5, off\n\t"      \
                     "global_load_dwordx4 %2, %5, off offset:1024\n\tglobal_load_dwordx4 %3, %5, off offset:2048\n\t" \
                     "global_load_dwordx4 %4, %5, off offset:3072"                                \
                     : "=&v"(Bb[slot][0]), "=&v"(Bb[slot][1]), "=&v"(Bb[slot][2]), "=&v"(Bb[slot][3]), "=&v"(Bb[slot][4]) : "v"(pb) : "memory"); \
        pa += 8192; pb += 10240;                                                                  \
    } while (0)
#define K2_STEP(u, WN)                                                                            \
    do {                                                                                          \
        asm volatile("s_waitcnt vmcnt(%9)" : "+v"(Ab[u][0]), "+v"(Ab[u][1]), "+v"(Ab[u][2]), "+v"(Ab[u][3]), \
                     "+v"(Bb[u][0]), "+v"(Bb[u][1]), "+v"(Bb[u][2]), "+v"(Bb[u][3]), "+v"(Bb[u][4]) : "n"(WN) : "memory"); \
        _Pragma("unroll") for (int r = 0; r < 4; ++r)                                             \
            _Pragma("unroll") for (int n = 0; n < 5; ++n)                                         \
                acc[r][n] = mfma16(__builtin_bit_cast(s16x8, Bb[u][n]), __builtin_bit_cast(s16x8, Ab[u][r]), acc[r][n]); \
        SCHED_FENCE();                                                                            \
    } while (0)
    K2_LOAD(0, ""); K2_LOAD(1, ""); K2_LOAD(2, "");

    f32x4 acc[4][5];
#pragma unroll
    for (int r = 0; r < 4; ++r)
#pragma unroll
        for (int n = 0; n < 5; ++n) acc[r][n] = (f32x4){0.f, 0.f, 0.f, 0.f};

    GAS unsigned* cnt = (GAS unsigned*)(ws + WS_CNT + RG * 256);
#define K2_WAITS(u, WN)                                                                           \
    asm volatile("s_waitcnt vmcnt(%9)" : "+v"(Ab[u][0]), "+v"(Ab[u][1]), "+v"(Ab[u][2]), "+v"(Ab[u][3]), \
                 "+v"(Bb[u][0]), "+v"(Bb[u][1]), "+v"(Bb[u][2]), "+v"(Bb[u][3]), "+v"(Bb[u][4]) : "n"(WN) : "memory")
#define K2_MMA(u)                                                                                 \
    do {                                                                                          \
        _Pragma("unroll") for (int r = 0; r < 4; ++r)                                             \
            _Pragma("unroll") for (int n = 0; n < 5; ++n)                                         \
                acc[r][n] = mfma16(__builtin_bit_cast(s16x8, Bb[u][n]), __builtin_bit_cast(s16x8, Ab[u][r]), acc[r][n]); \
    } while (0)
    K2_LOAD(3, ""); K2_WAITS(0, 27);
    asm volatile("" : "+v"(fx[0]), "+v"(fx[1]), "+v"(fx[2]), "+v"(fx[3]), "+v"(fx[4]), "+v"(agv[0]), "+v"(agv[1]), "+v"(agv[2]),
                 "+v"(agv[3]), "+v"(agv[4]), "+v"(agv[5]), "+v"(agv[6]), "+v"(agv[7]), "+v"(prv[0]), "+v"(prv[1]), "+v"(lam[0]), "+v"(lam[1]), "+v"(lam[2]));
    K2_MMA(0);
    const f32x2 g01 = *(const f32x2*)(raw + 4096 + 8 * t), g23 = *(const f32x2*)(raw + 4096 + 128 + 8 * t);
    {
        const float lr = bflo(lam[0]), li = bflo(lam[1]), ld = bflo(lam[2]);
        const float dt = __builtin_amdgcn_exp2f(ld * LOG2E);
        const float za = dt * lr * LOG2E, zb = dt * li * INV2PI;
        const f32x2 L1 = lam_bar(lr, li, ld);
        f32x2 Lp = L1;
#pragma unroll
        for (int e = 0; e < 5; ++e) Lp = cmul_pk(Lp, Lp);
        const f32x2 L32 = Lp, L64 = cmul_pk(L32, L32), L128 = cmul_pk(L64, L64);
        f32x2 cy = {0.f, 0.f};
        const int j0 = 8 * (lane >> 5);
#pragma unroll
        for (int j = 0; j < 8; ++j) {
            const f32x2 nx = cmul_pk(L128, cy) + __builtin_bit_cast(f32x2, agv[j]);
            cy = (j0 + j < rb) ? nx : cy;
        }
        f32x2 Lq = {1.f, 0.f};
#pragma unroll
        for (int j = 8; j < 15; ++j) { const f32x2 nx = cmul_pk(Lq, L128); Lq = (j < rb && lane < 32) ? nx : Lq; }
        cy = cmul_pk(cy, Lq);
        cy.x += __shfl_xor(cy.x, 32); cy.y += __shfl_xor(cy.y, 32);
        const f32x2 cy2 = cmul_pk(cy, L64);
        cy = wc0 ? cy2 : cy;
        const f32x2 c0 = __builtin_bit_cast(f32x2, prv[0]) + cy, c1 = cmul_pk(L32, cy) + __builtin_bit_cast(f32x2, prv[1]);
        f32x4* tp = (f32x4*)(tab + (lane & 31) * 32);
        tp[0] = (f32x4){za, zb, L1.x, L1.y};
        tp[1] = (f32x4){c0.x, c0.y, c1.x, c1.y};
    }
    SCHED_FENCE();
    const float fn = (float)(2 * t + 1), fnB = fn + (float)(wave & 1);
    const float gB = 2.0f * (wave == 0 ? g01.x : wave == 1 ? g01.y : wave == 2 ? g23.x : g23.y);
#define K2_FIX(e)                                                                                 \
    do {                                                                                          \
        const char* tp = tab + (2 * q + ((e) & 1) + 8 * ((e) >> 1)) * 32;                         \
        const f32x4 t0 = *(const f32x4*)tp, t1 = *(const f32x4*)(tp + 16);                        \
        const f32x4 u0 = *(const f32x4*)(tp + 512), u1 = *(const f32x4*)(tp + 528);               \
        {                                                                                         \
            const float mg = __builtin_amdgcn_exp2f(fn * t0[0]);                                  \
            const float rv = __builtin_amdgcn_fractf(fn * t0[1]);                                 \
            const f32x2 base = {mg * __builtin_amdgcn_cosf(rv), mg * __builtin_amdgcn_sinf(rv)};  \
            const f32x2 L1 = {t0[2], t0[3]};                                                      \
            _Pragma("unroll") for (int w = 0; w < 2; ++w) {                                       \
                const f32x2 cr = cmul_pk(base, (f32x2){t1[2 * w], t1[2 * w + 1]});                \
                const f32x2 cs = cmul_pk(cr, L1);                                                 \
                const float ga = 2.0f * (w ? g23.x : g01.x), gb = 2.0f * (w ? g23.y : g01.y);     \
                fx[2 * w][e] = pack2((bflo(fx[2 * w][e]) + cr.x) * ga, (bfhi(fx[2 * w][e]) - cr.y) * ga); \
                fx[2 * w + 1][e] = pack2((bflo(fx[2 * w + 1][e]) + cs.x) * gb, (bfhi(fx[2 * w + 1][e]) - cs.y) * gb); \
            }                                                                                     \
        }                                                                                         \
        {                                                                                         \
            const float mg = __builtin_amdgcn_exp2f(fnB * u0[0]);                                 \
            const float rv = __builtin_amdgcn_fractf(fnB * u0[1]);                                \
            const f32x2 base = {mg * __builtin_amdgcn_cosf(rv), mg * __builtin_amdgcn_sinf(rv)};  \
            const f32x2 cyB = (wave >> 1) ? (f32x2){u1[2], u1[3]} : (f32x2){u1[0], u1[1]};        \
            const f32x2 cr = cmul_pk(base, cyB);                                                  \
            fx[4][e] = pack2((bflo(fx[4][e]) + cr.x) * gB, (bfhi(fx[4][e]) - cr.y) * gB);         \
        }                                                                                         \
    } while (0)
    K2_LOAD(0, ""); K2_WAITS(1, 27); K2_MMA(1); K2_FIX(0); K2_FIX(1); SCHED_FENCE();
    K2_LOAD(1, ""); K2_WAITS(2, 27); K2_MMA(2); K2_FIX(2); K2_FIX(3); SCHED_FENCE();
    {
        char* da = ws + WS_A2C + ((size_t)rb * 40 + ksA) * 8192 + (RG & 1) * 4096 + lane * 16;
        char* db = ws + WS_A2C + ((size_t)rb * 40 + ksB) * 8192 + (RG & 1) * 4096 + wave * 1024 + lane * 16;
        asm volatile("global_store_dwordx4 %0, %2, off sc1\n\tglobal_store_dwordx4 %0, %3, off offset:1024 sc1\n\t"
                     "global_store_dwordx4 %0, %4, off offset:2048 sc1\n\tglobal_store_dwordx4 %0, %5, off offset:3072 sc1\n\t"
                     "global_store_dwordx4 %1, %6, off sc1\n\ts_nop 1"
                     :: "v"(da), "v"(db), "v"(fx[0]), "v"(fx[1]), "v"(fx[2]), "v"(fx[3]), "v"(fx[4]) : "memory");
    }
    K2_LOAD(2, ""); K2_STEP(3, 32);
    K2_LOAD(3, ""); K2_STEP(0, 18);
    if (lane == 0) __hip_atomic_fetch_add(cnt, 1u, __ATOMIC_RELAXED, __HIP_MEMORY_SCOPE_AGENT);
    K2_LOAD(0, ""); K2_STEP(1, 28);
    K2_LOAD(1, ""); K2_STEP(2, 28);
    K2_LOAD(2, ""); K2_STEP(3, 28);
    K2_LOAD(3, ""); K2_STEP(0, 27);
    if (tid == 0) {
        unsigned spins = 0;
        while (__hip_atomic_load(cnt, __ATOMIC_RELAXED, __HIP_MEMORY_SCOPE_AGENT) < 32u) {
            __builtin_amdgcn_s_sleep(1);
            if (++spins > (1u << 22)) break;
        }
    }
    asm volatile("" ::: "memory");
    __builtin_amdgcn_s_barrier();
    asm volatile("" ::: "memory");
    pa = ws + WS_A2C + ((size_t)rb * 40 + 10 * wave) * 8192 + (RG & 1) * 4096 + lane * 16;
    pb -= (size_t)(52 + 2 * wave) * 10240;
    K2_LOAD(0, " sc1"); K2_STEP(1, 27);
    K2_LOAD(1, " sc1"); K2_STEP(2, 27);
    K2_LOAD(2, " sc1"); K2_STEP(3, 27);
#pragma unroll
    for (int r = 0; r < 4; ++r) {
        const float g1 = 1.0f - (r == 0 ? g01.x : r == 1 ? g01.y : r == 2 ? g23.x : g23.y);
#pragma unroll
        for (int n = 0; n < 5; ++n) acc[r][n] *= g1;
    }
    K2_LOAD(3, " sc1"); K2_STEP(0, 27);
    K2_LOAD(0, " sc1"); K2_STEP(1, 27);
    K2_LOAD(1, " sc1"); K2_STEP(2, 27);
    K2_LOAD(2, " sc1"); K2_STEP(3, 27);
    K2_LOAD(3, " sc1"); K2_STEP(0, 27);
    K2_LOAD(0, " sc1"); K2_STEP(1, 27);
    K2_LOAD(1, " sc1"); K2_STEP(2, 27);
    K2_STEP(3, 18); K2_STEP(0, 9); K2_STEP(1, 0);

#pragma unroll
    for (int r = 0; r < 4; ++r)
#pragma unroll
        for (int n = 0; n < 5; ++n) *(f32x4*)(sm + K2_RED + ((r * 4 + wave) * 5 + n) * 1024 + lane * 16) = acc[r][n];
    __syncthreads();
    const int tm = 64 * RG + 32 * (wave >> 1) + 2 * t + (wave & 1);
    const float gme = wave == 0 ? g01.x : wave == 1 ? g01.y : wave == 2 ? g23.x : g23.y;
#pragma unroll
    for (int n = 0; n < 5; ++n) {
        const u32x2 dv = *(const u32x2*)(raw + (16 * n + 4 * q) * 2);
        const u32x2 xv = *(const u32x2*)(raw + 1024 + (2 * n + (q >> 1)) * 256 + t * 16 + (q & 1) * 8);
        f32x4 v = *(const f32x4*)(sm + K2_RED + (((wave * 4) * 5 + n) * 64 + lane) * 16);
#pragma unroll
        for (int k = 1; k < 4; ++k) v += *(const f32x4*)(sm + K2_RED + (((wave * 4 + k) * 5 + n) * 64 + lane) * 16);
        v[0] += gme * bflo(xv[0]) * bflo(dv[0]);
        v[1] += gme * bfhi(xv[0]) * bfhi(dv[0]);
        v[2] += gme * bflo(xv[1]) * bflo(dv[1]);
        v[3] += gme * bfhi(xv[1]) * bfhi(dv[1]);
        *(f32x4*)(out + (size_t)tm * DM + 80 * CB + 16 * n + 4 * q) = v;
    }
}

extern "C" void kernel_launch(void* const* d_in, const int* in_sizes, int n_in, void* d_out, int out_size, void* d_ws,
                              size_t ws_size, hipStream_t stream) {
    const u16* x = (const u16*)d_in[0];
    const u16* Lre = (const u16*)d_in[1];
    const u16* Lim = (const u16*)d_in[2];
    const u16* Bre = (const u16*)d_in[3];
    const u16* Bim = (const u16*)d_in[4];
    const u16* Cre = (const u16*)d_in[5];
    const u16* Cim = (const u16*)d_in[6];
    const u16* logdt = (const u16*)d_in[7];
    const u16* Dskip = (const u16*)d_in[8];
    const u16* w1 = (const u16*)d_in[10];
    const u16* b1 = (const u16*)d_in[11];
    const u16* w2 = (const u16*)d_in[12];
    const u16* gw = (const u16*)d_in[13];
    k1<<<256, 512, 0, stream>>>(x, Lre, Lim, Bre, Bim, Cre, Cim, logdt, w1, b1, w2, gw, (char*)d_ws);
    k2<<<256, 256, 0, stream>>>(x, Lre, Lim, logdt, Dskip, (char*)d_ws, (float*)d_out);
}
```

```cpp
#include <hip/hip_runtime.h>
#include <stdint.h>

typedef short s16x4 __attribute__((ext_vector_type(4)));
typedef short s16x8 __attribute__((ext_vector_type(8)));
typedef __bf16 bf16x2 __attribute__((ext_vector_type(2)));
typedef float f32x2 __attribute__((ext_vector_type(2)));
typedef float f32x4 __attribute__((ext_vector_type(4)));
typedef unsigned u32x2 __attribute__((ext_vector_type(2)));
typedef unsigned u32x4 __attribute__((ext_vector_type(4)));
typedef unsigned short u16;
#define LAS __attribute__((address_space(3)))
#define GAS __attribute__((address_space(1)))
#define DI __device__ __forceinline__

constexpr int SEQ = 2048, DM = 640, NP = 640, NF = 1536;
constexpr int NKS = 88;

constexpr size_t WS_A2 = 0;
constexpr size_t WS_W2 = WS_A2 + (size_t)16 * NKS * 8192;
constexpr size_t WS_G = WS_W2 + (size_t)4 * NKS * 10240;
constexpr size_t WS_AGG = WS_G + 2048 * 4;
constexpr size_t WS_PRE = WS_AGG + 16 * 640 * 8;
constexpr size_t WS_CNT = WS_PRE + 16 * 4 * 640 * 8;
constexpr size_t WS_A2C = WS_CNT + 64 * 256;

DI float bflo(unsigned v) { return __uint_as_float(v << 16); }
DI float bfhi(unsigned v) { return __uint_as_float(v & 0xffff0000u); }
DI unsigned pack2(float a, float b) {
    f32x2 p = {a, b};
    return __builtin_bit_cast(unsigned, __builtin_convertvector(p, bf16x2));
}
DI f32x4 mfma16(s16x8 a, s16x8 b, f32x4 c) { return __builtin_amdgcn_mfma_f32_16x16x32_bf16(a, b, c, 0, 0, 0); }
template <int S> DI float shr_zero(float v) {
    return __builtin_bit_cast(float, __builtin_amdgcn_update_dpp(0, __builtin_bit_cast(int, v), 0x110 + S, 0xf, 0xf, true));
}
DI f32x2 cmul(f32x2 a, f32x2 b) { return (f32x2){a.x * b.x - a.y * b.y, a.x * b.y + a.y * b.x}; }
DI f32x2 cfma(f32x2 a, f32x2 b, f32x2 c) {
    return (f32x2){fmaf(-a.y, b.y, fmaf(a.x, b.x, c.x)), fmaf(a.y, b.x, fmaf(a.x, b.y, c.y))};
}
DI f32x2 cmul_pk(f32x2 a, f32x2 b) { return (f32x2){fmaf(-a.y, b.y, a.x * b.x), fmaf(a.y, b.x, a.x * b.y)}; }
DI f32x2 cmul_sc(f32x2 a, f32x2 b) {
    float p = a.x * b.x, r = a.x * b.y;
    asm("" : "+v"(p));
    asm("" : "+v"(r));
    return (f32x2){fmaf(-a.y, b.y, p), fmaf(a.y, b.x, r)};
}
DI float dot2bf(unsigned a, unsigned b, float c) {
    return __builtin_amdgcn_fdot2_f32_bf16(__builtin_bit_cast(bf16x2, a), __builtin_bit_cast(bf16x2, b), c, false);
}
#define WAIT_VM(n) asm volatile("s_waitcnt vmcnt(%0)" ::"n"(n) : "memory")
#define WAIT_LGKM0() asm volatile("s_waitcnt lgkmcnt(0)" ::: "memory")
#define SCHED_FENCE() __builtin_amdgcn_sched_barrier(0)

DI void glds16(const void* gsrc, unsigned lds_dst) {
    unsigned keep;
    asm volatile("s_mov_b32 %0, m0\n\ts_mov_b32 m0, %2\n\ts_nop 0\n\tglobal_load_lds_dwordx4 %1, off\n\ts_mov_b32 m0, %0"
                 : "=&s"(keep) : "v"(gsrc), "s"(lds_dst) : "memory");
}
DI void glds16x4(const void* gsrc, unsigned lds_dst) {
    unsigned keep;
    asm volatile("s_mov_b32 %0, m0\n\ts_mov_b32 m0, %2\n\ts_nop 0\n\tglobal_load_lds_dwordx4 %1, off\n\t"
                 "global_load_lds_dwordx4 %1, off offset:1024\n\tglobal_load_lds_dwordx4 %1, off offset:2048\n\t"
                 "global_load_lds_dwordx4 %1, off offset:3072\n\ts_mov_b32 m0, %0"
                 : "=&s"(keep) : "v"(gsrc), "s"(lds_dst) : "memory");
}
DI void st128_wt(void* p, u32x4 v) { asm volatile("global_store_dwordx4 %0, %1, off sc1\n\ts_nop 1" :: "v"(p), "v"(v) : "memory"); }
DI void st64_wt(void* p, u32x2 v) { asm volatile("global_store_dwordx2 %0, %1, off sc1\n\ts_nop 1" :: "v"(p), "v"(v) : "memory"); }
DI unsigned ld32_asm(const void* p) { unsigned r; asm volatile("global_load_dword %0, %1, off" : "=v"(r) : "v"(p) : "memory"); return r; }
DI unsigned ld16_asm(const void* p) { unsigned r; asm volatile("global_load_ushort %0, %1, off" : "=v"(r) : "v"(p) : "memory"); return r; }
DI u32x2 ld64_asm(const void* p) { u32x2 r; asm volatile("global_load_dwordx2 %0, %1, off" : "=v"(r) : "v"(p) : "memory"); return r; }
DI u32x4 ld128_asm(const void* p) { u32x4 r; asm volatile("global_load_dwordx4 %0, %1, off" : "=v"(r) : "v"(p) : "memory"); return r; }
constexpr float LOG2E = 1.44269504088896341f, INV2PI = 0.15915494309189535f;
DI f32x2 lam_bar(float lr, float li, float ld) {
    const float dt = __builtin_amdgcn_exp2f(ld * LOG2E);
    const float m = __builtin_amdgcn_exp2f(dt * lr * LOG2E);
    const float rv = __builtin_amdgcn_fractf(dt * li * INV2PI);
    return (f32x2){m * __builtin_amdgcn_cosf(rv), m * __builtin_amdgcn_sinf(rv)};
}

constexpr int K1_STAGE = 38 * 1024;
constexpr int K1_GW = 4 * K1_STAGE;
constexpr int K1_PLR = K1_GW + 1280, K1_PLI = K1_GW + 1360, K1_PLD = K1_GW + 1440, K1_PB1 = K1_GW + 1536;
constexpr int K1_TAB = K1_GW + 2048;
constexpr int K1_XCH = K1_TAB + 2560;
constexpr int K1_CNT = K1_XCH + 1280;
constexpr int K1_B1 = K1_CNT + 16;
constexpr int K1_LDS = K1_B1 + 384;

__global__ __launch_bounds__(512, 2) void k1(const u16* __restrict__ x, const u16* __restrict__ Lre, const u16* __restrict__ Lim,
                                             const u16* __restrict__ Bre, const u16* __restrict__ Bim, const u16* __restrict__ Cre,
                                             const u16* __restrict__ Cim, const u16* __restrict__ logdt, const u16* __restrict__ w1,
                                             const u16* __restrict__ b1, const u16* __restrict__ w2, const u16* __restrict__ gw,
                                             char* __restrict__ ws) {
    __shared__ __attribute__((aligned(16))) char sm[K1_LDS];
    const int tid = threadIdx.x, lane = tid & 63;
    const int wave = __builtin_amdgcn_readfirstlane(tid >> 6);
    const int bid = blockIdx.x, xcd = bid & 7, idx = bid >> 3;
    const int rb = 8 * (xcd & 1) + (idx & 7);
    const int cb = 4 * (xcd >> 1) + (idx >> 3);
    const int t = lane & 15, q = lane >> 4;
    const unsigned smb = (unsigned)(size_t)(LAS char*)sm;
    const char* src[5];
    unsigned ldo[5];
    int incE, strE;
    {
        const int l8 = lane >> 3;
        auto xsrc = [&](int s) {
            const int p = 8 * s + l8;
            const int tm = 128 * rb + 32 * (p >> 5) + 2 * (p & 15) + ((p >> 4) & 1);
            return (const char*)(x + tm * DM + (((lane & 7) ^ ((p >> 1) & 7)) << 3));
        };
        auto wsrc = [&](int s) {
            const int p = 8 * s + l8;
            const int ch = 40 * cb + 8 * (p >> 4) + ((p & 15) >> 1);
            return (const char*)(((p & 1) ? Bim : Bre) + ch * DM + (((lane & 7) ^ ((p >> 1) & 7)) << 3));
        };
        auto msrc = [&](int s) {
            const int gI = 64 * s + lane, row = gI / 12, cp = gI - 12 * row;
            int j = (cp >> 1) - ((row >> 3) & 1);
            j += j < 0 ? 6 : 0;
            return (const char*)(w1 + row * NF + 96 * cb + 16 * j + 8 * (cp & 1));
        };
        src[0] = xsrc(2 * wave); ldo[0] = smb + 2048 * wave;
        src[1] = xsrc(2 * wave + 1); ldo[1] = smb + 2048 * wave + 1024;
        src[2] = wsrc(wave); ldo[2] = smb + 16384 + 1024 * wave;
        src[3] = msrc(wave); ldo[3] = smb + 26624 + 1024 * wave;
        if (wave < 2) { src[4] = wsrc(8 + wave); ldo[4] = smb + 16384 + 1024 * (8 + wave); incE = 128; strE = K1_STAGE; }
        else if (wave < 6) { src[4] = msrc(6 + wave); ldo[4] = smb + 26624 + 1024 * (6 + wave); incE = 64 * NF * 2; strE = K1_STAGE; }
        else {
            const u16* p = gw + lane * 8;
            if (wave == 7) {
                p = gw + 512 + t * 8;
                if (lane >= 16 && lane < 21) p = Lre + 40 * cb + (lane - 16) * 8;
                if (lane >= 21 && lane < 26) p = Lim + 40 * cb + (lane - 21) * 8;
                if (lane >= 26 && lane < 31) p = logdt + 40 * cb + (lane - 26) * 8;
                if (lane >= 32 && lane < 44) p = b1 + 96 * cb + (lane - 32) * 8;
            }
            src[4] = (const char*)p; ldo[4] = smb + K1_GW + (wave - 6) * 1024; incE = 0; strE = 0;
        }
    }
#define K1_SLOT(i, buf)                                                                  \
    do {                                                                                 \
        glds16(src[i], ldo[i] + (buf) * ((i) == 4 ? strE : K1_STAGE));                   \
        src[i] += (i) < 3 ? 128 : (i) == 3 ? 64 * NF * 2 : incE;                         \
    } while (0)
#pragma unroll
    for (int st = 0; st < 3; ++st) { K1_SLOT(0, st); K1_SLOT(1, st); K1_SLOT(2, st); K1_SLOT(3, st); K1_SLOT(4, st); }
    if (tid == 0) *(unsigned*)(sm + K1_CNT) = 0u;

    const int wc = wave & 3;
    const int xoff = (32 * wc + t) * 128;
    const int co0 = (q ^ ((t >> 1) & 7)) * 16, co1 = co0 ^ 64;
    f32x4 acc[6][2];
#pragma unroll
    for (int a = 0; a < 6; ++a) { acc[a][0] = (f32x4){0.f, 0.f, 0.f, 0.f}; acc[a][1] = (f32x4){0.f, 0.f, 0.f, 0.f}; }
    char* a2 = ws + WS_A2 + (size_t)rb * NKS * 8192 + (2 * wc) * 1024 + lane * 16;

    const int wgid = bid * 8 + wave;
    char* ptl = sm + 2 * K1_STAGE + wave * 2048;
    auto pretile_dma = [&]() {
        const int KS = wgid / 40, cn = wgid - 40 * KS;
        const u16* p0 = (KS < 40) ? ((lane >> 5) ? Cim : Cre) + (16 * cn + ((lane >> 1) & 15)) * NP + 16 * KS + 8 * (lane & 1)
                                  : w2 + (32 * (KS - 40) + (lane >> 1)) * DM + 16 * cn + 8 * (lane & 1);
        glds16(p0, smb + 2 * K1_STAGE + wave * 2048);
        const int f1 = wgid + 2048 < 3520 ? wgid + 2048 : 1600;
        const int KS1 = f1 / 40, cn1 = f1 - 40 * KS1;
        glds16(w2 + (32 * (KS1 - 40) + (lane >> 1)) * DM + 16 * cn1 + 8 * (lane & 1), smb + 2 * K1_STAGE + wave * 2048 + 1024);
    };
    auto pretile_out = [&]() {
        const int tro = (4 * q + (t >> 2)) * 32 + (lane & 3) * 8;
        const int KS = wgid / 40, cn = wgid - 40 * KS;
        u32x4 wf;
        if (KS < 40) {
            const unsigned r0 = *(const unsigned*)(ptl + t * 32 + 4 * q), r1 = *(const unsigned*)(ptl + t * 32 + 16 + 4 * q);
            const unsigned i0 = *(const unsigned*)(ptl + 512 + t * 32 + 4 * q), i1 = *(const unsigned*)(ptl + 512 + t * 32 + 16 + 4 * q);
            wf[0] = (r0 & 0xffffu) | (i0 << 16); wf[1] = (r0 >> 16) | (i0 & 0xffff0000u);
            wf[2] = (r1 & 0xffffu) | (i1 << 16); wf[3] = (r1 >> 16) | (i1 & 0xffff0000u);
        } else {
            const s16x4 lo = __builtin_amdgcn_ds_read_tr16_b64_v4i16((LAS s16x4*)(ptl + tro));
            const s16x4 hi = __builtin_amdgcn_ds_read_tr16_b64_v4i16((LAS s16x4*)(ptl + tro + 512));
            wf = __builtin_bit_cast(u32x4, __builtin_shufflevector(lo, hi, 0, 1, 2, 3, 4, 5, 6, 7));
        }
        st128_wt(ws + WS_W2 + ((size_t)(((cn / 10) * NKS + KS) * 10 + cn % 10) * 64 + lane) * 16, wf);
        const s16x4 lo = __builtin_amdgcn_ds_read_tr16_b64_v4i16((LAS s16x4*)(ptl + 1024 + tro));
        const s16x4 hi = __builtin_amdgcn_ds_read_tr16_b64_v4i16((LAS s16x4*)(ptl + 1024 + tro + 512));
        if (wgid + 2048 < 3520) {
            const int f = wgid + 2048, KS1 = f / 40, cn1 = f - 40 * KS1;
            st128_wt(ws + WS_W2 + ((size_t)(((cn1 / 10) * NKS + KS1) * 10 + cn1 % 10) * 64 + lane) * 16, __builtin_bit_cast(u32x4, __builtin_shufflevector(lo, hi, 0, 1, 2, 3, 4, 5, 6, 7)));
        }
    };
    auto side_tables = [&]() {
        if (wave == 0) {
            if (lane < 40) {
                const float lr = bflo(*(const u16*)(sm + K1_PLR + lane * 2)), li = bflo(*(const u16*)(sm + K1_PLI + lane * 2));
                f32x2 Lp = lam_bar(lr, li, bflo(*(const u16*)(sm + K1_PLD + lane * 2)));
                f32x2* tb = (f32x2*)(sm + K1_TAB + lane * 64);
                const float nr = Lp.x - 1.0f, ni = Lp.y;
                const float rden = 1.0f / (lr * lr + li * li);
                tb[6] = (f32x2){(nr * lr + ni * li) * rden, (ni * lr - nr * li) * rden};
#pragma unroll
                for (int e = 0; e < 6; ++e) { tb[e] = Lp; Lp = cmul(Lp, Lp); }
            }
        } else if (wave == 1) {
            if (lane < 48) {
                const unsigned v = *(const unsigned*)(sm + K1_PB1 + lane * 4);
                *(f32x2*)(sm + K1_B1 + lane * 8) = (f32x2){bflo(v), bfhi(v)};
            }
        }
    };

    if (wave < 4) {
        const bool do_gate = (cb == 0);
        float gs0 = 0.f, gs1 = 0.f;
        s16x8 fx[2][2], fw[2][5];
        const int woff = 16384 + t * 128;
#define S_READ(b, sb, co)                                                                         \
    do {                                                                                          \
        fx[b][0] = *(const s16x8*)((sb) + xoff + (co)); fx[b][1] = *(const s16x8*)((sb) + xoff + 2048 + (co)); \
        _Pragma("unroll") for (int T_ = 0; T_ < 5; ++T_) fw[b][T_] = *(const s16x8*)((sb) + woff + T_ * 2048 + (co)); \
    } while (0)
#define S_GATE(b, kk)                                                                             \
    do {                                                                                          \
        if (do_gate) {                                                                            \
            const u32x4 gv = *(const u32x4*)(sm + K1_GW + ((kk) + 8 * q) * 2);                    \
            const u32x4 xa = __builtin_bit_cast(u32x4, fx[b][0]), xb = __builtin_bit_cast(u32x4, fx[b][1]); \
            _Pragma("unroll") for (int e = 0; e < 4; ++e) { gs0 = dot2bf(xa[e], gv[e], gs0); gs1 = dot2bf(xb[e], gv[e], gs1); } \
        }                                                                                         \
    } while (0)
#define S_BODY(it, MODE)                                                                          \
    do {                                                                                          \
        const char* sb = sm + ((it) & 3) * K1_STAGE;                                              \
        const char* sn = sm + (((it) + 1) & 3) * K1_STAGE;                                        \
        S_READ(1, sb, co1);                                                                       \
        _Pragma("unroll") for (int T = 0; T < 5; ++T) { acc[T][0] = mfma16(fw[0][T], fx[0][0], acc[T][0]); acc[T][1] = mfma16(fw[0][T], fx[0][1], acc[T][1]); } \
        S_GATE(0, 64 * (it));                                                                     \
        if ((MODE) == 1) side_tables();                                                           \
        if ((MODE) < 4) {                                                                         \
            SCHED_FENCE();                                                                        \
            WAIT_VM((MODE) < 3 ? 5 : 2);                                                          \
            WAIT_LGKM0();                                                                         \
            __builtin_amdgcn_s_barrier();                                                         \
            SCHED_FENCE();                                                                        \
            S_READ(0, sn, co0);                                                                   \
        }                                                                                         \
        _Pragma("unroll") for (int T = 0; T < 5; ++T) {                                           \
            acc[T][0] = mfma16(fw[1][T], fx[1][0], acc[T][0]); acc[T][1] = mfma16(fw[1][T], fx[1][1], acc[T][1]); \
            if ((MODE) < 2) { SCHED_FENCE(); K1_SLOT(T, ((it) + 3) & 3); SCHED_FENCE(); }         \
        }                                                                                         \
        if ((MODE) == 2) pretile_dma();                                                           \
        S_GATE(1, 64 * (it) + 32);                                                                \
    } while (0)
        WAIT_VM(10);
        WAIT_LGKM0();
        __builtin_amdgcn_s_barrier();
        S_READ(0, sm, co0);
#pragma unroll 1
        for (int it = 0; it < 6; ++it) S_BODY(it, 0);
          S_BODY(6, 1);   S_BODY(7, 2);   S_BODY(8, 3);   S_BODY(9, 4);
        WAIT_VM(0);
        pretile_out();
        if (do_gate) {
            gs0 += __shfl_xor(gs0, 16); gs0 += __shfl_xor(gs0, 32);
            gs1 += __shfl_xor(gs1, 16); gs1 += __shfl_xor(gs1, 32);
            if (q == 0) {
                f32x2 gg = {1.0f / (1.0f + __expf(-gs0)), 1.0f / (1.0f + __expf(-gs1))};
                *(f32x2*)(ws + WS_G + (128 * rb + 32 * wc + 2 * t) * 4) = gg;
            }
        }
#pragma unroll
        for (int T = 0; T < 5; ++T) {
            u32x2 o0, o1;
#pragma unroll
            for (int cc = 0; cc < 2; ++cc) {
                const int chl = 8 * T + 2 * q + cc;
                const f32x4* tb = (const f32x4*)(sm + K1_TAB + chl * 64);
                const f32x4 t0 = tb[0], t1 = tb[1], t2 = tb[2], t3 = tb[3];
                const f32x2 L1 = {t0[0], t0[1]}, L2 = {t0[2], t0[3]}, L4 = {t1[0], t1[1]}, L8 = {t1[2], t1[3]}, L16 = {t2[0], t2[1]};
                const f32x2 cf = {t3[0], t3[1]};
                const f32x2 u0 = cmul_sc(cf, (f32x2){acc[T][0][2 * cc], acc[T][0][2 * cc + 1]});
                const f32x2 u1 = cmul_sc(cf, (f32x2){acc[T][1][2 * cc], acc[T][1][2 * cc + 1]});
                f32x2 a = cfma(L1, u0, u1);
                a = cfma(L2, (f32x2){shr_zero<1>(a.x), shr_zero<1>(a.y)}, a);
                a = cfma(L4, (f32x2){shr_zero<2>(a.x), shr_zero<2>(a.y)}, a);
                a = cfma(L8, (f32x2){shr_zero<4>(a.x), shr_zero<4>(a.y)}, a);
                a = cfma(L16, (f32x2){shr_zero<8>(a.x), shr_zero<8>(a.y)}, a);
                const f32x2 h0 = cfma(L1, (f32x2){shr_zero<1>(a.x), shr_zero<1>(a.y)}, u0);
                o0[cc] = pack2(h0.x, -h0.y);
                o1[cc] = pack2(a.x, -a.y);
                if (t == 15) *(f32x2*)(sm + K1_XCH + (wc * 40 + chl) * 8) = a;
            }
            const int Tg = 5 * cb + T;
            char* dst = a2 + (size_t)(Tg >> 1) * 8192 + (Tg & 1) * 8;
            st64_wt(dst, o0);
            st64_wt(dst + 1024, o1);
        }
        WAIT_LGKM0();
        unsigned old = 0;
        if (lane == 0) old = __hip_atomic_fetch_add((LAS unsigned*)(sm + K1_CNT), 1u, __ATOMIC_RELAXED, __HIP_MEMORY_SCOPE_WORKGROUP);
        old = __builtin_amdgcn_readfirstlane(old);
        asm volatile("" ::: "memory");
        if (old == 3 && lane < 40) {
            const f32x2 L32 = *(const f32x2*)(sm + K1_TAB + lane * 64 + 40);
            const f32x2* xc = (const f32x2*)(sm + K1_XCH);
            const int cg = cb >> 2, cl = 40 * (cb & 3) + lane;
            f32x2* pre = (f32x2*)(ws + WS_PRE) + (size_t)(rb * 4 + cg) * 640 + cl;
            f32x2 s = {0.f, 0.f};
#pragma unroll
            for (int w = 0; w < 4; ++w) { pre[w * 160] = s; s = cfma(L32, s, xc[w * 40 + lane]); }
            ((f32x2*)(ws + WS_AGG))[(cg * 16 + rb) * 160 + cl] = s;
        }
    } else {
        const int bb = q & 1;
        const int trb = 26624 + (8 * q + (t >> 2)) * 192 + (lane & 3) * 8;
        const int tra = trb + bb * 32;
        const int tr5 = trb + (bb ? 0 : 160);
        s16x8 fx[2][2], fw[2][6];
#define M_READ(b, sb, co, ks)                                                                     \
    do {                                                                                          \
        fx[b][0] = *(const s16x8*)((sb) + xoff + (co)); fx[b][1] = *(const s16x8*)((sb) + xoff + 2048 + (co)); \
        _Pragma("unroll") for (int j_ = 0; j_ < 6; ++j_) {                                        \
            const char* pa_ = (sb) + (j_ < 5 ? tra + j_ * 32 : tr5) + (ks) * 32 * 192;            \
            const s16x4 lo_ = __builtin_amdgcn_ds_read_tr16_b64_v4i16((LAS s16x4*)pa_);           \
            const s16x4 hi_ = __builtin_amdgcn_ds_read_tr16_b64_v4i16((LAS s16x4*)(pa_ + 4 * 192)); \
            fw[b][j_] = __builtin_shufflevector(lo_, hi_, 0, 1, 2, 3, 4, 5, 6, 7);                \
        }                                                                                         \
    } while (0)
#define M_BODY(it, MODE)                                                                          \
    do {                                                                                          \
        const char* sb = sm + ((it) & 3) * K1_STAGE;                                              \
        const char* sn = sm + (((it) + 1) & 3) * K1_STAGE;                                        \
        M_READ(1, sb, co1, 1);                                                                    \
        _Pragma("unroll") for (int j = 0; j < 6; ++j) { acc[j][0] = mfma16(fw[0][j], fx[0][0], acc[j][0]); acc[j][1] = mfma16(fw[0][j], fx[0][1], acc[j][1]); } \
        if ((MODE) < 4) {                                                                         \
            SCHED_FENCE();                                                                        \
            WAIT_VM((MODE) < 3 ? 5 : 2);                                                          \
            WAIT_LGKM0();                                                                         \
            __builtin_amdgcn_s_barrier();                                                         \
            SCHED_FENCE();                                                                        \
            M_READ(0, sn, co0, 0);                                                                \
        }                                                                                         \
        _Pragma("unroll") for (int j = 0; j < 6; ++j) {                                           \
            acc[j][0] = mfma16(fw[1][j], fx[1][0], acc[j][0]); acc[j][1] = mfma16(fw[1][j], fx[1][1], acc[j][1]); \
            if ((MODE) < 2 && j < 5) { SCHED_FENCE(); K1_SLOT(j, ((it) + 3) & 3); SCHED_FENCE(); } \
        }                                                                                         \
        if ((MODE) == 2) pretile_dma();                                                           \
    } while (0)
        WAIT_VM(10);
        __builtin_amdgcn_s_barrier();
        M_READ(0, sm, co0, 0);
#pragma unroll 1
        for (int it = 0; it < 6; ++it) M_BODY(it, 0);
          M_BODY(6, 1);   M_BODY(7, 2);   M_BODY(8, 3);   M_BODY(9, 4);
        WAIT_VM(0);
        pretile_out();
        if (tid == 256 && cb < 2) *(unsigned*)(ws + WS_CNT + (rb * 2 + cb) * 256) = 0u;
#pragma unroll
        for (int v = 0; v < 3; ++v) {
#pragma unroll
            for (int rt = 0; rt < 2; ++rt) {
                u32x4 o;
#pragma unroll
                for (int jj = 0; jj < 2; ++jj) {
                    const int j = 2 * v + jj;
                    const f32x4 bv = *(const f32x4*)(sm + K1_B1 + (16 * j + 4 * q) * 4);
                    float r[4];
#pragma unroll
                    for (int i = 0; i < 4; ++i) {
                        const float h = acc[j][rt][i] + bv[i];
                        const float e = __builtin_amdgcn_exp2f(fminf(h, 20.0f) * LOG2E);
                        const float n = e * (e + 2.0f);
                        r[i] = h * n * __builtin_amdgcn_rcpf(n + 2.0f);
                    }
                    o[2 * jj] = pack2(r[0], r[1]);
                    o[2 * jj + 1] = pack2(r[2], r[3]);
                }
                st128_wt(a2 + (size_t)(40 + 3 * cb + v) * 8192 + rt * 1024, o);
            }
        }
    }
}

constexpr int K2_RED = 0;
constexpr int K2_WV0 = 80 * 1024;
constexpr int K2_LDS = K2_WV0 + 4 * 6144;

__global__ __launch_bounds__(256, 1) void k2(const u16* __restrict__ x, const u16* __restrict__ Lre, const u16* __restrict__ Lim,
                                             const u16* __restrict__ logdt, const u16* __restrict__ Dskip,
                                             char* __restrict__ ws, float* __restrict__ out) {
    __shared__ __attribute__((aligned(16))) char sm[K2_LDS];
    const int tid = threadIdx.x, lane = tid & 63;
    const int wave = __builtin_amdgcn_readfirstlane(tid >> 6);
    const int bid = blockIdx.x, xcd = bid & 7, idx = bid >> 3;
    const int RG = 8 * (xcd >> 1) + (idx & 7);
    const int CB = 4 * (xcd & 1) + (idx >> 3);
    const int t = lane & 15, q = lane >> 4;
    const int rb = RG >> 1, wc0 = 2 * (RG & 1);
    char* raw = sm + K2_WV0 + wave * 6144;
    char* tab = raw + 5120;
    const unsigned rawb = (unsigned)(size_t)(LAS char*)sm + K2_WV0 + wave * 6144;

    const int ksA = 5 * CB + wave, ksB = 5 * CB + 4;
    const size_t fragA = ((size_t)rb * NKS + ksA) * 8192 + (RG & 1) * 4096 + lane * 16;
    const size_t fragB = ((size_t)rb * NKS + ksB) * 8192 + (RG & 1) * 4096 + wave * 1024 + lane * 16;
    u32x4 fx[5];
    u32x2 agv[8], prv[2];
    unsigned lam[3];
    {
        const char* pf = ws + WS_A2 + fragA;
        asm volatile("global_load_dwordx4 %0, %4, off\n\tglobal_load_dwordx4 %1, %4, off offset:1024\n\t"
                     "global_load_dwordx4 %2, %4, off offset:2048\n\tglobal_load_dwordx4 %3, %4, off offset:3072"
                     : "=&v"(fx[0]), "=&v"(fx[1]), "=&v"(fx[2]), "=&v"(fx[3]) : "v"(pf) : "memory");
        fx[4] = ld128_asm(ws + WS_A2 + fragB);
        const int ch = 16 * ((lane & 16) ? ksB : ksA) + t;
        const int cg = ch / 160, cl = ch - 160 * cg;
        const char* ap = ws + WS_AGG + ((size_t)(cg * 16 + 8 * (lane >> 5)) * 160 + cl) * 8;
#pragma unroll
        for (int j = 0; j < 8; ++j) agv[j] = ld64_asm(ap + j * 1280);
        const char* pp = ws + WS_PRE + ((size_t)((rb * 4 + cg) * 4 + wc0) * 160 + cl) * 8;
        prv[0] = ld64_asm(pp); prv[1] = ld64_asm(pp + 1280);
        lam[0] = ld16_asm(Lre + ch); lam[1] = ld16_asm(Lim + ch); lam[2] = ld16_asm(logdt + ch);
        glds16(Dskip + 80 * CB + (lane < 10 ? lane : 0) * 8, rawb);
        const u16* xr = x + (64 * RG + 32 * (wave >> 1) + 2 * t + (wave & 1)) * DM + 80 * CB;
#pragma unroll
        for (int i = 0; i < 3; ++i) glds16(xr + ((4 * i + q) < 10 ? 4 * i + q : 0) * 8, rawb + 1024 + i * 1024);
        glds16(ws + WS_G + (64 * RG + 4 * t) * 4, rawb + 4096);
    }

    const char* pa = ws + WS_A2 + ((size_t)rb * NKS + 40 + 12 * wave) * 8192 + (RG & 1) * 4096 + lane * 16;
    const char* pb = ws + WS_W2 + ((size_t)(CB >> 1) * NKS + 40 + 12 * wave) * 10240 + (CB & 1) * 5120 + 1024 + lane * 16;
    u32x4 Ab[4][4], Bb[4][5];
#define K2_LOAD(slot, SC)                                                                         \
    do {                                                                                          \
        asm volatile("global_load_dwordx4 %0, %4, off" SC "\n\tglobal_load_dwordx4 %1, %4, off offset:1024" SC "\n\t"       \
                     "global_load_dwordx4 %2, %4, off offset:2048" SC "\n\tglobal_load_dwordx4 %3, %4, off offset:3072" SC \
                     : "=&v"(Ab[slot][0]), "=&v"(Ab[slot][1]), "=&v"(Ab[slot][2]), "=&v"(Ab[slot][3]) : "v"(pa) : "memory"); \
        asm volatile("global_load_dwordx4 %0, %5, off offset:-1024\n\tglobal_load_dwordx4 %1, %5, off\n\t"      \
                     "global_load_dwordx4 %2, %5, off offset:1024\n\tglobal_load_dwordx4 %3, %5, off offset:2048\n\t" \
                     "global_load_dwordx4 %4, %5, off offset:3072"                                \
                     : "=&v"(Bb[slot][0]), "=&v"(Bb[slot][1]), "=&v"(Bb[slot][2]), "=&v"(Bb[slot][3]), "=&v"(Bb[slot][4]) : "v"(pb) : "memory"); \
        pa += 8192; pb += 10240;                                                                  \
    } while (0)
#define K2_STEP(u, WN)                                                                            \
    do {                                                                                          \
        asm volatile("s_waitcnt vmcnt(%9)" : "+v"(Ab[u][0]), "+v"(Ab[u][1]), "+v"(Ab[u][2]), "+v"(Ab[u][3]), \
                     "+v"(Bb[u][0]), "+v"(Bb[u][1]), "+v"(Bb[u][2]), "+v"(Bb[u][3]), "+v"(Bb[u][4]) : "n"(WN) : "memory"); \
        _Pragma("unroll") for (int r = 0; r < 4; ++r)                                             \
            _Pragma("unroll") for (int n = 0; n < 5; ++n)                                         \
                acc[r][n] = mfma16(__builtin_bit_cast(s16x8, Bb[u][n]), __builtin_bit_cast(s16x8, Ab[u][r]), acc[r][n]); \
        SCHED_FENCE();                                                                            \
    } while (0)
#define K2_L1(dst, P, OFF, SC) asm volatile("global_load_dwordx4 %0, %1, off offset:" #OFF SC : "=&v"(dst) : "v"(P) : "memory")
#define K2_M2(u, i)                                                                               \
    do {                                                                                          \
        acc[(i) / 5][(i) % 5] = mfma16(__builtin_bit_cast(s16x8, Bb[u][(i) % 5]), __builtin_bit_cast(s16x8, Ab[u][(i) / 5]), acc[(i) / 5][(i) % 5]); \
        acc[((i) + 1) / 5][((i) + 1) % 5] = mfma16(__builtin_bit_cast(s16x8, Bb[u][((i) + 1) % 5]), __builtin_bit_cast(s16x8, Ab[u][((i) + 1) / 5]), acc[((i) + 1) / 5][((i) + 1) % 5]); \
        SCHED_FENCE();                                                                            \
    } while (0)
#define K2_ISTEP(u, ls, SC)                                                                       \
    do {                                                                                          \
        SCHED_FENCE();                                                                            \
        K2_M2(u, 0); K2_L1(Ab[ls][0], pa, 0, SC); SCHED_FENCE();                                  \
        K2_M2(u, 2); K2_L1(Ab[ls][1], pa, 1024, SC); SCHED_FENCE();                               \
        K2_M2(u, 4); K2_L1(Ab[ls][2], pa, 2048, SC); SCHED_FENCE();                               \
        K2_M2(u, 6); K2_L1(Ab[ls][3], pa, 3072, SC); SCHED_FENCE();                               \
        K2_M2(u, 8); K2_L1(Bb[ls][0], pb, -1024, ""); SCHED_FENCE();                              \
        K2_M2(u, 10); K2_L1(Bb[ls][1], pb, 0, ""); SCHED_FENCE();                                 \
        K2_M2(u, 12); K2_L1(Bb[ls][2], pb, 1024, ""); SCHED_FENCE();                              \
        K2_M2(u, 14); K2_L1(Bb[ls][3], pb, 2048, ""); SCHED_FENCE();                              \
        K2_M2(u, 16); K2_L1(Bb[ls][4], pb, 3072, ""); SCHED_FENCE();                              \
        K2_M2(u, 18);                                                                             \
        pa += 8192; pb += 10240;                                                                  \
    } while (0)
    K2_LOAD(0, ""); K2_LOAD(1, ""); K2_LOAD(2, "");

    f32x4 acc[4][5];
#pragma unroll
    for (int r = 0; r < 4; ++r)
#pragma unroll
        for (int n = 0; n < 5; ++n) acc[r][n] = (f32x4){0.f, 0.f, 0.f, 0.f};

    GAS unsigned* cnt = (GAS unsigned*)(ws + WS_CNT + RG * 256);
#define K2_WAITS(u, WN)                                                                           \
    asm volatile("s_waitcnt vmcnt(%9)" : "+v"(Ab[u][0]), "+v"(Ab[u][1]), "+v"(Ab[u][2]), "+v"(Ab[u][3]), \
                 "+v"(Bb[u][0]), "+v"(Bb[u][1]), "+v"(Bb[u][2]), "+v"(Bb[u][3]), "+v"(Bb[u][4]) : "n"(WN) : "memory")
#define K2_MMA(u)                                                                                 \
    do {                                                                                          \
        _Pragma("unroll") for (int r = 0; r < 4; ++r)                                             \
            _Pragma("unroll") for (int n = 0; n < 5; ++n)                                         \
                acc[r][n] = mfma16(__builtin_bit_cast(s16x8, Bb[u][n]), __builtin_bit_cast(s16x8, Ab[u][r]), acc[r][n]); \
    } while (0)
    K2_LOAD(3, ""); K2_WAITS(0, 27);
    asm volatile("" : "+v"(fx[0]), "+v"(fx[1]), "+v"(fx[2]), "+v"(fx[3]), "+v"(fx[4]), "+v"(agv[0]), "+v"(agv[1]), "+v"(agv[2]),
                 "+v"(agv[3]), "+v"(agv[4]), "+v"(agv[5]), "+v"(agv[6]), "+v"(agv[7]), "+v"(prv[0]), "+v"(prv[1]), "+v"(lam[0]), "+v"(lam[1]), "+v"(lam[2]));
    K2_MMA(0);
    const f32x2 g01 = *(const f32x2*)(raw + 4096 + 8 * t), g23 = *(const f32x2*)(raw + 4096 + 128 + 8 * t);
    {
        const float lr = bflo(lam[0]), li = bflo(lam[1]), ld = bflo(lam[2]);
        const float dt = __builtin_amdgcn_exp2f(ld * LOG2E);
        const float za = dt * lr * LOG2E, zb = dt * li * INV2PI;
        const f32x2 L1 = lam_bar(lr, li, ld);
        f32x2 Lp = L1;
#pragma unroll
        for (int e = 0; e < 5; ++e) Lp = cmul_pk(Lp, Lp);
        const f32x2 L32 = Lp, L64 = cmul_pk(L32, L32), L128 = cmul_pk(L64, L64);
        f32x2 cy = {0.f, 0.f};
        const int j0 = 8 * (lane >> 5);
#pragma unroll
        for (int j = 0; j < 8; ++j) {
            const f32x2 nx = cmul_pk(L128, cy) + __builtin_bit_cast(f32x2, agv[j]);
            cy = (j0 + j < rb) ? nx : cy;
        }
        f32x2 Lq = {1.f, 0.f};
#pragma unroll
        for (int j = 8; j < 15; ++j) { const f32x2 nx = cmul_pk(Lq, L128); Lq = (j < rb && lane < 32) ? nx : Lq; }
        cy = cmul_pk(cy, Lq);
        cy.x += __shfl_xor(cy.x, 32); cy.y += __shfl_xor(cy.y, 32);
        const f32x2 cy2 = cmul_pk(cy, L64);
        cy = wc0 ? cy2 : cy;
        const f32x2 c0 = __builtin_bit_cast(f32x2, prv[0]) + cy, c1 = cmul_pk(L32, cy) + __builtin_bit_cast(f32x2, prv[1]);
        f32x4* tp = (f32x4*)(tab + (lane & 31) * 32);
        tp[0] = (f32x4){za, zb, L1.x, L1.y};
        tp[1] = (f32x4){c0.x, c0.y, c1.x, c1.y};
    }
    SCHED_FENCE();
    const float fn = (float)(2 * t + 1), fnB = fn + (float)(wave & 1);
    const float gB = 2.0f * (wave == 0 ? g01.x : wave == 1 ? g01.y : wave == 2 ? g23.x : g23.y);
#define K2_FIX(e)                                                                                 \
    do {                                                                                          \
        const char* tp = tab + (2 * q + ((e) & 1) + 8 * ((e) >> 1)) * 32;                         \
        const f32x4 t0 = *(const f32x4*)tp, t1 = *(const f32x4*)(tp + 16);                        \
        const f32x4 u0 = *(const f32x4*)(tp + 512), u1 = *(const f32x4*)(tp + 528);               \
        {                                                                                         \
            const float mg = __builtin_amdgcn_exp2f(fn * t0[0]);                                  \
            const float rv = __builtin_amdgcn_fractf(fn * t0[1]);                                 \
            const f32x2 base = {mg * __builtin_amdgcn_cosf(rv), mg * __builtin_amdgcn_sinf(rv)};  \
            const f32x2 L1 = {t0[2], t0[3]};                                                      \
            _Pragma("unroll") for (int w = 0; w < 2; ++w) {                                       \
                const f32x2 cr = cmul_pk(base, (f32x2){t1[2 * w], t1[2 * w + 1]});                \
                const f32x2 cs = cmul_pk(cr, L1);                                                 \
                const float ga = 2.0f * (w ? g23.x : g01.x), gb = 2.0f * (w ? g23.y : g01.y);     \
                fx[2 * w][e] = pack2((bflo(fx[2 * w][e]) + cr.x) * ga, (bfhi(fx[2 * w][e]) - cr.y) * ga); \
                fx[2 * w + 1][e] = pack2((bflo(fx[2 * w + 1][e]) + cs.x) * gb, (bfhi(fx[2 * w + 1][e]) - cs.y) * gb); \
            }                                                                                     \
        }                                                                                         \
        {                                                                                         \
            const float mg = __builtin_amdgcn_exp2f(fnB * u0[0]);                                 \
            const float rv = __builtin_amdgcn_fractf(fnB * u0[1]);                                \
            const f32x2 base = {mg * __builtin_amdgcn_cosf(rv), mg * __builtin_amdgcn_sinf(rv)};  \
            const f32x2 cyB = (wave >> 1) ? (f32x2){u1[2], u1[3]} : (f32x2){u1[0], u1[1]};        \
            const f32x2 cr = cmul_pk(base, cyB);                                                  \
            fx[4][e] = pack2((bflo(fx[4][e]) + cr.x) * gB, (bfhi(fx[4][e]) - cr.y) * gB);         \
        }                                                                                         \
    } while (0)
    K2_LOAD(0, ""); K2_WAITS(1, 27); K2_MMA(1); K2_FIX(0); K2_FIX(1); SCHED_FENCE();
    K2_LOAD(1, ""); K2_WAITS(2, 27); K2_MMA(2); K2_FIX(2); K2_FIX(3); SCHED_FENCE();
    {
        char* da = ws + WS_A2C + ((size_t)rb * 40 + ksA) * 8192 + (RG & 1) * 4096 + lane * 16;
        char* db = ws + WS_A2C + ((size_t)rb * 40 + ksB) * 8192 + (RG & 1) * 4096 + wave * 1024 + lane * 16;
        asm volatile("global_store_dwordx4 %0, %2, off sc1\n\tglobal_store_dwordx4 %0, %3, off offset:1024 sc1\n\t"
                     "global_store_dwordx4 %0, %4, off offset:2048 sc1\n\tglobal_store_dwordx4 %0, %5, off offset:3072 sc1\n\t"
                     "global_store_dwordx4 %1, %6, off sc1\n\ts_nop 1"
                     :: "v"(da), "v"(db), "v"(fx[0]), "v"(fx[1]), "v"(fx[2]), "v"(fx[3]), "v"(fx[4]) : "memory");
    }
    K2_WAITS(3, 23); K2_ISTEP(3, 2, "");
    K2_WAITS(0, 23); K2_ISTEP(0, 3, "");
    K2_WAITS(1, 18);
    if (lane == 0) __hip_atomic_fetch_add(cnt, 1u, __ATOMIC_RELAXED, __HIP_MEMORY_SCOPE_AGENT);
    K2_ISTEP(1, 0, "");
    K2_WAITS(2, 19); K2_ISTEP(2, 1, "");
    K2_WAITS(3, 19); K2_ISTEP(3, 2, "");
    K2_WAITS(0, 18); K2_ISTEP(0, 3, "");
    if (tid == 0) {
        unsigned spins = 0;
        while (__hip_atomic_load(cnt, __ATOMIC_RELAXED, __HIP_MEMORY_SCOPE_AGENT) < 32u) {
            __builtin_amdgcn_s_sleep(1);
            if (++spins > (1u << 22)) break;
        }
    }
    asm volatile("" ::: "memory");
    __builtin_amdgcn_s_barrier();
    asm volatile("" ::: "memory");
    pa = ws + WS_A2C + ((size_t)rb * 40 + 10 * wave) * 8192 + (RG & 1) * 4096 + lane * 16;
    pb -= (size_t)(52 + 2 * wave) * 10240;
    K2_WAITS(1, 18); K2_ISTEP(1, 0, " sc1");
    K2_WAITS(2, 18); K2_ISTEP(2, 1, " sc1");
    K2_WAITS(3, 18); K2_ISTEP(3, 2, " sc1");
#pragma unroll
    for (int r = 0; r < 4; ++r) {
        const float g1 = 1.0f - (r == 0 ? g01.x : r == 1 ? g01.y : r == 2 ? g23.x : g23.y);
#pragma unroll
        for (int n = 0; n < 5; ++n) acc[r][n] *= g1;
    }
    K2_WAITS(0, 18); K2_ISTEP(0, 3, " sc1");
    K2_WAITS(1, 18); K2_ISTEP(1, 0, " sc1");
    K2_WAITS(2, 18); K2_ISTEP(2, 1, " sc1");
    K2_WAITS(3, 18); K2_ISTEP(3, 2, " sc1");
    K2_WAITS(0, 18); K2_ISTEP(0, 3, " sc1");
    K2_WAITS(1, 18); K2_ISTEP(1, 0, " sc1");
    K2_WAITS(2, 18); K2_ISTEP(2, 1, " sc1");
    K2_STEP(3, 18); K2_STEP(0, 9); K2_STEP(1, 0);

#pragma unroll
    for (int r = 0; r < 4; ++r)
#pragma unroll
        for (int n = 0; n < 5; ++n) *(f32x4*)(sm + K2_RED + ((r * 4 + wave) * 5 + n) * 1024 + lane * 16) = acc[r][n];
    __syncthreads();
    const int tm = 64 * RG + 32 * (wave >> 1) + 2 * t + (wave & 1);
    const float gme = wave == 0 ? g01.x : wave == 1 ? g01.y : wave == 2 ? g23.x : g23.y;
#pragma unroll
    for (int n = 0; n < 5; ++n) {
        const u32x2 dv = *(const u32x2*)(raw + (16 * n + 4 * q) * 2);
        const u32x2 xv = *(const u32x2*)(raw + 1024 + (2 * n + (q >> 1)) * 256 + t * 16 + (q & 1) * 8);
        f32x4 v = *(const f32x4*)(sm + K2_RED + (((wave * 4) * 5 + n) * 64 + lane) * 16);
#pragma unroll
        for (int k = 1; k < 4; ++k) v += *(const f32x4*)(sm + K2_RED + (((wave * 4 + k) * 5 + n) * 64 + lane) * 16);
        v[0] += gme * bflo(xv[0]) * bflo(dv[0]);
        v[1] += gme * bfhi(xv[0]) * bfhi(dv[0]);
        v[2] += gme * bflo(xv[1]) * bflo(dv[1]);
        v[3] += gme * bfhi(xv[1]) * bfhi(dv[1]);
        st128_wt(out + (size_t)tm * DM + 80 * CB + 16 * n + 4 * q, __builtin_bit_cast(u32x4, v));
    }
}

extern "C" void kernel_launch(void* const* d_in, const int* in_sizes, int n_in, void* d_out, int out_size, void* d_ws,
                              size_t ws_size, hipStream_t stream) {
    const u16* x = (const u16*)d_in[0];
    const u16* Lre = (const u16*)d_in[1];
    const u16* Lim = (const u16*)d_in[2];
    const u16* Bre = (const u16*)d_in[3];
    const u16* Bim = (const u16*)d_in[4];
    const u16* Cre = (const u16*)d_in[5];
    const u16* Cim = (const u16*)d_in[6];
    const u16* logdt = (const u16*)d_in[7];
    const u16* Dskip = (const u16*)d_in[8];
    const u16* w1 = (const u16*)d_in[10];
    const u16* b1 = (const u16*)d_in[11];
    const u16* w2 = (const u16*)d_in[12];
    const u16* gw = (const u16*)d_in[13];
    k1<<<256, 512, 0, stream>>>(x, Lre, Lim, Bre, Bim, Cre, Cim, logdt, w1, b1, w2, gw, (char*)d_ws);
    k2<<<256, 256, 0, stream>>>(x, Lre, Lim, logdt, Dskip, (char*)d_ws, (float*)d_out);
}
```

```cpp
#include <hip/hip_runtime.h>
#include <stdint.h>

typedef short s16x4 __attribute__((ext_vector_type(4)));
typedef short s16x8 __attribute__((ext_vector_type(8)));
typedef __bf16 bf16x2 __attribute__((ext_vector_type(2)));
typedef float f32x2 __attribute__((ext_vector_type(2)));
typedef float f32x4 __attribute__((ext_vector_type(4)));
typedef unsigned u32x2 __attribute__((ext_vector_type(2)));
typedef unsigned u32x4 __attribute__((ext_vector_type(4)));
typedef unsigned short u16;
#define LAS __attribute__((address_space(3)))
#define GAS __attribute__((address_space(1)))
#define DI __device__ __forceinline__

constexpr int SEQ = 2048, DM = 640, NP = 640, NF = 1536;
constexpr int NKS = 88;

constexpr size_t WS_A2 = 0;
constexpr size_t WS_W2 = WS_A2 + (size_t)16 * NKS * 8192;
constexpr size_t WS_G = WS_W2 + (size_t)4 * NKS * 10240;
constexpr size_t WS_AGG = WS_G + 2048 * 4;
constexpr size_t WS_PRE = WS_AGG + 16 * 640 * 8;
constexpr size_t WS_CNT = WS_PRE + 16 * 4 * 640 * 8;
constexpr size_t WS_JUNK = WS_CNT + 64 * 256;
constexpr size_t WS_A2C = WS_JUNK + 1024;

DI float bflo(unsigned v) { return __uint_as_float(v << 16); }
DI float bfhi(unsigned v) { return __uint_as_float(v & 0xffff0000u); }
DI unsigned pack2(float a, float b) {
    f32x2 p = {a, b};
    return __builtin_bit_cast(unsigned, __builtin_convertvector(p, bf16x2));
}
DI f32x4 mfma16(s16x8 a, s16x8 b, f32x4 c) { return __builtin_amdgcn_mfma_f32_16x16x32_bf16(a, b, c, 0, 0, 0); }
template <int S> DI float shr_zero(float v) {
    return __builtin_bit_cast(float, __builtin_amdgcn_update_dpp(0, __builtin_bit_cast(int, v), 0x110 + S, 0xf, 0xf, true));
}
DI f32x2 cmul(f32x2 a, f32x2 b) { return (f32x2){a.x * b.x - a.y * b.y, a.x * b.y + a.y * b.x}; }
DI f32x2 cfma(f32x2 a, f32x2 b, f32x2 c) {
    return (f32x2){fmaf(-a.y, b.y, fmaf(a.x, b.x, c.x)), fmaf(a.y, b.x, fmaf(a.x, b.y, c.y))};
}
DI f32x2 cmul_pk(f32x2 a, f32x2 b) { return (f32x2){fmaf(-a.y, b.y, a.x * b.x), fmaf(a.y, b.x, a.x * b.y)}; }
DI f32x2 cmul_sc(f32x2 a, f32x2 b) {
    float p = a.x * b.x, r = a.x * b.y;
    asm("" : "+v"(p));
    asm("" : "+v"(r));
    return (f32x2){fmaf(-a.y, b.y, p), fmaf(a.y, b.x, r)};
}
DI float dot2bf(unsigned a, unsigned b, float c) {
    return __builtin_amdgcn_fdot2_f32_bf16(__builtin_bit_cast(bf16x2, a), __builtin_bit_cast(bf16x2, b), c, false);
}
#define WAIT_VM(n) asm volatile("s_waitcnt vmcnt(%0)" ::"n"(n) : "memory")
#define WAIT_LGKM0() asm volatile("s_waitcnt lgkmcnt(0)" ::: "memory")
#define SCHED_FENCE() __builtin_amdgcn_sched_barrier(0)

DI void glds16(const void* gsrc, unsigned lds_dst) {
    unsigned keep;
    asm volatile("s_mov_b32 %0, m0\n\ts_mov_b32 m0, %2\n\ts_nop 0\n\tglobal_load_lds_dwordx4 %1, off\n\ts_mov_b32 m0, %0"
                 : "=&s"(keep) : "v"(gsrc), "s"(lds_dst) : "memory");
}
DI void glds16x4(const void* gsrc, unsigned lds_dst) {
    unsigned keep;
    asm volatile("s_mov_b32 %0, m0\n\ts_mov_b32 m0, %2\n\ts_nop 0\n\tglobal_load_lds_dwordx4 %1, off\n\t"
                 "global_load_lds_dwordx4 %1, off offset:1024\n\tglobal_load_lds_dwordx4 %1, off offset:2048\n\t"
                 "global_load_lds_dwordx4 %1, off offset:3072\n\ts_mov_b32 m0, %0"
                 : "=&s"(keep) : "v"(gsrc), "s"(lds_dst) : "memory");
}
DI void st128_wt(void* p, u32x4 v) { asm volatile("global_store_dwordx4 %0, %1, off sc1\n\ts_nop 1" :: "v"(p), "v"(v) : "memory"); }
DI void st64_wt(void* p, u32x2 v) { asm volatile("global_store_dwordx2 %0, %1, off sc1\n\ts_nop 1" :: "v"(p), "v"(v) : "memory"); }
DI unsigned ld32_asm(const void* p) { unsigned r; asm volatile("global_load_dword %0, %1, off" : "=v"(r) : "v"(p) : "memory"); return r; }
DI unsigned ld16_asm(const void* p) { unsigned r; asm volatile("global_load_ushort %0, %1, off" : "=v"(r) : "v"(p) : "memory"); return r; }
DI u32x2 ld64_asm(const void* p) { u32x2 r; asm volatile("global_load_dwordx2 %0, %1, off" : "=v"(r) : "v"(p) : "memory"); return r; }
DI u32x4 ld128_asm(const void* p) { u32x4 r; asm volatile("global_load_dwordx4 %0, %1, off" : "=v"(r) : "v"(p) : "memory"); return r; }
constexpr float LOG2E = 1.44269504088896341f, INV2PI = 0.15915494309189535f;
DI f32x2 lam_bar(float lr, float li, float ld) {
    const float dt = __builtin_amdgcn_exp2f(ld * LOG2E);
    const float m = __builtin_amdgcn_exp2f(dt * lr * LOG2E);
    const float rv = __builtin_amdgcn_fractf(dt * li * INV2PI);
    return (f32x2){m * __builtin_amdgcn_cosf(rv), m * __builtin_amdgcn_sinf(rv)};
}

constexpr int K1_STAGE = 38 * 1024;
constexpr int K1_GW = 4 * K1_STAGE;
constexpr int K1_PLR = K1_GW + 1280, K1_PLI = K1_GW + 1360, K1_PLD = K1_GW + 1440, K1_PB1 = K1_GW + 1536;
constexpr int K1_TAB = K1_GW + 2048;
constexpr int K1_XCH = K1_TAB + 2560;
constexpr int K1_CNT = K1_XCH + 1280;
constexpr int K1_PT = K1_CNT + 16;
constexpr int K1_LDS = K1_PT + 2048;

__global__ __launch_bounds__(768, 3) void k1(const u16* __restrict__ x, const u16* __restrict__ Lre, const u16* __restrict__ Lim,
                                             const u16* __restrict__ Bre, const u16* __restrict__ Bim, const u16* __restrict__ Cre,
                                             const u16* __restrict__ Cim, const u16* __restrict__ logdt, const u16* __restrict__ w1,
                                             const u16* __restrict__ b1, const u16* __restrict__ w2, const u16* __restrict__ gw,
                                             char* __restrict__ ws) {
    __shared__ __attribute__((aligned(16))) char sm[K1_LDS];
    const int tid = threadIdx.x, lane = tid & 63;
    const int wave = __builtin_amdgcn_readfirstlane(tid >> 6);
    const int bid = blockIdx.x, xcd = bid & 7, idx = bid >> 3;
    const int rb = 8 * (xcd & 1) + (idx & 7);
    const int cb = 4 * (xcd >> 1) + (idx >> 3);
    const int t = lane & 15, q = lane >> 4;
    const unsigned smb = (unsigned)(size_t)(LAS char*)sm;

    if (wave >= 8) {
        const int ld = wave - 8;
        const int l8 = lane >> 3;
        const char* src[10];
#pragma unroll
        for (int i = 0; i < 4; ++i) {
            const int tm = 128 * rb + 32 * ld + 2 * (8 * (i & 1) + l8) + (i >> 1);
            src[i] = (const char*)(x + tm * DM + (((lane & 7) ^ (4 * (i & 1) + (l8 >> 1))) << 3));
        }
#pragma unroll
        for (int i = 0; i < 3; ++i) {
            const int gI = 64 * (3 * ld + i) + lane, row = gI / 12, cp = gI - 12 * row;
            int j = (cp >> 1) - ((row >> 3) & 1);
            j += j < 0 ? 6 : 0;
            src[4 + i] = (const char*)(w1 + row * NF + 96 * cb + 16 * j + 8 * (cp & 1));
        }
        {
            const u16* wb = ((l8 & 1) ? Bim : Bre) + (40 * cb + 4 * ld + (l8 >> 1)) * DM + (((lane & 7) ^ (4 * (ld & 1) + (l8 >> 1))) << 3);
#pragma unroll
            for (int i = 0; i < 3; ++i) src[7 + i] = (const char*)(wb + (ld + 4 * i < 10 ? 16 * i : 0) * DM);
        }
        const unsigned lx = smb + 4096 * ld, lm = smb + 26624 + 3072 * ld, lw = smb + 16384 + 1024 * ld;
#define K1_GROUP(buf, N)                                                                          \
    do {                                                                                          \
        _Pragma("unroll") for (int i_ = 0; i_ < (N); ++i_) {                                      \
            glds16(src[i_], (i_ < 4 ? lx + i_ * 1024 : i_ < 7 ? lm + (i_ - 4) * 1024 : lw + (i_ - 7) * 4096) + (buf) * K1_STAGE); \
            src[i_] += (i_ >= 4 && i_ < 7) ? 64 * NF * 2 : 128;                                   \
        }                                                                                         \
    } while (0)
        if (ld < 2) {
            K1_GROUP(0, 10); K1_GROUP(1, 10);
            if (tid == 512) *(unsigned*)(sm + K1_CNT) = 0u;
            WAIT_VM(10);
            WAIT_LGKM0();
            __builtin_amdgcn_s_barrier();
            K1_GROUP(2, 10); K1_GROUP(3, 10);
            WAIT_VM(20);
            __builtin_amdgcn_s_barrier();
#pragma unroll 1
            for (int it = 0; it < 6; ++it) {
                K1_GROUP(it & 3, 10);
                if (it == 1 && ld == 0 && lane < 40) {
                    const float lr = bflo(*(const u16*)(sm + K1_PLR + lane * 2)), li = bflo(*(const u16*)(sm + K1_PLI + lane * 2));
                    f32x2 Lp = lam_bar(lr, li, bflo(*(const u16*)(sm + K1_PLD + lane * 2)));
                    f32x2* tb = (f32x2*)(sm + K1_TAB + lane * 64);
                    const float nr = Lp.x - 1.0f, ni = Lp.y;
                    const float rden = 1.0f / (lr * lr + li * li);
                    tb[6] = (f32x2){(nr * lr + ni * li) * rden, (ni * lr - nr * li) * rden};
#pragma unroll
                    for (int e = 0; e < 6; ++e) { tb[e] = Lp; Lp = cmul(Lp, Lp); }
                }
                WAIT_VM(20);
                WAIT_LGKM0();
                __builtin_amdgcn_s_barrier();
            }
            WAIT_VM(10);
            __builtin_amdgcn_s_barrier();
            WAIT_VM(0);
            __builtin_amdgcn_s_barrier();
            if (tid == 576 && cb < 2) *(unsigned*)(ws + WS_CNT + (rb * 2 + cb) * 256) = 0u;
            return;
        }
        K1_GROUP(0, 9);
        {
            const u16* p = gw + lane * 8;
            if (ld == 3) {
                p = gw + 512 + t * 8;
                if (lane >= 16 && lane < 21) p = Lre + 40 * cb + (lane - 16) * 8;
                if (lane >= 21 && lane < 26) p = Lim + 40 * cb + (lane - 21) * 8;
                if (lane >= 26 && lane < 31) p = logdt + 40 * cb + (lane - 26) * 8;
                if (lane >= 32 && lane < 44) p = b1 + 96 * cb + (lane - 32) * 8;
            }
            glds16(p, smb + K1_GW + (ld - 2) * 1024);
        }
        K1_GROUP(1, 9);
        WAIT_VM(9);
        __builtin_amdgcn_s_barrier();
        K1_GROUP(2, 9); K1_GROUP(3, 9);
        WAIT_VM(18);
        __builtin_amdgcn_s_barrier();
#pragma unroll 1
        for (int it = 0; it < 6; ++it) {
            K1_GROUP(it & 3, 9);
            WAIT_VM(18);
            __builtin_amdgcn_s_barrier();
        }
        const int f0 = 14 * bid + 7 * (ld - 2);
        u32x4 pv[7];
#pragma unroll
        for (int i = 0; i < 7; ++i) {
            const int f = f0 + i < 3520 ? f0 + i : 0;
            const int KS = f / 40, cn = f - 40 * KS;
            const u16* p0 = (KS < 40) ? ((lane >> 5) ? Cim : Cre) + (16 * cn + ((lane >> 1) & 15)) * NP + 16 * KS + 8 * (lane & 1)
                                      : w2 + (32 * (KS - 40) + (lane >> 1)) * DM + 16 * cn + 8 * (lane & 1);
            pv[i] = ld128_asm(p0);
        }
        WAIT_VM(16);
        __builtin_amdgcn_s_barrier();
        WAIT_VM(7);
        __builtin_amdgcn_s_barrier();
        asm volatile("s_waitcnt vmcnt(0)" : "+v"(pv[0]), "+v"(pv[1]), "+v"(pv[2]), "+v"(pv[3]), "+v"(pv[4]), "+v"(pv[5]), "+v"(pv[6]) :: "memory");
        char* ptl = sm + K1_PT + (ld - 2) * 1024;
        const int tro = (4 * q + (t >> 2)) * 32 + (lane & 3) * 8;
#pragma unroll
        for (int i = 0; i < 7; ++i) {
            const int f = f0 + i;
            const int KS = f / 40, cn = f - 40 * KS;
            *(u32x4*)(ptl + lane * 16) = pv[i];
            WAIT_LGKM0();
            u32x4 wf;
            if (KS < 40) {
                const unsigned r0 = *(const unsigned*)(ptl + t * 32 + 4 * q), r1 = *(const unsigned*)(ptl + t * 32 + 16 + 4 * q);
                const unsigned i0 = *(const unsigned*)(ptl + 512 + t * 32 + 4 * q), i1 = *(const unsigned*)(ptl + 512 + t * 32 + 16 + 4 * q);
                wf[0] = (r0 & 0xffffu) | (i0 << 16); wf[1] = (r0 >> 16) | (i0 & 0xffff0000u);
                wf[2] = (r1 & 0xffffu) | (i1 << 16); wf[3] = (r1 >> 16) | (i1 & 0xffff0000u);
            } else {
                const s16x4 lo = __builtin_amdgcn_ds_read_tr16_b64_v4i16((LAS s16x4*)(ptl + tro));
                const s16x4 hi = __builtin_amdgcn_ds_read_tr16_b64_v4i16((LAS s16x4*)(ptl + tro + 512));
                wf = __builtin_bit_cast(u32x4, __builtin_shufflevector(lo, hi, 0, 1, 2, 3, 4, 5, 6, 7));
            }
            if (f < 3520) st128_wt(ws + WS_W2 + (size_t)(((cn / 10) * NKS + KS) * 10 + cn % 10) * 1024 + lane * 16, wf);
            WAIT_LGKM0();
        }
        return;
    }

    const int wc = wave & 3;
    const int xoff = (32 * wc + t) * 128;
    const int co0 = (q ^ ((t >> 1) & 7)) * 16, co1 = co0 ^ 64;
    f32x4 acc[6][2];
#pragma unroll
    for (int a = 0; a < 6; ++a) { acc[a][0] = (f32x4){0.f, 0.f, 0.f, 0.f}; acc[a][1] = (f32x4){0.f, 0.f, 0.f, 0.f}; }
    char* a2 = ws + WS_A2 + (size_t)rb * NKS * 8192 + (2 * wc) * 1024 + lane * 16;

    if (wave < 4) {
        const bool do_gate = (cb == 0);
        s16x8 fx[2][2], fw[2][5];
        const int woff = 16384 + t * 128;
#define S_READ(b, sb, co)                                                                         \
    do {                                                                                          \
        fx[b][0] = *(const s16x8*)((sb) + xoff + (co)); fx[b][1] = *(const s16x8*)((sb) + xoff + 2048 + (co)); \
        _Pragma("unroll") for (int T_ = 0; T_ < 5; ++T_) fw[b][T_] = *(const s16x8*)((sb) + woff + T_ * 2048 + (co)); \
    } while (0)
#define S_GATE(b, kk)                                                                             \
    do {                                                                                          \
        if (do_gate) {                                                                            \
            const s16x8 gv = *(const s16x8*)(sm + K1_GW + ((kk) + 8 * q) * 2);                    \
            acc[5][0] = mfma16(gv, fx[b][0], acc[5][0]); acc[5][1] = mfma16(gv, fx[b][1], acc[5][1]); \
        }                                                                                         \
    } while (0)
#define S_HALF(c, n, sb, co, RD)                                                                  \
    do {                                                                                          \
        _Pragma("unroll") for (int T = 0; T < 5; ++T) {                                           \
            acc[T][0] = mfma16(fw[c][T], fx[c][0], acc[T][0]); acc[T][1] = mfma16(fw[c][T], fx[c][1], acc[T][1]); \
            if (RD) {                                                                             \
                SCHED_FENCE();                                                                    \
                if (T == 0) { fx[n][0] = *(const s16x8*)((sb) + xoff + (co)); fx[n][1] = *(const s16x8*)((sb) + xoff + 2048 + (co)); fw[n][0] = *(const s16x8*)((sb) + woff + (co)); } \
                if (T == 1) { fw[n][1] = *(const s16x8*)((sb) + woff + 2048 + (co)); fw[n][2] = *(const s16x8*)((sb) + woff + 4096 + (co)); } \
                if (T == 2) { fw[n][3] = *(const s16x8*)((sb) + woff + 6144 + (co)); fw[n][4] = *(const s16x8*)((sb) + woff + 8192 + (co)); } \
                SCHED_FENCE();                                                                    \
            }                                                                                     \
        }                                                                                         \
    } while (0)
#define S_BODY(it, LAST)                                                                          \
    do {                                                                                          \
        const char* sb = sm + ((it) & 3) * K1_STAGE;                                              \
        const char* sn = sm + (((it) + 1) & 3) * K1_STAGE;                                        \
        S_HALF(0, 1, sb, co1, true);                                                              \
        S_GATE(0, 64 * (it));                                                                     \
        if (!(LAST)) {                                                                            \
            SCHED_FENCE();                                                                        \
            WAIT_LGKM0();                                                                         \
            __builtin_amdgcn_s_barrier();                                                         \
            asm volatile("" ::: "memory");                                                        \
            SCHED_FENCE();                                                                        \
        }                                                                                         \
        S_HALF(1, 0, sn, co0, !(LAST));                                                           \
        S_GATE(1, 64 * (it) + 32);                                                                \
    } while (0)
        asm volatile("" ::: "memory");
        __builtin_amdgcn_s_barrier();
        asm volatile("" ::: "memory");
        S_READ(0, sm, co0);
#pragma unroll 1
        for (int it = 0; it < 9; ++it) S_BODY(it, false);
        S_BODY(9, true);
        if (do_gate && q == 0) {
            f32x2 gg = {1.0f / (1.0f + __expf(-acc[5][0][0])), 1.0f / (1.0f + __expf(-acc[5][1][0]))};
            *(f32x2*)(ws + WS_G + (128 * rb + 32 * wc + 2 * t) * 4) = gg;
        }
#pragma unroll
        for (int T = 0; T < 5; ++T) {
            u32x2 o0, o1;
#pragma unroll
            for (int cc = 0; cc < 2; ++cc) {
                const int chl = 8 * T + 2 * q + cc;
                const f32x4* tb = (const f32x4*)(sm + K1_TAB + chl * 64);
                const f32x4 t0 = tb[0], t1 = tb[1], t2 = tb[2], t3 = tb[3];
                const f32x2 L1 = {t0[0], t0[1]}, L2 = {t0[2], t0[3]}, L4 = {t1[0], t1[1]}, L8 = {t1[2], t1[3]}, L16 = {t2[0], t2[1]};
                const f32x2 cf = {t3[0], t3[1]};
                const f32x2 u0 = cmul_sc(cf, (f32x2){acc[T][0][2 * cc], acc[T][0][2 * cc + 1]});
                const f32x2 u1 = cmul_sc(cf, (f32x2){acc[T][1][2 * cc], acc[T][1][2 * cc + 1]});
                f32x2 a = cfma(L1, u0, u1);
                a = cfma(L2, (f32x2){shr_zero<1>(a.x), shr_zero<1>(a.y)}, a);
                a = cfma(L4, (f32x2){shr_zero<2>(a.x), shr_zero<2>(a.y)}, a);
                a = cfma(L8, (f32x2){shr_zero<4>(a.x), shr_zero<4>(a.y)}, a);
                a = cfma(L16, (f32x2){shr_zero<8>(a.x), shr_zero<8>(a.y)}, a);
                const f32x2 h0 = cfma(L1, (f32x2){shr_zero<1>(a.x), shr_zero<1>(a.y)}, u0);
                o0[cc] = pack2(h0.x, -h0.y);
                o1[cc] = pack2(a.x, -a.y);
                if (t == 15) *(f32x2*)(sm + K1_XCH + (wc * 40 + chl) * 8) = a;
            }
            const int Tg = 5 * cb + T;
            char* dst = a2 + (size_t)(Tg >> 1) * 8192 + (Tg & 1) * 8;
            st64_wt(dst, o0);
            st64_wt(dst + 1024, o1);
        }
        WAIT_LGKM0();
        unsigned old = 0;
        if (lane == 0) old = __hip_atomic_fetch_add((LAS unsigned*)(sm + K1_CNT), 1u, __ATOMIC_RELAXED, __HIP_MEMORY_SCOPE_WORKGROUP);
        old = __builtin_amdgcn_readfirstlane(old);
        asm volatile("" ::: "memory");
        if (old == 3 && lane < 40) {
            const f32x2 L32 = *(const f32x2*)(sm + K1_TAB + lane * 64 + 40);
            const f32x2* xc = (const f32x2*)(sm + K1_XCH);
            const int cg = cb >> 2, cl = 40 * (cb & 3) + lane;
            f32x2* pre = (f32x2*)(ws + WS_PRE) + (size_t)(rb * 4 + cg) * 640 + cl;
            f32x2 s = {0.f, 0.f};
#pragma unroll
            for (int w = 0; w < 4; ++w) { pre[w * 160] = s; s = cfma(L32, s, xc[w * 40 + lane]); }
            ((f32x2*)(ws + WS_AGG))[(cg * 16 + rb) * 160 + cl] = s;
        }
    } else {
        const int bb = q & 1;
        const int trb = 26624 + (8 * q + (t >> 2)) * 192 + (lane & 3) * 8;
        const int tra = trb + bb * 32;
        const int tr5 = trb + (bb ? 0 : 160);
        s16x8 fx[2][2], fw[2][6];
#define M_READ(b, sb, co, ks)                                                                     \
    do {                                                                                          \
        fx[b][0] = *(const s16x8*)((sb) + xoff + (co)); fx[b][1] = *(const s16x8*)((sb) + xoff + 2048 + (co)); \
        _Pragma("unroll") for (int j_ = 0; j_ < 6; ++j_) {                                        \
            const char* pa_ = (sb) + (j_ < 5 ? tra + j_ * 32 : tr5) + (ks) * 32 * 192;            \
            const s16x4 lo_ = __builtin_amdgcn_ds_read_tr16_b64_v4i16((LAS s16x4*)pa_);           \
            const s16x4 hi_ = __builtin_amdgcn_ds_read_tr16_b64_v4i16((LAS s16x4*)(pa_ + 4 * 192)); \
            fw[b][j_] = __builtin_shufflevector(lo_, hi_, 0, 1, 2, 3, 4, 5, 6, 7);                \
        }                                                                                         \
    } while (0)
#define M_RD1(b, sb, ks, j_)                                                                      \
    do {                                                                                          \
        const char* pa_ = (sb) + ((j_) < 5 ? tra + (j_) * 32 : tr5) + (ks) * 32 * 192;            \
        const s16x4 lo_ = __builtin_amdgcn_ds_read_tr16_b64_v4i16((LAS s16x4*)pa_);               \
        const s16x4 hi_ = __builtin_amdgcn_ds_read_tr16_b64_v4i16((LAS s16x4*)(pa_ + 4 * 192));   \
        fw[b][j_] = __builtin_shufflevector(lo_, hi_, 0, 1, 2, 3, 4, 5, 6, 7);                    \
    } while (0)
#define M_HALF(c, n, sb, co, ks, RD)                                                              \
    do {                                                                                          \
        _Pragma("unroll") for (int j = 0; j < 6; ++j) {                                           \
            acc[j][0] = mfma16(fw[c][j], fx[c][0], acc[j][0]); acc[j][1] = mfma16(fw[c][j], fx[c][1], acc[j][1]); \
            if (RD) {                                                                             \
                SCHED_FENCE();                                                                    \
                if (j == 0) { fx[n][0] = *(const s16x8*)((sb) + xoff + (co)); fx[n][1] = *(const s16x8*)((sb) + xoff + 2048 + (co)); M_RD1(n, sb, ks, 0); } \
                if (j == 1) { M_RD1(n, sb, ks, 1); M_RD1(n, sb, ks, 2); }                         \
                if (j == 2) { M_RD1(n, sb, ks, 3); M_RD1(n, sb, ks, 4); }                         \
                if (j == 3) { M_RD1(n, sb, ks, 5); }                                              \
                SCHED_FENCE();                                                                    \
            }                                                                                     \
        }                                                                                         \
    } while (0)
#define M_BODY(it, LAST)                                                                          \
    do {                                                                                          \
        const char* sb = sm + ((it) & 3) * K1_STAGE;                                              \
        const char* sn = sm + (((it) + 1) & 3) * K1_STAGE;                                        \
        M_HALF(0, 1, sb, co1, 1, true);                                                           \
        if (!(LAST)) {                                                                            \
            SCHED_FENCE();                                                                        \
            WAIT_LGKM0();                                                                         \
            __builtin_amdgcn_s_barrier();                                                         \
            asm volatile("" ::: "memory");                                                        \
            SCHED_FENCE();                                                                        \
        }                                                                                         \
        M_HALF(1, 0, sn, co0, 0, !(LAST));                                                        \
    } while (0)
        asm volatile("" ::: "memory");
        __builtin_amdgcn_s_barrier();
        asm volatile("" ::: "memory");
        M_READ(0, sm, co0, 0);
#pragma unroll 1
        for (int it = 0; it < 9; ++it) M_BODY(it, false);
        M_BODY(9, true);
#pragma unroll
        for (int v = 0; v < 3; ++v) {
#pragma unroll
            for (int rt = 0; rt < 2; ++rt) {
                u32x4 o;
#pragma unroll
                for (int jj = 0; jj < 2; ++jj) {
                    const int j = 2 * v + jj;
                    const u32x2 bp = *(const u32x2*)(sm + K1_PB1 + (16 * j + 4 * q) * 2);
                    const float bv[4] = {bflo(bp[0]), bfhi(bp[0]), bflo(bp[1]), bfhi(bp[1])};
                    float r[4];
#pragma unroll
                    for (int i = 0; i < 4; ++i) {
                        const float h = acc[j][rt][i] + bv[i];
                        const float e = __builtin_amdgcn_exp2f(fminf(h, 20.0f) * LOG2E);
                        const float n = e * (e + 2.0f);
                        r[i] = h * n * __builtin_amdgcn_rcpf(n + 2.0f);
                    }
                    o[2 * jj] = pack2(r[0], r[1]);
                    o[2 * jj + 1] = pack2(r[2], r[3]);
                }
                st128_wt(a2 + (size_t)(40 + 3 * cb + v) * 8192 + rt * 1024, o);
            }
        }
    }
}

constexpr int K2_RED = 0;
constexpr int K2_WV0 = 80 * 1024;
constexpr int K2_LDS = K2_WV0 + 4 * 6144;

__global__ __launch_bounds__(256, 1) void k2(const u16* __restrict__ x, const u16* __restrict__ Lre, const u16* __restrict__ Lim,
                                             const u16* __restrict__ logdt, const u16* __restrict__ Dskip,
                                             char* __restrict__ ws, float* __restrict__ out) {
    __shared__ __attribute__((aligned(16))) char sm[K2_LDS];
    const int tid = threadIdx.x, lane = tid & 63;
    const int wave = __builtin_amdgcn_readfirstlane(tid >> 6);
    const int bid = blockIdx.x, xcd = bid & 7, idx = bid >> 3;
    const int RG = 8 * (xcd >> 1) + (idx & 7);
    const int CB = 4 * (xcd & 1) + (idx >> 3);
    const int t = lane & 15, q = lane >> 4;
    const int rb = RG >> 1, wc0 = 2 * (RG & 1);
    char* raw = sm + K2_WV0 + wave * 6144;
    char* tab = raw + 5120;
    const unsigned rawb = (unsigned)(size_t)(LAS char*)sm + K2_WV0 + wave * 6144;

    const int ksA = 5 * CB + wave, ksB = 5 * CB + 4;
    const size_t fragA = ((size_t)rb * NKS + ksA) * 8192 + (RG & 1) * 4096 + lane * 16;
    const size_t fragB = ((size_t)rb * NKS + ksB) * 8192 + (RG & 1) * 4096 + wave * 1024 + lane * 16;
    u32x4 fx[5];
    u32x2 agv[8], prv[2];
    unsigned lam[3];
    {
        const char* pf = ws + WS_A2 + fragA;
        asm volatile("global_load_dwordx4 %0, %4, off\n\tglobal_load_dwordx4 %1, %4, off offset:1024\n\t"
                     "global_load_dwordx4 %2, %4, off offset:2048\n\tglobal_load_dwordx4 %3, %4, off offset:3072"
                     : "=&v"(fx[0]), "=&v"(fx[1]), "=&v"(fx[2]), "=&v"(fx[3]) : "v"(pf) : "memory");
        fx[4] = ld128_asm(ws + WS_A2 + fragB);
        const int ch = 16 * ((lane & 16) ? ksB : ksA) + t;
        const int cg = ch / 160, cl = ch - 160 * cg;
        const char* ap = ws + WS_AGG + ((size_t)(cg * 16 + 8 * (lane >> 5)) * 160 + cl) * 8;
#pragma unroll
        for (int j = 0; j < 8; ++j) agv[j] = ld64_asm(ap + j * 1280);
        const char* pp = ws + WS_PRE + ((size_t)((rb * 4 + cg) * 4 + wc0) * 160 + cl) * 8;
        prv[0] = ld64_asm(pp); prv[1] = ld64_asm(pp + 1280);
        lam[0] = ld16_asm(Lre + ch); lam[1] = ld16_asm(Lim + ch); lam[2] = ld16_asm(logdt + ch);
        glds16(Dskip + 80 * CB + (lane < 10 ? lane : 0) * 8, rawb);
        const u16* xr = x + (64 * RG + 32 * (wave >> 1) + 2 * t + (wave & 1)) * DM + 80 * CB;
#pragma unroll
        for (int i = 0; i < 3; ++i) glds16(xr + ((4 * i + q) < 10 ? 4 * i + q : 0) * 8, rawb + 1024 + i * 1024);
        glds16(ws + WS_G + (64 * RG + 4 * t) * 4, rawb + 4096);
    }

    const char* pa = ws + WS_A2 + ((size_t)rb * NKS + 40 + 12 * wave) * 8192 + (RG & 1) * 4096 + lane * 16;
    const char* pb = ws + WS_W2 + ((size_t)(CB >> 1) * NKS + 40 + 12 * wave) * 10240 + (CB & 1) * 5120 + 1024 + lane * 16;
    u32x4 Ab[4][4], Bb[4][5];
#define K2_LOAD(slot, SC)                                                                         \
    do {                                                                                          \
        asm volatile("global_load_dwordx4 %0, %4, off" SC "\n\tglobal_load_dwordx4 %1, %4, off offset:1024" SC "\n\t"       \
                     "global_load_dwordx4 %2, %4, off offset:2048" SC "\n\tglobal_load_dwordx4 %3, %4, off offset:3072" SC \
                     : "=&v"(Ab[slot][0]), "=&v"(Ab[slot][1]), "=&v"(Ab[slot][2]), "=&v"(Ab[slot][3]) : "v"(pa) : "memory"); \
        asm volatile("global_load_dwordx4 %0, %5, off offset:-1024\n\tglobal_load_dwordx4 %1, %5, off\n\t"      \
                     "global_load_dwordx4 %2, %5, off offset:1024\n\tglobal_load_dwordx4 %3, %5, off offset:2048\n\t" \
                     "global_load_dwordx4 %4, %5, off offset:3072"                                \
                     : "=&v"(Bb[slot][0]), "=&v"(Bb[slot][1]), "=&v"(Bb[slot][2]), "=&v"(Bb[slot][3]), "=&v"(Bb[slot][4]) : "v"(pb) : "memory"); \
        pa += 8192; pb += 10240;                                                                  \
    } while (0)
#define K2_STEP(u, WN)                                                                            \
    do {                                                                                          \
        asm volatile("s_waitcnt vmcnt(%9)" : "+v"(Ab[u][0]), "+v"(Ab[u][1]), "+v"(Ab[u][2]), "+v"(Ab[u][3]), \
                     "+v"(Bb[u][0]), "+v"(Bb[u][1]), "+v"(Bb[u][2]), "+v"(Bb[u][3]), "+v"(Bb[u][4]) : "n"(WN) : "memory"); \
        _Pragma("unroll") for (int r = 0; r < 4; ++r)                                             \
            _Pragma("unroll") for (int n = 0; n < 5; ++n)                                         \
                acc[r][n] = mfma16(__builtin_bit_cast(s16x8, Bb[u][n]), __builtin_bit_cast(s16x8, Ab[u][r]), acc[r][n]); \
        SCHED_FENCE();                                                                            \
    } while (0)
#define K2_L1(dst, P, OFF, SC) asm volatile("global_load_dwordx4 %0, %1, off offset:" #OFF SC : "=&v"(dst) : "v"(P) : "memory")
#define K2_M2(u, i)                                                                               \
    do {                                                                                          \
        acc[(i) / 5][(i) % 5] = mfma16(__builtin_bit_cast(s16x8, Bb[u][(i) % 5]), __builtin_bit_cast(s16x8, Ab[u][(i) / 5]), acc[(i) / 5][(i) % 5]); \
        acc[((i) + 1) / 5][((i) + 1) % 5] = mfma16(__builtin_bit_cast(s16x8, Bb[u][((i) + 1) % 5]), __builtin_bit_cast(s16x8, Ab[u][((i) + 1) / 5]), acc[((i) + 1) / 5][((i) + 1) % 5]); \
        SCHED_FENCE();                                                                            \
    } while (0)
#define K2_ISTEP(u, ls, SC)                                                                       \
    do {                                                                                          \
        SCHED_FENCE();                                                                            \
        K2_M2(u, 0); K2_L1(Ab[ls][0], pa, 0, SC); SCHED_FENCE();                                  \
        K2_M2(u, 2); K2_L1(Ab[ls][1], pa, 1024, SC); SCHED_FENCE();                               \
        K2_M2(u, 4); K2_L1(Ab[ls][2], pa, 2048, SC); SCHED_FENCE();                               \
        K2_M2(u, 6); K2_L1(Ab[ls][3], pa, 3072, SC); SCHED_FENCE();                               \
        K2_M2(u, 8); K2_L1(Bb[ls][0], pb, -1024, ""); SCHED_FENCE();                              \
        K2_M2(u, 10); K2_L1(Bb[ls][1], pb, 0, ""); SCHED_FENCE();                                 \
        K2_M2(u, 12); K2_L1(Bb[ls][2], pb, 1024, ""); SCHED_FENCE();                              \
        K2_M2(u, 14); K2_L1(Bb[ls][3], pb, 2048, ""); SCHED_FENCE();                              \
        K2_M2(u, 16); K2_L1(Bb[ls][4], pb, 3072, ""); SCHED_FENCE();                              \
        K2_M2(u, 18);                                                                             \
        pa += 8192; pb += 10240;                                                                  \
    } while (0)
    K2_LOAD(0, ""); K2_LOAD(1, ""); K2_LOAD(2, "");

    f32x4 acc[4][5];
#pragma unroll
    for (int r = 0; r < 4; ++r)
#pragma unroll
        for (int n = 0; n < 5; ++n) acc[r][n] = (f32x4){0.f, 0.f, 0.f, 0.f};

    GAS unsigned* cnt = (GAS unsigned*)(ws + WS_CNT + RG * 256);
#define K2_WAITS(u, WN)                                                                           \
    asm volatile("s_waitcnt vmcnt(%9)" : "+v"(Ab[u][0]), "+v"(Ab[u][1]), "+v"(Ab[u][2]), "+v"(Ab[u][3]), \
                 "+v"(Bb[u][0]), "+v"(Bb[u][1]), "+v"(Bb[u][2]), "+v"(Bb[u][3]), "+v"(Bb[u][4]) : "n"(WN) : "memory")
#define K2_MMA(u)                                                                                 \
    do {                                                                                          \
        _Pragma("unroll") for (int r = 0; r < 4; ++r)                                             \
            _Pragma("unroll") for (int n = 0; n < 5; ++n)                                         \
                acc[r][n] = mfma16(__builtin_bit_cast(s16x8, Bb[u][n]), __builtin_bit_cast(s16x8, Ab[u][r]), acc[r][n]); \
    } while (0)
    K2_LOAD(3, ""); K2_WAITS(0, 27);
    asm volatile("" : "+v"(fx[0]), "+v"(fx[1]), "+v"(fx[2]), "+v"(fx[3]), "+v"(fx[4]), "+v"(agv[0]), "+v"(agv[1]), "+v"(agv[2]),
                 "+v"(agv[3]), "+v"(agv[4]), "+v"(agv[5]), "+v"(agv[6]), "+v"(agv[7]), "+v"(prv[0]), "+v"(prv[1]), "+v"(lam[0]), "+v"(lam[1]), "+v"(lam[2]));
    K2_MMA(0);
    const f32x2 g01 = *(const f32x2*)(raw + 4096 + 8 * t), g23 = *(const f32x2*)(raw + 4096 + 128 + 8 * t);
    {
        const float lr = bflo(lam[0]), li = bflo(lam[1]), ld = bflo(lam[2]);
        const float dt = __builtin_amdgcn_exp2f(ld * LOG2E);
        const float za = dt * lr * LOG2E, zb = dt * li * INV2PI;
        const f32x2 L1 = lam_bar(lr, li, ld);
        f32x2 Lp = L1;
#pragma unroll
        for (int e = 0; e < 5; ++e) Lp = cmul_pk(Lp, Lp);
        const f32x2 L32 = Lp, L64 = cmul_pk(L32, L32), L128 = cmul_pk(L64, L64);
        f32x2 cy = {0.f, 0.f};
        const int j0 = 8 * (lane >> 5);
#pragma unroll
        for (int j = 0; j < 8; ++j) {
            const f32x2 nx = cmul_pk(L128, cy) + __builtin_bit_cast(f32x2, agv[j]);
            cy = (j0 + j < rb) ? nx : cy;
        }
        f32x2 Lq = {1.f, 0.f};
#pragma unroll
        for (int j = 8; j < 15; ++j) { const f32x2 nx = cmul_pk(Lq, L128); Lq = (j < rb && lane < 32) ? nx : Lq; }
        cy = cmul_pk(cy, Lq);
        cy.x += __shfl_xor(cy.x, 32); cy.y += __shfl_xor(cy.y, 32);
        const f32x2 cy2 = cmul_pk(cy, L64);
        cy = wc0 ? cy2 : cy;
        const f32x2 c0 = __builtin_bit_cast(f32x2, prv[0]) + cy, c1 = cmul_pk(L32, cy) + __builtin_bit_cast(f32x2, prv[1]);
        f32x4* tp = (f32x4*)(tab + (lane & 31) * 32);
        tp[0] = (f32x4){za, zb, L1.x, L1.y};
        tp[1] = (f32x4){c0.x, c0.y, c1.x, c1.y};
    }
    SCHED_FENCE();
    const float fn = (float)(2 * t + 1), fnB = fn + (float)(wave & 1);
    const float gB = 2.0f * (wave == 0 ? g01.x : wave == 1 ? g01.y : wave == 2 ? g23.x : g23.y);
#define K2_FIX(e)                                                                                 \
    do {                                                                                          \
        const char* tp = tab + (2 * q + ((e) & 1) + 8 * ((e) >> 1)) * 32;                         \
        const f32x4 t0 = *(const f32x4*)tp, t1 = *(const f32x4*)(tp + 16);                        \
        const f32x4 u0 = *(const f32x4*)(tp + 512), u1 = *(const f32x4*)(tp + 528);               \
        {                                                                                         \
            const float mg = __builtin_amdgcn_exp2f(fn * t0[0]);                                  \
            const float rv = __builtin_amdgcn_fractf(fn * t0[1]);                                 \
            const f32x2 base = {mg * __builtin_amdgcn_cosf(rv), mg * __builtin_amdgcn_sinf(rv)};  \
            const f32x2 L1 = {t0[2], t0[3]};                                                      \
            _Pragma("unroll") for (int w = 0; w < 2; ++w) {                                       \
                const f32x2 cr = cmul_pk(base, (f32x2){t1[2 * w], t1[2 * w + 1]});                \
                const f32x2 cs = cmul_pk(cr, L1);                                                 \
                const float ga = 2.0f * (w ? g23.x : g01.x), gb = 2.0f * (w ? g23.y : g01.y);     \
                fx[2 * w][e] = pack2((bflo(fx[2 * w][e]) + cr.x) * ga, (bfhi(fx[2 * w][e]) - cr.y) * ga); \
                fx[2 * w + 1][e] = pack2((bflo(fx[2 * w + 1][e]) + cs.x) * gb, (bfhi(fx[2 * w + 1][e]) - cs.y) * gb); \
            }                                                                                     \
        }                                                                                         \
        {                                                                                         \
            const float mg = __builtin_amdgcn_exp2f(fnB * u0[0]);                                 \
            const float rv = __builtin_amdgcn_fractf(fnB * u0[1]);                                \
            const f32x2 base = {mg * __builtin_amdgcn_cosf(rv), mg * __builtin_amdgcn_sinf(rv)};  \
            const f32x2 cyB = (wave >> 1) ? (f32x2){u1[2], u1[3]} : (f32x2){u1[0], u1[1]};        \
            const f32x2 cr = cmul_pk(base, cyB);                                                  \
            fx[4][e] = pack2((bflo(fx[4][e]) + cr.x) * gB, (bfhi(fx[4][e]) - cr.y) * gB);         \
        }                                                                                         \
    } while (0)
    K2_LOAD(0, ""); K2_WAITS(1, 27); K2_MMA(1); K2_FIX(0); K2_FIX(1); SCHED_FENCE();
    K2_LOAD(1, ""); K2_WAITS(2, 27); K2_MMA(2); K2_FIX(2); K2_FIX(3); SCHED_FENCE();
    {
        char* da = ws + WS_A2C + ((size_t)rb * 40 + ksA) * 8192 + (RG & 1) * 4096 + lane * 16;
        char* db = ws + WS_A2C + ((size_t)rb * 40 + ksB) * 8192 + (RG & 1) * 4096 + wave * 1024 + lane * 16;
        asm volatile("global_store_dwordx4 %0, %2, off sc1\n\tglobal_store_dwordx4 %0, %3, off offset:1024 sc1\n\t"
                     "global_store_dwordx4 %0, %4, off offset:2048 sc1\n\tglobal_store_dwordx4 %0, %5, off offset:3072 sc1\n\t"
                     "global_store_dwordx4 %1, %6, off sc1\n\ts_nop 1"
                     :: "v"(da), "v"(db), "v"(fx[0]), "v"(fx[1]), "v"(fx[2]), "v"(fx[3]), "v"(fx[4]) : "memory");
    }
    K2_WAITS(3, 23); K2_ISTEP(3, 2, "");
    K2_WAITS(0, 23); K2_ISTEP(0, 3, "");
    K2_WAITS(1, 18);
    if (lane == 0) __hip_atomic_fetch_add(cnt, 1u, __ATOMIC_RELAXED, __HIP_MEMORY_SCOPE_AGENT);
    K2_ISTEP(1, 0, "");
    K2_WAITS(2, 19); K2_ISTEP(2, 1, "");
    K2_WAITS(3, 19); K2_ISTEP(3, 2, "");
    K2_WAITS(0, 18); K2_ISTEP(0, 3, "");
    if (tid == 0) {
        unsigned spins = 0;
        while (__hip_atomic_load(cnt, __ATOMIC_RELAXED, __HIP_MEMORY_SCOPE_AGENT) < 32u) {
            __builtin_amdgcn_s_sleep(1);
            if (++spins > (1u << 22)) break;
        }
    }
    asm volatile("" ::: "memory");
    __builtin_amdgcn_s_barrier();
    asm volatile("" ::: "memory");
    pa = ws + WS_A2C + ((size_t)rb * 40 + 10 * wave) * 8192 + (RG & 1) * 4096 + lane * 16;
    pb -= (size_t)(52 + 2 * wave) * 10240;
    K2_WAITS(1, 18); K2_ISTEP(1, 0, " sc1");
    K2_WAITS(2, 18); K2_ISTEP(2, 1, " sc1");
    K2_WAITS(3, 18); K2_ISTEP(3, 2, " sc1");
#pragma unroll
    for (int r = 0; r < 4; ++r) {
        const float g1 = 1.0f - (r == 0 ? g01.x : r == 1 ? g01.y : r == 2 ? g23.x : g23.y);
#pragma unroll
        for (int n = 0; n < 5; ++n) acc[r][n] *= g1;
    }
    K2_WAITS(0, 18); K2_ISTEP(0, 3, " sc1");
    K2_WAITS(1, 18); K2_ISTEP(1, 0, " sc1");
    K2_WAITS(2, 18); K2_ISTEP(2, 1, " sc1");
    K2_WAITS(3, 18); K2_ISTEP(3, 2, " sc1");
    K2_WAITS(0, 18); K2_ISTEP(0, 3, " sc1");
    K2_WAITS(1, 18); K2_ISTEP(1, 0, " sc1");
    K2_WAITS(2, 18); K2_ISTEP(2, 1, " sc1");
    K2_STEP(3, 18); K2_STEP(0, 9); K2_STEP(1, 0);

#pragma unroll
    for (int r = 0; r < 4; ++r)
#pragma unroll
        for (int n = 0; n < 5; ++n) *(f32x4*)(sm + K2_RED + ((r * 4 + wave) * 5 + n) * 1024 + lane * 16) = acc[r][n];
    __syncthreads();
    const int tm = 64 * RG + 32 * (wave >> 1) + 2 * t + (wave & 1);
    const float gme = wave == 0 ? g01.x : wave == 1 ? g01.y : wave == 2 ? g23.x : g23.y;
#pragma unroll
    for (int n = 0; n < 5; ++n) {
        const u32x2 dv = *(const u32x2*)(raw + (16 * n + 4 * q) * 2);
        const u32x2 xv = *(const u32x2*)(raw + 1024 + (2 * n + (q >> 1)) * 256 + t * 16 + (q & 1) * 8);
        f32x4 v = *(const f32x4*)(sm + K2_RED + (((wave * 4) * 5 + n) * 64 + lane) * 16);
#pragma unroll
        for (int k = 1; k < 4; ++k) v += *(const f32x4*)(sm + K2_RED + (((wave * 4 + k) * 5 + n) * 64 + lane) * 16);
        v[0] += gme * bflo(xv[0]) * bflo(dv[0]);
        v[1] += gme * bfhi(xv[0]) * bfhi(dv[0]);
        v[2] += gme * bflo(xv[1]) * bflo(dv[1]);
        v[3] += gme * bfhi(xv[1]) * bfhi(dv[1]);
        st128_wt(out + (size_t)tm * DM + 80 * CB + 16 * n + 4 * q, __builtin_bit_cast(u32x4, v));
    }
}

extern "C" void kernel_launch(void* const* d_in, const int* in_sizes, int n_in, void* d_out, int out_size, void* d_ws,
                              size_t ws_size, hipStream_t stream) {
    const u16* x = (const u16*)d_in[0];
    const u16* Lre = (const u16*)d_in[1];
    const u16* Lim = (const u16*)d_in[2];
    const u16* Bre = (const u16*)d_in[3];
    const u16* Bim = (const u16*)d_in[4];
    const u16* Cre = (const u16*)d_in[5];
    const u16* Cim = (const u16*)d_in[6];
    const u16* logdt = (const u16*)d_in[7];
    const u16* Dskip = (const u16*)d_in[8];
    const u16* w1 = (const u16*)d_in[10];
    const u16* b1 = (const u16*)d_in[11];
    const u16* w2 = (const u16*)d_in[12];
    const u16* gw = (const u16*)d_in[13];
    k1<<<256, 768, 0, stream>>>(x, Lre, Lim, Bre, Bim, Cre, Cim, logdt, w1, b1, w2, gw, (char*)d_ws);
    k2<<<256, 256, 0, stream>>>(x, Lre, Lim, logdt, Dskip, (char*)d_ws, (float*)d_out);
}
```

```cpp
#include <hip/hip_runtime.h>
#include <stdint.h>

typedef short s16x4 __attribute__((ext_vector_type(4)));
typedef short s16x8 __attribute__((ext_vector_type(8)));
typedef __bf16 bf16x2 __attribute__((ext_vector_type(2)));
typedef float f32x2 __attribute__((ext_vector_type(2)));
typedef float f32x4 __attribute__((ext_vector_type(4)));
typedef unsigned u32x2 __attribute__((ext_vector_type(2)));
typedef unsigned u32x4 __attribute__((ext_vector_type(4)));
typedef unsigned short u16;
#define LAS __attribute__((address_space(3)))
#define GAS __attribute__((address_space(1)))
#define DI __device__ __forceinline__

constexpr int SEQ = 2048, DM = 640, NP = 640, NF = 1536;
constexpr int NKS = 88;

constexpr size_t WS_A2 = 0;
constexpr size_t WS_W2 = WS_A2 + (size_t)16 * NKS * 8192;
constexpr size_t WS_G = WS_W2 + (size_t)8 * NKS * 5120;
constexpr size_t WS_AGG = WS_G + 2048 * 4;
constexpr size_t WS_EP = WS_AGG + 16 * 16 * 320;
constexpr size_t WS_FPT = WS_EP + 256;
constexpr size_t WS_FAG = WS_FPT + 2048;
constexpr size_t WS_FP1 = WS_FAG + 2048;

DI float bflo(unsigned v) { return __uint_as_float(v << 16); }
DI float bfhi(unsigned v) { return __uint_as_float(v & 0xffff0000u); }
DI unsigned pack2(float a, float b) {
    f32x2 p = {a, b};
    return __builtin_bit_cast(unsigned, __builtin_convertvector(p, bf16x2));
}
DI f32x4 mfma16(s16x8 a, s16x8 b, f32x4 c) { return __builtin_amdgcn_mfma_f32_16x16x32_bf16(a, b, c, 0, 0, 0); }
template <int S> DI float shr_zero(float v) {
    return __builtin_bit_cast(float, __builtin_amdgcn_update_dpp(0, __builtin_bit_cast(int, v), 0x110 + S, 0xf, 0xf, true));
}
DI f32x2 cmul(f32x2 a, f32x2 b) { return (f32x2){a.x * b.x - a.y * b.y, a.x * b.y + a.y * b.x}; }
DI f32x2 cfma(f32x2 a, f32x2 b, f32x2 c) {
    return (f32x2){fmaf(-a.y, b.y, fmaf(a.x, b.x, c.x)), fmaf(a.y, b.x, fmaf(a.x, b.y, c.y))};
}
DI f32x2 cmul_pk(f32x2 a, f32x2 b) { return (f32x2){fmaf(-a.y, b.y, a.x * b.x), fmaf(a.y, b.x, a.x * b.y)}; }
DI f32x2 cmul_sc(f32x2 a, f32x2 b) {
    float p = a.x * b.x, r = a.x * b.y;
    asm("" : "+v"(p));
    asm("" : "+v"(r));
    return (f32x2){fmaf(-a.y, b.y, p), fmaf(a.y, b.x, r)};
}
DI float dot2bf(unsigned a, unsigned b, float c) {
    return __builtin_amdgcn_fdot2_f32_bf16(__builtin_bit_cast(bf16x2, a), __builtin_bit_cast(bf16x2, b), c, false);
}
#define WAIT_VM(n) asm volatile("s_waitcnt vmcnt(%0)" ::"n"(n) : "memory")
#define WAIT_LGKM0() asm volatile("s_waitcnt lgkmcnt(0)" ::: "memory")
#define SCHED_FENCE() __builtin_amdgcn_sched_barrier(0)

DI void glds16(const void* gsrc, unsigned lds_dst) {
    unsigned keep;
    asm volatile("s_mov_b32 %0, m0\n\ts_mov_b32 m0, %2\n\ts_nop 0\n\tglobal_load_lds_dwordx4 %1, off\n\ts_mov_b32 m0, %0"
                 : "=&s"(keep) : "v"(gsrc), "s"(lds_dst) : "memory");
}
DI void glds16x4(const void* gsrc, unsigned lds_dst) {
    unsigned keep;
    asm volatile("s_mov_b32 %0, m0\n\ts_mov_b32 m0, %2\n\ts_nop 0\n\tglobal_load_lds_dwordx4 %1, off\n\t"
                 "global_load_lds_dwordx4 %1, off offset:1024\n\tglobal_load_lds_dwordx4 %1, off offset:2048\n\t"
                 "global_load_lds_dwordx4 %1, off offset:3072\n\ts_mov_b32 m0, %0"
                 : "=&s"(keep) : "v"(gsrc), "s"(lds_dst) : "memory");
}
DI void st128_wt(void* p, u32x4 v) { asm volatile("global_store_dwordx4 %0, %1, off sc1\n\ts_nop 1" :: "v"(p), "v"(v) : "memory"); }
DI void st64_wt(void* p, u32x2 v) { asm volatile("global_store_dwordx2 %0, %1, off sc1\n\ts_nop 1" :: "v"(p), "v"(v) : "memory"); }
DI unsigned ld32_asm(const void* p) { unsigned r; asm volatile("global_load_dword %0, %1, off" : "=v"(r) : "v"(p) : "memory"); return r; }
DI unsigned ld16_asm(const void* p) { unsigned r; asm volatile("global_load_ushort %0, %1, off" : "=v"(r) : "v"(p) : "memory"); return r; }
DI u32x2 ld64_asm(const void* p) { u32x2 r; asm volatile("global_load_dwordx2 %0, %1, off" : "=v"(r) : "v"(p) : "memory"); return r; }
DI u32x4 ld128_asm(const void* p) { u32x4 r; asm volatile("global_load_dwordx4 %0, %1, off" : "=v"(r) : "v"(p) : "memory"); return r; }
constexpr float LOG2E = 1.44269504088896341f, INV2PI = 0.15915494309189535f;
DI f32x2 lam_bar(float lr, float li, float ld) {
    const float dt = __builtin_amdgcn_exp2f(ld * LOG2E);
    const float m = __builtin_amdgcn_exp2f(dt * lr * LOG2E);
    const float rv = __builtin_amdgcn_fractf(dt * li * INV2PI);
    return (f32x2){m * __builtin_amdgcn_cosf(rv), m * __builtin_amdgcn_sinf(rv)};
}

typedef unsigned long long u64;
DI void glds16_c(const void* gsrc, unsigned lds_dst) {
    unsigned keep;
    asm volatile("s_mov_b32 %0, m0\n\ts_mov_b32 m0, %2\n\ts_nop 0\n\tglobal_load_lds_dwordx4 %1, off sc1\n\ts_mov_b32 m0, %0"
                 : "=&s"(keep) : "v"(gsrc), "s"(lds_dst) : "memory");
}
DI void glds16x4_c(const void* gsrc, unsigned lds_dst) {
    unsigned keep;
    asm volatile("s_mov_b32 %0, m0\n\ts_mov_b32 m0, %2\n\ts_nop 0\n\tglobal_load_lds_dwordx4 %1, off sc1\n\t"
                 "global_load_lds_dwordx4 %1, off offset:1024 sc1\n\tglobal_load_lds_dwordx4 %1, off offset:2048 sc1\n\t"
                 "global_load_lds_dwordx4 %1, off offset:3072 sc1\n\ts_mov_b32 m0, %0"
                 : "=&s"(keep) : "v"(gsrc), "s"(lds_dst) : "memory");
}
DI void flag_set(char* p, u64 tok) { __hip_atomic_store((GAS u64*)p, tok, __ATOMIC_RELAXED, __HIP_MEMORY_SCOPE_AGENT); }
DI void flag_wait(const char* p, bool need, u64 tok) {
    for (unsigned spins = 0; spins < (1u << 20); ++spins) {
        const u64 v = __hip_atomic_load((const GAS u64*)p, __ATOMIC_RELAXED, __HIP_MEMORY_SCOPE_AGENT);
        if (__all(!need || v == tok)) break;
        __builtin_amdgcn_s_sleep(2);
    }
    asm volatile("" ::: "memory");
}

constexpr int K1_STAGE = 38 * 1024;
constexpr int K1_GW = 3 * K1_STAGE;
constexpr int K1_PLR = K1_GW + 1280, K1_PLI = K1_GW + 1360, K1_PLD = K1_GW + 1440, K1_PB1 = K1_GW + 1536;
constexpr int K1_TAB = K1_GW + 2048;
constexpr int K1_XCH = K1_TAB + 2560;
constexpr int K1_CAR = K1_XCH + 1280;
constexpr int K1_CNT = K1_CAR + 1280;
constexpr int K1_PT = 122 * 1024;
constexpr int KF_LDS = 160 * 1024;

__global__ __launch_bounds__(512, 2) void kf(const u16* __restrict__ x, const u16* __restrict__ Lre, const u16* __restrict__ Lim,
                                             const u16* __restrict__ Bre, const u16* __restrict__ Bim, const u16* __restrict__ Cre,
                                             const u16* __restrict__ Cim, const u16* __restrict__ logdt, const u16* __restrict__ Dskip,
                                             const u16* __restrict__ w1, const u16* __restrict__ b1, const u16* __restrict__ w2,
                                             const u16* __restrict__ gw, char* __restrict__ ws, float* __restrict__ out) {
    __shared__ __attribute__((aligned(16))) char sm[KF_LDS];
    const int tid = threadIdx.x, lane = tid & 63;
    const int wave = __builtin_amdgcn_readfirstlane(tid >> 6);
    const int bid = blockIdx.x, xcd = bid & 7, idx = bid >> 3;
    const int rb = 8 * (xcd & 1) + (idx & 7);
    const int cb = 4 * (xcd >> 1) + (idx >> 3);
    const int RG = 8 * (xcd >> 1) + (idx & 7);
    const int CB = 4 * (xcd & 1) + (idx >> 3);
    const int t = lane & 15, q = lane >> 4;
    const unsigned smb = (unsigned)(size_t)(LAS char*)sm;
    u32x2 epv;
    asm volatile("global_load_dwordx2 %0, %1, off sc1" : "=v"(epv) : "v"(ws + WS_EP) : "memory");

    const int pf0 = 14 * bid + 2 * wave;
    char* ptl = sm + K1_PT + wave * 2048;
#pragma unroll
    for (int u = 0; u < 2; ++u) {
        int f = pf0 + u; f = (wave < 7 && f < 3520) ? f : 0;
        const int cK = f / 5, nt = f - 5 * cK, cB = cK / NKS, KS = cK - NKS * cB, d0 = 80 * cB + 16 * nt;
        const u16* p0 = (KS < 40) ? ((lane >> 5) ? Cim : Cre) + (d0 + ((lane >> 1) & 15)) * NP + 16 * KS + 8 * (lane & 1)
                                  : w2 + (32 * (KS - 40) + (lane >> 1)) * DM + d0 + 8 * (lane & 1);
        glds16(p0, smb + K1_PT + wave * 2048 + u * 1024);
    }
    const char* src[5];
    unsigned ldo[5];
    int incE, strE;
    {
        const int l8 = lane >> 3;
        auto xsrc = [&](int s) {
            const int p = 8 * s + l8;
            const int tm = 128 * rb + 32 * (p >> 5) + 2 * (p & 15) + ((p >> 4) & 1);
            return (const char*)(x + tm * DM + (((lane & 7) ^ ((p >> 1) & 7)) << 3));
        };
        auto wsrc = [&](int s) {
            const int p = 8 * s + l8;
            const int ch = 40 * cb + 8 * (p >> 4) + ((p & 15) >> 1);
            return (const char*)(((p & 1) ? Bim : Bre) + ch * DM + (((lane & 7) ^ ((p >> 1) & 7)) << 3));
        };
        auto msrc = [&](int s) {
            const int gI = 64 * s + lane, row = gI / 12, cp = gI - 12 * row;
            int j = (cp >> 1) - ((row >> 3) & 1);
            j += j < 0 ? 6 : 0;
            return (const char*)(w1 + row * NF + 96 * cb + 16 * j + 8 * (cp & 1));
        };
        src[0] = xsrc(2 * wave); ldo[0] = smb + 2048 * wave;
        src[1] = xsrc(2 * wave + 1); ldo[1] = smb + 2048 * wave + 1024;
        src[2] = wsrc(wave); ldo[2] = smb + 16384 + 1024 * wave;
        src[3] = msrc(wave); ldo[3] = smb + 26624 + 1024 * wave;
        if (wave < 2) { src[4] = wsrc(8 + wave); ldo[4] = smb + 16384 + 1024 * (8 + wave); incE = 128; strE = 1; }
        else if (wave < 6) { src[4] = msrc(6 + wave); ldo[4] = smb + 26624 + 1024 * (6 + wave); incE = 64 * NF * 2; strE = 1; }
        else {
            const u16* p = gw + lane * 8;
            if (wave == 7) {
                p = gw + 512 + t * 8;
                if (lane >= 16 && lane < 21) p = Lre + 40 * cb + (lane - 16) * 8;
                if (lane >= 21 && lane < 26) p = Lim + 40 * cb + (lane - 21) * 8;
                if (lane >= 26 && lane < 31) p = logdt + 40 * cb + (lane - 26) * 8;
                if (lane >= 32 && lane < 44) p = b1 + 96 * cb + (lane - 32) * 8;
            }
            src[4] = (const char*)p; ldo[4] = smb + K1_GW + (wave - 6) * 1024; incE = 0; strE = 0;
        }
    }
#define K1_SLOT(i, boff)                                                                 \
    do {                                                                                 \
        glds16(src[i], ldo[i] + ((i) == 4 ? strE * (boff) : (boff)));                    \
        src[i] += (i) < 3 ? 128 : (i) == 3 ? 64 * NF * 2 : incE;                         \
    } while (0)
#pragma unroll
    for (int st = 0; st < 3; ++st) {
        K1_SLOT(0, st * K1_STAGE); K1_SLOT(1, st * K1_STAGE); K1_SLOT(2, st * K1_STAGE); K1_SLOT(3, st * K1_STAGE); K1_SLOT(4, st * K1_STAGE);
    }
    if (tid == 0) *(u32x2*)(sm + K1_CNT) = (u32x2){0u, 0u};

    const int wc = wave & 3;
    const int xoff = (32 * wc + t) * 128;
    const int co0 = (q ^ ((t >> 1) & 7)) * 16, co1 = co0 ^ 64;
    char* a2 = ws + WS_A2 + (size_t)rb * NKS * 8192 + (2 * wc) * 1024 + lane * 16;

    asm volatile("s_waitcnt vmcnt(10)" : "+v"(epv) :: "memory");
    WAIT_LGKM0();
    __builtin_amdgcn_s_barrier();
    asm volatile("" ::: "memory");
    const u64 tok = (((u64)(unsigned)__builtin_amdgcn_readfirstlane(epv[1]) << 32) | (unsigned)__builtin_amdgcn_readfirstlane(epv[0])) + 1ull;
    auto pretile_out = [&]() {
        if (wave < 7) {
            const int tro = (4 * q + (t >> 2)) * 32 + (lane & 3) * 8;
#pragma unroll
            for (int u = 0; u < 2; ++u) {
                const int f = pf0 + u;
                const int cK = f / 5, KS = cK % NKS;
                const char* pp = ptl + u * 1024;
                u32x4 wf;
                if (KS < 40) {
                    const unsigned r0 = *(const unsigned*)(pp + t * 32 + 4 * q), r1 = *(const unsigned*)(pp + t * 32 + 16 + 4 * q);
                    const unsigned i0 = *(const unsigned*)(pp + 512 + t * 32 + 4 * q), i1 = *(const unsigned*)(pp + 512 + t * 32 + 16 + 4 * q);
                    wf[0] = (r0 & 0xffffu) | (i0 << 16); wf[1] = (r0 >> 16) | (i0 & 0xffff0000u);
                    wf[2] = (r1 & 0xffffu) | (i1 << 16); wf[3] = (r1 >> 16) | (i1 & 0xffff0000u);
                } else {
                    const s16x4 lo = __builtin_amdgcn_ds_read_tr16_b64_v4i16((LAS s16x4*)(pp + tro));
                    const s16x4 hi = __builtin_amdgcn_ds_read_tr16_b64_v4i16((LAS s16x4*)(pp + tro + 512));
                    wf = __builtin_bit_cast(u32x4, __builtin_shufflevector(lo, hi, 0, 1, 2, 3, 4, 5, 6, 7));
                }
                if (f < 3520) st128_wt(ws + WS_W2 + (size_t)f * 1024 + lane * 16, wf);
            }
        }
    };

    const int rb2 = RG >> 1, hh = RG & 1;
    const int ks0 = wave < 4 ? 10 * wave : 40 + 12 * (wave - 4);
    const char* pa = ws + WS_A2 + ((size_t)rb2 * NKS + ks0) * 8192 + hh * 4096 + lane * 16;
    const char* pb = ws + WS_W2 + ((size_t)CB * NKS + ks0) * 5120 + 1024 + lane * 16;
    const int bco = (wave < 7 ? wave * 15360 : K1_PT);
    u32x4 Ab[4][4], Bb[3][5];
    f32x4 ac2[4][5];
#define P2_L1(dst, P, OFF) asm volatile("global_load_dwordx4 %0, %1, off offset:" #OFF " sc1" : "=&v"(dst) : "v"(P) : "memory")
#define P2_LDB(slot, P)                                                                           \
    do { P2_L1(Bb[slot][0], P, -1024); P2_L1(Bb[slot][1], P, 0); P2_L1(Bb[slot][2], P, 1024); P2_L1(Bb[slot][3], P, 2048); P2_L1(Bb[slot][4], P, 3072); } while (0)
#define P2_LDA(slot, P)                                                                           \
    do { P2_L1(Ab[slot][0], P, 0); P2_L1(Ab[slot][1], P, 1024); P2_L1(Ab[slot][2], P, 2048); P2_L1(Ab[slot][3], P, 3072); } while (0)
    auto p2_prefetch = [&](int nsteps) {
        const int fa = (CB * NKS + ks0) * 5, w0 = fa / 14, w1_ = (fa + 5 * nsteps - 1) / 14;
        flag_wait(ws + WS_FPT + (size_t)(w0 + lane <= w1_ ? w0 + lane : w0) * 8, true, tok);
        P2_LDB(0, pb); P2_LDB(1, pb + 5120); P2_LDB(2, pb + 10240);
        const char* g3 = pb - 1024 + 3 * 5120;
        glds16x4_c(g3, smb + bco); glds16x4_c(g3 + 4096, smb + bco + 4096); glds16x4_c(g3 + 8192, smb + bco + 8192);
        glds16_c(g3 + 12288, smb + bco + 12288); glds16_c(g3 + 13312, smb + bco + 13312); glds16_c(g3 + 14336, smb + bco + 14336);
    };
#define P2_M2(ua, ub, i)                                                                          \
    do {                                                                                          \
        ac2[(i) / 5][(i) % 5] = mfma16(__builtin_bit_cast(s16x8, Bb[ub][(i) % 5]), __builtin_bit_cast(s16x8, Ab[ua][(i) / 5]), ac2[(i) / 5][(i) % 5]); \
        ac2[((i) + 1) / 5][((i) + 1) % 5] = mfma16(__builtin_bit_cast(s16x8, Bb[ub][((i) + 1) % 5]), __builtin_bit_cast(s16x8, Ab[ua][((i) + 1) / 5]), ac2[((i) + 1) / 5][((i) + 1) % 5]); \
        SCHED_FENCE();                                                                            \
    } while (0)
#define P2_STEP(j, NS, WN)                                                                        \
    do {                                                                                          \
        asm volatile("s_waitcnt vmcnt(%9)" : "+v"(Ab[(j) & 3][0]), "+v"(Ab[(j) & 3][1]), "+v"(Ab[(j) & 3][2]), "+v"(Ab[(j) & 3][3]), \
                     "+v"(Bb[(j) % 3][0]), "+v"(Bb[(j) % 3][1]), "+v"(Bb[(j) % 3][2]), "+v"(Bb[(j) % 3][3]), "+v"(Bb[(j) % 3][4]) : "n"(WN) : "memory"); \
        SCHED_FENCE();                                                                            \
        const char* pa_ = pa + ((j) + 3) * 8192;                                                  \
        const char* pb_ = pb + ((j) + 2) * 5120;                                                  \
        constexpr bool LA_ = (j) + 3 < (NS), LBG_ = (j) + 2 >= 6 && (j) + 2 < (NS), LBL_ = (j) + 2 >= 3 && (j) + 2 < 6; \
        P2_M2((j) & 3, (j) % 3, 0); if (LA_) { P2_L1(Ab[((j) + 3) & 3][0], pa_, 0); SCHED_FENCE(); }      \
        P2_M2((j) & 3, (j) % 3, 2); if (LA_) { P2_L1(Ab[((j) + 3) & 3][1], pa_, 1024); SCHED_FENCE(); }   \
        P2_M2((j) & 3, (j) % 3, 4); if (LA_) { P2_L1(Ab[((j) + 3) & 3][2], pa_, 2048); SCHED_FENCE(); }   \
        P2_M2((j) & 3, (j) % 3, 6); if (LA_) { P2_L1(Ab[((j) + 3) & 3][3], pa_, 3072); SCHED_FENCE(); }   \
        if (LBL_) { _Pragma("unroll") for (int n_ = 0; n_ < 5; ++n_) Bb[((j) + 2) % 3][n_] = *(const u32x4*)(sm + bco + (((j) + 2 - 3) * 5 + n_) * 1024 + lane * 16); SCHED_FENCE(); } \
        P2_M2((j) & 3, (j) % 3, 8); if (LBG_) { P2_L1(Bb[((j) + 2) % 3][0], pb_, -1024); SCHED_FENCE(); } \
        P2_M2((j) & 3, (j) % 3, 10); if (LBG_) { P2_L1(Bb[((j) + 2) % 3][1], pb_, 0); SCHED_FENCE(); }    \
        P2_M2((j) & 3, (j) % 3, 12); if (LBG_) { P2_L1(Bb[((j) + 2) % 3][2], pb_, 1024); SCHED_FENCE(); } \
        P2_M2((j) & 3, (j) % 3, 14); if (LBG_) { P2_L1(Bb[((j) + 2) % 3][3], pb_, 2048); SCHED_FENCE(); } \
        P2_M2((j) & 3, (j) % 3, 16); if (LBG_) { P2_L1(Bb[((j) + 2) % 3][4], pb_, 3072); SCHED_FENCE(); } \
        P2_M2((j) & 3, (j) % 3, 18);                                                              \
    } while (0)
#define P2_BEGIN(role)                                                                            \
    do {                                                                                          \
        WAIT_VM(0);                                                                               \
        if (lane == 0) flag_set(ws + WS_FP1 + (size_t)((((role) * 16 + rb) * 16 + cb) * 4 + wc) * 8, tok); \
        const int fi_ = (((role) * 16 + rb2) * 16 + 4 * wc + ((lane >> 1) & 3)) * 4 + 2 * hh + (lane & 1); \
        flag_wait(ws + WS_FP1 + (size_t)fi_ * 8, true, tok);                                      \
        P2_LDA(0, pa); P2_LDA(1, pa + 8192); P2_LDA(2, pa + 16384);                               \
        _Pragma("unroll") for (int r = 0; r < 4; ++r) _Pragma("unroll") for (int n = 0; n < 5; ++n) ac2[r][n] = (f32x4){0.f, 0.f, 0.f, 0.f}; \
    } while (0)

    if (wave < 4) {
        const bool do_gate = (cb == 0);
        f32x4 acc[6][2];
#pragma unroll
        for (int a = 0; a < 6; ++a) { acc[a][0] = (f32x4){0.f, 0.f, 0.f, 0.f}; acc[a][1] = (f32x4){0.f, 0.f, 0.f, 0.f}; }
        s16x8 fx[2][2], fw[2][5];
        const int woff = 16384 + t * 128;
#define S_READ(b, sb, co)                                                                         \
    do {                                                                                          \
        fx[b][0] = *(const s16x8*)((sb) + xoff + (co)); fx[b][1] = *(const s16x8*)((sb) + xoff + 2048 + (co)); \
        _Pragma("unroll") for (int T_ = 0; T_ < 5; ++T_) fw[b][T_] = *(const s16x8*)((sb) + woff + T_ * 2048 + (co)); \
    } while (0)
#define S_GATE(b, kk)                                                                             \
    do {                                                                                          \
        if (do_gate) {                                                                            \
            const s16x8 gv = *(const s16x8*)(sm + K1_GW + ((kk) + 8 * q) * 2);                    \
            acc[5][0] = mfma16(gv, fx[b][0], acc[5][0]); acc[5][1] = mfma16(gv, fx[b][1], acc[5][1]); \
        }                                                                                         \
    } while (0)
#define S_BODY(it, bo, bn, MODE)                                                                  \
    do {                                                                                          \
        S_READ(1, sm + (bo), co1);                                                                \
        _Pragma("unroll") for (int T = 0; T < 5; ++T) { acc[T][0] = mfma16(fw[0][T], fx[0][0], acc[T][0]); acc[T][1] = mfma16(fw[0][T], fx[0][1], acc[T][1]); } \
        S_GATE(0, 64 * (it));                                                                     \
        SCHED_FENCE();                                                                            \
        if ((MODE) < 3) WAIT_VM((MODE) < 2 ? 5 : 0);                                              \
        WAIT_LGKM0();                                                                             \
        __builtin_amdgcn_s_barrier();                                                             \
        asm volatile("" ::: "memory");                                                            \
        SCHED_FENCE();                                                                            \
        if ((MODE) < 3) S_READ(0, sm + (bn), co0);                                                \
        _Pragma("unroll") for (int T = 0; T < 5; ++T) {                                           \
            acc[T][0] = mfma16(fw[1][T], fx[1][0], acc[T][0]); acc[T][1] = mfma16(fw[1][T], fx[1][1], acc[T][1]); \
            if ((MODE) == 0) { SCHED_FENCE(); K1_SLOT(T, bo); SCHED_FENCE(); }                    \
        }                                                                                         \
        S_GATE(1, 64 * (it) + 32);                                                                \
    } while (0)
        S_READ(0, sm, co0);
        pretile_out();
        int bo = 0, bn = K1_STAGE;
#pragma unroll 1
        for (int it = 0; it < 7; ++it) {
            S_BODY(it, bo, bn, 0);
            if (it == 1 && tid == 0) flag_set(ws + WS_FPT + (size_t)bid * 8, tok);
            if (it == 2 && wave == 0 && lane < 40) {
                const float lr = bflo(*(const u16*)(sm + K1_PLR + lane * 2)), li = bflo(*(const u16*)(sm + K1_PLI + lane * 2));
                const float ld = bflo(*(const u16*)(sm + K1_PLD + lane * 2));
                const float dt = __builtin_amdgcn_exp2f(ld * LOG2E);
                f32x2 Lp = lam_bar(lr, li, ld);
                f32x2* tb = (f32x2*)(sm + K1_TAB + lane * 64);
                const float nr = Lp.x - 1.0f, ni = Lp.y;
                const float rden = 1.0f / (lr * lr + li * li);
                tb[6] = (f32x2){(nr * lr + ni * li) * rden, (ni * lr - nr * li) * rden};
                tb[7] = (f32x2){dt * lr * LOG2E, dt * li * INV2PI};
#pragma unroll
                for (int e = 0; e < 6; ++e) { tb[e] = Lp; Lp = cmul(Lp, Lp); }
            }
            bo = bn; bn = bn == 2 * K1_STAGE ? 0 : bn + K1_STAGE;
        }
        S_BODY(7, K1_STAGE, 2 * K1_STAGE, 1); S_BODY(8, 2 * K1_STAGE, 0, 2); S_BODY(9, 0, 0, 3);
        p2_prefetch(10);
        if (do_gate && q == 0) {
            f32x2 gg = {1.0f / (1.0f + __expf(-acc[5][0][0])), 1.0f / (1.0f + __expf(-acc[5][1][0]))};
            st64_wt(ws + WS_G + (128 * rb + 32 * wc + 2 * t) * 4, __builtin_bit_cast(u32x2, gg));
        }
        f32x2 h0[5][2], h1[5][2];
#pragma unroll
        for (int T = 0; T < 5; ++T) {
#pragma unroll
            for (int cc = 0; cc < 2; ++cc) {
                const int chl = 8 * T + 2 * q + cc;
                const f32x4* tb = (const f32x4*)(sm + K1_TAB + chl * 64);
                const f32x4 t0 = tb[0], t1 = tb[1], t2 = tb[2], t3 = tb[3];
                const f32x2 L1 = {t0[0], t0[1]}, L2 = {t0[2], t0[3]}, L4 = {t1[0], t1[1]}, L8 = {t1[2], t1[3]}, L16 = {t2[0], t2[1]};
                const f32x2 cf = {t3[0], t3[1]};
                const f32x2 u0 = cmul_sc(cf, (f32x2){acc[T][0][2 * cc], acc[T][0][2 * cc + 1]});
                const f32x2 u1 = cmul_sc(cf, (f32x2){acc[T][1][2 * cc], acc[T][1][2 * cc + 1]});
                f32x2 a = cfma(L1, u0, u1);
                a = cfma(L2, (f32x2){shr_zero<1>(a.x), shr_zero<1>(a.y)}, a);
                a = cfma(L4, (f32x2){shr_zero<2>(a.x), shr_zero<2>(a.y)}, a);
                a = cfma(L8, (f32x2){shr_zero<4>(a.x), shr_zero<4>(a.y)}, a);
                a = cfma(L16, (f32x2){shr_zero<8>(a.x), shr_zero<8>(a.y)}, a);
                h0[T][cc] = cfma(L1, (f32x2){shr_zero<1>(a.x), shr_zero<1>(a.y)}, u0);
                h1[T][cc] = a;
                if (t == 15) *(f32x2*)(sm + K1_XCH + (wc * 40 + chl) * 8) = a;
            }
        }
        WAIT_LGKM0();
        unsigned old = 0;
        if (lane == 0) old = __hip_atomic_fetch_add((LAS unsigned*)(sm + K1_CNT), 1u, __ATOMIC_RELAXED, __HIP_MEMORY_SCOPE_WORKGROUP);
        old = __builtin_amdgcn_readfirstlane(old);
        asm volatile("" ::: "memory");
        if (old == 3) {
            const int cl = lane < 40 ? lane : 0;
            const f32x2 L32 = *(const f32x2*)(sm + K1_TAB + cl * 64 + 40);
            const f32x2* xc = (const f32x2*)(sm + K1_XCH);
            f32x2 pre[4], s = {0.f, 0.f};
#pragma unroll
            for (int w = 0; w < 4; ++w) { pre[w] = s; s = cfma(L32, s, xc[w * 40 + cl]); }
            if (lane < 40) st64_wt(ws + WS_AGG + (size_t)(cb * 16 + rb) * 320 + lane * 8, __builtin_bit_cast(u32x2, s));
            WAIT_VM(0);
            if (lane == 0) flag_set(ws + WS_FAG + (size_t)(cb * 16 + rb) * 8, tok);
            f32x2 cy = {0.f, 0.f};
            if (rb > 0) {
                flag_wait(ws + WS_FAG + (size_t)(cb * 16 + (lane < rb ? lane : 0)) * 8, true, tok);
                u32x2 ag[15];
#pragma unroll
                for (int j = 0; j < 15; ++j)
                    asm volatile("global_load_dwordx2 %0, %1, off sc1" : "=v"(ag[j]) : "v"(ws + WS_AGG + (size_t)(cb * 16 + (j < rb ? j : 0)) * 320 + cl * 8) : "memory");
                const f32x2 L64 = cmul(L32, L32), L128 = cmul(L64, L64);
                asm volatile("s_waitcnt vmcnt(0)" : "+v"(ag[0]), "+v"(ag[1]), "+v"(ag[2]), "+v"(ag[3]), "+v"(ag[4]), "+v"(ag[5]), "+v"(ag[6]), "+v"(ag[7]),
                             "+v"(ag[8]), "+v"(ag[9]), "+v"(ag[10]), "+v"(ag[11]), "+v"(ag[12]), "+v"(ag[13]), "+v"(ag[14]) :: "memory");
#pragma unroll
                for (int j = 0; j < 15; ++j) {
                    const f32x2 nx = cfma(L128, cy, __builtin_bit_cast(f32x2, ag[j]));
                    cy = (j < rb) ? nx : cy;
                }
            }
            if (lane < 40) {
#pragma unroll
                for (int w = 0; w < 4; ++w) {
                    *(f32x2*)(sm + K1_CAR + (w * 40 + lane) * 8) = (f32x2){pre[w].x + cy.x, pre[w].y + cy.y};
                    cy = cmul(cy, L32);
                }
            }
            WAIT_LGKM0();
            if (lane == 0) __hip_atomic_store((LAS unsigned*)(sm + K1_CNT + 4), 1u, __ATOMIC_RELAXED, __HIP_MEMORY_SCOPE_WORKGROUP);
        } else {
            for (unsigned spins = 0; spins < (1u << 22); ++spins) {
                if (__hip_atomic_load((LAS unsigned*)(sm + K1_CNT + 4), __ATOMIC_RELAXED, __HIP_MEMORY_SCOPE_WORKGROUP)) break;
                __builtin_amdgcn_s_sleep(2);
            }
        }
        asm volatile("" ::: "memory");
        const float fn = (float)(2 * t + 1);
#pragma unroll
        for (int T = 0; T < 5; ++T) {
            u32x2 o0, o1;
#pragma unroll
            for (int cc = 0; cc < 2; ++cc) {
                const int chl = 8 * T + 2 * q + cc;
                const f32x2 L1 = *(const f32x2*)(sm + K1_TAB + chl * 64), zz = *(const f32x2*)(sm + K1_TAB + chl * 64 + 56);
                const f32x2 cy = *(const f32x2*)(sm + K1_CAR + (wc * 40 + chl) * 8);
                const float mg = __builtin_amdgcn_exp2f(fn * zz.x);
                const float rv = __builtin_amdgcn_fractf(fn * zz.y);
                const f32x2 c0 = cmul_sc((f32x2){mg * __builtin_amdgcn_cosf(rv), mg * __builtin_amdgcn_sinf(rv)}, cy);
                const f32x2 v1 = cfma(L1, c0, h1[T][cc]);
                o0[cc] = pack2(h0[T][cc].x + c0.x, -(h0[T][cc].y + c0.y));
                o1[cc] = pack2(v1.x, -v1.y);
            }
            const int Tg = 5 * cb + T;
            char* dst = a2 + (size_t)(Tg >> 1) * 8192 + (Tg & 1) * 8;
            st64_wt(dst, o0);
            st64_wt(dst + 1024, o1);
        }
        P2_BEGIN(0);
        P2_STEP(0, 10, 8); P2_STEP(1, 10, 8); P2_STEP(2, 10, 8); P2_STEP(3, 10, 8); P2_STEP(4, 10, 8); P2_STEP(5, 10, 13);
        P2_STEP(6, 10, 9); P2_STEP(7, 10, 9); P2_STEP(8, 10, 5); P2_STEP(9, 10, 0);
    } else {
        const int bb = q & 1;
        const int trb = 26624 + (8 * q + (t >> 2)) * 192 + (lane & 3) * 8;
        const int tra = trb + bb * 32;
        const int tr5 = trb + (bb ? 0 : 160);
        f32x4 acc[6][2];
#pragma unroll
        for (int j = 0; j < 6; ++j) {
            const u32x2 bp = *(const u32x2*)(sm + K1_PB1 + (16 * j + 4 * q) * 2);
            acc[j][0] = (f32x4){bflo(bp[0]), bfhi(bp[0]), bflo(bp[1]), bfhi(bp[1])};
            acc[j][1] = acc[j][0];
        }
        s16x8 fx[2][2], fw[2][6];
#define M_READ(b, sb, co, ks)                                                                     \
    do {                                                                                          \
        fx[b][0] = *(const s16x8*)((sb) + xoff + (co)); fx[b][1] = *(const s16x8*)((sb) + xoff + 2048 + (co)); \
        _Pragma("unroll") for (int j_ = 0; j_ < 6; ++j_) {                                        \
            const char* pa_ = (sb) + (j_ < 5 ? tra + j_ * 32 : tr5) + (ks) * 32 * 192;            \
            const s16x4 lo_ = __builtin_amdgcn_ds_read_tr16_b64_v4i16((LAS s16x4*)pa_);           \
            const s16x4 hi_ = __builtin_amdgcn_ds_read_tr16_b64_v4i16((LAS s16x4*)(pa_ + 4 * 192)); \
            fw[b][j_] = __builtin_shufflevector(lo_, hi_, 0, 1, 2, 3, 4, 5, 6, 7);                \
        }                                                                                         \
    } while (0)
#define M_BODY(bo, bn, MODE)                                                                      \
    do {                                                                                          \
        M_READ(1, sm + (bo), co1, 1);                                                             \
        _Pragma("unroll") for (int j = 0; j < 6; ++j) { acc[j][0] = mfma16(fw[0][j], fx[0][0], acc[j][0]); acc[j][1] = mfma16(fw[0][j], fx[0][1], acc[j][1]); } \
        SCHED_FENCE();                                                                            \
        if ((MODE) < 3) WAIT_VM((MODE) < 2 ? 5 : 0);                                              \
        WAIT_LGKM0();                                                                             \
        __builtin_amdgcn_s_barrier();                                                             \
        asm volatile("" ::: "memory");                                                            \
        SCHED_FENCE();                                                                            \
        if ((MODE) < 3) M_READ(0, sm + (bn), co0, 0);                                             \
        _Pragma("unroll") for (int j = 0; j < 6; ++j) {                                           \
            acc[j][0] = mfma16(fw[1][j], fx[1][0], acc[j][0]); acc[j][1] = mfma16(fw[1][j], fx[1][1], acc[j][1]); \
            if ((MODE) == 0 && j < 5) { SCHED_FENCE(); K1_SLOT(j, bo); SCHED_FENCE(); }           \
        }                                                                                         \
    } while (0)
        M_READ(0, sm, co0, 0);
        pretile_out();
        int bo = 0, bn = K1_STAGE;
#pragma unroll 1
        for (int it = 0; it < 7; ++it) {
            M_BODY(bo, bn, 0);
            bo = bn; bn = bn == 2 * K1_STAGE ? 0 : bn + K1_STAGE;
        }
        M_BODY(K1_STAGE, 2 * K1_STAGE, 1); M_BODY(2 * K1_STAGE, 0, 2); M_BODY(0, 0, 3);
        p2_prefetch(12);
#pragma unroll
        for (int v = 0; v < 3; ++v) {
#pragma unroll
            for (int rt = 0; rt < 2; ++rt) {
                u32x4 o;
#pragma unroll
                for (int jj = 0; jj < 2; ++jj) {
                    float r[4];
#pragma unroll
                    for (int i = 0; i < 4; ++i) {
                        const float h = acc[2 * v + jj][rt][i];
                        const float e = __builtin_amdgcn_exp2f(h * LOG2E);
                        const float d = fmaf(e, e + 2.0f, 2.0f);
                        r[i] = fmaf(-2.0f, h * __builtin_amdgcn_rcpf(d), h);
                    }
                    o[2 * jj] = pack2(r[0], r[1]);
                    o[2 * jj + 1] = pack2(r[2], r[3]);
                }
                st128_wt(a2 + (size_t)(40 + 3 * cb + v) * 8192 + rt * 1024, o);
            }
        }
        P2_BEGIN(1);
        P2_STEP(0, 12, 8); P2_STEP(1, 12, 8); P2_STEP(2, 12, 8); P2_STEP(3, 12, 8); P2_STEP(4, 12, 8); P2_STEP(5, 12, 13);
        P2_STEP(6, 12, 9); P2_STEP(7, 12, 9); P2_STEP(8, 12, 9); P2_STEP(9, 12, 9); P2_STEP(10, 12, 5); P2_STEP(11, 12, 0);
    }
    flag_wait(ws + WS_FP1 + (size_t)(rb2 * 64 + 2 * hh + (lane & 1)) * 8, true, tok);
    u32x2 xv[3], dv[3];
    float gv[3];
    int oo[3];
#pragma unroll
    for (int m = 0; m < 3; ++m) {
        int tile = wave + 8 * m; tile = tile < 20 ? tile : wave;
        const int r = tile / 5, n = tile - 5 * r;
        const int tm = 64 * RG + 32 * (r >> 1) + 2 * t + (r & 1), col = 80 * CB + 16 * n + 4 * q;
        oo[m] = tm * DM + col;
        xv[m] = *(const u32x2*)(x + oo[m]);
        dv[m] = *(const u32x2*)(Dskip + col);
        gv[m] = __uint_as_float(__hip_atomic_load((const GAS unsigned*)(ws + WS_G + tm * 4), __ATOMIC_RELAXED, __HIP_MEMORY_SCOPE_AGENT));
    }
    __syncthreads();
#pragma unroll
    for (int r = 0; r < 4; ++r)
#pragma unroll
        for (int n = 0; n < 5; ++n) *(f32x4*)(sm + ((r * 5 + n) * 8 + wave) * 1024 + lane * 16) = ac2[r][n];
    __syncthreads();
#pragma unroll
    for (int m = 0; m < 3; ++m) {
        const int tile = wave + 8 * m;
        if (tile < 20) {
            const char* rp = sm + tile * 8192 + lane * 16;
            const f32x4 vs = (*(const f32x4*)rp + *(const f32x4*)(rp + 1024)) + (*(const f32x4*)(rp + 2048) + *(const f32x4*)(rp + 3072));
            const f32x4 vm = (*(const f32x4*)(rp + 4096) + *(const f32x4*)(rp + 5120)) + (*(const f32x4*)(rp + 6144) + *(const f32x4*)(rp + 7168));
            const float g = gv[m];
            f32x4 v = vs * (2.0f * g) + vm * (1.0f - g);
            v[0] += g * bflo(xv[m][0]) * bflo(dv[m][0]);
            v[1] += g * bfhi(xv[m][0]) * bfhi(dv[m][0]);
            v[2] += g * bflo(xv[m][1]) * bflo(dv[m][1]);
            v[3] += g * bfhi(xv[m][1]) * bfhi(dv[m][1]);
            st128_wt(out + oo[m], __builtin_bit_cast(u32x4, v));
        }
    }
    if (bid == 0 && wave == 0) {
#pragma unroll 1
        for (int k = 0; k < 4; ++k) flag_wait(ws + WS_FPT + (size_t)(64 * k + lane) * 8, true, tok);
        if (lane == 0) flag_set(ws + WS_EP, tok);
    }
}

extern "C" void kernel_launch(void* const* d_in, const int* in_sizes, int n_in, void* d_out, int out_size, void* d_ws,
                              size_t ws_size, hipStream_t stream) {
    kf<<<256, 512, 0, stream>>>((const u16*)d_in[0], (const u16*)d_in[1], (const u16*)d_in[2], (const u16*)d_in[3], (const u16*)d_in[4],
                                (const u16*)d_in[5], (const u16*)d_in[6], (const u16*)d_in[7], (const u16*)d_in[8], (const u16*)d_in[10],
                                (const u16*)d_in[11], (const u16*)d_in[12], (const u16*)d_in[13], (char*)d_ws, (float*)d_out);
}
```

```cpp
#include <hip/hip_runtime.h>
#include <stdint.h>

typedef short s16x4 __attribute__((ext_vector_type(4)));
typedef short s16x8 __attribute__((ext_vector_type(8)));
typedef __bf16 bf16x2 __attribute__((ext_vector_type(2)));
typedef float f32x2 __attribute__((ext_vector_type(2)));
typedef float f32x4 __attribute__((ext_vector_type(4)));
typedef unsigned u32x2 __attribute__((ext_vector_type(2)));
typedef unsigned u32x4 __attribute__((ext_vector_type(4)));
typedef unsigned short u16;
#define LAS __attribute__((address_space(3)))
#define GAS __attribute__((address_space(1)))
#define DI __device__ __forceinline__

constexpr int SEQ = 2048, DM = 640, NP = 640, NF = 1536;
constexpr int NKS = 88;

constexpr size_t WS_A2 = 0;
constexpr size_t WS_W2 = WS_A2 + (size_t)16 * NKS * 8192;
constexpr size_t WS_G = WS_W2 + (size_t)8 * NKS * 5120;
constexpr size_t WS_AGG = WS_G + 2048 * 4;
constexpr size_t WS_EP = WS_AGG + 16 * 16 * 640;
constexpr size_t WS_FPT = WS_EP + 256;
constexpr size_t WS_FAG = WS_FPT + 2048;
constexpr size_t WS_FP1 = WS_FAG + 2048;
constexpr size_t WS_JUNK = WS_FP1 + 16384;

DI float bflo(unsigned v) { return __uint_as_float(v << 16); }
DI float bfhi(unsigned v) { return __uint_as_float(v & 0xffff0000u); }
DI unsigned pack2(float a, float b) {
    f32x2 p = {a, b};
    return __builtin_bit_cast(unsigned, __builtin_convertvector(p, bf16x2));
}
DI f32x4 mfma16(s16x8 a, s16x8 b, f32x4 c) { return __builtin_amdgcn_mfma_f32_16x16x32_bf16(a, b, c, 0, 0, 0); }
template <int S> DI float shr_zero(float v) {
    return __builtin_bit_cast(float, __builtin_amdgcn_update_dpp(0, __builtin_bit_cast(int, v), 0x110 + S, 0xf, 0xf, true));
}
DI f32x2 cmul(f32x2 a, f32x2 b) { return (f32x2){a.x * b.x - a.y * b.y, a.x * b.y + a.y * b.x}; }
DI f32x2 cfma(f32x2 a, f32x2 b, f32x2 c) {
    return (f32x2){fmaf(-a.y, b.y, fmaf(a.x, b.x, c.x)), fmaf(a.y, b.x, fmaf(a.x, b.y, c.y))};
}
DI f32x2 cmul_pk(f32x2 a, f32x2 b) { return (f32x2){fmaf(-a.y, b.y, a.x * b.x), fmaf(a.y, b.x, a.x * b.y)}; }
DI f32x2 cmul_sc(f32x2 a, f32x2 b) {
    float p = a.x * b.x, r = a.x * b.y;
    asm("" : "+v"(p));
    asm("" : "+v"(r));
    return (f32x2){fmaf(-a.y, b.y, p), fmaf(a.y, b.x, r)};
}
DI float dot2bf(unsigned a, unsigned b, float c) {
    return __builtin_amdgcn_fdot2_f32_bf16(__builtin_bit_cast(bf16x2, a), __builtin_bit_cast(bf16x2, b), c, false);
}
#define WAIT_VM(n) asm volatile("s_waitcnt vmcnt(%0)" ::"n"(n) : "memory")
#define WAIT_LGKM0() asm volatile("s_waitcnt lgkmcnt(0)" ::: "memory")
#define ACQUIRE() asm volatile("buffer_inv sc1" ::: "memory")
#define SCHED_FENCE() __builtin_amdgcn_sched_barrier(0)

DI void glds16(const void* gsrc, unsigned lds_dst) {
    unsigned keep;
    asm volatile("s_mov_b32 %0, m0\n\ts_mov_b32 m0, %2\n\ts_nop 0\n\tglobal_load_lds_dwordx4 %1, off\n\ts_mov_b32 m0, %0"
                 : "=&s"(keep) : "v"(gsrc), "s"(lds_dst) : "memory");
}
DI void glds16x4(const void* gsrc, unsigned lds_dst) {
    unsigned keep;
    asm volatile("s_mov_b32 %0, m0\n\ts_mov_b32 m0, %2\n\ts_nop 0\n\tglobal_load_lds_dwordx4 %1, off\n\t"
                 "global_load_lds_dwordx4 %1, off offset:1024\n\tglobal_load_lds_dwordx4 %1, off offset:2048\n\t"
                 "global_load_lds_dwordx4 %1, off offset:3072\n\ts_mov_b32 m0, %0"
                 : "=&s"(keep) : "v"(gsrc), "s"(lds_dst) : "memory");
}
DI void st128_wt(void* p, u32x4 v) { asm volatile("global_store_dwordx4 %0, %1, off sc1\n\ts_nop 1" :: "v"(p), "v"(v) : "memory"); }
DI void st64_wt(void* p, u32x2 v) { asm volatile("global_store_dwordx2 %0, %1, off sc1\n\ts_nop 1" :: "v"(p), "v"(v) : "memory"); }
DI unsigned ld32_asm(const void* p) { unsigned r; asm volatile("global_load_dword %0, %1, off" : "=v"(r) : "v"(p) : "memory"); return r; }
DI unsigned ld16_asm(const void* p) { unsigned r; asm volatile("global_load_ushort %0, %1, off" : "=v"(r) : "v"(p) : "memory"); return r; }
DI u32x2 ld64_asm(const void* p) { u32x2 r; asm volatile("global_load_dwordx2 %0, %1, off" : "=v"(r) : "v"(p) : "memory"); return r; }
DI u32x4 ld128_asm(const void* p) { u32x4 r; asm volatile("global_load_dwordx4 %0, %1, off" : "=v"(r) : "v"(p) : "memory"); return r; }
constexpr float LOG2E = 1.44269504088896341f, INV2PI = 0.15915494309189535f;
DI f32x2 lam_bar(float lr, float li, float ld) {
    const float dt = __builtin_amdgcn_exp2f(ld * LOG2E);
    const float m = __builtin_amdgcn_exp2f(dt * lr * LOG2E);
    const float rv = __builtin_amdgcn_fractf(dt * li * INV2PI);
    return (f32x2){m * __builtin_amdgcn_cosf(rv), m * __builtin_amdgcn_sinf(rv)};
}

typedef unsigned long long u64;
DI void glds16_c(const void* gsrc, unsigned lds_dst) {
    unsigned keep;
    lds_dst = __builtin_amdgcn_readfirstlane(lds_dst);
    asm volatile("s_mov_b32 %0, m0\n\ts_mov_b32 m0, %2\n\ts_nop 0\n\tglobal_load_lds_dwordx4 %1, off sc1\n\ts_mov_b32 m0, %0"
                 : "=&s"(keep) : "v"(gsrc), "s"(lds_dst) : "memory");
}
DI void glds16x4_c(const void* gsrc, unsigned lds_dst) {
    unsigned keep;
    asm volatile("s_mov_b32 %0, m0\n\ts_mov_b32 m0, %2\n\ts_nop 0\n\tglobal_load_lds_dwordx4 %1, off sc1\n\t"
                 "global_load_lds_dwordx4 %1, off offset:1024 sc1\n\tglobal_load_lds_dwordx4 %1, off offset:2048 sc1\n\t"
                 "global_load_lds_dwordx4 %1, off offset:3072 sc1\n\ts_mov_b32 m0, %0"
                 : "=&s"(keep) : "v"(gsrc), "s"(lds_dst) : "memory");
}
DI void flag_set(char* p, u64 tok) { __hip_atomic_store((GAS u64*)p, tok, __ATOMIC_RELAXED, __HIP_MEMORY_SCOPE_AGENT); }
DI void flag_wait(const char* p, bool need, u64 tok) {
    for (unsigned spins = 0; spins < (1u << 20); ++spins) {
        u64 v = tok;
        if (need) v = __hip_atomic_load((const GAS u64*)p, __ATOMIC_RELAXED, __HIP_MEMORY_SCOPE_AGENT);
        if (__all(v == tok)) break;
        __builtin_amdgcn_s_sleep(8);
    }
    asm volatile("" ::: "memory");
}

constexpr int K1_STAGE = 38 * 1024;
constexpr int K1_GW = 3 * K1_STAGE;
constexpr int K1_PLR = K1_GW + 1280, K1_PLI = K1_GW + 1360, K1_PLD = K1_GW + 1440, K1_PB1 = K1_GW + 1536;
constexpr int K1_TAB = K1_GW + 2048;
constexpr int K1_XCH = K1_TAB + 2560;
constexpr int K1_CAR = K1_XCH + 1280;
constexpr int K1_CNT = K1_CAR + 1280;
constexpr int K1_PT = 122 * 1024;
constexpr int K1_FM = 140 * 1024;
constexpr int KF_LDS = 160 * 1024;

__global__ __launch_bounds__(512, 2) void kf(const u16* __restrict__ x, const u16* __restrict__ Lre, const u16* __restrict__ Lim,
                                             const u16* __restrict__ Bre, const u16* __restrict__ Bim, const u16* __restrict__ Cre,
                                             const u16* __restrict__ Cim, const u16* __restrict__ logdt, const u16* __restrict__ Dskip,
                                             const u16* __restrict__ w1, const u16* __restrict__ b1, const u16* __restrict__ w2,
                                             const u16* __restrict__ gw, char* __restrict__ ws, float* __restrict__ out) {
    __shared__ __attribute__((aligned(16))) char sm[KF_LDS];
    const int tid = threadIdx.x, lane = tid & 63;
    const int wave = __builtin_amdgcn_readfirstlane(tid >> 6);
    const int bid = blockIdx.x, xcd = bid & 7, idx = bid >> 3;
    const int rb = 8 * (xcd & 1) + (idx & 7);
    const int cb = 4 * (xcd >> 1) + (idx >> 3);
    const int RG = 8 * (xcd >> 1) + (idx & 7);
    const int CB = 4 * (xcd & 1) + (idx >> 3);
    const int t = lane & 15, q = lane >> 4;
    const unsigned smb = (unsigned)(size_t)(LAS char*)sm;
    unsigned cjunk = 0;
    if (wave == 7) {
        u64 pc;
        asm volatile("s_getpc_b64 %0" : "=s"(pc));
        const char* cp = (const char*)(pc & ~255ull) + lane * 128;
#pragma unroll
        for (int k = 0; k < 4; ++k) asm volatile("global_load_dword %0, %1, off" : "+v"(cjunk) : "v"(cp + k * 8192) : "memory");
    }
    const char* src[5];
    unsigned ldo[5];
    int incE, strE;
    {
        const int l8 = lane >> 3;
        auto xsrc = [&](int s) {
            const int p = 8 * s + l8;
            const int tm = 128 * rb + 32 * (p >> 5) + 2 * (p & 15) + ((p >> 4) & 1);
            return (const char*)(x + tm * DM + (((lane & 7) ^ ((p >> 1) & 7)) << 3));
        };
        auto wsrc = [&](int s) {
            const int p = 8 * s + l8;
            const int ch = 40 * cb + 8 * (p >> 4) + ((p & 15) >> 1);
            return (const char*)(((p & 1) ? Bim : Bre) + ch * DM + (((lane & 7) ^ ((p >> 1) & 7)) << 3));
        };
        auto msrc = [&](int s) {
            const int gI = 64 * s + lane, row = gI / 12, cp = gI - 12 * row;
            int j = (cp >> 1) - ((row >> 3) & 1);
            j += j < 0 ? 6 : 0;
            return (const char*)(w1 + row * NF + 96 * cb + 16 * j + 8 * (cp & 1));
        };
        src[0] = xsrc(2 * wave); ldo[0] = smb + 2048 * wave;
        src[1] = xsrc(2 * wave + 1); ldo[1] = smb + 2048 * wave + 1024;
        src[2] = wsrc(wave); ldo[2] = smb + 16384 + 1024 * wave;
        src[3] = msrc(wave); ldo[3] = smb + 26624 + 1024 * wave;
        if (wave < 2) { src[4] = wsrc(8 + wave); ldo[4] = smb + 16384 + 1024 * (8 + wave); incE = 128; strE = 1; }
        else if (wave < 6) { src[4] = msrc(6 + wave); ldo[4] = smb + 26624 + 1024 * (6 + wave); incE = 64 * NF * 2; strE = 1; }
        else {
            const u16* p = gw + lane * 8;
            if (wave == 7) {
                p = gw + 512 + t * 8;
                if (lane >= 16 && lane < 21) p = Lre + 40 * cb + (lane - 16) * 8;
                if (lane >= 21 && lane < 26) p = Lim + 40 * cb + (lane - 21) * 8;
                if (lane >= 26 && lane < 31) p = logdt + 40 * cb + (lane - 26) * 8;
                if (lane >= 32 && lane < 44) p = b1 + 96 * cb + (lane - 32) * 8;
            }
            src[4] = (const char*)p; ldo[4] = smb + K1_GW + (wave - 6) * 1024; incE = 0; strE = 0;
        }
    }
#define K1_SLOT(i, boff)                                                                 \
    do {                                                                                 \
        glds16(src[i], ldo[i] + ((i) == 4 ? strE * (boff) : (boff)));                    \
        src[i] += (i) < 3 ? 128 : (i) == 3 ? 64 * NF * 2 : incE;                         \
    } while (0)
    K1_SLOT(0, 0); K1_SLOT(1, 0); K1_SLOT(2, 0); K1_SLOT(3, 0); K1_SLOT(4, 0);
    u32x2 epv;
    asm volatile("global_load_dwordx2 %0, %1, off sc1" : "=v"(epv) : "v"(ws + WS_EP) : "memory");

#pragma unroll
    for (int st = 1; st < 3; ++st) {
        K1_SLOT(0, st * K1_STAGE); K1_SLOT(1, st * K1_STAGE); K1_SLOT(2, st * K1_STAGE); K1_SLOT(3, st * K1_STAGE); K1_SLOT(4, st * K1_STAGE);
    }
    const int pf0 = 14 * bid + 2 * wave;
    char* ptl = sm + K1_PT + wave * 2048;
#pragma unroll
    for (int u = 0; u < 2; ++u) {
        int f = pf0 + u; f = (wave < 7 && f < 3520) ? f : 0;
        const int cK = f / 5, nt = f - 5 * cK, cB = cK / NKS, KS = cK - NKS * cB, d0 = 80 * cB + 16 * nt;
        const u16* p0 = (KS < 40) ? ((lane >> 5) ? Cim : Cre) + (d0 + ((lane >> 1) & 15)) * NP + 16 * KS + 8 * (lane & 1)
                                  : w2 + (32 * (KS - 40) + (lane >> 1)) * DM + d0 + 8 * (lane & 1);
        glds16(p0, smb + K1_PT + wave * 2048 + u * 1024);
    }
    if (tid == 0) *(u32x2*)(sm + K1_CNT) = (u32x2){0u, 0u};

    const int wc = wave & 3;
    const int xoff = (32 * wc + t) * 128;
    const int co0 = (q ^ ((t >> 1) & 7)) * 16, co1 = co0 ^ 64;
    char* a2 = ws + WS_A2 + (size_t)rb * NKS * 8192 + (2 * wc) * 1024 + lane * 16;

    asm volatile("s_waitcnt vmcnt(12)" : "+v"(epv), "+v"(cjunk) :: "memory");
    WAIT_LGKM0();
    __builtin_amdgcn_s_barrier();
    asm volatile("" ::: "memory");
    const u64 tok = (((u64)(unsigned)__builtin_amdgcn_readfirstlane(epv[1]) << 32) | (unsigned)__builtin_amdgcn_readfirstlane(epv[0])) + 1ull;
    auto pretile_out = [&]() {
        {
            const int tro = (4 * q + (t >> 2)) * 32 + (lane & 3) * 8;
#pragma unroll
            for (int u = 0; u < 2; ++u) {
                const int f = pf0 + u;
                const int cK = f / 5, KS = cK % NKS;
                const char* pp = ptl + u * 1024;
                u32x4 wf;
                if (KS < 40) {
                    const unsigned r0 = *(const unsigned*)(pp + t * 32 + 4 * q), r1 = *(const unsigned*)(pp + t * 32 + 16 + 4 * q);
                    const unsigned i0 = *(const unsigned*)(pp + 512 + t * 32 + 4 * q), i1 = *(const unsigned*)(pp + 512 + t * 32 + 16 + 4 * q);
                    wf[0] = (r0 & 0xffffu) | (i0 << 16); wf[1] = (r0 >> 16) | (i0 & 0xffff0000u);
                    wf[2] = (r1 & 0xffffu) | (i1 << 16); wf[3] = (r1 >> 16) | (i1 & 0xffff0000u);
                } else {
                    const s16x4 lo = __builtin_amdgcn_ds_read_tr16_b64_v4i16((LAS s16x4*)(pp + tro));
                    const s16x4 hi = __builtin_amdgcn_ds_read_tr16_b64_v4i16((LAS s16x4*)(pp + tro + 512));
                    wf = __builtin_bit_cast(u32x4, __builtin_shufflevector(lo, hi, 0, 1, 2, 3, 4, 5, 6, 7));
                }
                st128_wt(((wave < 7 && f < 3520) ? ws + WS_W2 + (size_t)f * 1024 : ws + WS_JUNK + (size_t)(bid * 16 + wave * 2 + u) * 1024) + lane * 16, wf);
            }
        }
    };

    const int rb2 = RG >> 1, hh = RG & 1;
    const int ks0 = wave < 4 ? 10 * wave : 40 + 12 * (wave - 4);
    const char* pa = ws + WS_A2 + ((size_t)rb2 * NKS + ks0) * 8192 + hh * 4096 + lane * 16;
    const char* pb = ws + WS_W2 + ((size_t)CB * NKS + ks0) * 5120 + 1024 + lane * 16;
    const int bco = (wc < 3 ? wc * 35840 : K1_PT);
    u32x4 Ab[4][4], Bb[3][5];
    f32x4 ac2[4][5];
#define P2_L1(dst, P, OFF) asm volatile("global_load_dwordx4 %0, %1, off offset:" #OFF " sc1" : "=&v"(dst) : "v"(P) : "memory")
#define P2_LDB(slot, P)                                                                           \
    do { P2_L1(Bb[slot][0], P, -1024); P2_L1(Bb[slot][1], P, 0); P2_L1(Bb[slot][2], P, 1024); P2_L1(Bb[slot][3], P, 2048); P2_L1(Bb[slot][4], P, 3072); } while (0)
#define P2_LDA(slot, P)                                                                           \
    do { P2_L1(Ab[slot][0], P, 0); P2_L1(Ab[slot][1], P, 1024); P2_L1(Ab[slot][2], P, 2048); P2_L1(Ab[slot][3], P, 3072); } while (0)
    auto pt_flag = [&](int nsteps, int i) {
        const int fa = (CB * NKS + ks0) * 5, w0 = fa / 14, w1_ = (fa + 5 * nsteps - 1) / 14;
        return ws + WS_FPT + (size_t)(w0 + i <= w1_ ? w0 + i : w0) * 8;
    };
#define P2_M2(ua, ub, i)                                                                          \
    do {                                                                                          \
        ac2[(i) / 5][(i) % 5] = mfma16(__builtin_bit_cast(s16x8, Bb[ub][(i) % 5]), __builtin_bit_cast(s16x8, Ab[ua][(i) / 5]), ac2[(i) / 5][(i) % 5]); \
        ac2[((i) + 1) / 5][((i) + 1) % 5] = mfma16(__builtin_bit_cast(s16x8, Bb[ub][((i) + 1) % 5]), __builtin_bit_cast(s16x8, Ab[ua][((i) + 1) / 5]), ac2[((i) + 1) / 5][((i) + 1) % 5]); \
        SCHED_FENCE();                                                                            \
    } while (0)
#define P2_STEP(j, NS, WN, BL)                                                                    \
    do {                                                                                          \
        asm volatile("s_waitcnt vmcnt(%9)" : "+v"(Ab[(j) & 3][0]), "+v"(Ab[(j) & 3][1]), "+v"(Ab[(j) & 3][2]), "+v"(Ab[(j) & 3][3]), \
                     "+v"(Bb[(j) % 3][0]), "+v"(Bb[(j) % 3][1]), "+v"(Bb[(j) % 3][2]), "+v"(Bb[(j) % 3][3]), "+v"(Bb[(j) % 3][4]) : "n"(WN) : "memory"); \
        SCHED_FENCE();                                                                            \
        const char* pa_ = pa + ((j) + 3) * 8192;                                                  \
        const char* pb_ = pb + ((j) + 2) * 5120;                                                  \
        constexpr bool LA_ = (j) + 3 < (NS), LBG_ = !(BL) && (j) + 2 < (NS), LBL_ = (BL) && (j) + 2 >= 3 && (j) + 2 < (NS); \
        P2_M2((j) & 3, (j) % 3, 0); if (LA_) { P2_L1(Ab[((j) + 3) & 3][0], pa_, 0); SCHED_FENCE(); }      \
        P2_M2((j) & 3, (j) % 3, 2); if (LA_) { P2_L1(Ab[((j) + 3) & 3][1], pa_, 1024); SCHED_FENCE(); }   \
        P2_M2((j) & 3, (j) % 3, 4); if (LA_) { P2_L1(Ab[((j) + 3) & 3][2], pa_, 2048); SCHED_FENCE(); }   \
        P2_M2((j) & 3, (j) % 3, 6); if (LA_) { P2_L1(Ab[((j) + 3) & 3][3], pa_, 3072); SCHED_FENCE(); }   \
        if (LBL_) { _Pragma("unroll") for (int n_ = 0; n_ < 5; ++n_) Bb[((j) + 2) % 3][n_] = *(const u32x4*)(sm + bco + (((j) + 2 - 3) * 5 + n_) * 1024 + lane * 16); SCHED_FENCE(); } \
        P2_M2((j) & 3, (j) % 3, 8); if (LBG_) { P2_L1(Bb[((j) + 2) % 3][0], pb_, -1024); SCHED_FENCE(); } \
        P2_M2((j) & 3, (j) % 3, 10); if (LBG_) { P2_L1(Bb[((j) + 2) % 3][1], pb_, 0); SCHED_FENCE(); }    \
        P2_M2((j) & 3, (j) % 3, 12); if (LBG_) { P2_L1(Bb[((j) + 2) % 3][2], pb_, 1024); SCHED_FENCE(); } \
        P2_M2((j) & 3, (j) % 3, 14); if (LBG_) { P2_L1(Bb[((j) + 2) % 3][3], pb_, 2048); SCHED_FENCE(); } \
        P2_M2((j) & 3, (j) % 3, 16); if (LBG_) { P2_L1(Bb[((j) + 2) % 3][4], pb_, 3072); SCHED_FENCE(); } \
        P2_M2((j) & 3, (j) % 3, 18);                                                              \
    } while (0)
    u32x2 xv[3], dv[3], gq[2];
    int oo[3];
#define P2_BEGIN(role)                                                                            \
    do {                                                                                          \
        WAIT_VM(0);                                                                               \
        if (lane == 0) flag_set(ws + WS_FP1 + (size_t)(((((role) * 16 + rb) * 2 + (wc >> 1)) * 16 + cb) * 2 + (wc & 1)) * 8, tok); \
        flag_wait(pt_flag((role) ? 12 : 10, lane), lane < 8, tok);                                \
        P2_LDB(0, pb); P2_LDB(1, pb + 5120); if ((role) == 0) P2_LDB(2, pb + 10240);              \
        _Pragma("unroll") for (int m = 0; m < 3; ++m) {                                           \
            int tile = wave + 8 * m; tile = tile < 20 ? tile : wave;                              \
            const int r = tile / 5, n = tile - 5 * r;                                             \
            const int col = 80 * CB + 16 * n + 4 * q;                                             \
            oo[m] = (64 * RG + 32 * (r >> 1) + 2 * t + (r & 1)) * DM + col;                       \
            xv[m] = ld64_asm(x + oo[m]); dv[m] = ld64_asm(Dskip + col);                           \
        }                                                                                         \
        const int fi_ = ((role) == 0 && lane >= 8) ? RG * 32 + (lane & 1)      \
                        : ((role) * 32 + RG) * 32 + 8 * wc + (lane & 7);                          \
        if ((role) == 0) {       \
            const char* g3 = pb - 1024 + 3 * 5120;                                                \
            bool done_ = false;                                                                   \
            _Pragma("unroll 1") for (int i = 0; i < 9; ++i) {                                     \
                const int o_ = i < 8 ? i * 4096 : 31744;                                          \
                glds16x4_c(g3 + o_, __builtin_amdgcn_readfirstlane(smb + bco + o_));              \
                if (!done_) {                                                                     \
                    u64 v_ = tok;                                                                 \
                    if (lane < 10) v_ = __hip_atomic_load((const GAS u64*)(ws + WS_FP1 + (size_t)fi_ * 8), __ATOMIC_RELAXED, __HIP_MEMORY_SCOPE_AGENT); \
                    done_ = __all(v_ == tok);                                                     \
                }                                                                                 \
            }                                                                                     \
        }                                                                                         \
        flag_wait(ws + WS_FP1 + (size_t)fi_ * 8, lane < ((role) ? 8 : 10), tok);                  \
        asm volatile("s_waitcnt vmcnt(0)" : "+v"(xv[0]), "+v"(xv[1]), "+v"(xv[2]), "+v"(dv[0]), "+v"(dv[1]), "+v"(dv[2]) :: "memory"); \
        if ((role) == 0) P2_GATES();                                                              \
        P2_LDA(0, pa); P2_LDA(1, pa + 8192); P2_LDA(2, pa + 16384);                               \
        _Pragma("unroll") for (int r = 0; r < 4; ++r) _Pragma("unroll") for (int n = 0; n < 5; ++n) ac2[r][n] = (f32x4){0.f, 0.f, 0.f, 0.f}; \
    } while (0)
#define P2_GATES()                                                                                \
    asm volatile("global_load_dwordx2 %0, %2, off sc1\n\tglobal_load_dwordx2 %1, %2, off offset:128 sc1" \
                 : "=&v"(gq[0]), "=&v"(gq[1]) : "v"(ws + WS_G + (64 * RG + 2 * t) * 4) : "memory")

    if (wave < 4) {
        const bool do_gate = (cb == 0);
        f32x4 acc[6][2];
#pragma unroll
        for (int a = 0; a < 6; ++a) { acc[a][0] = (f32x4){0.f, 0.f, 0.f, 0.f}; acc[a][1] = (f32x4){0.f, 0.f, 0.f, 0.f}; }
        s16x8 fx[2][2], fw[2][5];
        const int woff = 16384 + t * 128;
#define S_READ(b, sb, co)                                                                         \
    do {                                                                                          \
        fx[b][0] = *(const s16x8*)((sb) + xoff + (co)); fx[b][1] = *(const s16x8*)((sb) + xoff + 2048 + (co)); \
        _Pragma("unroll") for (int T_ = 0; T_ < 5; ++T_) fw[b][T_] = *(const s16x8*)((sb) + woff + T_ * 2048 + (co)); \
    } while (0)
#define S_GATE(b, kk)                                                                             \
    do {                                                                                          \
        if (do_gate) {                                                                            \
            const s16x8 gv = *(const s16x8*)(sm + K1_GW + ((kk) + 8 * q) * 2);                    \
            acc[5][0] = mfma16(gv, fx[b][0], acc[5][0]); acc[5][1] = mfma16(gv, fx[b][1], acc[5][1]); \
        }                                                                                         \
    } while (0)
#define S_BODY(it, bo, bn, MODE)                                                                  \
    do {                                                                                          \
        S_READ(1, sm + (bo), co1);                                                                \
        _Pragma("unroll") for (int T = 0; T < 5; ++T) { acc[T][0] = mfma16(fw[0][T], fx[0][0], acc[T][0]); acc[T][1] = mfma16(fw[0][T], fx[0][1], acc[T][1]); } \
        S_GATE(0, 64 * (it));                                                                     \
        SCHED_FENCE();                                                                            \
        if ((MODE) == 0) { if ((it) < 2) WAIT_VM(7); else WAIT_VM(5); }                           \
        else WAIT_VM((MODE) == 1 ? 5 : (MODE) == 2 ? 2 : 0);                                      \
        WAIT_LGKM0();                                                                             \
        __builtin_amdgcn_s_barrier();                                                             \
        asm volatile("" ::: "memory");                                                            \
        SCHED_FENCE();                                                                            \
        if ((MODE) < 3) S_READ(0, sm + (bn), co0);                                                \
        _Pragma("unroll") for (int T = 0; T < 5; ++T) {                                           \
            acc[T][0] = mfma16(fw[1][T], fx[1][0], acc[T][0]); acc[T][1] = mfma16(fw[1][T], fx[1][1], acc[T][1]); \
            if ((MODE) == 0) { SCHED_FENCE(); K1_SLOT(T, bo); SCHED_FENCE(); }                    \
        }                                                                                         \
        S_GATE(1, 64 * (it) + 32);                                                                \
        if ((MODE) == 1) pretile_out();                                                           \
    } while (0)
        S_READ(0, sm, co0);
        int bo = 0, bn = K1_STAGE;
#pragma unroll 1
        for (int it = 0; it < 7; ++it) {
            S_BODY(it, bo, bn, 0);
            if (it == 4 && lane < 40) {
                const float lr = bflo(*(const u16*)(sm + K1_PLR + lane * 2)), li = bflo(*(const u16*)(sm + K1_PLI + lane * 2));
                const float dt = __builtin_amdgcn_exp2f(bflo(*(const u16*)(sm + K1_PLD + lane * 2)) * LOG2E);
                const float za = dt * lr * LOG2E, zb = dt * li * INV2PI;
                f32x2* tb = (f32x2*)(sm + K1_TAB + lane * 64);
                const float n0 = wc == 0 ? 1.f : wc == 1 ? 2.f : wc == 2 ? 8.f : 32.f;
                const float m0 = __builtin_amdgcn_exp2f(n0 * za), r0 = __builtin_amdgcn_fractf(n0 * zb);
                const f32x2 p0 = {m0 * __builtin_amdgcn_cosf(r0), m0 * __builtin_amdgcn_sinf(r0)};
                if (wc == 0) {
                    const float nr = p0.x - 1.0f, ni = p0.y;
                    const float rden = 1.0f / (lr * lr + li * li);
                    tb[0] = p0;
                    tb[6] = (f32x2){(nr * lr + ni * li) * rden, (ni * lr - nr * li) * rden};
                    tb[7] = (f32x2){za, zb};
                } else if (wc == 3) {
                    tb[5] = p0;
                } else {
                    tb[2 * wc - 1] = p0;
                    tb[2 * wc] = cmul(p0, p0);
                }
            }
            bo = bn; bn = bn == 2 * K1_STAGE ? 0 : bn + K1_STAGE;
        }
        S_BODY(7, K1_STAGE, 2 * K1_STAGE, 1); S_BODY(8, 2 * K1_STAGE, 0, 2); S_BODY(9, 0, 0, 3);
        __builtin_amdgcn_s_setprio(3);
        if (do_gate && q == 0) {
            f32x2 gg = {1.0f / (1.0f + __expf(-acc[5][0][0])), 1.0f / (1.0f + __expf(-acc[5][1][0]))};
            st64_wt(ws + WS_G + (128 * rb + 32 * wc + 2 * t) * 4, __builtin_bit_cast(u32x2, gg));
        }
        f32x2 h0[5][2], h1[5][2];
#pragma unroll
        for (int T = 0; T < 5; ++T) {
#pragma unroll
            for (int cc = 0; cc < 2; ++cc) {
                const int chl = 8 * T + 2 * q + cc;
                const f32x4* tb = (const f32x4*)(sm + K1_TAB + chl * 64);
                const f32x4 t0 = tb[0], t1 = tb[1], t2 = tb[2], t3 = tb[3];
                const f32x2 L1 = {t0[0], t0[1]}, L2 = {t0[2], t0[3]}, L4 = {t1[0], t1[1]}, L8 = {t1[2], t1[3]}, L16 = {t2[0], t2[1]};
                const f32x2 cf = {t3[0], t3[1]};
                const f32x2 u0 = cmul_sc(cf, (f32x2){acc[T][0][2 * cc], acc[T][0][2 * cc + 1]});
                const f32x2 u1 = cmul_sc(cf, (f32x2){acc[T][1][2 * cc], acc[T][1][2 * cc + 1]});
                f32x2 a = cfma(L1, u0, u1);
                a = cfma(L2, (f32x2){shr_zero<1>(a.x), shr_zero<1>(a.y)}, a);
                a = cfma(L4, (f32x2){shr_zero<2>(a.x), shr_zero<2>(a.y)}, a);
                a = cfma(L8, (f32x2){shr_zero<4>(a.x), shr_zero<4>(a.y)}, a);
                a = cfma(L16, (f32x2){shr_zero<8>(a.x), shr_zero<8>(a.y)}, a);
                h0[T][cc] = cfma(L1, (f32x2){shr_zero<1>(a.x), shr_zero<1>(a.y)}, u0);
                h1[T][cc] = a;
                if (t == 15) *(f32x2*)(sm + K1_XCH + (wc * 40 + chl) * 8) = a;
            }
        }
        WAIT_LGKM0();
        unsigned old = 0;
        if (lane == 0) old = __hip_atomic_fetch_add((LAS unsigned*)(sm + K1_CNT), 1u, __ATOMIC_RELAXED, __HIP_MEMORY_SCOPE_WORKGROUP);
        old = __builtin_amdgcn_readfirstlane(old);
        asm volatile("" ::: "memory");
        if (old == 3) {
            const int cl = lane < 40 ? lane : 0;
            const f32x2 L32 = *(const f32x2*)(sm + K1_TAB + cl * 64 + 40);
            const f32x2* xc = (const f32x2*)(sm + K1_XCH);
            f32x2 pre[4], s = {0.f, 0.f};
#pragma unroll
            for (int w = 0; w < 4; ++w) { pre[w] = s; s = cfma(L32, s, xc[w * 40 + cl]); }
            if (lane < 40) st128_wt(ws + WS_AGG + (size_t)(cb * 16 + rb) * 640 + lane * 16,
                                    (u32x4){__float_as_uint(s.x), __float_as_uint(s.y), (unsigned)tok, (unsigned)(tok >> 32)});
            f32x2 cy = {0.f, 0.f};
            if (rb > 0) {
                const f32x2 L64 = cmul(L32, L32), L128 = cmul(L64, L64);
                const char* agp = ws + WS_AGG + (size_t)cb * 16 * 640 + cl * 16;
                for (unsigned spins = 0; spins < (1u << 20); ++spins) {
                    u32x4 ag[15];
#pragma unroll
                    for (int j = 0; j < 15; ++j)
                        asm volatile("global_load_dwordx4 %0, %1, off sc1" : "=v"(ag[j]) : "v"(agp + (j < rb ? j : 0) * 640) : "memory");
                    asm volatile("s_waitcnt vmcnt(0)" : "+v"(ag[0]), "+v"(ag[1]), "+v"(ag[2]), "+v"(ag[3]), "+v"(ag[4]), "+v"(ag[5]), "+v"(ag[6]), "+v"(ag[7]),
                                 "+v"(ag[8]), "+v"(ag[9]), "+v"(ag[10]), "+v"(ag[11]), "+v"(ag[12]), "+v"(ag[13]), "+v"(ag[14]) :: "memory");
                    bool ok = true;
                    cy = (f32x2){0.f, 0.f};
#pragma unroll
                    for (int j = 0; j < 15; ++j) {
                        ok = ok && ag[j][2] == (unsigned)tok && ag[j][3] == (unsigned)(tok >> 32);
                        const f32x2 nx = cfma(L128, cy, (f32x2){__uint_as_float(ag[j][0]), __uint_as_float(ag[j][1])});
                        cy = (j < rb) ? nx : cy;
                    }
                    if (__all(ok)) break;
                    __builtin_amdgcn_s_sleep(8);
                }
            }
            if (lane < 40) {
#pragma unroll
                for (int w = 0; w < 4; ++w) {
                    *(f32x2*)(sm + K1_CAR + (w * 40 + lane) * 8) = (f32x2){pre[w].x + cy.x, pre[w].y + cy.y};
                    cy = cmul(cy, L32);
                }
            }
            WAIT_LGKM0();
            if (lane == 0) __hip_atomic_store((LAS unsigned*)(sm + K1_CNT + 4), 1u, __ATOMIC_RELAXED, __HIP_MEMORY_SCOPE_WORKGROUP);
        } else {
            for (unsigned spins = 0; spins < (1u << 22); ++spins) {
                if (__hip_atomic_load((LAS unsigned*)(sm + K1_CNT + 4), __ATOMIC_RELAXED, __HIP_MEMORY_SCOPE_WORKGROUP)) break;
                __builtin_amdgcn_s_sleep(2);
            }
        }
        asm volatile("" ::: "memory");
        const float fn = (float)(2 * t + 1);
#pragma unroll
        for (int T = 0; T < 5; ++T) {
            u32x2 o0, o1;
#pragma unroll
            for (int cc = 0; cc < 2; ++cc) {
                const int chl = 8 * T + 2 * q + cc;
                const f32x2 L1 = *(const f32x2*)(sm + K1_TAB + chl * 64), zz = *(const f32x2*)(sm + K1_TAB + chl * 64 + 56);
                const f32x2 cy = *(const f32x2*)(sm + K1_CAR + (wc * 40 + chl) * 8);
                const float mg = __builtin_amdgcn_exp2f(fn * zz.x);
                const float rv = __builtin_amdgcn_fractf(fn * zz.y);
                const f32x2 c0 = cmul_sc((f32x2){mg * __builtin_amdgcn_cosf(rv), mg * __builtin_amdgcn_sinf(rv)}, cy);
                const f32x2 v1 = cfma(L1, c0, h1[T][cc]);
                o0[cc] = pack2(h0[T][cc].x + c0.x, -(h0[T][cc].y + c0.y));
                o1[cc] = pack2(v1.x, -v1.y);
            }
            const int Tg = 5 * cb + T;
            char* dst = a2 + (size_t)(Tg >> 1) * 8192 + (Tg & 1) * 8;
            st64_wt(dst, o0);
            st64_wt(dst + 1024, o1);
        }
        __builtin_amdgcn_s_setprio(0);
        P2_BEGIN(0);
        P2_STEP(0, 10, 8, true); P2_STEP(1, 10, 8, true); P2_STEP(2, 10, 8, true); P2_STEP(3, 10, 8, true); P2_STEP(4, 10, 8, true);
        P2_STEP(5, 10, 8, true); P2_STEP(6, 10, 8, true); P2_STEP(7, 10, 8, true); P2_STEP(8, 10, 4, true); P2_STEP(9, 10, 0, true);
    } else {
        const int bb = q & 1;
        const int trb = 26624 + (8 * q + (t >> 2)) * 192 + (lane & 3) * 8;
        const int tra = trb + bb * 32;
        const int tr5 = trb + (bb ? 0 : 160);
        f32x4 acc[6][2];
#pragma unroll
        for (int j = 0; j < 6; ++j) {
            const u32x2 bp = *(const u32x2*)(sm + K1_PB1 + (16 * j + 4 * q) * 2);
            acc[j][0] = (f32x4){bflo(bp[0]), bfhi(bp[0]), bflo(bp[1]), bfhi(bp[1])};
            acc[j][1] = acc[j][0];
        }
        s16x8 fx[2][2], fw[2][6];
#define M_READ(b, sb, co, ks)                                                                     \
    do {                                                                                          \
        fx[b][0] = *(const s16x8*)((sb) + xoff + (co)); fx[b][1] = *(const s16x8*)((sb) + xoff + 2048 + (co)); \
        _Pragma("unroll") for (int j_ = 0; j_ < 6; ++j_) {                                        \
            const char* pa_ = (sb) + (j_ < 5 ? tra + j_ * 32 : tr5) + (ks) * 32 * 192;            \
            const s16x4 lo_ = __builtin_amdgcn_ds_read_tr16_b64_v4i16((LAS s16x4*)pa_);           \
            const s16x4 hi_ = __builtin_amdgcn_ds_read_tr16_b64_v4i16((LAS s16x4*)(pa_ + 4 * 192)); \
            fw[b][j_] = __builtin_shufflevector(lo_, hi_, 0, 1, 2, 3, 4, 5, 6, 7);                \
        }                                                                                         \
    } while (0)
#define M_BODY(it, bo, bn, MODE)                                                                  \
    do {                                                                                          \
        M_READ(1, sm + (bo), co1, 1);                                                             \
        _Pragma("unroll") for (int j = 0; j < 6; ++j) { acc[j][0] = mfma16(fw[0][j], fx[0][0], acc[j][0]); acc[j][1] = mfma16(fw[0][j], fx[0][1], acc[j][1]); } \
        SCHED_FENCE();                                                                            \
        if ((MODE) == 0) { if ((it) < 2) WAIT_VM(7); else WAIT_VM(5); }                           \
        else WAIT_VM((MODE) == 1 ? 5 : (MODE) == 2 ? 2 : 0);                                      \
        WAIT_LGKM0();                                                                             \
        __builtin_amdgcn_s_barrier();                                                             \
        asm volatile("" ::: "memory");                                                            \
        SCHED_FENCE();                                                                            \
        if ((MODE) == 3 && tid == 256) flag_set(ws + WS_FPT + (size_t)bid * 8, tok);     \
        if ((MODE) < 3) M_READ(0, sm + (bn), co0, 0);                                             \
        _Pragma("unroll") for (int j = 0; j < 6; ++j) {                                           \
            acc[j][0] = mfma16(fw[1][j], fx[1][0], acc[j][0]); acc[j][1] = mfma16(fw[1][j], fx[1][1], acc[j][1]); \
            if ((MODE) == 0 && j < 5) { SCHED_FENCE(); K1_SLOT(j, bo); SCHED_FENCE(); }           \
        }                                                                                         \
        if ((MODE) == 1) pretile_out();                                                           \
    } while (0)
        M_READ(0, sm, co0, 0);
        int bo = 0, bn = K1_STAGE;
#pragma unroll 1
        for (int it = 0; it < 7; ++it) {
            M_BODY(it, bo, bn, 0);
            bo = bn; bn = bn == 2 * K1_STAGE ? 0 : bn + K1_STAGE;
        }
        M_BODY(7, K1_STAGE, 2 * K1_STAGE, 1); M_BODY(8, 2 * K1_STAGE, 0, 2); M_BODY(9, 0, 0, 3);
#pragma unroll
        for (int v = 0; v < 3; ++v) {
#pragma unroll
            for (int rt = 0; rt < 2; ++rt) {
                u32x4 o;
#pragma unroll
                for (int jj = 0; jj < 2; ++jj) {
                    float r[4];
#pragma unroll
                    for (int i = 0; i < 4; ++i) {
                        const float h = acc[2 * v + jj][rt][i];
                        const float e = __builtin_amdgcn_exp2f(h * LOG2E);
                        const float d = fmaf(e, e + 2.0f, 2.0f);
                        r[i] = fmaf(-2.0f, h * __builtin_amdgcn_rcpf(d), h);
                    }
                    o[2 * jj] = pack2(r[0], r[1]);
                    o[2 * jj + 1] = pack2(r[2], r[3]);
                }
                st128_wt(a2 + (size_t)(40 + 3 * cb + v) * 8192 + rt * 1024, o);
            }
        }
        P2_BEGIN(1);
        P2_STEP(0, 12, 8, false); P2_STEP(1, 12, 13, false); P2_STEP(2, 12, 9, false); P2_STEP(3, 12, 9, false); P2_STEP(4, 12, 9, false); P2_STEP(5, 12, 9, false);
        P2_STEP(6, 12, 9, false); P2_STEP(7, 12, 9, false); P2_STEP(8, 12, 9, false); P2_STEP(9, 12, 9, false); P2_STEP(10, 12, 5, false); P2_STEP(11, 12, 0, false);
        flag_wait(ws + WS_FP1 + (size_t)(RG * 32 + (lane & 1)) * 8, lane < 2, tok);
        P2_GATES();
    }
    asm volatile("s_waitcnt vmcnt(0)" : "+v"(gq[0]), "+v"(gq[1]) :: "memory");
    __syncthreads();
#pragma unroll
    for (int r = 0; r < 4; ++r)
#pragma unroll
        for (int n = 0; n < 5; ++n) *(f32x4*)(sm + ((r * 5 + n) * 8 + wave) * 1024 + lane * 16) = ac2[r][n];
    __syncthreads();
#pragma unroll
    for (int m = 0; m < 3; ++m) {
        const int tile = wave + 8 * m;
        if (tile < 20) {
            const char* rp = sm + tile * 8192 + lane * 16;
            const f32x4 vs = (*(const f32x4*)rp + *(const f32x4*)(rp + 1024)) + (*(const f32x4*)(rp + 2048) + *(const f32x4*)(rp + 3072));
            const f32x4 vm = (*(const f32x4*)(rp + 4096) + *(const f32x4*)(rp + 5120)) + (*(const f32x4*)(rp + 6144) + *(const f32x4*)(rp + 7168));
            const int r = tile / 5;
            const float g = __uint_as_float(r == 0 ? gq[0][0] : r == 1 ? gq[0][1] : r == 2 ? gq[1][0] : gq[1][1]);
            f32x4 v = vs * (2.0f * g) + vm * (1.0f - g);
            v[0] += g * bflo(xv[m][0]) * bflo(dv[m][0]);
            v[1] += g * bfhi(xv[m][0]) * bfhi(dv[m][0]);
            v[2] += g * bflo(xv[m][1]) * bflo(dv[m][1]);
            v[3] += g * bfhi(xv[m][1]) * bfhi(dv[m][1]);
            st128_wt(out + oo[m], __builtin_bit_cast(u32x4, v));
        }
    }
    if (bid == 0 && wave == 0) {
#pragma unroll 1
        for (int k = 0; k < 4; ++k) flag_wait(ws + WS_FPT + (size_t)(64 * k + lane) * 8, true, tok);
        if (lane == 0) flag_set(ws + WS_EP, tok);
    }
}

extern "C" void kernel_launch(void* const* d_in, const int* in_sizes, int n_in, void* d_out, int out_size, void* d_ws,
                              size_t ws_size, hipStream_t stream) {
    kf<<<256, 512, 0, stream>>>((const u16*)d_in[0], (const u16*)d_in[1], (const u16*)d_in[2], (const u16*)d_in[3], (const u16*)d_in[4],
                                (const u16*)d_in[5], (const u16*)d_in[6], (const u16*)d_in[7], (const u16*)d_in[8], (const u16*)d_in[10],
                                (const u16*)d_in[11], (const u16*)d_in[12], (const u16*)d_in[13], (char*)d_ws, (float*)d_out);
}
```

```cpp
#include <hip/hip_runtime.h>
#include <stdint.h>

typedef short s16x4 __attribute__((ext_vector_type(4)));
typedef short s16x8 __attribute__((ext_vector_type(8)));
typedef __bf16 bf16x2 __attribute__((ext_vector_type(2)));
typedef float f32x2 __attribute__((ext_vector_type(2)));
typedef float f32x4 __attribute__((ext_vector_type(4)));
typedef unsigned u32x2 __attribute__((ext_vector_type(2)));
typedef unsigned u32x4 __attribute__((ext_vector_type(4)));
typedef unsigned short u16;
#define LAS __attribute__((address_space(3)))
#define GAS __attribute__((address_space(1)))
#define DI __device__ __forceinline__

constexpr int SEQ = 2048, DM = 640, NP = 640, NF = 1536;
constexpr int NKS = 88;

constexpr size_t WS_A2 = 0;
constexpr size_t WS_W2 = WS_A2 + (size_t)16 * NKS * 8192;
constexpr size_t WS_G = WS_W2 + (size_t)8 * NKS * 5120;
constexpr size_t WS_AGG = WS_G + 2048 * 4;
constexpr size_t WS_EP = WS_AGG + 16 * 16 * 640;
constexpr size_t WS_FPT = WS_EP + 256;
constexpr size_t WS_FAG = WS_FPT + 2048;
constexpr size_t WS_FP1 = WS_FAG + 2048;
constexpr size_t WS_JUNK = WS_FP1 + 16384;

DI float bflo(unsigned v) { return __uint_as_float(v << 16); }
DI float bfhi(unsigned v) { return __uint_as_float(v & 0xffff0000u); }
DI unsigned pack2(float a, float b) {
    f32x2 p = {a, b};
    return __builtin_bit_cast(unsigned, __builtin_convertvector(p, bf16x2));
}
DI f32x4 mfma16(s16x8 a, s16x8 b, f32x4 c) { return __builtin_amdgcn_mfma_f32_16x16x32_bf16(a, b, c, 0, 0, 0); }
template <int S> DI float shr_zero(float v) {
    return __builtin_bit_cast(float, __builtin_amdgcn_update_dpp(0, __builtin_bit_cast(int, v), 0x110 + S, 0xf, 0xf, true));
}
DI f32x2 cmul(f32x2 a, f32x2 b) { return (f32x2){a.x * b.x - a.y * b.y, a.x * b.y + a.y * b.x}; }
DI f32x2 cfma(f32x2 a, f32x2 b, f32x2 c) {
    return (f32x2){fmaf(-a.y, b.y, fmaf(a.x, b.x, c.x)), fmaf(a.y, b.x, fmaf(a.x, b.y, c.y))};
}
DI f32x2 cmul_pk(f32x2 a, f32x2 b) { return (f32x2){fmaf(-a.y, b.y, a.x * b.x), fmaf(a.y, b.x, a.x * b.y)}; }
DI f32x2 cmul_sc(f32x2 a, f32x2 b) {
    float p = a.x * b.x, r = a.x * b.y;
    asm("" : "+v"(p));
    asm("" : "+v"(r));
    return (f32x2){fmaf(-a.y, b.y, p), fmaf(a.y, b.x, r)};
}
DI float dot2bf(unsigned a, unsigned b, float c) {
    return __builtin_amdgcn_fdot2_f32_bf16(__builtin_bit_cast(bf16x2, a), __builtin_bit_cast(bf16x2, b), c, false);
}
#define WAIT_VM(n) asm volatile("s_waitcnt vmcnt(%0)" ::"n"(n) : "memory")
#define WAIT_LGKM0() asm volatile("s_waitcnt lgkmcnt(0)" ::: "memory")
#define ACQUIRE() asm volatile("buffer_inv sc1" ::: "memory")
#define SCHED_FENCE() __builtin_amdgcn_sched_barrier(0)

DI void glds16(const void* gsrc, unsigned lds_dst) {
    unsigned keep;
    asm volatile("s_mov_b32 %0, m0\n\ts_mov_b32 m0, %2\n\ts_nop 0\n\tglobal_load_lds_dwordx4 %1, off\n\ts_mov_b32 m0, %0"
                 : "=&s"(keep) : "v"(gsrc), "s"(lds_dst) : "memory");
}
DI void glds16x4(const void* gsrc, unsigned lds_dst) {
    unsigned keep;
    asm volatile("s_mov_b32 %0, m0\n\ts_mov_b32 m0, %2\n\ts_nop 0\n\tglobal_load_lds_dwordx4 %1, off\n\t"
                 "global_load_lds_dwordx4 %1, off offset:1024\n\tglobal_load_lds_dwordx4 %1, off offset:2048\n\t"
                 "global_load_lds_dwordx4 %1, off offset:3072\n\ts_mov_b32 m0, %0"
                 : "=&s"(keep) : "v"(gsrc), "s"(lds_dst) : "memory");
}
DI void st128_wt(void* p, u32x4 v) { asm volatile("global_store_dwordx4 %0, %1, off sc1\n\ts_nop 1" :: "v"(p), "v"(v) : "memory"); }
DI void st64_wt(void* p, u32x2 v) { asm volatile("global_store_dwordx2 %0, %1, off sc1\n\ts_nop 1" :: "v"(p), "v"(v) : "memory"); }
DI unsigned ld32_asm(const void* p) { unsigned r; asm volatile("global_load_dword %0, %1, off" : "=v"(r) : "v"(p) : "memory"); return r; }
DI unsigned ld16_asm(const void* p) { unsigned r; asm volatile("global_load_ushort %0, %1, off" : "=v"(r) : "v"(p) : "memory"); return r; }
DI u32x2 ld64_asm(const void* p) { u32x2 r; asm volatile("global_load_dwordx2 %0, %1, off" : "=v"(r) : "v"(p) : "memory"); return r; }
DI u32x4 ld128_asm(const void* p) { u32x4 r; asm volatile("global_load_dwordx4 %0, %1, off" : "=v"(r) : "v"(p) : "memory"); return r; }
constexpr float LOG2E = 1.44269504088896341f, INV2PI = 0.15915494309189535f;
DI f32x2 lam_bar(float lr, float li, float ld) {
    const float dt = __builtin_amdgcn_exp2f(ld * LOG2E);
    const float m = __builtin_amdgcn_exp2f(dt * lr * LOG2E);
    const float rv = __builtin_amdgcn_fractf(dt * li * INV2PI);
    return (f32x2){m * __builtin_amdgcn_cosf(rv), m * __builtin_amdgcn_sinf(rv)};
}

typedef unsigned long long u64;
DI void glds16_c(const void* gsrc, unsigned lds_dst) {
    unsigned keep;
    lds_dst = __builtin_amdgcn_readfirstlane(lds_dst);
    asm volatile("s_mov_b32 %0, m0\n\ts_mov_b32 m0, %2\n\ts_nop 0\n\tglobal_load_lds_dwordx4 %1, off sc1\n\ts_mov_b32 m0, %0"
                 : "=&s"(keep) : "v"(gsrc), "s"(lds_dst) : "memory");
}
DI void glds16x4_c(const void* gsrc, unsigned lds_dst) {
    unsigned keep;
    asm volatile("s_mov_b32 %0, m0\n\ts_mov_b32 m0, %2\n\ts_nop 0\n\tglobal_load_lds_dwordx4 %1, off sc1\n\t"
                 "global_load_lds_dwordx4 %1, off offset:1024 sc1\n\tglobal_load_lds_dwordx4 %1, off offset:2048 sc1\n\t"
                 "global_load_lds_dwordx4 %1, off offset:3072 sc1\n\ts_mov_b32 m0, %0"
                 : "=&s"(keep) : "v"(gsrc), "s"(lds_dst) : "memory");
}
DI void flag_set(char* p, u64 tok) { __hip_atomic_store((GAS u64*)p, tok, __ATOMIC_RELAXED, __HIP_MEMORY_SCOPE_AGENT); }
DI void flag_wait(const char* p, bool need, u64 tok) {
    for (unsigned spins = 0; spins < (1u << 20); ++spins) {
        u64 v = tok;
        if (need) v = __hip_atomic_load((const GAS u64*)p, __ATOMIC_RELAXED, __HIP_MEMORY_SCOPE_AGENT);
        if (__all(v == tok)) break;
        __builtin_amdgcn_s_sleep(8);
    }
    asm volatile("" ::: "memory");
}

constexpr int K1_STAGE = 38 * 1024;
constexpr int K1_GW = 3 * K1_STAGE;
constexpr int K1_PLR = K1_GW + 1280, K1_PLI = K1_GW + 1360, K1_PLD = K1_GW + 1440, K1_PB1 = K1_GW + 1536;
constexpr int K1_TAB = K1_GW + 2048;
constexpr int K1_XCH = K1_TAB + 2560;
constexpr int K1_CAR = K1_XCH + 1280;
constexpr int K1_CNT = K1_CAR + 1280;
constexpr int K1_PT = 122 * 1024;
constexpr int K1_FM = 140 * 1024;
constexpr int KF_LDS = 160 * 1024;

__global__ __launch_bounds__(512, 2) void kf(const u16* __restrict__ x, const u16* __restrict__ Lre, const u16* __restrict__ Lim,
                                             const u16* __restrict__ Bre, const u16* __restrict__ Bim, const u16* __restrict__ Cre,
                                             const u16* __restrict__ Cim, const u16* __restrict__ logdt, const u16* __restrict__ Dskip,
                                             const u16* __restrict__ w1, const u16* __restrict__ b1, const u16* __restrict__ w2,
                                             const u16* __restrict__ gw, char* __restrict__ ws, float* __restrict__ out) {
    __shared__ __attribute__((aligned(16))) char sm[KF_LDS];
    const int tid = threadIdx.x, lane = tid & 63;
    const int wave = __builtin_amdgcn_readfirstlane(tid >> 6);
    const int bid = blockIdx.x, xcd = bid & 7, idx = bid >> 3;
    const int rb = 8 * (xcd & 1) + (idx & 7);
    const int cb = 4 * (xcd >> 1) + (idx >> 3);
    const int RG = 8 * (xcd >> 1) + (idx & 7);
    const int CB = 4 * (xcd & 1) + (idx >> 3);
    const int t = lane & 15, q = lane >> 4;
    const unsigned smb = (unsigned)(size_t)(LAS char*)sm;
    unsigned cjunk = 0;
    if (wave == 7) {
        u64 pc;
        asm volatile("s_getpc_b64 %0" : "=s"(pc));
        const char* cp = (const char*)(pc & ~255ull) + lane * 128;
#pragma unroll
        for (int k = 0; k < 4; ++k) asm volatile("global_load_dword %0, %1, off" : "+v"(cjunk) : "v"(cp + k * 8192) : "memory");
    }
    const char* src[5];
    unsigned ldo[5];
    int incE, strE;
    {
        const int l8 = lane >> 3;
        auto xsrc = [&](int s) {
            const int p = 8 * s + l8;
            const int tm = 128 * rb + 32 * (p >> 5) + 2 * (p & 15) + ((p >> 4) & 1);
            return (const char*)(x + tm * DM + (((lane & 7) ^ ((p >> 1) & 7)) << 3));
        };
        auto wsrc = [&](int s) {
            const int p = 8 * s + l8;
            const int ch = 40 * cb + 8 * (p >> 4) + ((p & 15) >> 1);
            return (const char*)(((p & 1) ? Bim : Bre) + ch * DM + (((lane & 7) ^ ((p >> 1) & 7)) << 3));
        };
        auto msrc = [&](int s) {
            const int gI = 64 * s + lane, row = gI / 12, cp = gI - 12 * row;
            int j = (cp >> 1) - ((row >> 3) & 1);
            j += j < 0 ? 6 : 0;
            return (const char*)(w1 + row * NF + 96 * cb + 16 * j + 8 * (cp & 1));
        };
        src[0] = xsrc(2 * wave); ldo[0] = smb + 2048 * wave;
        src[1] = xsrc(2 * wave + 1); ldo[1] = smb + 2048 * wave + 1024;
        src[2] = wsrc(wave); ldo[2] = smb + 16384 + 1024 * wave;
        src[3] = msrc(wave); ldo[3] = smb + 26624 + 1024 * wave;
        if (wave < 2) { src[4] = wsrc(8 + wave); ldo[4] = smb + 16384 + 1024 * (8 + wave); incE = 128; strE = 1; }
        else if (wave < 6) { src[4] = msrc(6 + wave); ldo[4] = smb + 26624 + 1024 * (6 + wave); incE = 64 * NF * 2; strE = 1; }
        else {
            const u16* p = gw + lane * 8;
            if (wave == 7) {
                p = gw + 512 + t * 8;
                if (lane >= 16 && lane < 21) p = Lre + 40 * cb + (lane - 16) * 8;
                if (lane >= 21 && lane < 26) p = Lim + 40 * cb + (lane - 21) * 8;
                if (lane >= 26 && lane < 31) p = logdt + 40 * cb + (lane - 26) * 8;
                if (lane >= 32 && lane < 44) p = b1 + 96 * cb + (lane - 32) * 8;
            }
            src[4] = (const char*)p; ldo[4] = smb + K1_GW + (wave - 6) * 1024; incE = 0; strE = 0;
        }
    }
#define K1_SLOT(i, boff)                                                                 \
    do {                                                                                 \
        glds16(src[i], ldo[i] + ((i) == 4 ? strE * (boff) : (boff)));                    \
        src[i] += (i) < 3 ? 128 : (i) == 3 ? 64 * NF * 2 : incE;                         \
    } while (0)
    K1_SLOT(0, 0); K1_SLOT(1, 0); K1_SLOT(2, 0); K1_SLOT(3, 0); K1_SLOT(4, 0);
    u32x2 epv;
    asm volatile("global_load_dwordx2 %0, %1, off sc1" : "=v"(epv) : "v"(ws + WS_EP) : "memory");

    const int pf0 = 14 * bid + 2 * wave;
    char* ptl = sm + K1_PT + wave * 2048;
#pragma unroll
    for (int u = 0; u < 2; ++u) {
        int f = pf0 + u; f = (wave < 7 && f < 3520) ? f : 0;
        const int cK = f / 5, nt = f - 5 * cK, cB = cK / NKS, KS = cK - NKS * cB, d0 = 80 * cB + 16 * nt;
        const u16* p0 = (KS < 40) ? ((lane >> 5) ? Cim : Cre) + (d0 + ((lane >> 1) & 15)) * NP + 16 * KS + 8 * (lane & 1)
                                  : w2 + (32 * (KS - 40) + (lane >> 1)) * DM + d0 + 8 * (lane & 1);
        glds16(p0, smb + K1_PT + wave * 2048 + u * 1024);
    }
#pragma unroll
    for (int st = 1; st < 3; ++st) {
        K1_SLOT(0, st * K1_STAGE); K1_SLOT(1, st * K1_STAGE); K1_SLOT(2, st * K1_STAGE); K1_SLOT(3, st * K1_STAGE); K1_SLOT(4, st * K1_STAGE);
    }
    if (tid == 0) *(u32x2*)(sm + K1_CNT) = (u32x2){0u, 0u};

    const int wc = wave & 3;
    const int xoff = (32 * wc + t) * 128;
    const int co0 = (q ^ ((t >> 1) & 7)) * 16, co1 = co0 ^ 64;
    char* a2 = ws + WS_A2 + (size_t)rb * NKS * 8192 + (2 * wc) * 1024 + lane * 16;

    asm volatile("s_waitcnt vmcnt(10)" : "+v"(epv), "+v"(cjunk) :: "memory");
    WAIT_LGKM0();
    __builtin_amdgcn_s_barrier();
    asm volatile("" ::: "memory");
    const u64 tok = (((u64)(unsigned)__builtin_amdgcn_readfirstlane(epv[1]) << 32) | (unsigned)__builtin_amdgcn_readfirstlane(epv[0])) + 1ull;
    auto pretile_out = [&]() {
        {
            const int tro = (4 * q + (t >> 2)) * 32 + (lane & 3) * 8;
#pragma unroll
            for (int u = 0; u < 2; ++u) {
                const int f = pf0 + u;
                const int cK = f / 5, KS = cK % NKS;
                const char* pp = ptl + u * 1024;
                u32x4 wf;
                if (KS < 40) {
                    const unsigned r0 = *(const unsigned*)(pp + t * 32 + 4 * q), r1 = *(const unsigned*)(pp + t * 32 + 16 + 4 * q);
                    const unsigned i0 = *(const unsigned*)(pp + 512 + t * 32 + 4 * q), i1 = *(const unsigned*)(pp + 512 + t * 32 + 16 + 4 * q);
                    wf[0] = (r0 & 0xffffu) | (i0 << 16); wf[1] = (r0 >> 16) | (i0 & 0xffff0000u);
                    wf[2] = (r1 & 0xffffu) | (i1 << 16); wf[3] = (r1 >> 16) | (i1 & 0xffff0000u);
                } else {
                    const s16x4 lo = __builtin_amdgcn_ds_read_tr16_b64_v4i16((LAS s16x4*)(pp + tro));
                    const s16x4 hi = __builtin_amdgcn_ds_read_tr16_b64_v4i16((LAS s16x4*)(pp + tro + 512));
                    wf = __builtin_bit_cast(u32x4, __builtin_shufflevector(lo, hi, 0, 1, 2, 3, 4, 5, 6, 7));
                }
                st128_wt(((wave < 7 && f < 3520) ? ws + WS_W2 + (size_t)f * 1024 : ws + WS_JUNK + (size_t)(bid * 16 + wave * 2 + u) * 1024) + lane * 16, wf);
            }
        }
    };

    const int rb2 = RG >> 1, hh = RG & 1;
    const int ks0 = wave < 4 ? 10 * wave : 40 + 12 * (wave - 4);
    const char* pa = ws + WS_A2 + ((size_t)rb2 * NKS + ks0) * 8192 + hh * 4096 + lane * 16;
    const char* pb = ws + WS_W2 + ((size_t)CB * NKS + ks0) * 5120 + 1024 + lane * 16;
    const int bco = wave < 4 ? wave * 20480 : wave < 6 ? 81920 + (wave - 4) * 15360 : K1_PT + (wave - 6) * 15360;
    u32x4 Ab[4][4], Bb[3][5];
    f32x4 ac2[4][5];
#define P2_L1(dst, P, OFF) asm volatile("global_load_dwordx4 %0, %1, off offset:" #OFF " sc1" : "=&v"(dst) : "v"(P) : "memory")
#define P2_LDB(slot, P)                                                                           \
    do { P2_L1(Bb[slot][0], P, -1024); P2_L1(Bb[slot][1], P, 0); P2_L1(Bb[slot][2], P, 1024); P2_L1(Bb[slot][3], P, 2048); P2_L1(Bb[slot][4], P, 3072); } while (0)
#define P2_LDA(slot, P)                                                                           \
    do { P2_L1(Ab[slot][0], P, 0); P2_L1(Ab[slot][1], P, 1024); P2_L1(Ab[slot][2], P, 2048); P2_L1(Ab[slot][3], P, 3072); } while (0)
    auto pt_flag = [&](int nsteps, int i) {
        const int fa = (CB * NKS + ks0) * 5, w0 = fa / 14, w1_ = (fa + 5 * nsteps - 1) / 14;
        return ws + WS_FPT + (size_t)(w0 + i <= w1_ ? w0 + i : w0) * 8;
    };
    auto pt_check = [&](int nsteps) {
        const int fa = (CB * NKS + ks0) * 5, w0 = fa / 14, w1_ = (fa + 5 * nsteps - 1) / 14;
        const int wi = w0 + lane <= w1_ ? w0 + lane : w0;
        const u64 v = *(const u64*)(sm + K1_FM + (wi - (((440 * CB) / 14) & ~1)) * 8);
        if (!__all(v == tok)) flag_wait(pt_flag(nsteps, lane), lane < 8, tok);
    };
    const char* g3 = pb - 1024 + 3 * 5120;
#define P2_CACHE(i) glds16_c(g3 + (i) * 1024, smb + bco + (i) * 1024)
#define P2_M2(ua, ub, i)                                                                          \
    do {                                                                                          \
        ac2[(i) / 5][(i) % 5] = mfma16(__builtin_bit_cast(s16x8, Bb[ub][(i) % 5]), __builtin_bit_cast(s16x8, Ab[ua][(i) / 5]), ac2[(i) / 5][(i) % 5]); \
        ac2[((i) + 1) / 5][((i) + 1) % 5] = mfma16(__builtin_bit_cast(s16x8, Bb[ub][((i) + 1) % 5]), __builtin_bit_cast(s16x8, Ab[ua][((i) + 1) / 5]), ac2[((i) + 1) / 5][((i) + 1) % 5]); \
        SCHED_FENCE();                                                                            \
    } while (0)
#define P2_STEP(j, NS, WN, NL)                                                                    \
    do {                                                                                          \
        asm volatile("s_waitcnt vmcnt(%9)" : "+v"(Ab[(j) & 3][0]), "+v"(Ab[(j) & 3][1]), "+v"(Ab[(j) & 3][2]), "+v"(Ab[(j) & 3][3]), \
                     "+v"(Bb[(j) % 3][0]), "+v"(Bb[(j) % 3][1]), "+v"(Bb[(j) % 3][2]), "+v"(Bb[(j) % 3][3]), "+v"(Bb[(j) % 3][4]) : "n"(WN) : "memory"); \
        SCHED_FENCE();                                                                            \
        const char* pa_ = pa + ((j) + 3) * 8192;                                                  \
        const char* pb_ = pb + ((j) + 2) * 5120;                                                  \
        constexpr bool LA_ = (j) + 3 < (NS), LBG_ = (j) + 2 >= (NL) && (j) + 2 < (NS), LBL_ = (j) + 2 >= 3 && (j) + 2 < (NL); \
        P2_M2((j) & 3, (j) % 3, 0); if (LA_) { P2_L1(Ab[((j) + 3) & 3][0], pa_, 0); SCHED_FENCE(); }      \
        P2_M2((j) & 3, (j) % 3, 2); if (LA_) { P2_L1(Ab[((j) + 3) & 3][1], pa_, 1024); SCHED_FENCE(); }   \
        P2_M2((j) & 3, (j) % 3, 4); if (LA_) { P2_L1(Ab[((j) + 3) & 3][2], pa_, 2048); SCHED_FENCE(); }   \
        P2_M2((j) & 3, (j) % 3, 6); if (LA_) { P2_L1(Ab[((j) + 3) & 3][3], pa_, 3072); SCHED_FENCE(); }   \
        if (LBL_) { _Pragma("unroll") for (int n_ = 0; n_ < 5; ++n_) Bb[((j) + 2) % 3][n_] = *(const u32x4*)(sm + bco + (((j) + 2 - 3) * 5 + n_) * 1024 + lane * 16); SCHED_FENCE(); } \
        P2_M2((j) & 3, (j) % 3, 8); if (LBG_) { P2_L1(Bb[((j) + 2) % 3][0], pb_, -1024); SCHED_FENCE(); } \
        P2_M2((j) & 3, (j) % 3, 10); if (LBG_) { P2_L1(Bb[((j) + 2) % 3][1], pb_, 0); SCHED_FENCE(); }    \
        P2_M2((j) & 3, (j) % 3, 12); if (LBG_) { P2_L1(Bb[((j) + 2) % 3][2], pb_, 1024); SCHED_FENCE(); } \
        P2_M2((j) & 3, (j) % 3, 14); if (LBG_) { P2_L1(Bb[((j) + 2) % 3][3], pb_, 2048); SCHED_FENCE(); } \
        P2_M2((j) & 3, (j) % 3, 16); if (LBG_) { P2_L1(Bb[((j) + 2) % 3][4], pb_, 3072); SCHED_FENCE(); } \
        P2_M2((j) & 3, (j) % 3, 18);                                                              \
    } while (0)
    u32x2 xv[3], dv[3], gq[2];
    int oo[3];
#define P2_BEGIN(role)                                                                            \
    do {                                                                                          \
        WAIT_VM(0);                                                                               \
        if (lane == 0) flag_set(ws + WS_FP1 + (size_t)(((((role) * 16 + rb) * 2 + (wc >> 1)) * 16 + cb) * 2 + (wc & 1)) * 8, tok); \
        if ((role) == 0) { _Pragma("unroll") for (int i = 0; i < 5; ++i) glds16x4_c(g3 + i * 4096, __builtin_amdgcn_readfirstlane(smb + bco + i * 4096)); } \
        _Pragma("unroll") for (int m = 0; m < 3; ++m) {                                           \
            int tile = wave + 8 * m; tile = tile < 20 ? tile : wave;                              \
            const int r = tile / 5, n = tile - 5 * r;                                             \
            const int col = 80 * CB + 16 * n + 4 * q;                                             \
            oo[m] = (64 * RG + 32 * (r >> 1) + 2 * t + (r & 1)) * DM + col;                       \
            xv[m] = ld64_asm(x + oo[m]); dv[m] = ld64_asm(Dskip + col);                           \
        }                                                                                         \
        const int fi_ = ((role) == 0 && lane >= 8) ? RG * 32 + (lane & 1)      \
                        : ((role) * 32 + RG) * 32 + 8 * wc + (lane & 7);                          \
        flag_wait(ws + WS_FP1 + (size_t)fi_ * 8, lane < ((role) ? 8 : 10), tok);                  \
        asm volatile("s_waitcnt vmcnt(0)" : "+v"(xv[0]), "+v"(xv[1]), "+v"(xv[2]), "+v"(dv[0]), "+v"(dv[1]), "+v"(dv[2]) :: "memory"); \
        if ((role) == 0) P2_GATES();                                                              \
        P2_LDA(0, pa); P2_LDA(1, pa + 8192); P2_LDA(2, pa + 16384);                               \
        _Pragma("unroll") for (int r = 0; r < 4; ++r) _Pragma("unroll") for (int n = 0; n < 5; ++n) ac2[r][n] = (f32x4){0.f, 0.f, 0.f, 0.f}; \
    } while (0)
#define P2_GATES()                                                                                \
    asm volatile("global_load_dwordx2 %0, %2, off sc1\n\tglobal_load_dwordx2 %1, %2, off offset:128 sc1" \
                 : "=&v"(gq[0]), "=&v"(gq[1]) : "v"(ws + WS_G + (64 * RG + 2 * t) * 4) : "memory")

    if (wave < 4) {
        const bool do_gate = (cb == 0);
        f32x4 acc[6][2];
#pragma unroll
        for (int a = 0; a < 6; ++a) { acc[a][0] = (f32x4){0.f, 0.f, 0.f, 0.f}; acc[a][1] = (f32x4){0.f, 0.f, 0.f, 0.f}; }
        s16x8 fx[2][2], fw[2][5];
        const int woff = 16384 + t * 128;
#define S_READ(b, sb, co)                                                                         \
    do {                                                                                          \
        fx[b][0] = *(const s16x8*)((sb) + xoff + (co)); fx[b][1] = *(const s16x8*)((sb) + xoff + 2048 + (co)); \
        _Pragma("unroll") for (int T_ = 0; T_ < 5; ++T_) fw[b][T_] = *(const s16x8*)((sb) + woff + T_ * 2048 + (co)); \
    } while (0)
#define S_GATE(b, kk)                                                                             \
    do {                                                                                          \
        if (do_gate) {                                                                            \
            const s16x8 gv = *(const s16x8*)(sm + K1_GW + ((kk) + 8 * q) * 2);                    \
            acc[5][0] = mfma16(gv, fx[b][0], acc[5][0]); acc[5][1] = mfma16(gv, fx[b][1], acc[5][1]); \
        }                                                                                         \
    } while (0)
#define S_BODY(it, bo, bn, MODE)                                                                  \
    do {                                                                                          \
        S_READ(1, sm + (bo), co1);                                                                \
        _Pragma("unroll") for (int T = 0; T < 5; ++T) { acc[T][0] = mfma16(fw[0][T], fx[0][0], acc[T][0]); acc[T][1] = mfma16(fw[0][T], fx[0][1], acc[T][1]); } \
        S_GATE(0, 64 * (it));                                                                     \
        SCHED_FENCE();                                                                            \
        if ((MODE) == 0) { if ((it) < 2) WAIT_VM(7); else if ((it) == 3 && wave == 0) WAIT_VM(6); else WAIT_VM(5); } \
        else if ((MODE) < 3) WAIT_VM(5);                                                          \
        WAIT_LGKM0();                                                                             \
        __builtin_amdgcn_s_barrier();                                                             \
        asm volatile("" ::: "memory");                                                            \
        SCHED_FENCE();                                                                            \
        if ((MODE) == 0 && (it) == 2 && tid == 0) flag_set(ws + WS_FPT + (size_t)bid * 8, tok);    \
        if ((MODE) == 1) pt_check(10);                                                            \
        if ((MODE) < 3) S_READ(0, sm + (bn), co0);                                                \
        _Pragma("unroll") for (int T = 0; T < 5; ++T) {                                           \
            acc[T][0] = mfma16(fw[1][T], fx[1][0], acc[T][0]); acc[T][1] = mfma16(fw[1][T], fx[1][1], acc[T][1]); \
            SCHED_FENCE();                                                                        \
            if ((MODE) == 0) K1_SLOT(T, bo);                                                      \
            else asm volatile("global_load_dwordx4 %0, %1, off sc1" : "=&v"(Bb[(MODE) > 0 ? (MODE)-1 : 0][T]) : "v"(pb + ((MODE)-1) * 5120 + (T - 1) * 1024) : "memory"); \
            SCHED_FENCE();                                                                        \
        }                                                                                         \
        S_GATE(1, 64 * (it) + 32);                                                                \
    } while (0)
        S_READ(0, sm, co0);
        pretile_out();
        int bo = 0, bn = K1_STAGE;
#pragma unroll 1
        for (int it = 0; it < 7; ++it) {
            S_BODY(it, bo, bn, 0);
            if (it == 4 && lane < 40) {
                const float lr = bflo(*(const u16*)(sm + K1_PLR + lane * 2)), li = bflo(*(const u16*)(sm + K1_PLI + lane * 2));
                const float dt = __builtin_amdgcn_exp2f(bflo(*(const u16*)(sm + K1_PLD + lane * 2)) * LOG2E);
                const float za = dt * lr * LOG2E, zb = dt * li * INV2PI;
                f32x2* tb = (f32x2*)(sm + K1_TAB + lane * 64);
                const float n0 = wc == 0 ? 1.f : wc == 1 ? 2.f : wc == 2 ? 8.f : 32.f;
                const float m0 = __builtin_amdgcn_exp2f(n0 * za), r0 = __builtin_amdgcn_fractf(n0 * zb);
                const f32x2 p0 = {m0 * __builtin_amdgcn_cosf(r0), m0 * __builtin_amdgcn_sinf(r0)};
                if (wc == 0) {
                    const float nr = p0.x - 1.0f, ni = p0.y;
                    const float rden = 1.0f / (lr * lr + li * li);
                    tb[0] = p0;
                    tb[6] = (f32x2){(nr * lr + ni * li) * rden, (ni * lr - nr * li) * rden};
                    tb[7] = (f32x2){za, zb};
                } else if (wc == 3) {
                    tb[5] = p0;
                } else {
                    tb[2 * wc - 1] = p0;
                    tb[2 * wc] = cmul(p0, p0);
                }
            }
            bo = bn; bn = bn == 2 * K1_STAGE ? 0 : bn + K1_STAGE;
        }
        S_BODY(7, K1_STAGE, 2 * K1_STAGE, 1); S_BODY(8, 2 * K1_STAGE, 0, 2); S_BODY(9, 0, 0, 3);
        __builtin_amdgcn_s_setprio(3);
        if (do_gate && q == 0) {
            f32x2 gg = {1.0f / (1.0f + __expf(-acc[5][0][0])), 1.0f / (1.0f + __expf(-acc[5][1][0]))};
            st64_wt(ws + WS_G + (128 * rb + 32 * wc + 2 * t) * 4, __builtin_bit_cast(u32x2, gg));
        }
        f32x2 h0[5][2], h1[5][2];
#pragma unroll
        for (int T = 0; T < 5; ++T) {
#pragma unroll
            for (int cc = 0; cc < 2; ++cc) {
                const int chl = 8 * T + 2 * q + cc;
                const f32x4* tb = (const f32x4*)(sm + K1_TAB + chl * 64);
                const f32x4 t0 = tb[0], t1 = tb[1], t2 = tb[2], t3 = tb[3];
                const f32x2 L1 = {t0[0], t0[1]}, L2 = {t0[2], t0[3]}, L4 = {t1[0], t1[1]}, L8 = {t1[2], t1[3]}, L16 = {t2[0], t2[1]};
                const f32x2 cf = {t3[0], t3[1]};
                const f32x2 u0 = cmul_sc(cf, (f32x2){acc[T][0][2 * cc], acc[T][0][2 * cc + 1]});
                const f32x2 u1 = cmul_sc(cf, (f32x2){acc[T][1][2 * cc], acc[T][1][2 * cc + 1]});
                f32x2 a = cfma(L1, u0, u1);
                a = cfma(L2, (f32x2){shr_zero<1>(a.x), shr_zero<1>(a.y)}, a);
                a = cfma(L4, (f32x2){shr_zero<2>(a.x), shr_zero<2>(a.y)}, a);
                a = cfma(L8, (f32x2){shr_zero<4>(a.x), shr_zero<4>(a.y)}, a);
                a = cfma(L16, (f32x2){shr_zero<8>(a.x), shr_zero<8>(a.y)}, a);
                h0[T][cc] = cfma(L1, (f32x2){shr_zero<1>(a.x), shr_zero<1>(a.y)}, u0);
                h1[T][cc] = a;
                if (t == 15) *(f32x2*)(sm + K1_XCH + (wc * 40 + chl) * 8) = a;
            }
        }
        WAIT_LGKM0();
        unsigned old = 0;
        if (lane == 0) old = __hip_atomic_fetch_add((LAS unsigned*)(sm + K1_CNT), 1u, __ATOMIC_RELAXED, __HIP_MEMORY_SCOPE_WORKGROUP);
        old = __builtin_amdgcn_readfirstlane(old);
        asm volatile("" ::: "memory");
        if (old == 3) {
            const int cl = lane < 40 ? lane : 0;
            const f32x2 L32 = *(const f32x2*)(sm + K1_TAB + cl * 64 + 40);
            const f32x2* xc = (const f32x2*)(sm + K1_XCH);
            f32x2 pre[4], s = {0.f, 0.f};
#pragma unroll
            for (int w = 0; w < 4; ++w) { pre[w] = s; s = cfma(L32, s, xc[w * 40 + cl]); }
            if (lane < 40) st128_wt(ws + WS_AGG + (size_t)(cb * 16 + rb) * 640 + lane * 16,
                                    (u32x4){__float_as_uint(s.x), __float_as_uint(s.y), (unsigned)tok, (unsigned)(tok >> 32)});
            f32x2 cy = {0.f, 0.f};
            if (rb > 0) {
                const f32x2 L64 = cmul(L32, L32), L128 = cmul(L64, L64);
                const char* agp = ws + WS_AGG + (size_t)cb * 16 * 640 + cl * 16;
                for (unsigned spins = 0; spins < (1u << 20); ++spins) {
                    bool ok = true;
                    cy = (f32x2){0.f, 0.f};
#pragma unroll
                    for (int hb = 0; hb < 2; ++hb) {
                        if (8 * hb < rb) {
                            u32x4 ag[8];
#pragma unroll
                            for (int j = 0; j < 8; ++j)
                                asm volatile("global_load_dwordx4 %0, %1, off sc1" : "=v"(ag[j]) : "v"(agp + (8 * hb + j < rb ? 8 * hb + j : 0) * 640) : "memory");
                            asm volatile("s_waitcnt vmcnt(0)" : "+v"(ag[0]), "+v"(ag[1]), "+v"(ag[2]), "+v"(ag[3]), "+v"(ag[4]), "+v"(ag[5]), "+v"(ag[6]), "+v"(ag[7]) :: "memory");
#pragma unroll
                            for (int j = 0; j < 8; ++j) {
                                ok = ok && ag[j][2] == (unsigned)tok && ag[j][3] == (unsigned)(tok >> 32);
                                const f32x2 nx = cfma(L128, cy, (f32x2){__uint_as_float(ag[j][0]), __uint_as_float(ag[j][1])});
                                cy = (8 * hb + j < rb) ? nx : cy;
                            }
                        }
                    }
                    if (__all(ok)) break;
                    __builtin_amdgcn_s_sleep(8);
                }
            }
            if (lane < 40) {
#pragma unroll
                for (int w = 0; w < 4; ++w) {
                    *(f32x2*)(sm + K1_CAR + (w * 40 + lane) * 8) = (f32x2){pre[w].x + cy.x, pre[w].y + cy.y};
                    cy = cmul(cy, L32);
                }
            }
            WAIT_LGKM0();
            if (lane == 0) __hip_atomic_store((LAS unsigned*)(sm + K1_CNT + 4), 1u, __ATOMIC_RELAXED, __HIP_MEMORY_SCOPE_WORKGROUP);
        } else {
            for (unsigned spins = 0; spins < (1u << 22); ++spins) {
                if (__hip_atomic_load((LAS unsigned*)(sm + K1_CNT + 4), __ATOMIC_RELAXED, __HIP_MEMORY_SCOPE_WORKGROUP)) break;
                __builtin_amdgcn_s_sleep(2);
            }
        }
        asm volatile("" ::: "memory");
        const float fn = (float)(2 * t + 1);
#pragma unroll
        for (int T = 0; T < 5; ++T) {
            u32x2 o0, o1;
#pragma unroll
            for (int cc = 0; cc < 2; ++cc) {
                const int chl = 8 * T + 2 * q + cc;
                const f32x2 L1 = *(const f32x2*)(sm + K1_TAB + chl * 64), zz = *(const f32x2*)(sm + K1_TAB + chl * 64 + 56);
                const f32x2 cy = *(const f32x2*)(sm + K1_CAR + (wc * 40 + chl) * 8);
                const float mg = __builtin_amdgcn_exp2f(fn * zz.x);
                const float rv = __builtin_amdgcn_fractf(fn * zz.y);
                const f32x2 c0 = cmul_sc((f32x2){mg * __builtin_amdgcn_cosf(rv), mg * __builtin_amdgcn_sinf(rv)}, cy);
                const f32x2 v1 = cfma(L1, c0, h1[T][cc]);
                o0[cc] = pack2(h0[T][cc].x + c0.x, -(h0[T][cc].y + c0.y));
                o1[cc] = pack2(v1.x, -v1.y);
            }
            const int Tg = 5 * cb + T;
            char* dst = a2 + (size_t)(Tg >> 1) * 8192 + (Tg & 1) * 8;
            st64_wt(dst, o0);
            st64_wt(dst + 1024, o1);
        }
        __builtin_amdgcn_s_setprio(0);
        P2_BEGIN(0);
        P2_STEP(0, 10, 8, 7); P2_STEP(1, 10, 8, 7); P2_STEP(2, 10, 8, 7); P2_STEP(3, 10, 8, 7); P2_STEP(4, 10, 8, 7);
        P2_STEP(5, 10, 8, 7); P2_STEP(6, 10, 13, 7); P2_STEP(7, 10, 9, 7); P2_STEP(8, 10, 5, 7); P2_STEP(9, 10, 0, 7);
    } else {
        const int bb = q & 1;
        const int trb = 26624 + (8 * q + (t >> 2)) * 192 + (lane & 3) * 8;
        const int tra = trb + bb * 32;
        const int tr5 = trb + (bb ? 0 : 160);
        f32x4 acc[6][2];
#pragma unroll
        for (int j = 0; j < 6; ++j) {
            const u32x2 bp = *(const u32x2*)(sm + K1_PB1 + (16 * j + 4 * q) * 2);
            acc[j][0] = (f32x4){bflo(bp[0]), bfhi(bp[0]), bflo(bp[1]), bfhi(bp[1])};
            acc[j][1] = acc[j][0];
        }
        s16x8 fx[2][2], fw[2][6];
#define M_READ(b, sb, co, ks)                                                                     \
    do {                                                                                          \
        fx[b][0] = *(const s16x8*)((sb) + xoff + (co)); fx[b][1] = *(const s16x8*)((sb) + xoff + 2048 + (co)); \
        _Pragma("unroll") for (int j_ = 0; j_ < 6; ++j_) {                                        \
            const char* pa_ = (sb) + (j_ < 5 ? tra + j_ * 32 : tr5) + (ks) * 32 * 192;            \
            const s16x4 lo_ = __builtin_amdgcn_ds_read_tr16_b64_v4i16((LAS s16x4*)pa_);           \
            const s16x4 hi_ = __builtin_amdgcn_ds_read_tr16_b64_v4i16((LAS s16x4*)(pa_ + 4 * 192)); \
            fw[b][j_] = __builtin_shufflevector(lo_, hi_, 0, 1, 2, 3, 4, 5, 6, 7);                \
        }                                                                                         \
    } while (0)
#define M_BODY(it, bo, bn, MODE)                                                                  \
    do {                                                                                          \
        M_READ(1, sm + (bo), co1, 1);                                                             \
        _Pragma("unroll") for (int j = 0; j < 6; ++j) { acc[j][0] = mfma16(fw[0][j], fx[0][0], acc[j][0]); acc[j][1] = mfma16(fw[0][j], fx[0][1], acc[j][1]); } \
        SCHED_FENCE();                                                                            \
        if ((MODE) == 0) { if ((it) < 2) WAIT_VM(7); else WAIT_VM(5); }                           \
        else if ((MODE) < 3) WAIT_VM(5);                                                          \
        WAIT_LGKM0();                                                                             \
        __builtin_amdgcn_s_barrier();                                                             \
        asm volatile("" ::: "memory");                                                            \
        SCHED_FENCE();                                                                            \
        if ((MODE) == 1) pt_check(12);                                                            \
        if ((MODE) < 3) M_READ(0, sm + (bn), co0, 0);                                             \
        _Pragma("unroll") for (int j = 0; j < 6; ++j) {                                           \
            acc[j][0] = mfma16(fw[1][j], fx[1][0], acc[j][0]); acc[j][1] = mfma16(fw[1][j], fx[1][1], acc[j][1]); \
            if (j < 5) {                                                                          \
                SCHED_FENCE();                                                                    \
                if ((MODE) == 0) {                                                                \
                    if (j == 4 && (it) == 5 && wave == 7) glds16_c(ws + WS_FPT + (size_t)(((440 * CB) / 14) & ~1) * 8 + (lane < 20 ? lane : 0) * 16, smb + K1_FM); \
                    else K1_SLOT(j, bo);                                                          \
                }                                                                                 \
                else asm volatile("global_load_dwordx4 %0, %1, off sc1" : "=&v"(Bb[(MODE) > 0 ? (MODE)-1 : 0][j < 5 ? j : 0]) : "v"(pb + ((MODE)-1) * 5120 + (j - 1) * 1024) : "memory"); \
                SCHED_FENCE();                                                                    \
            }                                                                                     \
        }                                                                                         \
    } while (0)
        M_READ(0, sm, co0, 0);
        pretile_out();
        int bo = 0, bn = K1_STAGE;
#pragma unroll 1
        for (int it = 0; it < 7; ++it) {
            M_BODY(it, bo, bn, 0);
            bo = bn; bn = bn == 2 * K1_STAGE ? 0 : bn + K1_STAGE;
        }
        M_BODY(7, K1_STAGE, 2 * K1_STAGE, 1); M_BODY(8, 2 * K1_STAGE, 0, 2); M_BODY(9, 0, 0, 3);
#pragma unroll
        for (int v = 0; v < 3; ++v) {
#pragma unroll
            for (int rt = 0; rt < 2; ++rt) {
                u32x4 o;
                if (2 * v + rt < 5) { P2_CACHE(3 * (2 * v + rt)); P2_CACHE(3 * (2 * v + rt) + 1); P2_CACHE(3 * (2 * v + rt) + 2); }
#pragma unroll
                for (int jj = 0; jj < 2; ++jj) {
                    float r[4];
#pragma unroll
                    for (int i = 0; i < 4; ++i) {
                        const float h = acc[2 * v + jj][rt][i];
                        const float e = __builtin_amdgcn_exp2f(h * LOG2E);
                        const float d = fmaf(e, e + 2.0f, 2.0f);
                        r[i] = fmaf(-2.0f, h * __builtin_amdgcn_rcpf(d), h);
                    }
                    o[2 * jj] = pack2(r[0], r[1]);
                    o[2 * jj + 1] = pack2(r[2], r[3]);
                }
                st128_wt(a2 + (size_t)(40 + 3 * cb + v) * 8192 + rt * 1024, o);
            }
        }
        P2_BEGIN(1);
        P2_STEP(0, 12, 8, 6); P2_STEP(1, 12, 8, 6); P2_STEP(2, 12, 8, 6); P2_STEP(3, 12, 8, 6); P2_STEP(4, 12, 8, 6); P2_STEP(5, 12, 13, 6);
        P2_STEP(6, 12, 9, 6); P2_STEP(7, 12, 9, 6); P2_STEP(8, 12, 9, 6); P2_STEP(9, 12, 9, 6); P2_STEP(10, 12, 5, 6); P2_STEP(11, 12, 0, 6);
        flag_wait(ws + WS_FP1 + (size_t)(RG * 32 + (lane & 1)) * 8, lane < 2, tok);
        P2_GATES();
    }
    asm volatile("s_waitcnt vmcnt(0)" : "+v"(gq[0]), "+v"(gq[1]) :: "memory");
    __syncthreads();
#pragma unroll
    for (int r = 0; r < 4; ++r)
#pragma unroll
        for (int n = 0; n < 5; ++n) *(f32x4*)(sm + ((r * 5 + n) * 8 + wave) * 1024 + lane * 16) = ac2[r][n];
    __syncthreads();
#pragma unroll
    for (int m = 0; m < 3; ++m) {
        const int tile = wave + 8 * m;
        if (tile < 20) {
            const char* rp = sm + tile * 8192 + lane * 16;
            const f32x4 vs = (*(const f32x4*)rp + *(const f32x4*)(rp + 1024)) + (*(const f32x4*)(rp + 2048) + *(const f32x4*)(rp + 3072));
            const f32x4 vm = (*(const f32x4*)(rp + 4096) + *(const f32x4*)(rp + 5120)) + (*(const f32x4*)(rp + 6144) + *(const f32x4*)(rp + 7168));
            const int r = tile / 5;
            const float g = __uint_as_float(r == 0 ? gq[0][0] : r == 1 ? gq[0][1] : r == 2 ? gq[1][0] : gq[1][1]);
            f32x4 v = vs * (2.0f * g) + vm * (1.0f - g);
            v[0] += g * bflo(xv[m][0]) * bflo(dv[m][0]);
            v[1] += g * bfhi(xv[m][0]) * bfhi(dv[m][0]);
            v[2] += g * bflo(xv[m][1]) * bflo(dv[m][1]);
            v[3] += g * bfhi(xv[m][1]) * bfhi(dv[m][1]);
            st128_wt(out + oo[m], __builtin_bit_cast(u32x4, v));
        }
    }
    if (bid == 0 && wave == 0) {
#pragma unroll 1
        for (int k = 0; k < 4; ++k) flag_wait(ws + WS_FPT + (size_t)(64 * k + lane) * 8, true, tok);
        if (lane == 0) flag_set(ws + WS_EP, tok);
    }
}

extern "C" void kernel_launch(void* const* d_in, const int* in_sizes, int n_in, void* d_out, int out_size, void* d_ws,
                              size_t ws_size, hipStream_t stream) {
    kf<<<256, 512, 0, stream>>>((const u16*)d_in[0], (const u16*)d_in[1], (const u16*)d_in[2], (const u16*)d_in[3], (const u16*)d_in[4],
                                (const u16*)d_in[5], (const u16*)d_in[6], (const u16*)d_in[7], (const u16*)d_in[8], (const u16*)d_in[10],
                                (const u16*)d_in[11], (const u16*)d_in[12], (const u16*)d_in[13], (char*)d_ws, (float*)d_out);
}
```

```cpp
#include <hip/hip_runtime.h>
#include <stdint.h>

typedef short s16x4 __attribute__((ext_vector_type(4)));
typedef short s16x8 __attribute__((ext_vector_type(8)));
typedef __bf16 bf16x2 __attribute__((ext_vector_type(2)));
typedef float f32x2 __attribute__((ext_vector_type(2)));
typedef float f32x4 __attribute__((ext_vector_type(4)));
typedef unsigned u32x2 __attribute__((ext_vector_type(2)));
typedef unsigned u32x4 __attribute__((ext_vector_type(4)));
typedef unsigned short u16;
#define LAS __attribute__((address_space(3)))
#define GAS __attribute__((address_space(1)))
#define DI __device__ __forceinline__

constexpr int SEQ = 2048, DM = 640, NP = 640, NF = 1536;
constexpr int NKS = 88;

constexpr size_t WS_A2 = 0;
constexpr size_t WS_W2 = WS_A2 + (size_t)16 * NKS * 8192;
constexpr size_t WS_G = WS_W2 + (size_t)8 * NKS * 5120;
constexpr size_t WS_AGG = WS_G + 2048 * 4;
constexpr size_t WS_EP = WS_AGG + 16 * 16 * 640;
constexpr size_t WS_FPT = WS_EP + 256;
constexpr size_t WS_FAG = WS_FPT + 2048;
constexpr size_t WS_FP1 = WS_FAG + 2048;
constexpr size_t WS_JUNK = WS_FP1 + 16384;

DI unsigned f2u(float v) { return __builtin_bit_cast(unsigned, v); }
DI float u2f(unsigned v) { return __builtin_bit_cast(float, v); }
DI bool wave_all(bool p) { return __builtin_amdgcn_ballot_w64(!p) == 0ull; }
DI void wg_sync() {
    __builtin_amdgcn_fence(__ATOMIC_RELEASE, "workgroup");
    __builtin_amdgcn_s_barrier();
    __builtin_amdgcn_fence(__ATOMIC_ACQUIRE, "workgroup");
}
DI float sigmoidf_(float v) { return 1.0f / (1.0f + __builtin_amdgcn_exp2f(v * -1.44269504088896341f)); }
DI float bflo(unsigned v) { return __uint_as_float(v << 16); }
DI float bfhi(unsigned v) { return __uint_as_float(v & 0xffff0000u); }
DI unsigned pack2(float a, float b) {
    f32x2 p = {a, b};
    return __builtin_bit_cast(unsigned, __builtin_convertvector(p, bf16x2));
}
DI f32x4 mfma16(s16x8 a, s16x8 b, f32x4 c) { return __builtin_amdgcn_mfma_f32_16x16x32_bf16(a, b, c, 0, 0, 0); }
template <int S> DI float shr_zero(float v) {
    return __builtin_bit_cast(float, __builtin_amdgcn_update_dpp(0, __builtin_bit_cast(int, v), 0x110 + S, 0xf, 0xf, true));
}
DI f32x2 cmul(f32x2 a, f32x2 b) { return (f32x2){a.x * b.x - a.y * b.y, a.x * b.y + a.y * b.x}; }
DI f32x2 cfma(f32x2 a, f32x2 b, f32x2 c) {
    return (f32x2){fmaf(-a.y, b.y, fmaf(a.x, b.x, c.x)), fmaf(a.y, b.x, fmaf(a.x, b.y, c.y))};
}
DI f32x2 cmul_pk(f32x2 a, f32x2 b) { return (f32x2){fmaf(-a.y, b.y, a.x * b.x), fmaf(a.y, b.x, a.x * b.y)}; }
DI f32x2 cmul_sc(f32x2 a, f32x2 b) {
    float p = a.x * b.x, r = a.x * b.y;
    asm("" : "+v"(p));
    asm("" : "+v"(r));
    return (f32x2){fmaf(-a.y, b.y, p), fmaf(a.y, b.x, r)};
}
DI float dot2bf(unsigned a, unsigned b, float c) {
    return __builtin_amdgcn_fdot2_f32_bf16(__builtin_bit_cast(bf16x2, a), __builtin_bit_cast(bf16x2, b), c, false);
}
#define WAIT_VM(n) asm volatile("s_waitcnt vmcnt(%0)" ::"n"(n) : "memory")
#define WAIT_LGKM0() asm volatile("s_waitcnt lgkmcnt(0)" ::: "memory")
#define ACQUIRE() asm volatile("buffer_inv sc1" ::: "memory")
#define SCHED_FENCE() __builtin_amdgcn_sched_barrier(0)

DI void glds16(const void* gsrc, unsigned lds_dst) {
    unsigned keep;
    asm volatile("s_mov_b32 %0, m0\n\ts_mov_b32 m0, %2\n\ts_nop 0\n\tglobal_load_lds_dwordx4 %1, off\n\ts_mov_b32 m0, %0"
                 : "=&s"(keep) : "v"(gsrc), "s"(lds_dst) : "memory");
}
DI void glds16x4(const void* gsrc, unsigned lds_dst) {
    unsigned keep;
    asm volatile("s_mov_b32 %0, m0\n\ts_mov_b32 m0, %2\n\ts_nop 0\n\tglobal_load_lds_dwordx4 %1, off\n\t"
                 "global_load_lds_dwordx4 %1, off offset:1024\n\tglobal_load_lds_dwordx4 %1, off offset:2048\n\t"
                 "global_load_lds_dwordx4 %1, off offset:3072\n\ts_mov_b32 m0, %0"
                 : "=&s"(keep) : "v"(gsrc), "s"(lds_dst) : "memory");
}
DI void st128_wt(void* p, u32x4 v) { asm volatile("global_store_dwordx4 %0, %1, off sc1\n\ts_nop 1" :: "v"(p), "v"(v) : "memory"); }
DI void st64_wt(void* p, u32x2 v) { asm volatile("global_store_dwordx2 %0, %1, off sc1\n\ts_nop 1" :: "v"(p), "v"(v) : "memory"); }
DI unsigned ld32_asm(const void* p) { unsigned r; asm volatile("global_load_dword %0, %1, off" : "=v"(r) : "v"(p) : "memory"); return r; }
DI unsigned ld16_asm(const void* p) { unsigned r; asm volatile("global_load_ushort %0, %1, off" : "=v"(r) : "v"(p) : "memory"); return r; }
DI u32x2 ld64_asm(const void* p) { u32x2 r; asm volatile("global_load_dwordx2 %0, %1, off" : "=v"(r) : "v"(p) : "memory"); return r; }
DI u32x4 ld128_asm(const void* p) { u32x4 r; asm volatile("global_load_dwordx4 %0, %1, off" : "=v"(r) : "v"(p) : "memory"); return r; }
constexpr float LOG2E = 1.44269504088896341f, INV2PI = 0.15915494309189535f;
DI f32x2 lam_bar(float lr, float li, float ld) {
    const float dt = __builtin_amdgcn_exp2f(ld * LOG2E);
    const float m = __builtin_amdgcn_exp2f(dt * lr * LOG2E);
    const float rv = __builtin_amdgcn_fractf(dt * li * INV2PI);
    return (f32x2){m * __builtin_amdgcn_cosf(rv), m * __builtin_amdgcn_sinf(rv)};
}

typedef unsigned long long u64;
DI void glds16_c(const void* gsrc, unsigned lds_dst) {
    unsigned keep;
    lds_dst = __builtin_amdgcn_readfirstlane(lds_dst);
    asm volatile("s_mov_b32 %0, m0\n\ts_mov_b32 m0, %2\n\ts_nop 0\n\tglobal_load_lds_dwordx4 %1, off sc1\n\ts_mov_b32 m0, %0"
                 : "=&s"(keep) : "v"(gsrc), "s"(lds_dst) : "memory");
}
DI void glds16x4_c(const void* gsrc, unsigned lds_dst) {
    unsigned keep;
    asm volatile("s_mov_b32 %0, m0\n\ts_mov_b32 m0, %2\n\ts_nop 0\n\tglobal_load_lds_dwordx4 %1, off sc1\n\t"
                 "global_load_lds_dwordx4 %1, off offset:1024 sc1\n\tglobal_load_lds_dwordx4 %1, off offset:2048 sc1\n\t"
                 "global_load_lds_dwordx4 %1, off offset:3072 sc1\n\ts_mov_b32 m0, %0"
                 : "=&s"(keep) : "v"(gsrc), "s"(lds_dst) : "memory");
}
DI void flag_set(char* p, u64 tok) { __hip_atomic_store((GAS u64*)p, tok, __ATOMIC_RELAXED, __HIP_MEMORY_SCOPE_AGENT); }
DI void flag_wait(const char* p, bool need, u64 tok) {
    for (unsigned spins = 0; spins < (1u << 20); ++spins) {
        u64 v = tok;
        if (need) v = __hip_atomic_load((const GAS u64*)p, __ATOMIC_RELAXED, __HIP_MEMORY_SCOPE_AGENT);
        if (__all(v == tok)) break;
        __builtin_amdgcn_s_sleep(8);
    }
    asm volatile("" ::: "memory");
}

constexpr int K1_STAGE = 38 * 1024;
constexpr int K1_GW = 3 * K1_STAGE;
constexpr int K1_PLR = K1_GW + 1280, K1_PLI = K1_GW + 1360, K1_PLD = K1_GW + 1440, K1_PB1 = K1_GW + 1536;
constexpr int K1_TAB = K1_GW + 2048;
constexpr int K1_XCH = K1_TAB + 2560;
constexpr int K1_CAR = K1_XCH + 1280;
constexpr int K1_CNT = K1_CAR + 1280;
constexpr int K1_PRT = 159 * 1024 - 512;
constexpr int K1_PT = 122 * 1024;
constexpr int K1_FM = 140 * 1024;
constexpr int KF_LDS = 160 * 1024;

__global__ __launch_bounds__(512, 2) __attribute__((target("no-packed-fp32-ops"))) void kf(const u16* __restrict__ x, const u16* __restrict__ Lre, const u16* __restrict__ Lim,
                                             const u16* __restrict__ Bre, const u16* __restrict__ Bim, const u16* __restrict__ Cre,
                                             const u16* __restrict__ Cim, const u16* __restrict__ logdt, const u16* __restrict__ Dskip,
                                             const u16* __restrict__ w1, const u16* __restrict__ b1, const u16* __restrict__ w2,
                                             const u16* __restrict__ gw, char* __restrict__ ws, float* __restrict__ out) {
    __shared__ __attribute__((aligned(16))) char sm[KF_LDS];
    const int tid = threadIdx.x, lane = tid & 63;
    const int wave = __builtin_amdgcn_readfirstlane(tid >> 6);
    const int bid = blockIdx.x, xcd = bid & 7, idx = bid >> 3;
    const int rb = 8 * (xcd & 1) + (idx & 7);
    const int cb = 4 * (xcd >> 1) + (idx >> 3);
    const int RG = 8 * (xcd >> 1) + (idx & 7);
    const int CB = 4 * (xcd & 1) + (idx >> 3);
    const int t = lane & 15, q = lane >> 4;
    const unsigned smb = (unsigned)(size_t)(LAS char*)sm;
    unsigned cjunk = 0;
    if (wave == 7) {
        u64 pc;
        asm volatile("s_getpc_b64 %0" : "=s"(pc));
        const char* cp = (const char*)(pc & ~255ull) + lane * 128;
#pragma unroll
        for (int k = 0; k < 4; ++k) asm volatile("global_load_dword %0, %1, off" : "+v"(cjunk) : "v"(cp + k * 8192) : "memory");
    }
    const char* src[5];
    unsigned ldo[5];
    int incE, strE;
    {
        const int l8 = lane >> 3;
        auto xsrc = [&](int s) __attribute__((always_inline)) {
            const int p = 8 * s + l8;
            const int tm = 128 * rb + 32 * (p >> 5) + 2 * (p & 15) + ((p >> 4) & 1);
            return (const char*)(x + tm * DM + (((lane & 7) ^ ((p >> 1) & 7)) << 3));
        };
        auto wsrc = [&](int s) __attribute__((always_inline)) {
            const int p = 8 * s + l8;
            const int ch = 40 * cb + 8 * (p >> 4) + ((p & 15) >> 1);
            return (const char*)(((p & 1) ? Bim : Bre) + ch * DM + (((lane & 7) ^ ((p >> 1) & 7)) << 3));
        };
        auto msrc = [&](int s) __attribute__((always_inline)) {
            const int gI = 64 * s + lane, row = gI / 12, cp = gI - 12 * row;
            int j = (cp >> 1) - ((row >> 3) & 1);
            j += j < 0 ? 6 : 0;
            return (const char*)(w1 + row * NF + 96 * cb + 16 * j + 8 * (cp & 1));
        };
        src[0] = xsrc(2 * wave); ldo[0] = smb + 2048 * wave;
        src[1] = xsrc(2 * wave + 1); ldo[1] = smb + 2048 * wave + 1024;
        src[2] = wsrc(wave); ldo[2] = smb + 16384 + 1024 * wave;
        src[3] = msrc(wave); ldo[3] = smb + 26624 + 1024 * wave;
        if (wave < 2) { src[4] = wsrc(8 + wave); ldo[4] = smb + 16384 + 1024 * (8 + wave); incE = 128; strE = 1; }
        else if (wave < 6) { src[4] = msrc(6 + wave); ldo[4] = smb + 26624 + 1024 * (6 + wave); incE = 64 * NF * 2; strE = 1; }
        else {
            const u16* p = gw + lane * 8;
            if (wave == 7) {
                p = gw + 512 + t * 8;
                if (lane >= 16 && lane < 21) p = Lre + 40 * cb + (lane - 16) * 8;
                if (lane >= 21 && lane < 26) p = Lim + 40 * cb + (lane - 21) * 8;
                if (lane >= 26 && lane < 31) p = logdt + 40 * cb + (lane - 26) * 8;
                if (lane >= 32 && lane < 44) p = b1 + 96 * cb + (lane - 32) * 8;
            }
            src[4] = (const char*)p; ldo[4] = smb + K1_GW + (wave - 6) * 1024; incE = 0; strE = 0;
        }
    }
#define K1_SLOT(i, boff)                                                                 \
    do {                                                                                 \
        glds16(src[i], ldo[i] + ((i) == 4 ? strE * (boff) : (boff)));                    \
        src[i] += (i) < 3 ? 128 : (i) == 3 ? 64 * NF * 2 : incE;                         \
    } while (0)
    K1_SLOT(0, 0); K1_SLOT(1, 0); K1_SLOT(2, 0); K1_SLOT(3, 0); K1_SLOT(4, 0);
    u32x2 epv;
    asm volatile("global_load_dwordx2 %0, %1, off sc1" : "=v"(epv) : "v"(ws + WS_EP) : "memory");

    const int pf0 = 14 * bid + 2 * wave;
    char* ptl = sm + K1_PT + wave * 2048;
#pragma unroll
    for (int u = 0; u < 2; ++u) {
        int f = pf0 + u; f = (wave < 7 && f < 3520) ? f : 0;
        const int cK = f / 5, nt = f - 5 * cK, cB = cK / NKS, KS = cK - NKS * cB, d0 = 80 * cB + 16 * nt;
        const u16* p0 = (KS < 40) ? ((lane >> 5) ? Cim : Cre) + (d0 + ((lane >> 1) & 15)) * NP + 16 * KS + 8 * (lane & 1)
                                  : w2 + (32 * (KS - 40) + (lane >> 1)) * DM + d0 + 8 * (lane & 1);
        glds16(p0, smb + K1_PT + wave * 2048 + u * 1024);
    }
#pragma unroll
    for (int st = 1; st < 3; ++st) {
        K1_SLOT(0, st * K1_STAGE); K1_SLOT(1, st * K1_STAGE); K1_SLOT(2, st * K1_STAGE); K1_SLOT(3, st * K1_STAGE); K1_SLOT(4, st * K1_STAGE);
    }
    if (tid == 0) *(u32x2*)(sm + K1_CNT) = (u32x2){0u, 0u};

    const int wc = wave & 3;
    const int xoff = (32 * wc + t) * 128;
    const int co0 = (q ^ ((t >> 1) & 7)) * 16, co1 = co0 ^ 64;
    char* a2 = ws + WS_A2 + (size_t)rb * NKS * 8192 + (2 * wc) * 1024 + lane * 16;

    asm volatile("s_waitcnt vmcnt(10)" : "+v"(epv), "+v"(cjunk) :: "memory");
    WAIT_LGKM0();
    __builtin_amdgcn_s_barrier();
    asm volatile("" ::: "memory");
    const u64 tok = (((u64)(unsigned)__builtin_amdgcn_readfirstlane(epv[1]) << 32) | (unsigned)__builtin_amdgcn_readfirstlane(epv[0])) + 1ull;
    auto pretile_out = [&]() __attribute__((always_inline)) {
        {
            const int tro = (4 * q + (t >> 2)) * 32 + (lane & 3) * 8;
#pragma unroll
            for (int u = 0; u < 2; ++u) {
                const int f = pf0 + u;
                const int cK = f / 5, KS = cK % NKS;
                const char* pp = ptl + u * 1024;
                u32x4 wf;
                if (KS < 40) {
                    const unsigned r0 = *(const unsigned*)(pp + t * 32 + 4 * q), r1 = *(const unsigned*)(pp + t * 32 + 16 + 4 * q);
                    const unsigned i0 = *(const unsigned*)(pp + 512 + t * 32 + 4 * q), i1 = *(const unsigned*)(pp + 512 + t * 32 + 16 + 4 * q);
                    wf[0] = (r0 & 0xffffu) | (i0 << 16); wf[1] = (r0 >> 16) | (i0 & 0xffff0000u);
                    wf[2] = (r1 & 0xffffu) | (i1 << 16); wf[3] = (r1 >> 16) | (i1 & 0xffff0000u);
                } else {
                    const s16x4 lo = __builtin_amdgcn_ds_read_tr16_b64_v4i16((LAS s16x4*)(pp + tro));
                    const s16x4 hi = __builtin_amdgcn_ds_read_tr16_b64_v4i16((LAS s16x4*)(pp + tro + 512));
                    wf = __builtin_bit_cast(u32x4, __builtin_shufflevector(lo, hi, 0, 1, 2, 3, 4, 5, 6, 7));
                }
                st128_wt(((wave < 7 && f < 3520) ? ws + WS_W2 + (size_t)f * 1024 : ws + WS_JUNK + (size_t)(bid * 16 + wave * 2 + u) * 1024) + lane * 16, wf);
            }
        }
    };

    const int rb2 = RG >> 1, hh = RG & 1;
    const int ks0 = wave < 4 ? 10 * wave : 40 + 12 * (wave - 4);
    const char* pa = ws + WS_A2 + ((size_t)rb2 * NKS + ks0) * 8192 + hh * 4096 + lane * 16;
    const char* pb = ws + WS_W2 + ((size_t)CB * NKS + ks0) * 5120 + 1024 + lane * 16;
    const int bco = wave < 4 ? wave * 20480 : wave < 6 ? 81920 + (wave - 4) * 15360 : K1_PT + (wave - 6) * 15360;
    u32x4 Ab[4][4], Bb[3][5];
    f32x4 ac2[4][5];
#define P2_L1(dst, P, OFF) asm volatile("global_load_dwordx4 %0, %1, off offset:" #OFF " sc1" : "=&v"(dst) : "v"(P) : "memory")
#define P2_LDB(slot, P)                                                                           \
    do { P2_L1(Bb[slot][0], P, -1024); P2_L1(Bb[slot][1], P, 0); P2_L1(Bb[slot][2], P, 1024); P2_L1(Bb[slot][3], P, 2048); P2_L1(Bb[slot][4], P, 3072); } while (0)
#define P2_LDA(slot, P)                                                                           \
    do { P2_L1(Ab[slot][0], P, 0); P2_L1(Ab[slot][1], P, 1024); P2_L1(Ab[slot][2], P, 2048); P2_L1(Ab[slot][3], P, 3072); } while (0)
    auto pt_flag = [&](int nsteps, int i) __attribute__((always_inline)) {
        const int fa = (CB * NKS + ks0) * 5, w0 = fa / 14, w1_ = (fa + 5 * nsteps - 1) / 14;
        return ws + WS_FPT + (size_t)(w0 + i <= w1_ ? w0 + i : w0) * 8;
    };
    auto pt_check = [&](int nsteps) __attribute__((always_inline)) {
        const int fa = (CB * NKS + ks0) * 5, w0 = fa / 14, w1_ = (fa + 5 * nsteps - 1) / 14;
        const int wi = w0 + lane <= w1_ ? w0 + lane : w0;
        const u64 v = *(const u64*)(sm + K1_FM + (wi - (((440 * CB) / 14) & ~1)) * 8);
        if (!wave_all(v == tok)) flag_wait(pt_flag(nsteps, lane), lane < 8, tok);
    };
    const char* g3 = pb - 1024 + 3 * 5120;
#define P2_CACHE(i) glds16_c(g3 + (i) * 1024, smb + bco + (i) * 1024)
#define P2_M2(ua, ub, i)                                                                          \
    do {                                                                                          \
        ac2[(i) / 5][(i) % 5] = mfma16(__builtin_bit_cast(s16x8, Bb[ub][(i) % 5]), __builtin_bit_cast(s16x8, Ab[ua][(i) / 5]), ac2[(i) / 5][(i) % 5]); \
        ac2[((i) + 1) / 5][((i) + 1) % 5] = mfma16(__builtin_bit_cast(s16x8, Bb[ub][((i) + 1) % 5]), __builtin_bit_cast(s16x8, Ab[ua][((i) + 1) / 5]), ac2[((i) + 1) / 5][((i) + 1) % 5]); \
        SCHED_FENCE();                                                                            \
    } while (0)
#define P2_STEP(j, NS, WN, NL)                                                                    \
    do {                                                                                          \
        asm volatile("s_waitcnt vmcnt(%9)" : "+v"(Ab[(j) & 3][0]), "+v"(Ab[(j) & 3][1]), "+v"(Ab[(j) & 3][2]), "+v"(Ab[(j) & 3][3]), \
                     "+v"(Bb[(j) % 3][0]), "+v"(Bb[(j) % 3][1]), "+v"(Bb[(j) % 3][2]), "+v"(Bb[(j) % 3][3]), "+v"(Bb[(j) % 3][4]) : "n"(WN) : "memory"); \
        SCHED_FENCE();                                                                            \
        const char* pa_ = pa + ((j) + 3) * 8192;                                                  \
        const char* pb_ = pb + ((j) + 2) * 5120;                                                  \
        constexpr bool LA_ = (j) + 3 < (NS), LBG_ = (j) + 2 >= (NL) && (j) + 2 < (NS), LBL_ = (j) + 2 >= 3 && (j) + 2 < (NL); \
        P2_M2((j) & 3, (j) % 3, 0); if (LA_) { P2_L1(Ab[((j) + 3) & 3][0], pa_, 0); SCHED_FENCE(); }      \
        P2_M2((j) & 3, (j) % 3, 2); if (LA_) { P2_L1(Ab[((j) + 3) & 3][1], pa_, 1024); SCHED_FENCE(); }   \
        P2_M2((j) & 3, (j) % 3, 4); if (LA_) { P2_L1(Ab[((j) + 3) & 3][2], pa_, 2048); SCHED_FENCE(); }   \
        P2_M2((j) & 3, (j) % 3, 6); if (LA_) { P2_L1(Ab[((j) + 3) & 3][3], pa_, 3072); SCHED_FENCE(); }   \
        if (LBL_) { _Pragma("unroll") for (int n_ = 0; n_ < 5; ++n_) Bb[((j) + 2) % 3][n_] = *(const u32x4*)(sm + bco + (((j) + 2 - 3) * 5 + n_) * 1024 + lane * 16); SCHED_FENCE(); } \
        P2_M2((j) & 3, (j) % 3, 8); if (LBG_) { P2_L1(Bb[((j) + 2) % 3][0], pb_, -1024); SCHED_FENCE(); } \
        P2_M2((j) & 3, (j) % 3, 10); if (LBG_) { P2_L1(Bb[((j) + 2) % 3][1], pb_, 0); SCHED_FENCE(); }    \
        P2_M2((j) & 3, (j) % 3, 12); if (LBG_) { P2_L1(Bb[((j) + 2) % 3][2], pb_, 1024); SCHED_FENCE(); } \
        P2_M2((j) & 3, (j) % 3, 14); if (LBG_) { P2_L1(Bb[((j) + 2) % 3][3], pb_, 2048); SCHED_FENCE(); } \
        P2_M2((j) & 3, (j) % 3, 16); if (LBG_) { P2_L1(Bb[((j) + 2) % 3][4], pb_, 3072); SCHED_FENCE(); } \
        P2_M2((j) & 3, (j) % 3, 18);                                                              \
    } while (0)
    u32x2 xv[3], dv[3], gq[2];
    int oo[3];
#define P2_BEGIN(role)                                                                            \
    do {                                                                                          \
        WAIT_VM(0);                                                                               \
        if (lane == 0) flag_set(ws + WS_FP1 + (size_t)(((((role) * 16 + rb) * 2 + (wc >> 1)) * 16 + cb) * 2 + (wc & 1)) * 8, tok); \
        if ((role) == 0) { _Pragma("unroll") for (int i = 0; i < 5; ++i) glds16x4_c(g3 + i * 4096, __builtin_amdgcn_readfirstlane(smb + bco + i * 4096)); } \
        _Pragma("unroll") for (int m = 0; m < 3; ++m) {                                           \
            int tile = wave + 8 * m; tile = tile < 20 ? tile : wave;                              \
            const int r = tile / 5, n = tile - 5 * r;                                             \
            const int col = 80 * CB + 16 * n + 4 * q;                                             \
            oo[m] = (64 * RG + 32 * (r >> 1) + 2 * t + (r & 1)) * DM + col;                       \
            xv[m] = ld64_asm(x + oo[m]); dv[m] = ld64_asm(Dskip + col);                           \
        }                                                                                         \
        const int fi_ = ((role) == 0 && lane >= 8) ? RG * 32 + (lane & 1)      \
                        : ((role) * 32 + RG) * 32 + 8 * wc + (lane & 7);                          \
        flag_wait(ws + WS_FP1 + (size_t)fi_ * 8, lane < ((role) ? 8 : 10), tok);                  \
        asm volatile("s_waitcnt vmcnt(0)" : "+v"(xv[0]), "+v"(xv[1]), "+v"(xv[2]), "+v"(dv[0]), "+v"(dv[1]), "+v"(dv[2]) :: "memory"); \
        if ((role) == 0) P2_GATES();                                                              \
        P2_LDA(0, pa); P2_LDA(1, pa + 8192); P2_LDA(2, pa + 16384);                               \
        _Pragma("unroll") for (int r = 0; r < 4; ++r) _Pragma("unroll") for (int n = 0; n < 5; ++n) ac2[r][n] = (f32x4){0.f, 0.f, 0.f, 0.f}; \
    } while (0)
#define P2_GATES()                                                                                \
    asm volatile("global_load_dwordx2 %0, %2, off sc1\n\tglobal_load_dwordx2 %1, %2, off offset:128 sc1" \
                 : "=&v"(gq[0]), "=&v"(gq[1]) : "v"(ws + WS_G + (64 * RG + 2 * t) * 4) : "memory")

    if (wave < 4) {
        const bool do_gate = (cb == 0);
        f32x4 acc[6][2];
#pragma unroll
        for (int a = 0; a < 6; ++a) { acc[a][0] = (f32x4){0.f, 0.f, 0.f, 0.f}; acc[a][1] = (f32x4){0.f, 0.f, 0.f, 0.f}; }
        s16x8 fx[2][2], fw[2][5];
        const int woff = 16384 + t * 128;
#define S_READ(b, sb, co)                                                                         \
    do {                                                                                          \
        fx[b][0] = *(const s16x8*)((sb) + xoff + (co)); fx[b][1] = *(const s16x8*)((sb) + xoff + 2048 + (co)); \
        _Pragma("unroll") for (int T_ = 0; T_ < 5; ++T_) fw[b][T_] = *(const s16x8*)((sb) + woff + T_ * 2048 + (co)); \
    } while (0)
#define S_GATE(b, kk)                                                                             \
    do {                                                                                          \
        if (do_gate) {                                                                            \
            const s16x8 gv = *(const s16x8*)(sm + K1_GW + ((kk) + 8 * q) * 2);                    \
            acc[5][0] = mfma16(gv, fx[b][0], acc[5][0]); acc[5][1] = mfma16(gv, fx[b][1], acc[5][1]); \
        }                                                                                         \
    } while (0)
#define S_BODY(it, bo, bn, MODE)                                                                  \
    do {                                                                                          \
        S_READ(1, sm + (bo), co1);                                                                \
        _Pragma("unroll") for (int T = 0; T < 5; ++T) { acc[T][0] = mfma16(fw[0][T], fx[0][0], acc[T][0]); acc[T][1] = mfma16(fw[0][T], fx[0][1], acc[T][1]); } \
        S_GATE(0, 64 * (it));                                                                     \
        SCHED_FENCE();                                                                            \
        if ((MODE) == 0) { if ((it) < 2) WAIT_VM(7); else if ((it) == 3 && wave == 0) WAIT_VM(6); else WAIT_VM(5); } \
        else if ((MODE) < 3) WAIT_VM(5);                                                          \
        WAIT_LGKM0();                                                                             \
        __builtin_amdgcn_s_barrier();                                                             \
        asm volatile("" ::: "memory");                                                            \
        SCHED_FENCE();                                                                            \
        if ((MODE) == 0 && (it) == 2 && tid == 0) flag_set(ws + WS_FPT + (size_t)bid * 8, tok);    \
        if ((MODE) == 1) pt_check(10);                                                            \
        if ((MODE) < 3) S_READ(0, sm + (bn), co0);                                                \
        _Pragma("unroll") for (int T = 0; T < 5; ++T) {                                           \
            acc[T][0] = mfma16(fw[1][T], fx[1][0], acc[T][0]); acc[T][1] = mfma16(fw[1][T], fx[1][1], acc[T][1]); \
            SCHED_FENCE();                                                                        \
            if ((MODE) == 0) K1_SLOT(T, bo);                                                      \
            else asm volatile("global_load_dwordx4 %0, %1, off sc1" : "=&v"(Bb[(MODE) > 0 ? (MODE)-1 : 0][T]) : "v"(pb + ((MODE)-1) * 5120 + (T - 1) * 1024) : "memory"); \
            SCHED_FENCE();                                                                        \
        }                                                                                         \
        S_GATE(1, 64 * (it) + 32);                                                                \
    } while (0)
        S_READ(0, sm, co0);
        pretile_out();
        int bo = 0, bn = K1_STAGE;
#pragma unroll 1
        for (int it = 0; it < 7; ++it) {
            S_BODY(it, bo, bn, 0);
            if (it == 4 && lane < 40) {
                const float lr = bflo(*(const u16*)(sm + K1_PLR + lane * 2)), li = bflo(*(const u16*)(sm + K1_PLI + lane * 2));
                const float dt = __builtin_amdgcn_exp2f(bflo(*(const u16*)(sm + K1_PLD + lane * 2)) * LOG2E);
                const float za = dt * lr * LOG2E, zb = dt * li * INV2PI;
                f32x2* tb = (f32x2*)(sm + K1_TAB + lane * 64);
                const float n0 = wc == 0 ? 1.f : wc == 1 ? 2.f : wc == 2 ? 8.f : 32.f;
                const float m0 = __builtin_amdgcn_exp2f(n0 * za), r0 = __builtin_amdgcn_fractf(n0 * zb);
                const f32x2 p0 = {m0 * __builtin_amdgcn_cosf(r0), m0 * __builtin_amdgcn_sinf(r0)};
                if (wc == 0) {
                    const float nr = p0.x - 1.0f, ni = p0.y;
                    const float rden = 1.0f / (lr * lr + li * li);
                    tb[0] = p0;
                    tb[6] = (f32x2){(nr * lr + ni * li) * rden, (ni * lr - nr * li) * rden};
                    tb[7] = (f32x2){za, zb};
                } else if (wc == 3) {
                    tb[5] = p0;
                } else {
                    tb[2 * wc - 1] = p0;
                    tb[2 * wc] = cmul(p0, p0);
                }
            }
            bo = bn; bn = bn == 2 * K1_STAGE ? 0 : bn + K1_STAGE;
        }
        S_BODY(7, K1_STAGE, 2 * K1_STAGE, 1); S_BODY(8, 2 * K1_STAGE, 0, 2); S_BODY(9, 0, 0, 3);
        __builtin_amdgcn_s_setprio(3);
        if (do_gate && q == 0) {
            f32x2 gg = {sigmoidf_(acc[5][0][0]), sigmoidf_(acc[5][1][0])};
            st64_wt(ws + WS_G + (128 * rb + 32 * wc + 2 * t) * 4, __builtin_bit_cast(u32x2, gg));
        }
        f32x2 h0[5][2], h1[5][2];
#pragma unroll
        for (int T = 0; T < 5; ++T) {
#pragma unroll
            for (int cc = 0; cc < 2; ++cc) {
                const int chl = 8 * T + 2 * q + cc;
                const f32x4* tb = (const f32x4*)(sm + K1_TAB + chl * 64);
                const f32x4 t0 = tb[0], t1 = tb[1], t2 = tb[2], t3 = tb[3];
                const f32x2 L1 = {t0[0], t0[1]}, L2 = {t0[2], t0[3]}, L4 = {t1[0], t1[1]}, L8 = {t1[2], t1[3]}, L16 = {t2[0], t2[1]};
                const f32x2 cf = {t3[0], t3[1]};
                const f32x2 u0 = cmul_sc(cf, (f32x2){acc[T][0][2 * cc], acc[T][0][2 * cc + 1]});
                const f32x2 u1 = cmul_sc(cf, (f32x2){acc[T][1][2 * cc], acc[T][1][2 * cc + 1]});
                f32x2 a = cfma(L1, u0, u1);
                a = cfma(L2, (f32x2){shr_zero<1>(a.x), shr_zero<1>(a.y)}, a);
                a = cfma(L4, (f32x2){shr_zero<2>(a.x), shr_zero<2>(a.y)}, a);
                a = cfma(L8, (f32x2){shr_zero<4>(a.x), shr_zero<4>(a.y)}, a);
                a = cfma(L16, (f32x2){shr_zero<8>(a.x), shr_zero<8>(a.y)}, a);
                h0[T][cc] = cfma(L1, (f32x2){shr_zero<1>(a.x), shr_zero<1>(a.y)}, u0);
                h1[T][cc] = a;
                if (t == 15) *(f32x2*)(sm + K1_XCH + (wc * 40 + chl) * 8) = a;
            }
        }
        WAIT_LGKM0();
        unsigned old = 0;
        if (lane == 0) old = __hip_atomic_fetch_add((LAS unsigned*)(sm + K1_CNT), 1u, __ATOMIC_RELAXED, __HIP_MEMORY_SCOPE_WORKGROUP);
        old = __builtin_amdgcn_readfirstlane(old);
        asm volatile("" ::: "memory");
        {
            const int cl = lane < 40 ? lane : 0;
            const f32x2 L32 = *(const f32x2*)(sm + K1_TAB + cl * 64 + 40), zz = *(const f32x2*)(sm + K1_TAB + cl * 64 + 56);
            const f32x2* xc = (const f32x2*)(sm + K1_XCH);
            if (old == 3) {
                f32x2 s = {0.f, 0.f};
#pragma unroll
                for (int w = 0; w < 4; ++w) s = cfma(L32, s, xc[w * 40 + cl]);
                if (lane < 40) st128_wt(ws + WS_AGG + (size_t)(cb * 16 + rb) * 640 + lane * 16,
                                        (u32x4){f2u(s.x), f2u(s.y), (unsigned)tok, (unsigned)(tok >> 32)});
            }
            if (bid == 255 && old == 0) {
#pragma unroll 1
                for (int k = 0; k < 4; ++k) flag_wait(ws + WS_FPT + (size_t)(64 * k + lane) * 8, true, tok);
                if (lane == 0) flag_set(ws + WS_EP, tok);
            }
            f32x2 cy = {0.f, 0.f};
            if (4 * wc < rb) {
                const f32x2 L64 = cmul(L32, L32), L128 = cmul(L64, L64);
                const char* agp = ws + WS_AGG + (size_t)(cb * 16 + 4 * wc) * 640 + cl * 16;
                for (unsigned spins = 0; spins < (1u << 20); ++spins) {
                    u32x4 ag[4];
#pragma unroll
                    for (int j = 0; j < 4; ++j)
                        asm volatile("global_load_dwordx4 %0, %1, off sc1" : "=v"(ag[j]) : "v"(agp + (4 * wc + j < rb ? j : 0) * 640) : "memory");
                    asm volatile("s_waitcnt vmcnt(0)" : "+v"(ag[0]), "+v"(ag[1]), "+v"(ag[2]), "+v"(ag[3]) :: "memory");
                    bool ok = true;
                    cy = (f32x2){0.f, 0.f};
#pragma unroll
                    for (int j = 0; j < 4; ++j) {
                        ok = ok && ag[j][2] == (unsigned)tok && ag[j][3] == (unsigned)(tok >> 32);
                        const f32x2 nx = cfma(L128, cy, (f32x2){u2f(ag[j][0]), u2f(ag[j][1])});
                        cy = (4 * wc + j < rb) ? nx : cy;
                    }
                    if (wave_all(ok)) break;
                    __builtin_amdgcn_s_sleep(4);
                }
                for (int j = 4 * wc + 4; j < rb; ++j) cy = cmul(cy, L128);
            }
            (void)zz;
            if (lane < 40) *(f32x2*)(sm + K1_PRT + (wc * 40 + lane) * 8) = cy;
            WAIT_LGKM0();
            if (lane == 0) __hip_atomic_fetch_add((LAS unsigned*)(sm + K1_CNT + 4), 1u, __ATOMIC_RELAXED, __HIP_MEMORY_SCOPE_WORKGROUP);
            for (unsigned spins = 0; spins < (1u << 22); ++spins) {
                if (__hip_atomic_load((LAS unsigned*)(sm + K1_CNT + 4), __ATOMIC_RELAXED, __HIP_MEMORY_SCOPE_WORKGROUP) >= 4u) break;
                __builtin_amdgcn_s_sleep(1);
            }
            asm volatile("" ::: "memory");
            const f32x2* pp = (const f32x2*)(sm + K1_PRT);
            f32x2 c = {(pp[cl].x + pp[40 + cl].x) + (pp[80 + cl].x + pp[120 + cl].x), (pp[cl].y + pp[40 + cl].y) + (pp[80 + cl].y + pp[120 + cl].y)};
            for (int w = 0; w < wc; ++w) c = cfma(L32, c, xc[w * 40 + cl]);
            if (lane < 40) *(f32x2*)(sm + K1_CAR + (wc * 40 + lane) * 8) = c;
        }
        asm volatile("" ::: "memory");
        const float fn = (float)(2 * t + 1);
#pragma unroll
        for (int T = 0; T < 5; ++T) {
            u32x2 o0, o1;
#pragma unroll
            for (int cc = 0; cc < 2; ++cc) {
                const int chl = 8 * T + 2 * q + cc;
                const f32x2 L1 = *(const f32x2*)(sm + K1_TAB + chl * 64), zz = *(const f32x2*)(sm + K1_TAB + chl * 64 + 56);
                const f32x2 cy = *(const f32x2*)(sm + K1_CAR + (wc * 40 + chl) * 8);
                const float mg = __builtin_amdgcn_exp2f(fn * zz.x);
                const float rv = __builtin_amdgcn_fractf(fn * zz.y);
                const f32x2 c0 = cmul_sc((f32x2){mg * __builtin_amdgcn_cosf(rv), mg * __builtin_amdgcn_sinf(rv)}, cy);
                const f32x2 v1 = cfma(L1, c0, h1[T][cc]);
                o0[cc] = pack2(h0[T][cc].x + c0.x, -(h0[T][cc].y + c0.y));
                o1[cc] = pack2(v1.x, -v1.y);
            }
            const int Tg = 5 * cb + T;
            char* dst = a2 + (size_t)(Tg >> 1) * 8192 + (Tg & 1) * 8;
            st64_wt(dst, o0);
            st64_wt(dst + 1024, o1);
        }
        __builtin_amdgcn_s_setprio(0);
        P2_BEGIN(0);
        P2_STEP(0, 10, 8, 7); P2_STEP(1, 10, 8, 7); P2_STEP(2, 10, 8, 7); P2_STEP(3, 10, 8, 7); P2_STEP(4, 10, 8, 7);
        P2_STEP(5, 10, 8, 7); P2_STEP(6, 10, 13, 7); P2_STEP(7, 10, 9, 7); P2_STEP(8, 10, 5, 7); P2_STEP(9, 10, 0, 7);
    } else {
        const int bb = q & 1;
        const int trb = 26624 + (8 * q + (t >> 2)) * 192 + (lane & 3) * 8;
        const int tra = trb + bb * 32;
        const int tr5 = trb + (bb ? 0 : 160);
        f32x4 acc[6][2];
#pragma unroll
        for (int j = 0; j < 6; ++j) {
            const u32x2 bp = *(const u32x2*)(sm + K1_PB1 + (16 * j + 4 * q) * 2);
            acc[j][0] = (f32x4){bflo(bp[0]), bfhi(bp[0]), bflo(bp[1]), bfhi(bp[1])};
            acc[j][1] = acc[j][0];
        }
        s16x8 fx[2][2], fw[2][6];
#define M_READ(b, sb, co, ks)                                                                     \
    do {                                                                                          \
        fx[b][0] = *(const s16x8*)((sb) + xoff + (co)); fx[b][1] = *(const s16x8*)((sb) + xoff + 2048 + (co)); \
        _Pragma("unroll") for (int j_ = 0; j_ < 6; ++j_) {                                        \
            const char* pa_ = (sb) + (j_ < 5 ? tra + j_ * 32 : tr5) + (ks) * 32 * 192;            \
            const s16x4 lo_ = __builtin_amdgcn_ds_read_tr16_b64_v4i16((LAS s16x4*)pa_);           \
            const s16x4 hi_ = __builtin_amdgcn_ds_read_tr16_b64_v4i16((LAS s16x4*)(pa_ + 4 * 192)); \
            fw[b][j_] = __builtin_shufflevector(lo_, hi_, 0, 1, 2, 3, 4, 5, 6, 7);                \
        }                                                                                         \
    } while (0)
#define M_BODY(it, bo, bn, MODE)                                                                  \
    do {                                                                                          \
        M_READ(1, sm + (bo), co1, 1);                                                             \
        _Pragma("unroll") for (int j = 0; j < 6; ++j) { acc[j][0] = mfma16(fw[0][j], fx[0][0], acc[j][0]); acc[j][1] = mfma16(fw[0][j], fx[0][1], acc[j][1]); } \
        SCHED_FENCE();                                                                            \
        if ((MODE) == 0) { if ((it) < 2) WAIT_VM(7); else WAIT_VM(5); }                           \
        else if ((MODE) < 3) WAIT_VM(5);                                                          \
        WAIT_LGKM0();                                                                             \
        __builtin_amdgcn_s_barrier();                                                             \
        asm volatile("" ::: "memory");                                                            \
        SCHED_FENCE();                                                                            \
        if ((MODE) == 1) pt_check(12);                                                            \
        if ((MODE) < 3) M_READ(0, sm + (bn), co0, 0);                                             \
        _Pragma("unroll") for (int j = 0; j < 6; ++j) {                                           \
            acc[j][0] = mfma16(fw[1][j], fx[1][0], acc[j][0]); acc[j][1] = mfma16(fw[1][j], fx[1][1], acc[j][1]); \
            if (j < 5) {                                                                          \
                SCHED_FENCE();                                                                    \
                if ((MODE) == 0) {                                                                \
                    if (j == 4 && (it) == 5 && wave == 7) glds16_c(ws + WS_FPT + (size_t)(((440 * CB) / 14) & ~1) * 8 + (lane < 20 ? lane : 0) * 16, smb + K1_FM); \
                    else K1_SLOT(j, bo);                                                          \
                }                                                                                 \
                else asm volatile("global_load_dwordx4 %0, %1, off sc1" : "=&v"(Bb[(MODE) > 0 ? (MODE)-1 : 0][j < 5 ? j : 0]) : "v"(pb + ((MODE)-1) * 5120 + (j - 1) * 1024) : "memory"); \
                SCHED_FENCE();                                                                    \
            }                                                                                     \
        }                                                                                         \
    } while (0)
        M_READ(0, sm, co0, 0);
        pretile_out();
        int bo = 0, bn = K1_STAGE;
#pragma unroll 1
        for (int it = 0; it < 7; ++it) {
            M_BODY(it, bo, bn, 0);
            bo = bn; bn = bn == 2 * K1_STAGE ? 0 : bn + K1_STAGE;
        }
        M_BODY(7, K1_STAGE, 2 * K1_STAGE, 1); M_BODY(8, 2 * K1_STAGE, 0, 2); M_BODY(9, 0, 0, 3);
#pragma unroll
        for (int v = 0; v < 3; ++v) {
#pragma unroll
            for (int rt = 0; rt < 2; ++rt) {
                u32x4 o;
                if (2 * v + rt < 5) { P2_CACHE(3 * (2 * v + rt)); P2_CACHE(3 * (2 * v + rt) + 1); P2_CACHE(3 * (2 * v + rt) + 2); }
#pragma unroll
                for (int jj = 0; jj < 2; ++jj) {
                    float r[4];
#pragma unroll
                    for (int i = 0; i < 4; ++i) {
                        const float h = acc[2 * v + jj][rt][i];
                        const float e = __builtin_amdgcn_exp2f(h * LOG2E);
                        const float d = fmaf(e, e + 2.0f, 2.0f);
                        r[i] = fmaf(-2.0f, h * __builtin_amdgcn_rcpf(d), h);
                    }
                    o[2 * jj] = pack2(r[0], r[1]);
                    o[2 * jj + 1] = pack2(r[2], r[3]);
                }
                st128_wt(a2 + (size_t)(40 + 3 * cb + v) * 8192 + rt * 1024, o);
            }
        }
        P2_BEGIN(1);
        P2_STEP(0, 12, 8, 6); P2_STEP(1, 12, 8, 6); P2_STEP(2, 12, 8, 6); P2_STEP(3, 12, 8, 6); P2_STEP(4, 12, 8, 6); P2_STEP(5, 12, 13, 6);
        P2_STEP(6, 12, 9, 6); P2_STEP(7, 12, 9, 6); P2_STEP(8, 12, 9, 6); P2_STEP(9, 12, 9, 6); P2_STEP(10, 12, 5, 6); P2_STEP(11, 12, 0, 6);
        flag_wait(ws + WS_FP1 + (size_t)(RG * 32 + (lane & 1)) * 8, lane < 2, tok);
        P2_GATES();
    }
    asm volatile("s_waitcnt vmcnt(0)" : "+v"(gq[0]), "+v"(gq[1]) :: "memory");
    wg_sync();
#pragma unroll
    for (int r = 0; r < 4; ++r)
#pragma unroll
        for (int n = 0; n < 5; ++n) *(f32x4*)(sm + ((r * 5 + n) * 8 + wave) * 1024 + lane * 16) = ac2[r][n];
    wg_sync();
#pragma unroll
    for (int m = 0; m < 3; ++m) {
        const int tile = wave + 8 * m;
        if (tile < 20) {
            const char* rp = sm + tile * 8192 + lane * 16;
            const f32x4 vs = (*(const f32x4*)rp + *(const f32x4*)(rp + 1024)) + (*(const f32x4*)(rp + 2048) + *(const f32x4*)(rp + 3072));
            const f32x4 vm = (*(const f32x4*)(rp + 4096) + *(const f32x4*)(rp + 5120)) + (*(const f32x4*)(rp + 6144) + *(const f32x4*)(rp + 7168));
            const int r = tile / 5;
            const float g = u2f(r == 0 ? gq[0][0] : r == 1 ? gq[0][1] : r == 2 ? gq[1][0] : gq[1][1]);
            f32x4 v = vs * (2.0f * g) + vm * (1.0f - g);
            v[0] += g * bflo(xv[m][0]) * bflo(dv[m][0]);
            v[1] += g * bfhi(xv[m][0]) * bfhi(dv[m][0]);
            v[2] += g * bflo(xv[m][1]) * bflo(dv[m][1]);
            v[3] += g * bfhi(xv[m][1]) * bfhi(dv[m][1]);
            st128_wt(out + oo[m], __builtin_bit_cast(u32x4, v));
        }
    }
}

extern "C" void kernel_launch(void* const* d_in, const int* in_sizes, int n_in, void* d_out, int out_size, void* d_ws,
                              size_t ws_size, hipStream_t stream) {
    kf<<<256, 512, 0, stream>>>((const u16*)d_in[0], (const u16*)d_in[1], (const u16*)d_in[2], (const u16*)d_in[3], (const u16*)d_in[4],
                                (const u16*)d_in[5], (const u16*)d_in[6], (const u16*)d_in[7], (const u16*)d_in[8], (const u16*)d_in[10],
                                (const u16*)d_in[11], (const u16*)d_in[12], (const u16*)d_in[13], (char*)d_ws, (float*)d_out);
}
```

```cpp
#include <hip/hip_runtime.h>
#include <stdint.h>

typedef short s16x4 __attribute__((ext_vector_type(4)));
typedef short s16x8 __attribute__((ext_vector_type(8)));
typedef __bf16 bf16x2 __attribute__((ext_vector_type(2)));
typedef float f32x2 __attribute__((ext_vector_type(2)));
typedef float f32x4 __attribute__((ext_vector_type(4)));
typedef unsigned u32x2 __attribute__((ext_vector_type(2)));
typedef unsigned u32x4 __attribute__((ext_vector_type(4)));
typedef unsigned short u16;
#define LAS __attribute__((address_space(3)))
#define GAS __attribute__((address_space(1)))
#define DI __device__ __forceinline__

constexpr int SEQ = 2048, DM = 640, NP = 640, NF = 1536;
constexpr int NKS = 88;

constexpr size_t WS_A2 = 0;
constexpr size_t WS_W2 = WS_A2 + (size_t)16 * NKS * 8192;
constexpr size_t WS_G = WS_W2 + (size_t)8 * NKS * 5120;
constexpr size_t WS_AGG = WS_G + 2048 * 4;
constexpr size_t WS_EP = WS_AGG + 16 * 16 * 640;
constexpr size_t WS_FPT = WS_EP + 256;
constexpr size_t WS_FAG = WS_FPT + 2048;
constexpr size_t WS_FP1 = WS_FAG + 2048;
constexpr size_t WS_JUNK = WS_FP1 + 16384;

DI unsigned f2u(float v) { return __builtin_bit_cast(unsigned, v); }
DI float u2f(unsigned v) { return __builtin_bit_cast(float, v); }
DI bool wave_all(bool p) { return __builtin_amdgcn_ballot_w64(!p) == 0ull; }
DI void wg_sync() {
    __builtin_amdgcn_fence(__ATOMIC_RELEASE, "workgroup");
    __builtin_amdgcn_s_barrier();
    __builtin_amdgcn_fence(__ATOMIC_ACQUIRE, "workgroup");
}
DI float sigmoidf_(float v) { return 1.0f / (1.0f + __builtin_amdgcn_exp2f(v * -1.44269504088896341f)); }
DI float bflo(unsigned v) { return __uint_as_float(v << 16); }
DI float bfhi(unsigned v) { return __uint_as_float(v & 0xffff0000u); }
DI unsigned pack2(float a, float b) {
    f32x2 p = {a, b};
    return __builtin_bit_cast(unsigned, __builtin_convertvector(p, bf16x2));
}
DI f32x4 mfma16(s16x8 a, s16x8 b, f32x4 c) { return __builtin_amdgcn_mfma_f32_16x16x32_bf16(a, b, c, 0, 0, 0); }
template <int S> DI float shr_zero(float v) {
    return __builtin_bit_cast(float, __builtin_amdgcn_update_dpp(0, __builtin_bit_cast(int, v), 0x110 + S, 0xf, 0xf, true));
}
DI f32x2 cmul(f32x2 a, f32x2 b) { return (f32x2){a.x * b.x - a.y * b.y, a.x * b.y + a.y * b.x}; }
DI f32x2 cfma(f32x2 a, f32x2 b, f32x2 c) {
    return (f32x2){fmaf(-a.y, b.y, fmaf(a.x, b.x, c.x)), fmaf(a.y, b.x, fmaf(a.x, b.y, c.y))};
}
DI f32x2 cmul_pk(f32x2 a, f32x2 b) { return (f32x2){fmaf(-a.y, b.y, a.x * b.x), fmaf(a.y, b.x, a.x * b.y)}; }
DI f32x2 cmul_sc(f32x2 a, f32x2 b) {
    float p = a.x * b.x, r = a.x * b.y;
    asm("" : "+v"(p));
    asm("" : "+v"(r));
    return (f32x2){fmaf(-a.y, b.y, p), fmaf(a.y, b.x, r)};
}
DI float dot2bf(unsigned a, unsigned b, float c) {
    return __builtin_amdgcn_fdot2_f32_bf16(__builtin_bit_cast(bf16x2, a), __builtin_bit_cast(bf16x2, b), c, false);
}
#define WAIT_VM(n) asm volatile("s_waitcnt vmcnt(%0)" ::"n"(n) : "memory")
#define WAIT_LGKM0() asm volatile("s_waitcnt lgkmcnt(0)" ::: "memory")
#define ACQUIRE() asm volatile("buffer_inv sc1" ::: "memory")
#define SCHED_FENCE() __builtin_amdgcn_sched_barrier(0)

DI void glds16(const void* gsrc, unsigned lds_dst) {
    unsigned keep;
    asm volatile("s_mov_b32 %0, m0\n\ts_mov_b32 m0, %2\n\ts_nop 0\n\tglobal_load_lds_dwordx4 %1, off\n\ts_mov_b32 m0, %0"
                 : "=&s"(keep) : "v"(gsrc), "s"(lds_dst) : "memory");
}
DI void glds16x4(const void* gsrc, unsigned lds_dst) {
    unsigned keep;
    asm volatile("s_mov_b32 %0, m0\n\ts_mov_b32 m0, %2\n\ts_nop 0\n\tglobal_load_lds_dwordx4 %1, off\n\t"
                 "global_load_lds_dwordx4 %1, off offset:1024\n\tglobal_load_lds_dwordx4 %1, off offset:2048\n\t"
                 "global_load_lds_dwordx4 %1, off offset:3072\n\ts_mov_b32 m0, %0"
                 : "=&s"(keep) : "v"(gsrc), "s"(lds_dst) : "memory");
}
DI void st128_wt(void* p, u32x4 v) { asm volatile("global_store_dwordx4 %0, %1, off sc1\n\ts_nop 1" :: "v"(p), "v"(v) : "memory"); }
DI void st64_wt(void* p, u32x2 v) { asm volatile("global_store_dwordx2 %0, %1, off sc1\n\ts_nop 1" :: "v"(p), "v"(v) : "memory"); }
DI unsigned ld32_asm(const void* p) { unsigned r; asm volatile("global_load_dword %0, %1, off" : "=v"(r) : "v"(p) : "memory"); return r; }
DI unsigned ld16_asm(const void* p) { unsigned r; asm volatile("global_load_ushort %0, %1, off" : "=v"(r) : "v"(p) : "memory"); return r; }
DI u32x2 ld64_asm(const void* p) { u32x2 r; asm volatile("global_load_dwordx2 %0, %1, off" : "=v"(r) : "v"(p) : "memory"); return r; }
DI u32x4 ld128_asm(const void* p) { u32x4 r; asm volatile("global_load_dwordx4 %0, %1, off" : "=v"(r) : "v"(p) : "memory"); return r; }
constexpr float LOG2E = 1.44269504088896341f, INV2PI = 0.15915494309189535f;
DI f32x2 lam_bar(float lr, float li, float ld) {
    const float dt = __builtin_amdgcn_exp2f(ld * LOG2E);
    const float m = __builtin_amdgcn_exp2f(dt * lr * LOG2E);
    const float rv = __builtin_amdgcn_fractf(dt * li * INV2PI);
    return (f32x2){m * __builtin_amdgcn_cosf(rv), m * __builtin_amdgcn_sinf(rv)};
}

typedef unsigned long long u64;
DI void glds16_c(const void* gsrc, unsigned lds_dst) {
    unsigned keep;
    lds_dst = __builtin_amdgcn_readfirstlane(lds_dst);
    asm volatile("s_mov_b32 %0, m0\n\ts_mov_b32 m0, %2\n\ts_nop 0\n\tglobal_load_lds_dwordx4 %1, off sc1\n\ts_mov_b32 m0, %0"
                 : "=&s"(keep) : "v"(gsrc), "s"(lds_dst) : "memory");
}
DI void glds16x4_c(const void* gsrc, unsigned lds_dst) {
    unsigned keep;
    asm volatile("s_mov_b32 %0, m0\n\ts_mov_b32 m0, %2\n\ts_nop 0\n\tglobal_load_lds_dwordx4 %1, off sc1\n\t"
                 "global_load_lds_dwordx4 %1, off offset:1024 sc1\n\tglobal_load_lds_dwordx4 %1, off offset:2048 sc1\n\t"
                 "global_load_lds_dwordx4 %1, off offset:3072 sc1\n\ts_mov_b32 m0, %0"
                 : "=&s"(keep) : "v"(gsrc), "s"(lds_dst) : "memory");
}
DI void flag_set(char* p, u64 tok) { __hip_atomic_store((GAS u64*)p, tok, __ATOMIC_RELAXED, __HIP_MEMORY_SCOPE_AGENT); }
DI void flag_wait(const char* p, bool need, u64 tok) {
    for (unsigned spins = 0; spins < (1u << 20); ++spins) {
        u64 v = tok;
        if (need) v = __hip_atomic_load((const GAS u64*)p, __ATOMIC_RELAXED, __HIP_MEMORY_SCOPE_AGENT);
        if (__all(v == tok)) break;
        __builtin_amdgcn_s_sleep(8);
    }
    asm volatile("" ::: "memory");
}

constexpr int K1_STAGE = 38 * 1024;
constexpr int K1_GW = 3 * K1_STAGE;
constexpr int K1_PLR = K1_GW + 1280, K1_PLI = K1_GW + 1360, K1_PLD = K1_GW + 1440, K1_PB1 = K1_GW + 1536;
constexpr int K1_TAB = K1_GW + 2048;
constexpr int K1_XCH = K1_TAB + 2560;
constexpr int K1_CAR = K1_XCH + 1280;
constexpr int K1_CNT = K1_CAR + 1280;
constexpr int K1_PRT = K1_CNT + 16;
constexpr int K1_PT = 123 * 1024;
constexpr int K1_FM = 140 * 1024;
constexpr int KF_LDS = 160 * 1024;

__global__ __launch_bounds__(512, 2) __attribute__((target("no-packed-fp32-ops"))) void kf(const u16* __restrict__ x, const u16* __restrict__ Lre, const u16* __restrict__ Lim,
                                             const u16* __restrict__ Bre, const u16* __restrict__ Bim, const u16* __restrict__ Cre,
                                             const u16* __restrict__ Cim, const u16* __restrict__ logdt, const u16* __restrict__ Dskip,
                                             const u16* __restrict__ w1, const u16* __restrict__ b1, const u16* __restrict__ w2,
                                             const u16* __restrict__ gw, char* __restrict__ ws, float* __restrict__ out) {
    __shared__ __attribute__((aligned(16))) char sm[KF_LDS];
    const int tid = threadIdx.x, lane = tid & 63;
    const int wave = __builtin_amdgcn_readfirstlane(tid >> 6);
    const int bid = blockIdx.x, xcd = bid & 7, idx = bid >> 3;
    const int rb = 8 * (xcd & 1) + (idx & 7);
    const int cb = 4 * (xcd >> 1) + (idx >> 3);
    const int RG = 8 * (xcd >> 1) + (idx & 7);
    const int CB = 4 * (xcd & 1) + (idx >> 3);
    const int t = lane & 15, q = lane >> 4;
    const unsigned smb = (unsigned)(size_t)(LAS char*)sm;
    if (wave == 7) {
        u64 pc;
        asm volatile("s_getpc_b64 %0" : "=s"(pc));
        const char* cp = (const char*)(pc & ~255ull) + lane * 128;
#pragma unroll
        for (int k = 0; k < 4; ++k) glds16(cp + k * 8192, smb + K1_FM);
    }
    const char* src[5];
    unsigned ldo[5];
    int incE, strE;
    {
        const int l8 = lane >> 3;
        auto xsrc = [&](int s) __attribute__((always_inline)) {
            const int p = 8 * s + l8;
            const int tm = 128 * rb + 32 * (p >> 5) + 2 * (p & 15) + ((p >> 4) & 1);
            return (const char*)(x + tm * DM + (((lane & 7) ^ ((p >> 1) & 7)) << 3));
        };
        auto wsrc = [&](int s) __attribute__((always_inline)) {
            const int p = 8 * s + l8;
            const int ch = 40 * cb + 8 * (p >> 4) + ((p & 15) >> 1);
            return (const char*)(((p & 1) ? Bim : Bre) + ch * DM + (((lane & 7) ^ ((p >> 1) & 7)) << 3));
        };
        auto msrc = [&](int s) __attribute__((always_inline)) {
            const int gI = 64 * s + lane, row = gI / 12, cp = gI - 12 * row;
            int j = (cp >> 1) - ((row >> 3) & 1);
            j += j < 0 ? 6 : 0;
            return (const char*)(w1 + row * NF + 96 * cb + 16 * j + 8 * (cp & 1));
        };
        src[0] = xsrc(2 * wave); ldo[0] = smb + 2048 * wave;
        src[1] = xsrc(2 * wave + 1); ldo[1] = smb + 2048 * wave + 1024;
        src[2] = wsrc(wave); ldo[2] = smb + 16384 + 1024 * wave;
        src[3] = msrc(wave); ldo[3] = smb + 26624 + 1024 * wave;
        if (wave < 2) { src[4] = wsrc(8 + wave); ldo[4] = smb + 16384 + 1024 * (8 + wave); incE = 128; strE = 1; }
        else if (wave < 6) { src[4] = msrc(6 + wave); ldo[4] = smb + 26624 + 1024 * (6 + wave); incE = 64 * NF * 2; strE = 1; }
        else {
            const u16* p = gw + lane * 8;
            if (wave == 7) {
                p = gw + 512 + t * 8;
                if (lane >= 16 && lane < 21) p = Lre + 40 * cb + (lane - 16) * 8;
                if (lane >= 21 && lane < 26) p = Lim + 40 * cb + (lane - 21) * 8;
                if (lane >= 26 && lane < 31) p = logdt + 40 * cb + (lane - 26) * 8;
                if (lane >= 32 && lane < 44) p = b1 + 96 * cb + (lane - 32) * 8;
            }
            src[4] = (const char*)p; ldo[4] = smb + K1_GW + (wave - 6) * 1024; incE = 0; strE = 0;
        }
    }
#define K1_SLOT(i, boff)                                                                 \
    do {                                                                                 \
        glds16(src[i], ldo[i] + ((i) == 4 ? strE * (boff) : (boff)));                    \
        src[i] += (i) < 3 ? 128 : (i) == 3 ? 64 * NF * 2 : incE;                         \
    } while (0)
    K1_SLOT(0, 0); K1_SLOT(1, 0); K1_SLOT(2, 0); K1_SLOT(3, 0); K1_SLOT(4, 0);
    u32x2 epv;
    asm volatile("global_load_dwordx2 %0, %1, off sc1" : "=v"(epv) : "v"(ws + WS_EP) : "memory");

    const int pu = 4 * bid + wave;
    char* ptl = sm + K1_PT + (wave & 3) * 4096;
    if (wave < 4) {
        const int u = pu < 880 ? pu : 0;
#pragma unroll
        for (int i = 0; i < 4; ++i) {
            const u16* p0;
            if (u < 400) { const int kg = u / 40, nt = u - 40 * kg; p0 = ((i >> 1) ? Cim : Cre) + (16 * nt + 8 * (i & 1) + (lane >> 3)) * NP + 64 * kg + (lane & 7) * 8; }
            else { const int v = u - 400, tg = v / 48, ks = v - 48 * tg; p0 = w2 + (32 * ks + 8 * i + (lane >> 3)) * DM + 64 * tg + (lane & 7) * 8; }
            glds16(p0, smb + K1_PT + wave * 4096 + i * 1024);
        }
    }
#pragma unroll
    for (int st = 1; st < 3; ++st) {
        K1_SLOT(0, st * K1_STAGE); K1_SLOT(1, st * K1_STAGE); K1_SLOT(2, st * K1_STAGE); K1_SLOT(3, st * K1_STAGE); K1_SLOT(4, st * K1_STAGE);
    }
    if (tid == 0) *(u32x4*)(sm + K1_CNT) = (u32x4){0u, 0u, 0u, 0u};

    const int wc = wave & 3;
    const int xoff = (32 * wc + t) * 128;
    const int co0 = (q ^ ((t >> 1) & 7)) * 16, co1 = co0 ^ 64;
    char* a2 = ws + WS_A2 + (size_t)rb * NKS * 8192 + (2 * wc) * 1024 + lane * 16;

    asm volatile("s_waitcnt vmcnt(10)" : "+v"(epv) :: "memory");
    WAIT_LGKM0();
    __builtin_amdgcn_s_barrier();
    asm volatile("" ::: "memory");
    const u64 tok = (((u64)(unsigned)__builtin_amdgcn_readfirstlane(epv[1]) << 32) | (unsigned)__builtin_amdgcn_readfirstlane(epv[0])) + 1ull;
    auto pretile_out = [&]() __attribute__((always_inline)) {
#pragma unroll
        for (int i = 0; i < 4; ++i) {
            u32x4 wf;
            int KS, g;
            if (pu < 400) {
                const int kg = pu / 40;
                g = pu - 40 * kg; KS = 4 * kg + i;
                const int c0 = (KS % 5 == 2) ? 8 * q : 4 * q, c1 = (KS % 5 == 2) ? 8 * q + 4 : 16 + 4 * q;
                const char* pp = ptl + t * 128 + i * 32;
                const unsigned r0 = *(const unsigned*)(pp + c0), r1 = *(const unsigned*)(pp + c1);
                const unsigned i0 = *(const unsigned*)(pp + 2048 + c0), i1 = *(const unsigned*)(pp + 2048 + c1);
                wf[0] = (r0 & 0xffffu) | (i0 << 16); wf[1] = (r0 >> 16) | (i0 & 0xffff0000u);
                wf[2] = (r1 & 0xffffu) | (i1 << 16); wf[3] = (r1 >> 16) | (i1 & 0xffff0000u);
            } else {
                const int v = pu - 400, tg = v / 48;
                KS = 40 + v - 48 * tg; g = 4 * tg + i;
                const char* pp = ptl + (4 * q + (t >> 2)) * 128 + i * 32 + (lane & 3) * 8;
                const s16x4 lo = __builtin_amdgcn_ds_read_tr16_b64_v4i16((LAS s16x4*)pp);
                const s16x4 hi = __builtin_amdgcn_ds_read_tr16_b64_v4i16((LAS s16x4*)(pp + 2048));
                wf = __builtin_bit_cast(u32x4, __builtin_shufflevector(lo, hi, 0, 1, 2, 3, 4, 5, 6, 7));
            }
            const int cB = g / 5;
            st128_wt((pu < 880 ? ws + WS_W2 + (size_t)((cB * NKS + KS) * 5 + g - 5 * cB) * 1024 : ws + WS_JUNK + (size_t)(pu * 4 + i) * 1024) + lane * 16, wf);
        }
    };

    const int rb2 = RG >> 1, hh = RG & 1;
    const int ks0 = wave < 4 ? 10 * wave : 40 + 12 * (wave - 4);
    const char* pa = ws + WS_A2 + ((size_t)rb2 * NKS + ks0) * 8192 + hh * 4096 + lane * 16;
    const char* pb = ws + WS_W2 + ((size_t)CB * NKS + ks0) * 5120 + 1024 + lane * 16;
    const int bco = wave == 6 ? 6 * 20480 + 4096 : wave * 20480;
    u32x4 Ab[4][4], Bb[3][5];
    f32x4 ac2[4][5];
#define P2_L1(dst, P, OFF) asm volatile("global_load_dwordx4 %0, %1, off offset:" #OFF " sc1" : "=&v"(dst) : "v"(P) : "memory")
#define P2_LDB(slot, P)                                                                           \
    do { P2_L1(Bb[slot][0], P, -1024); P2_L1(Bb[slot][1], P, 0); P2_L1(Bb[slot][2], P, 1024); P2_L1(Bb[slot][3], P, 2048); P2_L1(Bb[slot][4], P, 3072); } while (0)
#define P2_LDA(slot, P)                                                                           \
    do { P2_L1(Ab[slot][0], P, 0); P2_L1(Ab[slot][1], P, 1024); P2_L1(Ab[slot][2], P, 2048); P2_L1(Ab[slot][3], P, 3072); } while (0)
    auto pt_check = [&]() __attribute__((always_inline)) {
        const u32x4 v0 = *(const u32x4*)(sm + K1_FM + lane * 16), v1 = *(const u32x4*)(sm + K1_FM + 1024 + lane * 16);
        const unsigned lo = (unsigned)tok, hi = (unsigned)(tok >> 32);
        if (!wave_all(v0[0] == lo && v0[1] == hi && v0[2] == lo && v0[3] == hi && v1[0] == lo && v1[1] == hi && v1[2] == lo && v1[3] == hi)) {
#pragma unroll 1
            for (int k = 0; k < 4; ++k) flag_wait(ws + WS_FPT + (size_t)(64 * k + lane) * 8, true, tok);
        }
    };
    const char* g3 = pb - 1024 + 3 * 5120;
#define P2_CACHE(i) glds16_c(g3 + (i) * 1024, smb + bco + (i) * 1024)
#define P2_M2(ua, ub, i)                                                                          \
    do {                                                                                          \
        ac2[(i) / 5][(i) % 5] = mfma16(__builtin_bit_cast(s16x8, Bb[ub][(i) % 5]), __builtin_bit_cast(s16x8, Ab[ua][(i) / 5]), ac2[(i) / 5][(i) % 5]); \
        ac2[((i) + 1) / 5][((i) + 1) % 5] = mfma16(__builtin_bit_cast(s16x8, Bb[ub][((i) + 1) % 5]), __builtin_bit_cast(s16x8, Ab[ua][((i) + 1) / 5]), ac2[((i) + 1) / 5][((i) + 1) % 5]); \
        SCHED_FENCE();                                                                            \
    } while (0)
#define P2_STEP(j, NS, WN, NL)                                                                    \
    do {                                                                                          \
        asm volatile("s_waitcnt vmcnt(%9)" : "+v"(Ab[(j) & 3][0]), "+v"(Ab[(j) & 3][1]), "+v"(Ab[(j) & 3][2]), "+v"(Ab[(j) & 3][3]), \
                     "+v"(Bb[(j) % 3][0]), "+v"(Bb[(j) % 3][1]), "+v"(Bb[(j) % 3][2]), "+v"(Bb[(j) % 3][3]), "+v"(Bb[(j) % 3][4]) : "n"(WN) : "memory"); \
        SCHED_FENCE();                                                                            \
        const char* pa_ = pa + ((j) + 3) * 8192;                                                  \
        const char* pb_ = pb + ((j) + 2) * 5120;                                                  \
        constexpr bool LA_ = (j) + 3 < (NS), LBG_ = (j) + 2 >= (NL) && (j) + 2 < (NS), LBL_ = (j) + 2 >= 3 && (j) + 2 < (NL); \
        P2_M2((j) & 3, (j) % 3, 0); if (LA_) { P2_L1(Ab[((j) + 3) & 3][0], pa_, 0); SCHED_FENCE(); }      \
        P2_M2((j) & 3, (j) % 3, 2); if (LA_) { P2_L1(Ab[((j) + 3) & 3][1], pa_, 1024); SCHED_FENCE(); }   \
        P2_M2((j) & 3, (j) % 3, 4); if (LA_) { P2_L1(Ab[((j) + 3) & 3][2], pa_, 2048); SCHED_FENCE(); }   \
        P2_M2((j) & 3, (j) % 3, 6); if (LA_) { P2_L1(Ab[((j) + 3) & 3][3], pa_, 3072); SCHED_FENCE(); }   \
        if (LBL_) { _Pragma("unroll") for (int n_ = 0; n_ < 5; ++n_) Bb[((j) + 2) % 3][n_] = *(const u32x4*)(sm + bco + (((j) + 2 - 3) * 5 + n_) * 1024 + lane * 16); SCHED_FENCE(); } \
        P2_M2((j) & 3, (j) % 3, 8); if (LBG_) { P2_L1(Bb[((j) + 2) % 3][0], pb_, -1024); SCHED_FENCE(); } \
        P2_M2((j) & 3, (j) % 3, 10); if (LBG_) { P2_L1(Bb[((j) + 2) % 3][1], pb_, 0); SCHED_FENCE(); }    \
        P2_M2((j) & 3, (j) % 3, 12); if (LBG_) { P2_L1(Bb[((j) + 2) % 3][2], pb_, 1024); SCHED_FENCE(); } \
        P2_M2((j) & 3, (j) % 3, 14); if (LBG_) { P2_L1(Bb[((j) + 2) % 3][3], pb_, 2048); SCHED_FENCE(); } \
        P2_M2((j) & 3, (j) % 3, 16); if (LBG_) { P2_L1(Bb[((j) + 2) % 3][4], pb_, 3072); SCHED_FENCE(); } \
        P2_M2((j) & 3, (j) % 3, 18);                                                              \
    } while (0)
    u32x2 xv[3], dv[3], gq[2];
    int oo[3];
#define P2_BEGIN(role)                                                                            \
    do {                                                                                          \
        WAIT_VM(0);                                                                               \
        if (lane == 0) flag_set(ws + WS_FP1 + (size_t)(((((role) * 16 + rb) * 2 + (wc >> 1)) * 16 + cb) * 2 + (wc & 1)) * 8, tok); \
        if ((role) == 0) { _Pragma("unroll") for (int i = 0; i < 5; ++i) glds16x4_c(g3 + i * 4096, __builtin_amdgcn_readfirstlane(smb + bco + i * 4096)); } \
        _Pragma("unroll") for (int m = 0; m < 3; ++m) {                                           \
            int tile = wave + 8 * m; tile = tile < 20 ? tile : wave;                              \
            const int r = tile / 5, n = tile - 5 * r;                                             \
            const int col = 80 * CB + 16 * n + 4 * q;                                             \
            oo[m] = (64 * RG + 32 * (r >> 1) + 2 * t + (r & 1)) * DM + col;                       \
            xv[m] = ld64_asm(x + oo[m]); dv[m] = ld64_asm(Dskip + col);                           \
        }                                                                                         \
        const int fi_ = ((role) == 0 && lane >= 8) ? RG * 32 + (lane & 1)      \
                        : ((role) * 32 + RG) * 32 + 8 * wc + (lane & 7);                          \
        flag_wait(ws + WS_FP1 + (size_t)fi_ * 8, lane < ((role) ? 8 : 10), tok);                  \
        asm volatile("s_waitcnt vmcnt(0)" : "+v"(xv[0]), "+v"(xv[1]), "+v"(xv[2]), "+v"(dv[0]), "+v"(dv[1]), "+v"(dv[2]) :: "memory"); \
        if ((role) == 0) P2_GATES();                                                              \
        P2_LDA(0, pa); P2_LDA(1, pa + 8192); P2_LDA(2, pa + 16384);                               \
        _Pragma("unroll") for (int r = 0; r < 4; ++r) _Pragma("unroll") for (int n = 0; n < 5; ++n) ac2[r][n] = (f32x4){0.f, 0.f, 0.f, 0.f}; \
    } while (0)
#define P2_GATES()                                                                                \
    asm volatile("global_load_dwordx2 %0, %2, off sc1\n\tglobal_load_dwordx2 %1, %2, off offset:128 sc1" \
                 : "=&v"(gq[0]), "=&v"(gq[1]) : "v"(ws + WS_G + (64 * RG + 2 * t) * 4) : "memory")

    if (wave < 4) {
        const bool do_gate = (cb == 0);
        f32x4 acc[6][2];
#pragma unroll
        for (int a = 0; a < 6; ++a) { acc[a][0] = (f32x4){0.f, 0.f, 0.f, 0.f}; acc[a][1] = (f32x4){0.f, 0.f, 0.f, 0.f}; }
        s16x8 fx[2][2], fw[2][5];
        const int woff = 16384 + t * 128;
#define S_READ(b, sb, co)                                                                         \
    do {                                                                                          \
        fx[b][0] = *(const s16x8*)((sb) + xoff + (co)); fx[b][1] = *(const s16x8*)((sb) + xoff + 2048 + (co)); \
        _Pragma("unroll") for (int T_ = 0; T_ < 5; ++T_) fw[b][T_] = *(const s16x8*)((sb) + woff + T_ * 2048 + (co)); \
    } while (0)
#define S_GATE(b, kk)                                                                             \
    do {                                                                                          \
        if (do_gate) {                                                                            \
            const s16x8 gv = *(const s16x8*)(sm + K1_GW + ((kk) + 8 * q) * 2);                    \
            acc[5][0] = mfma16(gv, fx[b][0], acc[5][0]); acc[5][1] = mfma16(gv, fx[b][1], acc[5][1]); \
        }                                                                                         \
    } while (0)
#define S_BODY(it, bo, bn, MODE)                                                                  \
    do {                                                                                          \
        S_READ(1, sm + (bo), co1);                                                                \
        _Pragma("unroll") for (int T = 0; T < 5; ++T) { acc[T][0] = mfma16(fw[0][T], fx[0][0], acc[T][0]); acc[T][1] = mfma16(fw[0][T], fx[0][1], acc[T][1]); } \
        S_GATE(0, 64 * (it));                                                                     \
        SCHED_FENCE();                                                                            \
        if ((MODE) == 0) { if ((it) < 2) WAIT_VM(9); else if ((it) == 3 && wave == 0) WAIT_VM(6); else WAIT_VM(5); } \
        else if ((MODE) < 3) WAIT_VM(5);                                                          \
        WAIT_LGKM0();                                                                             \
        __builtin_amdgcn_s_barrier();                                                             \
        asm volatile("" ::: "memory");                                                            \
        SCHED_FENCE();                                                                            \
        if ((MODE) == 0 && (it) == 2 && tid == 0) flag_set(ws + WS_FPT + (size_t)bid * 8, tok);    \
        if ((MODE) == 1) pt_check();                                                              \
        if ((MODE) < 3) S_READ(0, sm + (bn), co0);                                                \
        _Pragma("unroll") for (int T = 0; T < 5; ++T) {                                           \
            acc[T][0] = mfma16(fw[1][T], fx[1][0], acc[T][0]); acc[T][1] = mfma16(fw[1][T], fx[1][1], acc[T][1]); \
            SCHED_FENCE();                                                                        \
            if ((MODE) == 0) K1_SLOT(T, bo);                                                      \
            else asm volatile("global_load_dwordx4 %0, %1, off sc1" : "=&v"(Bb[(MODE) > 0 ? (MODE)-1 : 0][T]) : "v"(pb + ((MODE)-1) * 5120 + (T - 1) * 1024) : "memory"); \
            SCHED_FENCE();                                                                        \
        }                                                                                         \
        S_GATE(1, 64 * (it) + 32);                                                                \
    } while (0)
        S_READ(0, sm, co0);
        pretile_out();
        int bo = 0, bn = K1_STAGE;
#pragma unroll 1
        for (int it = 0; it < 7; ++it) {
            S_BODY(it, bo, bn, 0);
            if (it == 4 && lane < 40) {
                const float lr = bflo(*(const u16*)(sm + K1_PLR + lane * 2)), li = bflo(*(const u16*)(sm + K1_PLI + lane * 2));
                const float dt = __builtin_amdgcn_exp2f(bflo(*(const u16*)(sm + K1_PLD + lane * 2)) * LOG2E);
                const float za = dt * lr * LOG2E, zb = dt * li * INV2PI;
                f32x2* tb = (f32x2*)(sm + K1_TAB + lane * 64);
                const float n0 = wc == 0 ? 1.f : wc == 1 ? 2.f : wc == 2 ? 8.f : 32.f;
                const float m0 = __builtin_amdgcn_exp2f(n0 * za), r0 = __builtin_amdgcn_fractf(n0 * zb);
                const f32x2 p0 = {m0 * __builtin_amdgcn_cosf(r0), m0 * __builtin_amdgcn_sinf(r0)};
                if (wc == 0) {
                    const float nr = p0.x - 1.0f, ni = p0.y;
                    const float rden = 1.0f / (lr * lr + li * li);
                    tb[0] = p0;
                    tb[6] = (f32x2){(nr * lr + ni * li) * rden, (ni * lr - nr * li) * rden};
                    tb[7] = (f32x2){za, zb};
                } else if (wc == 3) {
                    tb[5] = p0;
                } else {
                    tb[2 * wc - 1] = p0;
                    tb[2 * wc] = cmul(p0, p0);
                }
            }
            bo = bn; bn = bn == 2 * K1_STAGE ? 0 : bn + K1_STAGE;
        }
        S_BODY(7, K1_STAGE, 2 * K1_STAGE, 1); S_BODY(8, 2 * K1_STAGE, 0, 2); S_BODY(9, 0, 0, 3);
        __builtin_amdgcn_s_setprio(3);
        if (do_gate && q == 0) {
            f32x2 gg = {sigmoidf_(acc[5][0][0]), sigmoidf_(acc[5][1][0])};
            st64_wt(ws + WS_G + (128 * rb + 32 * wc + 2 * t) * 4, __builtin_bit_cast(u32x2, gg));
        }
        f32x2 h0[5][2], h1[5][2];
#pragma unroll
        for (int T = 0; T < 5; ++T) {
#pragma unroll
            for (int cc = 0; cc < 2; ++cc) {
                const int chl = 8 * T + 2 * q + cc;
                const f32x4* tb = (const f32x4*)(sm + K1_TAB + chl * 64);
                const f32x4 t0 = tb[0], t1 = tb[1], t2 = tb[2], t3 = tb[3];
                const f32x2 L1 = {t0[0], t0[1]}, L2 = {t0[2], t0[3]}, L4 = {t1[0], t1[1]}, L8 = {t1[2], t1[3]}, L16 = {t2[0], t2[1]};
                const f32x2 cf = {t3[0], t3[1]};
                const f32x2 u0 = cmul_sc(cf, (f32x2){acc[T][0][2 * cc], acc[T][0][2 * cc + 1]});
                const f32x2 u1 = cmul_sc(cf, (f32x2){acc[T][1][2 * cc], acc[T][1][2 * cc + 1]});
                f32x2 a = cfma(L1, u0, u1);
                a = cfma(L2, (f32x2){shr_zero<1>(a.x), shr_zero<1>(a.y)}, a);
                a = cfma(L4, (f32x2){shr_zero<2>(a.x), shr_zero<2>(a.y)}, a);
                a = cfma(L8, (f32x2){shr_zero<4>(a.x), shr_zero<4>(a.y)}, a);
                a = cfma(L16, (f32x2){shr_zero<8>(a.x), shr_zero<8>(a.y)}, a);
                h0[T][cc] = cfma(L1, (f32x2){shr_zero<1>(a.x), shr_zero<1>(a.y)}, u0);
                h1[T][cc] = a;
                if (t == 15) *(f32x2*)(sm + K1_XCH + (wc * 40 + chl) * 8) = a;
            }
        }
        WAIT_LGKM0();
        unsigned old = 0;
        if (lane == 0) old = __hip_atomic_fetch_add((LAS unsigned*)(sm + K1_CNT), 1u, __ATOMIC_RELAXED, __HIP_MEMORY_SCOPE_WORKGROUP);
        old = __builtin_amdgcn_readfirstlane(old);
        asm volatile("" ::: "memory");
        {
            const int cl = lane < 40 ? lane : 0;
            const f32x2 L32 = *(const f32x2*)(sm + K1_TAB + cl * 64 + 40), zz = *(const f32x2*)(sm + K1_TAB + cl * 64 + 56);
            const f32x2* xc = (const f32x2*)(sm + K1_XCH);
            if (old == 3) {
                f32x2 s = {0.f, 0.f};
#pragma unroll
                for (int w = 0; w < 4; ++w) s = cfma(L32, s, xc[w * 40 + cl]);
                if (lane < 40) st128_wt(ws + WS_AGG + (size_t)(cb * 16 + rb) * 640 + lane * 16,
                                        (u32x4){f2u(s.x), f2u(s.y), (unsigned)tok, (unsigned)(tok >> 32)});
            }
            if (bid == 255 && old == 0) {
#pragma unroll 1
                for (int k = 0; k < 4; ++k) flag_wait(ws + WS_FPT + (size_t)(64 * k + lane) * 8, true, tok);
                if (lane == 0) flag_set(ws + WS_EP, tok);
            }
            f32x2 cy = {0.f, 0.f};
            if (4 * wc < rb) {
                const f32x2 L64 = cmul(L32, L32), L128 = cmul(L64, L64);
                const char* agp = ws + WS_AGG + (size_t)(cb * 16 + 4 * wc) * 640 + cl * 16;
                for (unsigned spins = 0; spins < (1u << 20); ++spins) {
                    u32x4 ag[4];
#pragma unroll
                    for (int j = 0; j < 4; ++j)
                        asm volatile("global_load_dwordx4 %0, %1, off sc1" : "=v"(ag[j]) : "v"(agp + (4 * wc + j < rb ? j : 0) * 640) : "memory");
                    asm volatile("s_waitcnt vmcnt(0)" : "+v"(ag[0]), "+v"(ag[1]), "+v"(ag[2]), "+v"(ag[3]) :: "memory");
                    bool ok = true;
                    cy = (f32x2){0.f, 0.f};
#pragma unroll
                    for (int j = 0; j < 4; ++j) {
                        ok = ok && ag[j][2] == (unsigned)tok && ag[j][3] == (unsigned)(tok >> 32);
                        const f32x2 nx = cfma(L128, cy, (f32x2){u2f(ag[j][0]), u2f(ag[j][1])});
                        cy = (4 * wc + j < rb) ? nx : cy;
                    }
                    if (wave_all(ok)) break;
                    __builtin_amdgcn_s_sleep(4);
                }
                for (int j = 4 * wc + 4; j < rb; ++j) cy = cmul(cy, L128);
            }
            (void)zz;
            if (lane < 40) *(f32x2*)(sm + K1_PRT + (wc * 40 + lane) * 8) = cy;
            WAIT_LGKM0();
            if (lane == 0) __hip_atomic_fetch_add((LAS unsigned*)(sm + K1_CNT + 4), 1u, __ATOMIC_RELAXED, __HIP_MEMORY_SCOPE_WORKGROUP);
            for (unsigned spins = 0; spins < (1u << 22); ++spins) {
                if (__hip_atomic_load((LAS unsigned*)(sm + K1_CNT + 4), __ATOMIC_RELAXED, __HIP_MEMORY_SCOPE_WORKGROUP) >= 4u) break;
                __builtin_amdgcn_s_sleep(1);
            }
            asm volatile("" ::: "memory");
            const f32x2* pp = (const f32x2*)(sm + K1_PRT);
            f32x2 c = {(pp[cl].x + pp[40 + cl].x) + (pp[80 + cl].x + pp[120 + cl].x), (pp[cl].y + pp[40 + cl].y) + (pp[80 + cl].y + pp[120 + cl].y)};
            for (int w = 0; w < wc; ++w) c = cfma(L32, c, xc[w * 40 + cl]);
            if (lane < 40) *(f32x2*)(sm + K1_CAR + (wc * 40 + lane) * 8) = c;
        }
        asm volatile("" ::: "memory");
        const float fn = (float)(2 * t + 1);
        u32x2 o0[5], o1[5];
#pragma unroll
        for (int T = 0; T < 5; ++T) {
#pragma unroll
            for (int cc = 0; cc < 2; ++cc) {
                const int chl = 8 * T + 2 * q + cc;
                const f32x2 L1 = *(const f32x2*)(sm + K1_TAB + chl * 64), zz = *(const f32x2*)(sm + K1_TAB + chl * 64 + 56);
                const f32x2 cy = *(const f32x2*)(sm + K1_CAR + (wc * 40 + chl) * 8);
                const float mg = __builtin_amdgcn_exp2f(fn * zz.x);
                const float rv = __builtin_amdgcn_fractf(fn * zz.y);
                const f32x2 c0 = cmul_sc((f32x2){mg * __builtin_amdgcn_cosf(rv), mg * __builtin_amdgcn_sinf(rv)}, cy);
                const f32x2 v1 = cfma(L1, c0, h1[T][cc]);
                o0[T][cc] = pack2(h0[T][cc].x + c0.x, -(h0[T][cc].y + c0.y));
                o1[T][cc] = pack2(v1.x, -v1.y);
            }
        }
        {
            char* dst = a2 + (size_t)((5 * cb) >> 1) * 8192;
            char* dsh = dst - lane * 16 + ((q >> 1) * 16 + t) * 16 + (q & 1) * 8;
            if (cb & 1) {
                st64_wt(dsh + 512, o0[0]); st64_wt(dsh + 1536, o1[0]);
                st128_wt(dst + 8192, (u32x4){o0[1][0], o0[1][1], o0[2][0], o0[2][1]}); st128_wt(dst + 9216, (u32x4){o1[1][0], o1[1][1], o1[2][0], o1[2][1]});
                st128_wt(dst + 16384, (u32x4){o0[3][0], o0[3][1], o0[4][0], o0[4][1]}); st128_wt(dst + 17408, (u32x4){o1[3][0], o1[3][1], o1[4][0], o1[4][1]});
            } else {
                st128_wt(dst, (u32x4){o0[0][0], o0[0][1], o0[1][0], o0[1][1]}); st128_wt(dst + 1024, (u32x4){o1[0][0], o1[0][1], o1[1][0], o1[1][1]});
                st128_wt(dst + 8192, (u32x4){o0[2][0], o0[2][1], o0[3][0], o0[3][1]}); st128_wt(dst + 9216, (u32x4){o1[2][0], o1[2][1], o1[3][0], o1[3][1]});
                st64_wt(dsh + 16384, o0[4]); st64_wt(dsh + 17408, o1[4]);
            }
        }
        WAIT_LGKM0();
        if (lane == 0) __hip_atomic_fetch_add((LAS unsigned*)(sm + K1_CNT + 8), 1u, __ATOMIC_RELAXED, __HIP_MEMORY_SCOPE_WORKGROUP);
        __builtin_amdgcn_s_setprio(0);
        P2_BEGIN(0);
        P2_STEP(0, 10, 8, 7); P2_STEP(1, 10, 8, 7); P2_STEP(2, 10, 8, 7); P2_STEP(3, 10, 8, 7); P2_STEP(4, 10, 8, 7);
        P2_STEP(5, 10, 8, 7); P2_STEP(6, 10, 13, 7); P2_STEP(7, 10, 9, 7); P2_STEP(8, 10, 5, 7); P2_STEP(9, 10, 0, 7);
    } else {
        const int bb = q & 1;
        const int trb = 26624 + (8 * q + (t >> 2)) * 192 + (lane & 3) * 8;
        const int tra = trb + bb * 32;
        const int tr5 = trb + (bb ? 0 : 160);
        f32x4 acc[6][2];
#pragma unroll
        for (int j = 0; j < 6; ++j) {
            const u32x2 bp = *(const u32x2*)(sm + K1_PB1 + (16 * j + 4 * q) * 2);
            acc[j][0] = (f32x4){bflo(bp[0]), bfhi(bp[0]), bflo(bp[1]), bfhi(bp[1])};
            acc[j][1] = acc[j][0];
        }
        s16x8 fx[2][2], fw[2][6];
#define M_READ(b, sb, co, ks)                                                                     \
    do {                                                                                          \
        fx[b][0] = *(const s16x8*)((sb) + xoff + (co)); fx[b][1] = *(const s16x8*)((sb) + xoff + 2048 + (co)); \
        _Pragma("unroll") for (int j_ = 0; j_ < 6; ++j_) {                                        \
            const char* pa_ = (sb) + (j_ < 5 ? tra + j_ * 32 : tr5) + (ks) * 32 * 192;            \
            const s16x4 lo_ = __builtin_amdgcn_ds_read_tr16_b64_v4i16((LAS s16x4*)pa_);           \
            const s16x4 hi_ = __builtin_amdgcn_ds_read_tr16_b64_v4i16((LAS s16x4*)(pa_ + 4 * 192)); \
            fw[b][j_] = __builtin_shufflevector(lo_, hi_, 0, 1, 2, 3, 4, 5, 6, 7);                \
        }                                                                                         \
    } while (0)
#define M_BODY(it, bo, bn, MODE)                                                                  \
    do {                                                                                          \
        M_READ(1, sm + (bo), co1, 1);                                                             \
        _Pragma("unroll") for (int j = 0; j < 6; ++j) { acc[j][0] = mfma16(fw[0][j], fx[0][0], acc[j][0]); acc[j][1] = mfma16(fw[0][j], fx[0][1], acc[j][1]); } \
        SCHED_FENCE();                                                                            \
        if ((MODE) < 3) WAIT_VM(5);                                                               \
        WAIT_LGKM0();                                                                             \
        __builtin_amdgcn_s_barrier();                                                             \
        asm volatile("" ::: "memory");                                                            \
        SCHED_FENCE();                                                                            \
        if ((MODE) == 1) pt_check();                                                              \
        if ((MODE) < 3) M_READ(0, sm + (bn), co0, 0);                                             \
        _Pragma("unroll") for (int j = 0; j < 6; ++j) {                                           \
            acc[j][0] = mfma16(fw[1][j], fx[1][0], acc[j][0]); acc[j][1] = mfma16(fw[1][j], fx[1][1], acc[j][1]); \
            if (j < 5) {                                                                          \
                SCHED_FENCE();                                                                    \
                if ((MODE) == 0) {                                                                \
                    if (j == 4 && (it) == 5 && wave >= 6) glds16_c(ws + WS_FPT + (wave - 6) * 1024 + lane * 16, smb + K1_FM + (wave - 6) * 1024); \
                    else K1_SLOT(j, bo);                                                          \
                }                                                                                 \
                else asm volatile("global_load_dwordx4 %0, %1, off sc1" : "=&v"(Bb[(MODE) > 0 ? (MODE)-1 : 0][j < 5 ? j : 0]) : "v"(pb + ((MODE)-1) * 5120 + (j - 1) * 1024) : "memory"); \
                SCHED_FENCE();                                                                    \
            }                                                                                     \
        }                                                                                         \
    } while (0)
        M_READ(0, sm, co0, 0);
        int bo = 0, bn = K1_STAGE;
#pragma unroll 1
        for (int it = 0; it < 7; ++it) {
            M_BODY(it, bo, bn, 0);
            bo = bn; bn = bn == 2 * K1_STAGE ? 0 : bn + K1_STAGE;
        }
        M_BODY(7, K1_STAGE, 2 * K1_STAGE, 1); M_BODY(8, 2 * K1_STAGE, 0, 2); M_BODY(9, 0, 0, 3);
#pragma unroll
        for (int v = 0; v < 3; ++v) {
#pragma unroll
            for (int rt = 0; rt < 2; ++rt) {
                u32x4 o;
                if (2 * v + rt < 5) { P2_CACHE(3 * (2 * v + rt)); P2_CACHE(3 * (2 * v + rt) + 1); P2_CACHE(3 * (2 * v + rt) + 2); }
#pragma unroll
                for (int jj = 0; jj < 2; ++jj) {
                    float r[4];
#pragma unroll
                    for (int i = 0; i < 4; ++i) {
                        const float h = acc[2 * v + jj][rt][i];
                        const float e = __builtin_amdgcn_exp2f(h * LOG2E);
                        const float d = fmaf(e, e + 2.0f, 2.0f);
                        r[i] = fmaf(-2.0f, h * __builtin_amdgcn_rcpf(d), h);
                    }
                    o[2 * jj] = pack2(r[0], r[1]);
                    o[2 * jj + 1] = pack2(r[2], r[3]);
                }
                st128_wt(a2 + (size_t)(40 + 3 * cb + v) * 8192 + rt * 1024, o);
            }
        }
        P2_BEGIN(1);
        P2_STEP(0, 12, 8, 6); P2_STEP(1, 12, 8, 6); P2_STEP(2, 12, 8, 6); P2_STEP(3, 12, 8, 6); P2_STEP(4, 12, 8, 6); P2_STEP(5, 12, 13, 6);
        P2_STEP(6, 12, 9, 6); P2_STEP(7, 12, 9, 6); P2_STEP(8, 12, 9, 6); P2_STEP(9, 12, 9, 6); P2_STEP(10, 12, 5, 6); P2_STEP(11, 12, 0, 6);
        flag_wait(ws + WS_FP1 + (size_t)(RG * 32 + (lane & 1)) * 8, lane < 2, tok);
        P2_GATES();
        asm volatile("s_waitcnt vmcnt(0)" : "+v"(gq[0]), "+v"(gq[1]) :: "memory");
    }
    asm volatile("s_waitcnt vmcnt(0)" : "+v"(gq[0]), "+v"(gq[1]) :: "memory");
    if (wave == 5 || wave == 6) {
        for (unsigned spins = 0; spins < (1u << 22); ++spins) {
            if (__hip_atomic_load((LAS unsigned*)(sm + K1_CNT + 8), __ATOMIC_RELAXED, __HIP_MEMORY_SCOPE_WORKGROUP) >= 4u) break;
            __builtin_amdgcn_s_sleep(1);
        }
        if (lane == 0) __hip_atomic_fetch_add((LAS unsigned*)(sm + K1_CNT + 12), 1u, __ATOMIC_RELAXED, __HIP_MEMORY_SCOPE_WORKGROUP);
        if (wave == 6) {
            for (unsigned spins = 0; spins < (1u << 22); ++spins) {
                if (__hip_atomic_load((LAS unsigned*)(sm + K1_CNT + 12), __ATOMIC_RELAXED, __HIP_MEMORY_SCOPE_WORKGROUP) >= 2u) break;
                __builtin_amdgcn_s_sleep(1);
            }
        }
        asm volatile("" ::: "memory");
    }
#pragma unroll
    for (int r = 0; r < 4; ++r)
#pragma unroll
        for (int n = 0; n < 5; ++n) *(f32x4*)(sm + wave * 20480 + (r * 5 + n) * 1024 + lane * 16) = ac2[r][n];
    wg_sync();
#pragma unroll
    for (int m = 0; m < 3; ++m) {
        const int tile = wave + 8 * m;
        if (tile < 20) {
            const char* rp = sm + tile * 1024 + lane * 16;
            const f32x4 vs = (*(const f32x4*)rp + *(const f32x4*)(rp + 20480)) + (*(const f32x4*)(rp + 40960) + *(const f32x4*)(rp + 61440));
            const f32x4 vm = (*(const f32x4*)(rp + 81920) + *(const f32x4*)(rp + 102400)) + (*(const f32x4*)(rp + 122880) + *(const f32x4*)(rp + 143360));
            const int r = tile / 5;
            const float g = u2f(r == 0 ? gq[0][0] : r == 1 ? gq[0][1] : r == 2 ? gq[1][0] : gq[1][1]);
            f32x4 v = vs * (2.0f * g) + vm * (1.0f - g);
            v[0] += g * bflo(xv[m][0]) * bflo(dv[m][0]);
            v[1] += g * bfhi(xv[m][0]) * bfhi(dv[m][0]);
            v[2] += g * bflo(xv[m][1]) * bflo(dv[m][1]);
            v[3] += g * bfhi(xv[m][1]) * bfhi(dv[m][1]);
            st128_wt(out + oo[m], __builtin_bit_cast(u32x4, v));
        }
    }
}

extern "C" void kernel_launch(void* const* d_in, const int* in_sizes, int n_in, void* d_out, int out_size, void* d_ws,
                              size_t ws_size, hipStream_t stream) {
    kf<<<256, 512, 0, stream>>>((const u16*)d_in[0], (const u16*)d_in[1], (const u16*)d_in[2], (const u16*)d_in[3], (const u16*)d_in[4],
                                (const u16*)d_in[5], (const u16*)d_in[6], (const u16*)d_in[7], (const u16*)d_in[8], (const u16*)d_in[10],
                                (const u16*)d_in[11], (const u16*)d_in[12], (const u16*)d_in[13], (char*)d_ws, (float*)d_out);
}
```

```cpp
#include <hip/hip_runtime.h>
#include <stdint.h>

typedef short s16x4 __attribute__((ext_vector_type(4)));
typedef short s16x8 __attribute__((ext_vector_type(8)));
typedef __bf16 bf16x2 __attribute__((ext_vector_type(2)));
typedef float f32x2 __attribute__((ext_vector_type(2)));
typedef float f32x4 __attribute__((ext_vector_type(4)));
typedef unsigned u32x2 __attribute__((ext_vector_type(2)));
typedef unsigned u32x4 __attribute__((ext_vector_type(4)));
typedef unsigned short u16;
#define LAS __attribute__((address_space(3)))
#define GAS __attribute__((address_space(1)))
#define DI __device__ __forceinline__

constexpr int SEQ = 2048, DM = 640, NP = 640, NF = 1536;
constexpr int NKS = 88;

constexpr size_t WS_A2 = 0;
constexpr size_t WS_W2 = WS_A2 + (size_t)16 * NKS * 8192;
constexpr size_t WS_G = WS_W2 + (size_t)8 * NKS * 5120;
constexpr size_t WS_AGG = WS_G + 10 * 2048 * 4;
constexpr size_t WS_EP = WS_AGG + 16 * 16 * 640;
constexpr size_t WS_FPT = WS_EP + 256;
constexpr size_t WS_FAG = WS_FPT + 2048;
constexpr size_t WS_FP1 = WS_FAG + 2048;
constexpr size_t WS_JUNK = WS_FP1 + 16384;

DI unsigned f2u(float v) { return __builtin_bit_cast(unsigned, v); }
DI float u2f(unsigned v) { return __builtin_bit_cast(float, v); }
DI bool wave_all(bool p) { return __builtin_amdgcn_ballot_w64(!p) == 0ull; }
DI void wg_sync() {
    __builtin_amdgcn_fence(__ATOMIC_RELEASE, "workgroup");
    __builtin_amdgcn_s_barrier();
    __builtin_amdgcn_fence(__ATOMIC_ACQUIRE, "workgroup");
}
DI float sigmoidf_(float v) { return 1.0f / (1.0f + __builtin_amdgcn_exp2f(v * -1.44269504088896341f)); }
DI float bflo(unsigned v) { return __uint_as_float(v << 16); }
DI float bfhi(unsigned v) { return __uint_as_float(v & 0xffff0000u); }
DI unsigned pack2(float a, float b) {
    f32x2 p = {a, b};
    return __builtin_bit_cast(unsigned, __builtin_convertvector(p, bf16x2));
}
DI f32x4 mfma16(s16x8 a, s16x8 b, f32x4 c) { return __builtin_amdgcn_mfma_f32_16x16x32_bf16(a, b, c, 0, 0, 0); }
template <int S> DI float shr_zero(float v) {
    return __builtin_bit_cast(float, __builtin_amdgcn_update_dpp(0, __builtin_bit_cast(int, v), 0x110 + S, 0xf, 0xf, true));
}
DI float swp(float v) {
    return __builtin_bit_cast(float, __builtin_amdgcn_update_dpp(0, __builtin_bit_cast(int, v), 0xB1, 0xf, 0xf, true));
}
DI f32x2 cmul(f32x2 a, f32x2 b) { return (f32x2){a.x * b.x - a.y * b.y, a.x * b.y + a.y * b.x}; }
DI f32x2 cfma(f32x2 a, f32x2 b, f32x2 c) {
    return (f32x2){fmaf(-a.y, b.y, fmaf(a.x, b.x, c.x)), fmaf(a.y, b.x, fmaf(a.x, b.y, c.y))};
}
DI f32x2 cmul_pk(f32x2 a, f32x2 b) { return (f32x2){fmaf(-a.y, b.y, a.x * b.x), fmaf(a.y, b.x, a.x * b.y)}; }
DI f32x2 cmul_sc(f32x2 a, f32x2 b) {
    float p = a.x * b.x, r = a.x * b.y;
    asm("" : "+v"(p));
    asm("" : "+v"(r));
    return (f32x2){fmaf(-a.y, b.y, p), fmaf(a.y, b.x, r)};
}
DI float dot2bf(unsigned a, unsigned b, float c) {
    return __builtin_amdgcn_fdot2_f32_bf16(__builtin_bit_cast(bf16x2, a), __builtin_bit_cast(bf16x2, b), c, false);
}
#define WAIT_VM(n) asm volatile("s_waitcnt vmcnt(%0)" ::"n"(n) : "memory")
#define WAIT_LGKM0() asm volatile("s_waitcnt lgkmcnt(0)" ::: "memory")
#define ACQUIRE() asm volatile("buffer_inv sc1" ::: "memory")
#define SCHED_FENCE() __builtin_amdgcn_sched_barrier(0)

DI void glds16(const void* gsrc, unsigned lds_dst) {
    unsigned keep;
    asm volatile("s_mov_b32 %0, m0\n\ts_mov_b32 m0, %2\n\ts_nop 0\n\tglobal_load_lds_dwordx4 %1, off\n\ts_mov_b32 m0, %0"
                 : "=&s"(keep) : "v"(gsrc), "s"(lds_dst) : "memory");
}
DI void glds16x4(const void* gsrc, unsigned lds_dst) {
    unsigned keep;
    asm volatile("s_mov_b32 %0, m0\n\ts_mov_b32 m0, %2\n\ts_nop 0\n\tglobal_load_lds_dwordx4 %1, off\n\t"
                 "global_load_lds_dwordx4 %1, off offset:1024\n\tglobal_load_lds_dwordx4 %1, off offset:2048\n\t"
                 "global_load_lds_dwordx4 %1, off offset:3072\n\ts_mov_b32 m0, %0"
                 : "=&s"(keep) : "v"(gsrc), "s"(lds_dst) : "memory");
}
DI void st128_wt(void* p, u32x4 v) { asm volatile("global_store_dwordx4 %0, %1, off sc1\n\ts_nop 1" :: "v"(p), "v"(v) : "memory"); }
DI void st64_wt(void* p, u32x2 v) { asm volatile("global_store_dwordx2 %0, %1, off sc1\n\ts_nop 1" :: "v"(p), "v"(v) : "memory"); }
DI unsigned ld32_asm(const void* p) { unsigned r; asm volatile("global_load_dword %0, %1, off" : "=v"(r) : "v"(p) : "memory"); return r; }
DI unsigned ld16_asm(const void* p) { unsigned r; asm volatile("global_load_ushort %0, %1, off" : "=v"(r) : "v"(p) : "memory"); return r; }
DI u32x2 ld64_asm(const void* p) { u32x2 r; asm volatile("global_load_dwordx2 %0, %1, off" : "=v"(r) : "v"(p) : "memory"); return r; }
DI u32x4 ld128_asm(const void* p) { u32x4 r; asm volatile("global_load_dwordx4 %0, %1, off" : "=v"(r) : "v"(p) : "memory"); return r; }
constexpr float LOG2E = 1.44269504088896341f, INV2PI = 0.15915494309189535f;
DI f32x2 lam_bar(float lr, float li, float ld) {
    const float dt = __builtin_amdgcn_exp2f(ld * LOG2E);
    const float m = __builtin_amdgcn_exp2f(dt * lr * LOG2E);
    const float rv = __builtin_amdgcn_fractf(dt * li * INV2PI);
    return (f32x2){m * __builtin_amdgcn_cosf(rv), m * __builtin_amdgcn_sinf(rv)};
}

typedef unsigned long long u64;
DI void glds16_c(const void* gsrc, unsigned lds_dst) {
    unsigned keep;
    lds_dst = __builtin_amdgcn_readfirstlane(lds_dst);
    asm volatile("s_mov_b32 %0, m0\n\ts_mov_b32 m0, %2\n\ts_nop 0\n\tglobal_load_lds_dwordx4 %1, off sc1\n\ts_mov_b32 m0, %0"
                 : "=&s"(keep) : "v"(gsrc), "s"(lds_dst) : "memory");
}
DI void glds16x4_c(const void* gsrc, unsigned lds_dst) {
    unsigned keep;
    asm volatile("s_mov_b32 %0, m0\n\ts_mov_b32 m0, %2\n\ts_nop 0\n\tglobal_load_lds_dwordx4 %1, off sc1\n\t"
                 "global_load_lds_dwordx4 %1, off offset:1024 sc1\n\tglobal_load_lds_dwordx4 %1, off offset:2048 sc1\n\t"
                 "global_load_lds_dwordx4 %1, off offset:3072 sc1\n\ts_mov_b32 m0, %0"
                 : "=&s"(keep) : "v"(gsrc), "s"(lds_dst) : "memory");
}
DI void flag_set(char* p, u64 tok) { __hip_atomic_store((GAS u64*)p, tok, __ATOMIC_RELAXED, __HIP_MEMORY_SCOPE_AGENT); }
DI void flag_wait(const char* p, bool need, u64 tok) {
    for (unsigned spins = 0; spins < (1u << 20); ++spins) {
        u64 v = tok;
        if (need) v = __hip_atomic_load((const GAS u64*)p, __ATOMIC_RELAXED, __HIP_MEMORY_SCOPE_AGENT);
        if (__all(v == tok)) break;
        __builtin_amdgcn_s_sleep(8);
    }
    asm volatile("" ::: "memory");
}

template <int V> struct IC { static constexpr int value = V; };
constexpr int K1_STAGE = 38 * 1024;
constexpr int K1_GW = 0;
constexpr int K1_PLR = K1_GW + 1280, K1_PLI = K1_GW + 1360, K1_PLD = K1_GW + 1440;
constexpr int K1_RING = 2048;
#define BUF(it) (K1_RING + ((it) % 3) * K1_STAGE)
constexpr int K1_TAB = K1_RING + 3 * K1_STAGE;
constexpr int K1_XCH = K1_TAB + 5120;
constexpr int K1_CNT = K1_XCH + 1280;
constexpr int K1_PRT = K1_CNT + 16;
constexpr int K1_PT = 123 * 1024;
constexpr int K1_FM = 140 * 1024;
constexpr int KF_LDS = 160 * 1024;

__global__ __launch_bounds__(512, 2) __attribute__((target("no-packed-fp32-ops"))) void kf(const u16* __restrict__ x, const u16* __restrict__ Lre, const u16* __restrict__ Lim,
                                             const u16* __restrict__ Bre, const u16* __restrict__ Bim, const u16* __restrict__ Cre,
                                             const u16* __restrict__ Cim, const u16* __restrict__ logdt, const u16* __restrict__ Dskip,
                                             const u16* __restrict__ w1, const u16* __restrict__ b1, const u16* __restrict__ w2,
                                             const u16* __restrict__ gw, char* __restrict__ ws, float* __restrict__ out) {
    __shared__ __attribute__((aligned(16))) char sm[KF_LDS];
    const int tid = threadIdx.x, lane = tid & 63;
    const int wave = __builtin_amdgcn_readfirstlane(tid >> 6);
    const int bid = blockIdx.x, xcd = bid & 7, idx = bid >> 3;
    const int rb = 8 * (xcd & 1) + (idx & 7);
    const int cb = 4 * (xcd >> 1) + (idx >> 3);
    const int RG = 8 * (xcd >> 1) + (idx & 7);
    const int CB = 4 * (xcd & 1) + (idx >> 3);
    const int t = lane & 15, q = lane >> 4;
    const unsigned smb = (unsigned)(size_t)(LAS char*)sm;
    u64 pc0;
    asm volatile("s_getpc_b64 %0" : "=s"(pc0));
    asm volatile("" :: "s"(x), "s"(Lre), "s"(Lim), "s"(Bre), "s"(Bim), "s"(Cre), "s"(Cim), "s"(logdt), "s"(Dskip), "s"(w1), "s"(b1), "s"(w2),
                 "s"(gw), "s"(ws), "s"(out));
    f32x4 ac2[4][5];
    u32x2 xv[3], dv[3];
    unsigned gl[4];
    int oo[3];
    auto run = [&](auto ROLE_) __attribute__((always_inline)) {
    constexpr int ROLE = decltype(ROLE_)::value;
    int lane = tid & 63;
    asm volatile("" : "+v"(lane));
    const int t = lane & 15, q = lane >> 4;
    u32x2 epv;
    asm volatile("global_load_dwordx2 %0, %1, off sc1" : "=v"(epv) : "v"(ws + WS_EP) : "memory");
#define G_M0(base, c) asm volatile("s_add_i32 m0, %0, %1" :: "s"(base), "n"(c) : "scc")
#define G_LDS(vo, sb, c) asm volatile("global_load_lds_dwordx4 %0, %1 offset:%2" :: "v"(vo), "s"(sb), "n"(c) : "memory")
#define G_LDV(va, c) asm volatile("global_load_lds_dwordx4 %0, off offset:%1" :: "v"(va), "n"(c) : "memory")
#define G_NOP() asm volatile("s_nop 0")
    const int l8 = lane >> 3;
    unsigned vox[4];
    const char* wsr[3];
    const char* w1s = (const char*)w1;
    const unsigned lw = smb + 1024 * (wave & 3);
    const unsigned lm = smb + (ROLE == 0 ? 3072 : 4096) * (wave & 3);
#define X_M0(st, i) G_M0(lm, BUF(st) + 1024 * (i) - 128 * (st))
#define X_LD(st, i) G_LDS(vox[i], x, 128 * (st))
#define W_M0(st, j) G_M0(lw, BUF(st) + 16384 + 4096 * (j) - 128 * (st))
#define W_LD(st, j) G_LDV(wsr[j], 128 * (st))
#define M_M0(st, i) G_M0(lm, BUF(st) + 26624 + 1024 * (i))
#define M_LD(i) G_LDS(vox[i], w1s, 0)
    if constexpr (ROLE == 1) {
#pragma unroll
        for (int i = 0; i < 4; ++i) {
            const int p = 8 * i + l8;
            vox[i] = ((128 * rb + 32 * (wave & 3) + 8 * ((p & 15) >> 2) + 4 * (p >> 4) + (p & 3)) * DM + (((lane & 7) ^ ((p >> 1) & 7)) << 3)) * 2;
            if (i == 0) X_M0(0, 0); else if (i == 1) X_M0(0, 1); else if (i == 2) X_M0(0, 2); else X_M0(0, 3);
            G_NOP();
            G_LDS(vox[i], x, 0);
            SCHED_FENCE();
        }
    } else {
#pragma unroll
        for (int j = 0; j < 3; ++j) {
            const int p = 8 * (4 * j + wave) + l8;
            const int ch = 40 * cb + 8 * (p >> 4) + ((p & 15) >> 1);
            wsr[j] = (const char*)(((p & 1) ? Bim : Bre) + ch * DM + (((lane & 7) ^ ((p >> 1) & 7)) << 3));
            if (j < 2) {
                if (j == 0) W_M0(0, 0); else W_M0(0, 1);
                G_NOP();
                G_LDV(wsr[j], 0);
                SCHED_FENCE();
            }
        }
#pragma unroll
        for (int i = 0; i < 3; ++i) {
            const int gI = 64 * (3 * wave + i) + lane, row = gI / 12, cp = gI - 12 * row;
            int j = (cp >> 1) - ((row >> 3) & 1);
            j += j < 0 ? 6 : 0;
            vox[i] = (row * NF + 96 * cb + 16 * j + 8 * (cp & 1)) * 2;
            if (i == 0) M_M0(0, 0); else if (i == 1) M_M0(0, 1); else M_M0(0, 2);
            G_NOP();
            M_LD(i);
            SCHED_FENCE();
        }
        const u16* p = gw + lane * 8;
        if (wave == 3) {
            p = gw + 512 + t * 8;
            if (lane >= 16 && lane < 21) p = Lre + 40 * cb + (lane - 16) * 8;
            if (lane >= 21 && lane < 26) p = Lim + 40 * cb + (lane - 21) * 8;
            if (lane >= 26 && lane < 31) p = logdt + 40 * cb + (lane - 26) * 8;
        }
        glds16(wave < 2 ? wsr[2] : (const char*)p, wave < 2 ? lw + BUF(0) + 16384 + 8192 : smb + K1_GW + (wave - 2) * 1024);
    }
    const int pu = 4 * bid + wave;
    char* ptl = sm + K1_PT + (wave & 3) * 4096;
    const int wc = wave & 3;
    const int xoff = (32 * wc + t) * 128;
    const int co0 = (q ^ ((t >> 1) & 7)) * 16, co1 = co0 ^ 64;
    char* a2 = ws + WS_A2 + (size_t)rb * NKS * 8192 + (2 * wc) * 1024 + lane * 16;
    const int rb2 = RG >> 1, hh = RG & 1;
    const int ks0 = ROLE == 0 ? 10 * wave : 40 + 12 * (wave - 4);
    const char* pa = ws + WS_A2 + ((size_t)rb2 * NKS + ks0) * 8192 + hh * 4096 + lane * 16;
    const char* pb = ws + WS_W2 + ((size_t)CB * NKS + ks0) * 5120 + 1024 + lane * 16;
    u64 tok;
    u32x4 ptf[4];
    auto pretile_read = [&]() __attribute__((always_inline)) {
        if (pu < 400) {
#pragma unroll
            for (int i = 0; i < 4; ++i) {
                const char* pp = ptl + t * 128 + (((2 * i + (q >> 1)) ^ (t & 7)) << 4) + 8 * (q & 1);
                const u32x2 rr = *(const u32x2*)pp, ii = *(const u32x2*)(pp + 2048);
                const unsigned r0 = rr[0], r1 = rr[1], i0 = ii[0] ^ 0x80008000u, i1 = ii[1] ^ 0x80008000u;
                ptf[i] = (u32x4){(r0 & 0xffffu) | (i0 << 16), (r0 >> 16) | (i0 & 0xffff0000u), (r1 & 0xffffu) | (i1 << 16), (r1 >> 16) | (i1 & 0xffff0000u)};
            }
        } else {
            const int r = 4 * q + (t >> 2);
#pragma unroll
            for (int i = 0; i < 4; ++i) {
                const char* pp = ptl + r * 128 + (((2 * i + ((lane >> 1) & 1)) ^ (r & 7)) << 4) + (lane & 1) * 8;
                const s16x4 lo = __builtin_amdgcn_ds_read_tr16_b64_v4i16((LAS s16x4*)pp);
                const s16x4 hi = __builtin_amdgcn_ds_read_tr16_b64_v4i16((LAS s16x4*)(pp + 2048));
                ptf[i] = __builtin_bit_cast(u32x4, __builtin_shufflevector(lo, hi, 0, 1, 2, 3, 4, 5, 6, 7));
            }
        }
    };
    auto pretile_dst = [&](int i) __attribute__((always_inline)) {
        const int kg = pu / 40, v = pu - 400, tg = v / 48;
        const int g = pu < 400 ? pu - 40 * kg : 4 * tg + i, KS = pu < 400 ? 4 * kg + i : 40 + v - 48 * tg;
        const int cB = g / 5;
        return (unsigned)(pu < 880 ? WS_W2 + (size_t)((cB * NKS + KS) * 5 + g - 5 * cB) * 1024 : WS_JUNK + (size_t)(pu * 4 + i) * 1024);
    };

    const int bco = wave == 6 ? 6 * 20480 + 4096 : wave * 20480;
    u32x4 Ab[4][4], Bb[3][5];
#define P2_L1(dst, P, OFF) asm volatile("global_load_dwordx4 %0, %1, off offset:" #OFF " sc1" : "=&v"(dst) : "v"(P) : "memory")
#define P2_LDB(slot, P)                                                                           \
    do { P2_L1(Bb[slot][0], P, -1024); P2_L1(Bb[slot][1], P, 0); P2_L1(Bb[slot][2], P, 1024); P2_L1(Bb[slot][3], P, 2048); P2_L1(Bb[slot][4], P, 3072); } while (0)
#define P2_LDA(slot, P)                                                                           \
    do { P2_L1(Ab[slot][0], P, 0); P2_L1(Ab[slot][1], P, 1024); P2_L1(Ab[slot][2], P, 2048); P2_L1(Ab[slot][3], P, 3072); } while (0)
    auto pt_check = [&]() __attribute__((always_inline)) {
        const u32x4 v0 = *(const u32x4*)(sm + K1_FM + lane * 16), v1 = *(const u32x4*)(sm + K1_FM + 1024 + lane * 16);
        const unsigned lo = (unsigned)tok, hi = (unsigned)(tok >> 32);
        if (!wave_all(v0[0] == lo && v0[1] == hi && v0[2] == lo && v0[3] == hi && v1[0] == lo && v1[1] == hi && v1[2] == lo && v1[3] == hi)) {
#pragma unroll 1
            for (int k = 0; k < 4; ++k) flag_wait(ws + WS_FPT + (size_t)(64 * k + lane) * 8, true, tok);
        }
    };
    const char* g3 = pb - 1024 + 3 * 5120;
#define P2_CACHE(i) glds16_c(g3 + (i) * 1024, smb + bco + (i) * 1024)
#define P2_M2(ua, ub, i)                                                                          \
    do {                                                                                          \
        ac2[(i) / 5][(i) % 5] = mfma16(__builtin_bit_cast(s16x8, Bb[ub][(i) % 5]), __builtin_bit_cast(s16x8, Ab[ua][(i) / 5]), ac2[(i) / 5][(i) % 5]); \
        ac2[((i) + 1) / 5][((i) + 1) % 5] = mfma16(__builtin_bit_cast(s16x8, Bb[ub][((i) + 1) % 5]), __builtin_bit_cast(s16x8, Ab[ua][((i) + 1) / 5]), ac2[((i) + 1) / 5][((i) + 1) % 5]); \
        SCHED_FENCE();                                                                            \
    } while (0)
#define P2_STEP(j, NS, WN, NL)                                                                    \
    do {                                                                                          \
        asm volatile("s_waitcnt vmcnt(%9)" : "+v"(Ab[(j) & 3][0]), "+v"(Ab[(j) & 3][1]), "+v"(Ab[(j) & 3][2]), "+v"(Ab[(j) & 3][3]), \
                     "+v"(Bb[(j) % 3][0]), "+v"(Bb[(j) % 3][1]), "+v"(Bb[(j) % 3][2]), "+v"(Bb[(j) % 3][3]), "+v"(Bb[(j) % 3][4]) : "n"(WN) : "memory"); \
        SCHED_FENCE();                                                                            \
        const char* pa_ = pa + ((j) + 3) * 8192;                                                  \
        const char* pb_ = pb + ((j) + 2) * 5120;                                                  \
        constexpr bool LA_ = (j) + 3 < (NS), LBG_ = (j) + 2 >= (NL) && (j) + 2 < (NS), LBL_ = (j) + 2 >= 3 && (j) + 2 < (NL); \
        P2_M2((j) & 3, (j) % 3, 0); if (LA_) { P2_L1(Ab[((j) + 3) & 3][0], pa_, 0); SCHED_FENCE(); }      \
        P2_M2((j) & 3, (j) % 3, 2); if (LA_) { P2_L1(Ab[((j) + 3) & 3][1], pa_, 1024); SCHED_FENCE(); }   \
        P2_M2((j) & 3, (j) % 3, 4); if (LA_) { P2_L1(Ab[((j) + 3) & 3][2], pa_, 2048); SCHED_FENCE(); }   \
        P2_M2((j) & 3, (j) % 3, 6); if (LA_) { P2_L1(Ab[((j) + 3) & 3][3], pa_, 3072); SCHED_FENCE(); }   \
        if (LBL_) { _Pragma("unroll") for (int n_ = 0; n_ < 5; ++n_) Bb[((j) + 2) % 3][n_] = *(const u32x4*)(sm + bco + (((j) + 2 - 3) * 5 + n_) * 1024 + lane * 16); SCHED_FENCE(); } \
        P2_M2((j) & 3, (j) % 3, 8); if (LBG_) { P2_L1(Bb[((j) + 2) % 3][0], pb_, -1024); SCHED_FENCE(); } \
        P2_M2((j) & 3, (j) % 3, 10); if (LBG_) { P2_L1(Bb[((j) + 2) % 3][1], pb_, 0); SCHED_FENCE(); }    \
        P2_M2((j) & 3, (j) % 3, 12); if (LBG_) { P2_L1(Bb[((j) + 2) % 3][2], pb_, 1024); SCHED_FENCE(); } \
        P2_M2((j) & 3, (j) % 3, 14); if (LBG_) { P2_L1(Bb[((j) + 2) % 3][3], pb_, 2048); SCHED_FENCE(); } \
        P2_M2((j) & 3, (j) % 3, 16); if (LBG_) { P2_L1(Bb[((j) + 2) % 3][4], pb_, 3072); SCHED_FENCE(); } \
        P2_M2((j) & 3, (j) % 3, 18);                                                              \
    } while (0)
#define P2_BEGIN(role)                                                                            \
    do {                                                                                          \
        WAIT_VM(0);                                                                               \
        if (lane == 0) flag_set(ws + WS_FP1 + (size_t)(((((role) * 16 + rb) * 2 + (wc >> 1)) * 16 + cb) * 2 + (wc & 1)) * 8, tok); \
        if ((role) == 0) { _Pragma("unroll") for (int i = 0; i < 5; ++i) glds16x4_c(g3 + i * 4096, __builtin_amdgcn_readfirstlane(smb + bco + i * 4096)); } \
        _Pragma("unroll") for (int m = 0; m < 3; ++m) {                                           \
            int tile = wave + 8 * m; tile = tile < 20 ? tile : wave;                              \
            const int r = tile / 5, n = tile - 5 * r;                                             \
            const int col = 80 * CB + 16 * n + 4 * q;                                             \
            oo[m] = (64 * RG + 32 * (r >> 1) + 8 * (t >> 2) + 4 * (r & 1) + (t & 3)) * DM + col;  \
            xv[m] = ld64_asm(x + oo[m]); dv[m] = ld64_asm(Dskip + col);                           \
        }                                                                                         \
        flag_wait(ws + WS_FP1 + (size_t)(((role) * 32 + RG) * 32 + 8 * wc + (lane & 7)) * 8, lane < 8, tok); \
        asm volatile("s_waitcnt vmcnt(0)" : "+v"(xv[0]), "+v"(xv[1]), "+v"(xv[2]), "+v"(dv[0]), "+v"(dv[1]), "+v"(dv[2]) :: "memory"); \
        if ((role) == 0) P2_GATES();                                                              \
        P2_LDA(0, pa); P2_LDA(1, pa + 8192); P2_LDA(2, pa + 16384);                               \
        _Pragma("unroll") for (int r = 0; r < 4; ++r) _Pragma("unroll") for (int n = 0; n < 5; ++n) ac2[r][n] = (f32x4){0.f, 0.f, 0.f, 0.f}; \
    } while (0)
#define P2_GATES()                                                                                \
    _Pragma("unroll") for (int j_ = 0; j_ < 4; ++j_)                                              \
        asm volatile("global_load_dword %0, %1, off sc1" : "=&v"(gl[j_])                          \
                     : "v"(ws + WS_G + ((4 * wc + j_ < 10 ? 4 * wc + j_ : 9) * 2048 + 64 * RG + lane) * 4) : "memory")

    if constexpr (ROLE == 0) {
        f32x4 acc[6][2];
#pragma unroll
        for (int a = 0; a < 6; ++a) { acc[a][0] = (f32x4){0.f, 0.f, 0.f, 0.f}; acc[a][1] = (f32x4){0.f, 0.f, 0.f, 0.f}; }
        s16x8 fx[2][2], fw[2][5];
        const int woff = 16384 + t * 128;
#define S_RX(b, sb, co) do { fx[b][0] = *(const s16x8*)(sm + (sb) + xoff + (co)); fx[b][1] = *(const s16x8*)(sm + (sb) + xoff + 2048 + (co)); } while (0)
#define S_RW(b, sb, co, T) fw[b][T] = *(const s16x8*)(sm + (sb) + woff + (T) * 2048 + (co))
#define S_MM(b, T) do { acc[T][0] = mfma16(fx[b][0], fw[b][T], acc[T][0]); acc[T][1] = mfma16(fx[b][1], fw[b][T], acc[T][1]); SCHED_FENCE(); } while (0)
#define S_LB(slot, T) asm volatile("global_load_dwordx4 %0, %1, off sc1" : "=&v"(Bb[slot][T]) : "v"(pb + (slot) * 5120 + ((T) - 1) * 1024) : "memory")
#define S_PT(it, i) do { if ((it) == 0) asm volatile("global_store_dwordx4 %0, %1, %2 sc1\n\ts_nop 1" :: "v"(pto[i]), "v"(ptf[i]), "s"(ws) : "memory"); } while (0)
#define S_BODY(it, WN, XA0, XA1, XA2, XA3)                                                        \
    do {                                                                                          \
        constexpr bool RF_ = (it) < 7, RN_ = (it) < 9;                                            \
        constexpr int SB_ = (it) < 7 ? 0 : (it) - 7;                                              \
        S_MM(0, 0); S_RX(1, BUF(it), co1); S_RW(1, BUF(it), co1, 0); SCHED_FENCE();               \
        S_MM(0, 1); S_RW(1, BUF(it), co1, 1); S_RW(1, BUF(it), co1, 2); XA0; SCHED_FENCE();       \
        S_MM(0, 2); S_RW(1, BUF(it), co1, 3); S_RW(1, BUF(it), co1, 4); XA1; SCHED_FENCE();       \
        S_MM(0, 3); XA2; SCHED_FENCE();                                                           \
        S_MM(0, 4); XA3; SCHED_FENCE();                                                           \
        if (RN_) WAIT_VM(WN);                                                                     \
        WAIT_LGKM0();                                                                             \
        __builtin_amdgcn_s_barrier();                                                             \
        asm volatile("" ::: "memory");                                                            \
        SCHED_FENCE();                                                                            \
        S_MM(1, 0);                                                                               \
        if (__builtin_expect((it) == cb, 0)) {                                                    \
            const s16x8 g0_ = *(const s16x8*)(sm + K1_GW + (64 * (it) + 8 * q) * 2), g1_ = *(const s16x8*)(sm + K1_GW + (64 * (it) + 32 + 8 * q) * 2); \
            acc[5][0] = mfma16(fx[1][0], g1_, mfma16(fx[0][0], g0_, acc[5][0])); acc[5][1] = mfma16(fx[1][1], g1_, mfma16(fx[0][1], g0_, acc[5][1])); \
        }                                                                                         \
        if ((it) == 0) pretile_read();                                                            \
        if ((it) == 7) pt_check();                                                                \
        if (RF_) {                                                                                \
            if (wave < 2) { W_M0((it) + 3, 2); G_NOP(); W_LD((it) + 3, 2); }                      \
            else if ((it) == 2) { if (lane == 0) flag_set(ws + WS_FPT + (size_t)bid * 8, tok); }    \
            else if ((it) == 6) glds16_c(ws + WS_FPT + (wave - 2) * 1024 + lane * 16, smb + K1_FM + (wave - 2) * 1024); \
            w1s += 64 * NF * 2;                                                                   \
            W_M0((it) + 3, 0);                                                                    \
        }                                                                                         \
        if (RN_) { S_RX(0, BUF((it) + 1), co0); S_RW(0, BUF((it) + 1), co0, 0); }                 \
        SCHED_FENCE();                                                                            \
        if (RF_) { W_LD((it) + 3, 0); W_M0((it) + 3, 1); } else S_LB(SB_, 0);                     \
        SCHED_FENCE();                                                                            \
        S_MM(1, 1); if (RN_) { S_RW(0, BUF((it) + 1), co0, 1); S_RW(0, BUF((it) + 1), co0, 2); }  \
        SCHED_FENCE();                                                                            \
        if (RF_) { W_LD((it) + 3, 1); M_M0((it) + 3, 0); } else S_LB(SB_, 1);                     \
        S_PT(it, 0); SCHED_FENCE();                                                               \
        S_MM(1, 2); if (RN_) { S_RW(0, BUF((it) + 1), co0, 3); S_RW(0, BUF((it) + 1), co0, 4); }  \
        SCHED_FENCE();                                                                            \
        if (RF_) { M_LD(0); M_M0((it) + 3, 1); } else S_LB(SB_, 2);                               \
        S_PT(it, 1); SCHED_FENCE();                                                               \
        S_MM(1, 3);                                                                               \
        if (RF_) { M_LD(1); M_M0((it) + 3, 2); } else S_LB(SB_, 3);                               \
        S_PT(it, 2); SCHED_FENCE();                                                               \
        S_MM(1, 4);                                                                               \
        if (RF_) M_LD(2); else S_LB(SB_, 4);                                                      \
        S_PT(it, 3); SCHED_FENCE();                                                               \
    } while (0)
#define S_STAGE(st)                                                                               \
    do {                                                                                          \
        w1s += 64 * NF * 2;                                                                       \
        if (wave < 2) { W_M0(st, 2); G_NOP(); W_LD(st, 2); }                                      \
        W_M0(st, 0); G_NOP(); W_LD(st, 0); W_M0(st, 1); G_NOP(); W_LD(st, 1);                     \
        M_M0(st, 0); G_NOP(); M_LD(0); M_M0(st, 1); G_NOP(); M_LD(1); M_M0(st, 2); G_NOP(); M_LD(2); \
    } while (0)
        {
            const int u = pu < 880 ? pu : 0;
            const int cx = ((lane & 7) ^ l8) * 8;
#pragma unroll
            for (int i = 0; i < 4; ++i) {
                const u16* p0;
                if (u < 400) { const int kg = u / 40, nt = u - 40 * kg; p0 = ((i >> 1) ? Cim : Cre) + (16 * nt + 8 * (i & 1) + l8) * NP + 64 * kg + cx; }
                else { const int v = u - 400, tg = v / 48, ks = v - 48 * tg; p0 = w2 + (32 * ks + 8 * i + l8) * DM + 64 * tg + cx; }
                glds16(p0, smb + K1_PT + wave * 4096 + i * 1024);
            }
        }
        S_STAGE(1); S_STAGE(2);
        if (wave == 0 && lane == 0) *(u32x4*)(sm + K1_CNT) = (u32x4){0u, 0u, 0u, 0u};
        unsigned pto[4];
#pragma unroll
        for (int i = 0; i < 4; ++i) pto[i] = pretile_dst(i) + lane * 16;
        asm volatile("" : "+v"(a2), "+v"(pa), "+v"(pb), "+v"(pto[0]), "+v"(pto[1]), "+v"(pto[2]), "+v"(pto[3]));
        if (wave < 2) WAIT_VM(16); else WAIT_VM(14);
        asm volatile("" : "+v"(epv) :: "memory");
        WAIT_LGKM0();
        __builtin_amdgcn_s_barrier();
        asm volatile("" ::: "memory");
        S_RX(0, BUF(0), co0); S_RW(0, BUF(0), co0, 0); S_RW(0, BUF(0), co0, 1); S_RW(0, BUF(0), co0, 2); S_RW(0, BUF(0), co0, 3); S_RW(0, BUF(0), co0, 4);
        tok = (((u64)(unsigned)__builtin_amdgcn_readfirstlane(epv[1]) << 32) | (unsigned)__builtin_amdgcn_readfirstlane(epv[0])) + 1ull;
        const int cl = lane < 40 ? lane : 39;
        float lr, li, za, zb;
        f32x2 pA, pB;
#define TB0() do { lr = bflo(*(const u16*)(sm + K1_PLR + cl * 2)); li = bflo(*(const u16*)(sm + K1_PLI + cl * 2)); za = bflo(*(const u16*)(sm + K1_PLD + cl * 2)); } while (0)
#define TB1() do { const float dt = __builtin_amdgcn_exp2f(za * LOG2E); za = dt * lr * LOG2E; zb = dt * li * INV2PI;                   \
                   const float nA = wc == 1 ? 8.f : wc == 2 ? 24.f : 1.f;                                                             \
                   const float mA = __builtin_amdgcn_exp2f(nA * za), rA = __builtin_amdgcn_fractf(nA * zb);                           \
                   pA = (f32x2){mA * __builtin_amdgcn_cosf(rA), mA * __builtin_amdgcn_sinf(rA)}; } while (0)
#define TB2() do { const float nB = wc == 1 ? 16.f : wc == 2 ? 32.f : 0.f;                                                            \
                   const float mB = __builtin_amdgcn_exp2f(nB * za), rB = __builtin_amdgcn_fractf(nB * zb);                           \
                   pB = (f32x2){mB * __builtin_amdgcn_cosf(rB), mB * __builtin_amdgcn_sinf(rB)}; } while (0)
#define TB3() do { const float nr = pA.x - 1.0f, ni = pA.y, rden = 1.0f / (lr * lr + li * li);                                        \
                   const float z8 = __builtin_amdgcn_fractf(8.f * zb);                                                                \
                   if (wc == 3) pA = (f32x2){(nr * lr + ni * li) * rden, (ni * lr - nr * li) * rden};                 \
                   f32x4* tb = (f32x4*)(sm + K1_TAB + cl * 16);                                              \
                   tb[wc == 0 ? 0 : wc == 1 ? 120 : wc == 2 ? 200 : 40] = (f32x4){pA.x, -pA.y, pA.x, pA.y};                           \
                   tb[wc == 0 ? 80 : wc == 1 ? 160 : wc == 2 ? 240 : 280] = wc == 3 ? (f32x4){8.f * za, z8, 8.f * za, z8} : (f32x4){pB.x, -pB.y, pB.x, pB.y}; } while (0)
        S_BODY(0, 5, , , , );   S_BODY(1, 9, , , , );   S_BODY(2, 5, , , , );   S_BODY(3, 6, , , , );
        S_BODY(4, 5, TB0(), TB1(), TB2(), TB3());   S_BODY(5, 5, , , , );   S_BODY(6, 5, , , , );
        S_BODY(7, 5, , , , );   S_BODY(8, 5, , , , );
        const f32x2* tab = (const f32x2*)(sm + K1_TAB) + t;
        f32x2 Lc[5], Sc[5];
#pragma unroll
        for (int T = 0; T < 5; ++T) { Lc[T] = tab[16 * T]; Sc[T] = tab[(5 - q) * 80 + 16 * T]; }
        const f32x2 L32 = *(const f32x2*)(sm + K1_TAB + (6 * 80 + 2 * cl + 1) * 8);
        S_BODY(9, 0, , , , );
        __builtin_amdgcn_s_setprio(3);
        float ag8[5];
#pragma unroll
        for (int T = 0; T < 5; ++T) ag8[T] = acc[T][0][0];
#pragma unroll
        for (int j = 1; j < 8; ++j)
#pragma unroll
            for (int T = 0; T < 5; ++T) ag8[T] = fmaf(Lc[T].y, swp(ag8[T]), fmaf(Lc[T].x, ag8[T], acc[T][j >> 2][j & 3]));
        {
            unsigned sv[5];
#pragma unroll
            for (int T = 0; T < 5; ++T) sv[T] = f2u(fmaf(Sc[T].y, swp(ag8[T]), Sc[T].x * ag8[T]));
            const auto r01 = __builtin_amdgcn_permlane32_swap(sv[0], sv[1], false, false);
            const auto r23 = __builtin_amdgcn_permlane32_swap(sv[2], sv[3], false, false);
            const auto r44 = __builtin_amdgcn_permlane32_swap(sv[4], sv[4], false, false);
            const unsigned p4 = f2u(u2f(r44[0]) + u2f(r44[1]));
            const auto ra = __builtin_amdgcn_permlane16_swap(f2u(u2f(r01[0]) + u2f(r01[1])), f2u(u2f(r23[0]) + u2f(r23[1])), false, false);
            const auto rb4 = __builtin_amdgcn_permlane16_swap(p4, p4, false, false);
            float* xa = (float*)(sm + K1_XCH) + wc * 80 + t;
            xa[16 * (((q & 1) << 1) | (q >> 1))] = u2f(ra[0]) + u2f(ra[1]);
            xa[64] = u2f(rb4[0]) + u2f(rb4[1]);
        }
        asm volatile("" ::: "memory");
        unsigned old = 0;
        if (lane == 0) old = __hip_atomic_fetch_add((LAS unsigned*)(sm + K1_CNT), 1u, __ATOMIC_RELAXED, __HIP_MEMORY_SCOPE_WORKGROUP);
        old = __builtin_amdgcn_readfirstlane(old);
        asm volatile("" ::: "memory");
        if (old == 3) {
            const f32x2* xc = (const f32x2*)(sm + K1_XCH) + cl;
            const f32x2 s = cfma(L32, cfma(L32, cfma(L32, xc[0], xc[40]), xc[80]), xc[120]);
            if (lane < 40) st128_wt(ws + WS_AGG + (size_t)(cb * 16 + rb) * 640 + lane * 16,
                                    (u32x4){f2u(s.x), f2u(s.y), (unsigned)tok, (unsigned)(tok >> 32)});
        }
        SCHED_FENCE();
        char* own = sm + wave * 20480 + 2048;
#pragma unroll
        for (int T = 0; T < 5; ++T) *(float*)(own + 8192 + (16 * T + t) * 16 + q * 4) = ag8[T];
        if (cb < 10 && t < 2) {
            const f32x4 gv = t ? acc[5][1] : acc[5][0];
            st128_wt(ws + WS_G + (cb * 2048 + 128 * rb + 32 * wc + 8 * q + 4 * t) * 4, __builtin_bit_cast(u32x4, gv));
        }
        if (bid == 255 && old == 0) {
#pragma unroll 1
            for (int k = 0; k < 4; ++k) flag_wait(ws + WS_FPT + (size_t)(64 * k + lane) * 8, true, tok);
            if (lane == 0) flag_set(ws + WS_EP, tok);
        }
        f32x2 Wj[4];
        {
            const f32x2 zz = *(const f32x2*)(sm + K1_TAB + (7 * 80 + 2 * cl) * 8);
            const float za = 16.f * zz.x, zb = __builtin_amdgcn_fractf(16.f * zz.y);
#pragma unroll
            for (int j = 0; j < 4; ++j) {
                const int n = rb - 1 - 4 * wc - j;
                const float fn = (float)(n < 0 ? 0 : n);
                const float mg = n < 0 ? 0.f : __builtin_amdgcn_exp2f(fn * za), rv = fn * zb;
                Wj[j] = (f32x2){mg * __builtin_amdgcn_cosf(rv), mg * __builtin_amdgcn_sinf(rv)};
            }
        }
        float pre[5], Ma[5], Mb[5];
        {
            f32x2 Cc[5], L8c[5];
            const float fs = (float)(4 * wc + q), qr = (t & 1) ? -0.25f : 0.f, sg = (t & 1) ? 1.f : -1.f;
#pragma unroll
            for (int T = 0; T < 5; ++T) {
                Cc[T] = tab[80 + 16 * T]; L8c[T] = tab[240 + 16 * T];
                const f32x2 zz = tab[560 + 16 * T];
                const float e = __builtin_amdgcn_exp2f(fs * zz.x) * __builtin_amdgcn_cosf(fmaf(fs, zz.y, qr));
                Ma[T] = fmaf(Cc[T].y, swp(e), Cc[T].x * e);
                Mb[T] = sg * swp(Ma[T]);
#pragma unroll
                for (int j = 0; j < 8; ++j) {
                    const float u = acc[T][j >> 2][j & 3];
                    acc[T][j >> 2][j & 3] = fmaf(Cc[T].y, swp(u), Cc[T].x * u);
                }
                pre[T] = 0.f;
            }
            {
                for (unsigned spins = 0; spins < (1u << 22); ++spins) {
                    if (__hip_atomic_load((LAS unsigned*)(sm + K1_CNT), __ATOMIC_RELAXED, __HIP_MEMORY_SCOPE_WORKGROUP) >= 4u) break;
                    __builtin_amdgcn_s_sleep(1);
                }
                asm volatile("" ::: "memory");
                const float* xa = (const float*)(sm + K1_XCH) + t;
#pragma unroll 1
                for (int w = 0; w < wc; ++w)
#pragma unroll
                    for (int T = 0; T < 5; ++T) {
                        const f32x2 z = tab[480 + 16 * T];
                        pre[T] = fmaf(z.y, swp(pre[T]), fmaf(z.x, pre[T], xa[w * 80 + 16 * T]));
                    }
            }
#pragma unroll
            for (int T = 0; T < 5; ++T) {
                const f32x4 xs = *(const f32x4*)(own + 8192 + (16 * T + t) * 16);
#pragma unroll
                for (int k = 0; k < 3; ++k) {
                    const float nx = fmaf(L8c[T].y, swp(pre[T]), fmaf(L8c[T].x, pre[T], xs[k]));
                    pre[T] = k < q ? nx : pre[T];
                }
                pre[T] = fmaf(Cc[T].y, swp(pre[T]), Cc[T].x * pre[T]);
            }
        }
        SCHED_FENCE();
        if (rb > 0) {
            f32x2 cy = {0.f, 0.f};
            if (4 * wc < rb) {
                const char* agp = ws + WS_AGG + (size_t)(cb * 16 + 4 * wc) * 640 + cl * 16;
                for (unsigned spins = 0; spins < (1u << 20); ++spins) {
                    u32x4 ag[4];
#pragma unroll
                    for (int j = 0; j < 4; ++j)
                        asm volatile("global_load_dwordx4 %0, %1, off sc1" : "=v"(ag[j]) : "v"(agp + (4 * wc + j < rb ? j : 0) * 640) : "memory");
                    asm volatile("s_waitcnt vmcnt(0)" : "+v"(ag[0]), "+v"(ag[1]), "+v"(ag[2]), "+v"(ag[3]) :: "memory");
                    bool ok = true;
#pragma unroll
                    for (int j = 0; j < 4; ++j) ok = ok && ag[j][2] == (unsigned)tok && ag[j][3] == (unsigned)(tok >> 32);
                    const f32x2 c0 = cfma(Wj[1], (f32x2){u2f(ag[1][0]), u2f(ag[1][1])}, cmul_pk(Wj[0], (f32x2){u2f(ag[0][0]), u2f(ag[0][1])}));
                    const f32x2 c1 = cfma(Wj[3], (f32x2){u2f(ag[3][0]), u2f(ag[3][1])}, cmul_pk(Wj[2], (f32x2){u2f(ag[2][0]), u2f(ag[2][1])}));
                    cy = (f32x2){c0.x + c1.x, c0.y + c1.y};
                    if (wave_all(ok)) break;
                    __builtin_amdgcn_s_sleep(4);
                }
            }
            if (lane < 40) *(f32x2*)(sm + K1_PRT + (lane * 4 + wc) * 8) = cy;
            asm volatile("" ::: "memory");
            if (lane == 0) __hip_atomic_fetch_add((LAS unsigned*)(sm + K1_CNT + 4), 1u, __ATOMIC_RELAXED, __HIP_MEMORY_SCOPE_WORKGROUP);
            for (unsigned spins = 0; spins < (1u << 22); ++spins) {
                if (__hip_atomic_load((LAS unsigned*)(sm + K1_CNT + 4), __ATOMIC_RELAXED, __HIP_MEMORY_SCOPE_WORKGROUP) >= 4u) break;
                __builtin_amdgcn_s_sleep(1);
            }
            asm volatile("" ::: "memory");
#pragma unroll
            for (int T = 0; T < 5; ++T) {
                const f32x4* pp = (const f32x4*)(sm + K1_PRT + (8 * T + (t >> 1)) * 32);
                const f32x4 p0 = pp[0], p1 = pp[1];
                pre[T] = fmaf(Mb[T], (p0[1] + p0[3]) + (p1[1] + p1[3]), fmaf(Ma[T], (p0[0] + p0[2]) + (p1[0] + p1[2]), pre[T]));
            }
        }
#pragma unroll
        for (int j = 0; j < 8; ++j)
#pragma unroll
            for (int T = 0; T < 5; ++T) {
                pre[T] = fmaf(Lc[T].y, swp(pre[T]), fmaf(Lc[T].x, pre[T], acc[T][j >> 2][j & 3]));
                acc[T][j >> 2][j & 3] = pre[T];
            }
#pragma unroll
        for (int T = 0; T < 5; ++T)
            *(u32x4*)(own + (16 * T + t) * 80 + q * 16) = (u32x4){pack2(acc[T][0][0], acc[T][0][1]), pack2(acc[T][0][2], acc[T][0][3]),
                                                                  pack2(acc[T][1][0], acc[T][1][1]), pack2(acc[T][1][2], acc[T][1][3])};
        asm volatile("" ::: "memory");
        {
            const int od = cb & 1;
            char* dst = a2 + (size_t)(((5 * cb) >> 1) + od) * 8192;
            const char* rp = own + (16 * od + 8 * q + (t >> 2)) * 80 + (t & 3) * 16;
#pragma unroll
            for (int m = 0; m < 2; ++m)
#pragma unroll
                for (int rt = 0; rt < 2; ++rt) {
                    const s16x4 lo = __builtin_amdgcn_ds_read_tr16_b64_v4i16((LAS s16x4*)(rp + m * 2560 + rt * 8));
                    const s16x4 hi = __builtin_amdgcn_ds_read_tr16_b64_v4i16((LAS s16x4*)(rp + m * 2560 + rt * 8 + 320));
                    st128_wt(dst + m * 8192 + rt * 1024, __builtin_bit_cast(u32x4, __builtin_shufflevector(lo, hi, 0, 1, 2, 3, 4, 5, 6, 7)));
                }
            const char* rl = own + ((od ? 0 : 64) + 8 * (q & 1) + (t >> 2)) * 80 + (t & 3) * 16 + (q >> 1) * 8;
            const s16x4 lo = __builtin_amdgcn_ds_read_tr16_b64_v4i16((LAS s16x4*)rl);
            const s16x4 hi = __builtin_amdgcn_ds_read_tr16_b64_v4i16((LAS s16x4*)(rl + 320));
            st128_wt(dst + (od ? -8192 + 512 : 16384) + (q >> 1) * 512, __builtin_bit_cast(u32x4, __builtin_shufflevector(lo, hi, 0, 1, 2, 3, 4, 5, 6, 7)));
        }
        WAIT_LGKM0();
        if (lane == 0) __hip_atomic_fetch_add((LAS unsigned*)(sm + K1_CNT + 8), 1u, __ATOMIC_RELAXED, __HIP_MEMORY_SCOPE_WORKGROUP);
        __builtin_amdgcn_s_setprio(0);
        P2_BEGIN(0);
        P2_STEP(0, 10, 8, 7); P2_STEP(1, 10, 8, 7); P2_STEP(2, 10, 8, 7); P2_STEP(3, 10, 8, 7); P2_STEP(4, 10, 8, 7);
        P2_STEP(5, 10, 8, 7); P2_STEP(6, 10, 13, 7); P2_STEP(7, 10, 9, 7); P2_STEP(8, 10, 5, 7); P2_STEP(9, 10, 0, 7);
        asm volatile("" : "+v"(gl[0]), "+v"(gl[1]), "+v"(gl[2]), "+v"(gl[3]) :: "memory");
    } else {
        const int bb = q & 1;
        const int trb = 26624 + (8 * q + (t >> 2)) * 192 + (lane & 3) * 8;
        const int tra = trb + bb * 32;
        const int tr5 = trb + (bb ? 0 : 160);
        f32x4 acc[6][2];
        s16x8 fx[2][2], fw[2][6];
#define M_RW(b, sb, ks, j)                                                                        \
    do {                                                                                          \
        const char* pa_ = sm + (sb) + ((j) < 5 ? tra + (j) * 32 : tr5) + (ks) * 32 * 192;         \
        const s16x4 lo_ = __builtin_amdgcn_ds_read_tr16_b64_v4i16((LAS s16x4*)pa_);               \
        const s16x4 hi_ = __builtin_amdgcn_ds_read_tr16_b64_v4i16((LAS s16x4*)(pa_ + 4 * 192));   \
        fw[b][j] = __builtin_shufflevector(lo_, hi_, 0, 1, 2, 3, 4, 5, 6, 7);                     \
    } while (0)
#define M_MM(b, j) do { acc[j][0] = mfma16(fw[b][j], fx[b][0], acc[j][0]); acc[j][1] = mfma16(fw[b][j], fx[b][1], acc[j][1]); SCHED_FENCE(); } while (0)
#define M_BODY(it, WN)                                                                            \
    do {                                                                                          \
        constexpr bool RF_ = (it) < 8, RN_ = (it) < 9;                                            \
        constexpr int SB_ = (it) < 8 ? 0 : (it) - 7;                                              \
        M_MM(0, 0); if (RF_) X_M0((it) + 2, 0); S_RX(1, BUF(it), co1); M_RW(1, BUF(it), 1, 0); SCHED_FENCE(); \
        if (RF_) { X_LD((it) + 2, 0); X_M0((it) + 2, 1); } else S_LB(SB_, 0);                     \
        SCHED_FENCE();                                                                            \
        M_MM(0, 1); M_RW(1, BUF(it), 1, 1); M_RW(1, BUF(it), 1, 2); SCHED_FENCE();                \
        if (RF_) { X_LD((it) + 2, 1); X_M0((it) + 2, 2); } else S_LB(SB_, 1);                     \
        SCHED_FENCE();                                                                            \
        M_MM(0, 2); M_RW(1, BUF(it), 1, 3); M_RW(1, BUF(it), 1, 4); SCHED_FENCE();                \
        if (RF_) { X_LD((it) + 2, 2); X_M0((it) + 2, 3); } else S_LB(SB_, 2);                     \
        SCHED_FENCE();                                                                            \
        M_MM(0, 3); M_RW(1, BUF(it), 1, 5); SCHED_FENCE();                                        \
        if (RF_) X_LD((it) + 2, 3); else S_LB(SB_, 3);                                            \
        SCHED_FENCE();                                                                            \
        M_MM(0, 4); if (!RF_) S_LB(SB_, 4);                                                       \
        SCHED_FENCE();                                                                            \
        M_MM(0, 5);                                                                               \
        if (RN_) WAIT_VM(WN);                                                                     \
        WAIT_LGKM0();                                                                             \
        __builtin_amdgcn_s_barrier();                                                             \
        asm volatile("" ::: "memory");                                                            \
        SCHED_FENCE();                                                                            \
        M_MM(1, 0); if (RN_) { S_RX(0, BUF((it) + 1), co0); M_RW(0, BUF((it) + 1), 0, 0); }       \
        if ((it) == 7) { pt_check(); S_LB(0, 0); }                                                \
        SCHED_FENCE();                                                                            \
        M_MM(1, 1); if (RN_) { M_RW(0, BUF((it) + 1), 0, 1); M_RW(0, BUF((it) + 1), 0, 2); } if ((it) == 7) S_LB(0, 1); SCHED_FENCE(); \
        M_MM(1, 2); if (RN_) { M_RW(0, BUF((it) + 1), 0, 3); M_RW(0, BUF((it) + 1), 0, 4); } if ((it) == 7) S_LB(0, 2); SCHED_FENCE(); \
        M_MM(1, 3); if (RN_) M_RW(0, BUF((it) + 1), 0, 5); if ((it) == 7) S_LB(0, 3); SCHED_FENCE(); \
        M_MM(1, 4); if ((it) == 7) S_LB(0, 4); SCHED_FENCE();                                     \
        M_MM(1, 5);                                                                               \
    } while (0)
        u32x2 bp[6];
        asm volatile("global_load_dwordx2 %0, %6, off\n\tglobal_load_dwordx2 %1, %6, off offset:32\n\tglobal_load_dwordx2 %2, %6, off offset:64\n\t"
                     "global_load_dwordx2 %3, %6, off offset:96\n\tglobal_load_dwordx2 %4, %6, off offset:128\n\tglobal_load_dwordx2 %5, %6, off offset:160"
                     : "=&v"(bp[0]), "=&v"(bp[1]), "=&v"(bp[2]), "=&v"(bp[3]), "=&v"(bp[4]), "=&v"(bp[5]) : "v"(b1 + 96 * cb + 4 * q) : "memory");
        X_M0(1, 0); G_NOP(); X_LD(1, 0); X_M0(1, 1); G_NOP(); X_LD(1, 1); X_M0(1, 2); G_NOP(); X_LD(1, 2); X_M0(1, 3); G_NOP(); X_LD(1, 3);
        {
            const char* cp = (const char*)(pc0 & ~255ull) + (wave - 4) * 8192 + lane * 128;
            glds16(cp, smb + K1_FM);
        }
        asm volatile("" : "+v"(a2), "+v"(pa), "+v"(pb));
        asm volatile("s_waitcnt vmcnt(5)" : "+v"(epv), "+v"(bp[0]), "+v"(bp[1]), "+v"(bp[2]), "+v"(bp[3]), "+v"(bp[4]), "+v"(bp[5]) :: "memory");
#pragma unroll
        for (int j = 0; j < 6; ++j) {
            acc[j][0] = (f32x4){bflo(bp[j][0]), bfhi(bp[j][0]), bflo(bp[j][1]), bfhi(bp[j][1])};
            asm volatile("" : "+v"(acc[j][0]));
        }
        tok = (((u64)(unsigned)__builtin_amdgcn_readfirstlane(epv[1]) << 32) | (unsigned)__builtin_amdgcn_readfirstlane(epv[0])) + 1ull;
        __builtin_amdgcn_s_barrier();
        asm volatile("" ::: "memory");
        S_RX(0, BUF(0), co0); M_RW(0, BUF(0), 0, 0); M_RW(0, BUF(0), 0, 1); M_RW(0, BUF(0), 0, 2); M_RW(0, BUF(0), 0, 3); M_RW(0, BUF(0), 0, 4); M_RW(0, BUF(0), 0, 5);
#pragma unroll
        for (int j = 0; j < 6; ++j) acc[j][1] = acc[j][0];
        M_BODY(0, 4);   M_BODY(1, 4);   M_BODY(2, 4);   M_BODY(3, 4);   M_BODY(4, 4);   M_BODY(5, 4);   M_BODY(6, 4);
        M_BODY(7, 4);   M_BODY(8, 10);   M_BODY(9, 0);
#pragma unroll
        for (int v = 0; v < 3; ++v) {
#pragma unroll
            for (int rt = 0; rt < 2; ++rt) {
                u32x4 o;
                if (2 * v + rt < 5) { P2_CACHE(3 * (2 * v + rt)); P2_CACHE(3 * (2 * v + rt) + 1); P2_CACHE(3 * (2 * v + rt) + 2); }
#pragma unroll
                for (int jj = 0; jj < 2; ++jj) {
                    float r[4];
#pragma unroll
                    for (int i = 0; i < 4; ++i) {
                        const float h = acc[2 * v + jj][rt][i];
                        const float e = __builtin_amdgcn_exp2f(h * LOG2E);
                        const float d = fmaf(e, e + 2.0f, 2.0f);
                        r[i] = fmaf(-2.0f, h * __builtin_amdgcn_rcpf(d), h);
                    }
                    o[2 * jj] = pack2(r[0], r[1]);
                    o[2 * jj + 1] = pack2(r[2], r[3]);
                }
                st128_wt(a2 + (size_t)(40 + 3 * cb + v) * 8192 + rt * 1024, o);
            }
        }
        P2_BEGIN(1);
        P2_STEP(0, 12, 8, 6); P2_STEP(1, 12, 8, 6); P2_STEP(2, 12, 8, 6); P2_STEP(3, 12, 8, 6); P2_STEP(4, 12, 8, 6); P2_STEP(5, 12, 13, 6);
        P2_STEP(6, 12, 9, 6); P2_STEP(7, 12, 9, 6); P2_STEP(8, 12, 9, 6); P2_STEP(9, 12, 9, 6); P2_STEP(10, 12, 5, 6); P2_STEP(11, 12, 0, 6);
        gl[0] = gl[1] = gl[2] = gl[3] = 0u;
    }
    };
    if (wave < 4) run(IC<0>{}); else run(IC<1>{});
    if (wave == 5 || wave == 6) {
        for (unsigned spins = 0; spins < (1u << 22); ++spins) {
            if (__hip_atomic_load((LAS unsigned*)(sm + K1_CNT + 8), __ATOMIC_RELAXED, __HIP_MEMORY_SCOPE_WORKGROUP) >= 4u) break;
            __builtin_amdgcn_s_sleep(1);
        }
        if (lane == 0) __hip_atomic_fetch_add((LAS unsigned*)(sm + K1_CNT + 12), 1u, __ATOMIC_RELAXED, __HIP_MEMORY_SCOPE_WORKGROUP);
        if (wave == 6) {
            for (unsigned spins = 0; spins < (1u << 22); ++spins) {
                if (__hip_atomic_load((LAS unsigned*)(sm + K1_CNT + 12), __ATOMIC_RELAXED, __HIP_MEMORY_SCOPE_WORKGROUP) >= 2u) break;
                __builtin_amdgcn_s_sleep(1);
            }
        }
        asm volatile("" ::: "memory");
    }
#pragma unroll
    for (int r = 0; r < 4; ++r)
#pragma unroll
        for (int n = 0; n < 5; ++n) {
            if (r == 0 && n < 3 && wave == n) *(float*)(sm + wave * 21504 + lane * 4) = (u2f(gl[0]) + u2f(gl[1])) + (wave < 2 ? u2f(gl[2]) + u2f(gl[3]) : 0.f);
            else *(f32x4*)(sm + wave * 20480 + (r * 5 + n) * 1024 + lane * 16) = ac2[r][n];
        }
    wg_sync();
#pragma unroll
    for (int m = 0; m < 3; ++m) {
        const int tile = wave + 8 * m;
        if (tile < 20) {
            const char* rp = sm + tile * 1024 + lane * 16;
            f32x4 p0 = *(const f32x4*)rp, p1 = *(const f32x4*)(rp + 20480), p2 = *(const f32x4*)(rp + 40960);
            if (m == 0) { p0 = wave == 0 ? ac2[0][0] : p0; p1 = wave == 1 ? ac2[0][1] : p1; p2 = wave == 2 ? ac2[0][2] : p2; }
            const f32x4 vs = (p0 + p1) + (p2 + *(const f32x4*)(rp + 61440));
            const f32x4 vm = (*(const f32x4*)(rp + 81920) + *(const f32x4*)(rp + 102400)) + (*(const f32x4*)(rp + 122880) + *(const f32x4*)(rp + 143360));
            const int r = tile / 5;
            const float* gp = (const float*)sm + 32 * (r >> 1) + 8 * (t >> 2) + 4 * (r & 1) + (t & 3);
            const float g = sigmoidf_((gp[0] + gp[5376]) + gp[10752]);
            f32x4 v = vs * (2.0f * g) + vm * (1.0f - g);
            v[0] += g * bflo(xv[m][0]) * bflo(dv[m][0]);
            v[1] += g * bfhi(xv[m][0]) * bfhi(dv[m][0]);
            v[2] += g * bflo(xv[m][1]) * bflo(dv[m][1]);
            v[3] += g * bfhi(xv[m][1]) * bfhi(dv[m][1]);
            st128_wt(out + oo[m], __builtin_bit_cast(u32x4, v));
        }
    }
}

extern "C" void kernel_launch(void* const* d_in, const int* in_sizes, int n_in, void* d_out, int out_size, void* d_ws,
                              size_t ws_size, hipStream_t stream) {
    kf<<<256, 512, 0, stream>>>((const u16*)d_in[0], (const u16*)d_in[1], (const u16*)d_in[2], (const u16*)d_in[3], (const u16*)d_in[4],
                                (const u16*)d_in[5], (const u16*)d_in[6], (const u16*)d_in[7], (const u16*)d_in[8], (const u16*)d_in[10],
                                (const u16*)d_in[11], (const u16*)d_in[12], (const u16*)d_in[13], (char*)d_ws, (float*)d_out);
}
```

```cpp
#include <hip/hip_runtime.h>
#include <stdint.h>

typedef short s16x4 __attribute__((ext_vector_type(4)));
typedef short s16x8 __attribute__((ext_vector_type(8)));
typedef __bf16 bf16x2 __attribute__((ext_vector_type(2)));
typedef float f32x2 __attribute__((ext_vector_type(2)));
typedef float f32x4 __attribute__((ext_vector_type(4)));
typedef unsigned u32x2 __attribute__((ext_vector_type(2)));
typedef unsigned u32x4 __attribute__((ext_vector_type(4)));
typedef unsigned short u16;
#define LAS __attribute__((address_space(3)))
#define GAS __attribute__((address_space(1)))
#define DI __device__ __forceinline__

constexpr int SEQ = 2048, DM = 640, NP = 640, NF = 1536;
constexpr int NKS = 88;

constexpr size_t WS_A2 = 0;
constexpr size_t WS_W2 = WS_A2 + (size_t)16 * NKS * 8192;
constexpr size_t WS_G = WS_W2 + (size_t)8 * NKS * 5120;
constexpr size_t WS_AGG = WS_G + 10 * 2048 * 4;
constexpr size_t WS_EP = WS_AGG + 16 * 16 * 640;
constexpr size_t WS_FPT = WS_EP + 256;
constexpr size_t WS_FAG = WS_FPT + 2048;
constexpr size_t WS_FP1 = WS_FAG + 2048;
constexpr size_t WS_JUNK = WS_FP1 + 16384;

DI unsigned f2u(float v) { return __builtin_bit_cast(unsigned, v); }
DI float u2f(unsigned v) { return __builtin_bit_cast(float, v); }
DI bool wave_all(bool p) { return __builtin_amdgcn_ballot_w64(!p) == 0ull; }
DI void wg_sync() {
    __builtin_amdgcn_fence(__ATOMIC_RELEASE, "workgroup");
    __builtin_amdgcn_s_barrier();
    __builtin_amdgcn_fence(__ATOMIC_ACQUIRE, "workgroup");
}
DI float sigmoidf_(float v) { return 1.0f / (1.0f + __builtin_amdgcn_exp2f(v * -1.44269504088896341f)); }
DI float bflo(unsigned v) { return __uint_as_float(v << 16); }
DI float bfhi(unsigned v) { return __uint_as_float(v & 0xffff0000u); }
DI unsigned pack2(float a, float b) {
    f32x2 p = {a, b};
    return __builtin_bit_cast(unsigned, __builtin_convertvector(p, bf16x2));
}
DI f32x4 mfma16(s16x8 a, s16x8 b, f32x4 c) { return __builtin_amdgcn_mfma_f32_16x16x32_bf16(a, b, c, 0, 0, 0); }
template <int S> DI float shr_zero(float v) {
    return __builtin_bit_cast(float, __builtin_amdgcn_update_dpp(0, __builtin_bit_cast(int, v), 0x110 + S, 0xf, 0xf, true));
}
DI float swp(float v) {
    return __builtin_bit_cast(float, __builtin_amdgcn_update_dpp(0, __builtin_bit_cast(int, v), 0xB1, 0xf, 0xf, true));
}
DI f32x2 cmul(f32x2 a, f32x2 b) { return (f32x2){a.x * b.x - a.y * b.y, a.x * b.y + a.y * b.x}; }
DI f32x2 cfma(f32x2 a, f32x2 b, f32x2 c) {
    return (f32x2){fmaf(-a.y, b.y, fmaf(a.x, b.x, c.x)), fmaf(a.y, b.x, fmaf(a.x, b.y, c.y))};
}
DI f32x2 cmul_pk(f32x2 a, f32x2 b) { return (f32x2){fmaf(-a.y, b.y, a.x * b.x), fmaf(a.y, b.x, a.x * b.y)}; }
DI f32x2 cmul_sc(f32x2 a, f32x2 b) {
    float p = a.x * b.x, r = a.x * b.y;
    asm("" : "+v"(p));
    asm("" : "+v"(r));
    return (f32x2){fmaf(-a.y, b.y, p), fmaf(a.y, b.x, r)};
}
DI float dot2bf(unsigned a, unsigned b, float c) {
    return __builtin_amdgcn_fdot2_f32_bf16(__builtin_bit_cast(bf16x2, a), __builtin_bit_cast(bf16x2, b), c, false);
}
#define WAIT_VM(n) asm volatile("s_waitcnt vmcnt(%0)" ::"n"(n) : "memory")
#define WAIT_LGKM0() asm volatile("s_waitcnt lgkmcnt(0)" ::: "memory")
#define ACQUIRE() asm volatile("buffer_inv sc1" ::: "memory")
#define SCHED_FENCE() __builtin_amdgcn_sched_barrier(0)

DI void glds16(const void* gsrc, unsigned lds_dst) {
    unsigned keep;
    asm volatile("s_mov_b32 %0, m0\n\ts_mov_b32 m0, %2\n\ts_nop 0\n\tglobal_load_lds_dwordx4 %1, off\n\ts_mov_b32 m0, %0"
                 : "=&s"(keep) : "v"(gsrc), "s"(lds_dst) : "memory");
}
DI void glds4(const void* gsrc, unsigned lds_dst) {
    unsigned keep;
    asm volatile("s_mov_b32 %0, m0\n\ts_mov_b32 m0, %2\n\ts_nop 0\n\tglobal_load_lds_dword %1, off\n\ts_mov_b32 m0, %0"
                 : "=&s"(keep) : "v"(gsrc), "s"(lds_dst) : "memory");
}
DI void glds16x4(const void* gsrc, unsigned lds_dst) {
    unsigned keep;
    asm volatile("s_mov_b32 %0, m0\n\ts_mov_b32 m0, %2\n\ts_nop 0\n\tglobal_load_lds_dwordx4 %1, off\n\t"
                 "global_load_lds_dwordx4 %1, off offset:1024\n\tglobal_load_lds_dwordx4 %1, off offset:2048\n\t"
                 "global_load_lds_dwordx4 %1, off offset:3072\n\ts_mov_b32 m0, %0"
                 : "=&s"(keep) : "v"(gsrc), "s"(lds_dst) : "memory");
}
DI void st128_wt(void* p, u32x4 v) { asm volatile("global_store_dwordx4 %0, %1, off sc1\n\ts_nop 1" :: "v"(p), "v"(v) : "memory"); }
DI void st64_wt(void* p, u32x2 v) { asm volatile("global_store_dwordx2 %0, %1, off sc1\n\ts_nop 1" :: "v"(p), "v"(v) : "memory"); }
DI unsigned ld32_asm(const void* p) { unsigned r; asm volatile("global_load_dword %0, %1, off" : "=v"(r) : "v"(p) : "memory"); return r; }
DI unsigned ld16_asm(const void* p) { unsigned r; asm volatile("global_load_ushort %0, %1, off" : "=v"(r) : "v"(p) : "memory"); return r; }
DI u32x2 ld64_asm(const void* p) { u32x2 r; asm volatile("global_load_dwordx2 %0, %1, off" : "=v"(r) : "v"(p) : "memory"); return r; }
DI u32x4 ld128_asm(const void* p) { u32x4 r; asm volatile("global_load_dwordx4 %0, %1, off" : "=v"(r) : "v"(p) : "memory"); return r; }
constexpr float LOG2E = 1.44269504088896341f, INV2PI = 0.15915494309189535f;
DI f32x2 lam_bar(float lr, float li, float ld) {
    const float dt = __builtin_amdgcn_exp2f(ld * LOG2E);
    const float m = __builtin_amdgcn_exp2f(dt * lr * LOG2E);
    const float rv = __builtin_amdgcn_fractf(dt * li * INV2PI);
    return (f32x2){m * __builtin_amdgcn_cosf(rv), m * __builtin_amdgcn_sinf(rv)};
}

typedef unsigned long long u64;
DI void glds16_c(const void* gsrc, unsigned lds_dst) {
    unsigned keep;
    lds_dst = __builtin_amdgcn_readfirstlane(lds_dst);
    asm volatile("s_mov_b32 %0, m0\n\ts_mov_b32 m0, %2\n\ts_nop 0\n\tglobal_load_lds_dwordx4 %1, off sc1\n\ts_mov_b32 m0, %0"
                 : "=&s"(keep) : "v"(gsrc), "s"(lds_dst) : "memory");
}
DI void glds16x4_c(const void* gsrc, unsigned lds_dst) {
    unsigned keep;
    asm volatile("s_mov_b32 %0, m0\n\ts_mov_b32 m0, %2\n\ts_nop 0\n\tglobal_load_lds_dwordx4 %1, off sc1\n\t"
                 "global_load_lds_dwordx4 %1, off offset:1024 sc1\n\tglobal_load_lds_dwordx4 %1, off offset:2048 sc1\n\t"
                 "global_load_lds_dwordx4 %1, off offset:3072 sc1\n\ts_mov_b32 m0, %0"
                 : "=&s"(keep) : "v"(gsrc), "s"(lds_dst) : "memory");
}
DI void flag_set(char* p, u64 tok) { __hip_atomic_store((GAS u64*)p, tok, __ATOMIC_RELAXED, __HIP_MEMORY_SCOPE_AGENT); }
DI void flag_wait(const char* p, bool need, u64 tok) {
    for (unsigned spins = 0; spins < (1u << 20); ++spins) {
        u64 v = tok;
        if (need) v = __hip_atomic_load((const GAS u64*)p, __ATOMIC_RELAXED, __HIP_MEMORY_SCOPE_AGENT);
        if (__all(v == tok)) break;
        __builtin_amdgcn_s_sleep(8);
    }
    asm volatile("" ::: "memory");
}

template <int V> struct IC { static constexpr int value = V; };
#define ROTF ((((rb + cb) & 3) * 5 + 1) >> 1)
constexpr int K1_STAGE = 38 * 1024;
constexpr int K1_GW = 0;
constexpr int K1_PLR = K1_GW + 1280, K1_PLI = K1_GW + 1360, K1_PLD = K1_GW + 1440;
constexpr int K1_RING = 2048;
#define BUF(it) (K1_RING + ((it) % 3) * K1_STAGE)
constexpr int K1_TAB = K1_RING + 3 * K1_STAGE;
constexpr int K1_XCH = K1_TAB + 5120;
constexpr int K1_CNT = K1_XCH + 1280;
constexpr int K1_PRT = K1_CNT + 16;
constexpr int K1_PT = 123 * 1024;
constexpr int K1_FM = 140 * 1024;
constexpr int KF_LDS = 160 * 1024;

__global__ __launch_bounds__(512, 2) __attribute__((target("no-packed-fp32-ops"))) void kf(const u16* __restrict__ x, const u16* __restrict__ Lre, const u16* __restrict__ Lim,
                                             const u16* __restrict__ Bre, const u16* __restrict__ Bim, const u16* __restrict__ Cre,
                                             const u16* __restrict__ Cim, const u16* __restrict__ logdt, const u16* __restrict__ Dskip,
                                             const u16* __restrict__ w1, const u16* __restrict__ b1, const u16* __restrict__ w2,
                                             const u16* __restrict__ gw, char* __restrict__ ws, float* __restrict__ out) {
    __shared__ __attribute__((aligned(16))) char sm[KF_LDS];
    const int tid = threadIdx.x, lane = tid & 63;
    const int wave = __builtin_amdgcn_readfirstlane(tid >> 6);
    const int bid = blockIdx.x, xcd = bid & 7, idx = bid >> 3;
    const int rb = 8 * (xcd & 1) + (idx & 7);
    const int cb = 4 * (xcd >> 1) + (idx >> 3);
    const int RG = 8 * (xcd >> 1) + (idx & 7);
    const int CB = 4 * (xcd & 1) + (idx >> 3);
    const int t = lane & 15, q = lane >> 4;
    const unsigned smb = (unsigned)(size_t)(LAS char*)sm;
    if (wave < 5) {
        u64 pc0;
        asm volatile("s_getpc_b64 %0" : "=s"(pc0));
        glds4((const char*)(pc0 & ~255ull) + wave * 8192 + lane * 128, smb + K1_FM);
    }
    asm volatile("" :: "s"(x), "s"(Lre), "s"(Lim), "s"(Bre), "s"(Bim), "s"(Cre), "s"(Cim), "s"(logdt), "s"(Dskip), "s"(w1), "s"(b1), "s"(w2),
                 "s"(gw), "s"(ws), "s"(out));
    f32x4 ac2[4][5];
    u32x2 xv[3], dv[3];
    unsigned gl[4];
    int oo[3];
    auto run = [&](auto ROLE_) __attribute__((always_inline)) {
    constexpr int ROLE = decltype(ROLE_)::value;
    int lane = tid & 63;
    asm volatile("" : "+v"(lane));
    const int t = lane & 15, q = lane >> 4;
    u32x2 epv;
    asm volatile("global_load_dwordx2 %0, %1, off sc1" : "=v"(epv) : "v"(ws + WS_EP) : "memory");
#define G_M0(base, c) asm volatile("s_add_i32 m0, %0, %1" :: "s"(base), "n"(c) : "scc")
#define G_LDS(vo, sb, c) asm volatile("global_load_lds_dwordx4 %0, %1 offset:%2" :: "v"(vo), "s"(sb), "n"(c) : "memory")
#define G_LDV(va, c) asm volatile("global_load_lds_dwordx4 %0, off offset:%1" :: "v"(va), "n"(c) : "memory")
#define G_NOP() asm volatile("s_nop 0")
    const int l8 = lane >> 3;
    unsigned vox[4];
    const char* wsr[3];
    const int rot = ROTF;
    const char* w1s = (const char*)w1 + (size_t)rot * (64 * NF * 2);
    const char* xs = (const char*)x + rot * 128;
#define K_WRAP(st) (((st) == 2 || (st) == 5 || (st) == 7) && rot == 10 - (st))
#define W_NEXT(st)                                                                                \
    do {                                                                                          \
        w1s += K_WRAP(st) ? -9 * (64 * NF * 2) : 64 * NF * 2;                                     \
        if ((st) == 2 || (st) == 5 || (st) == 7) { const long d_ = K_WRAP(st) ? -1280 : 0; wsr[0] += d_; wsr[1] += d_; wsr[2] += d_; } \
    } while (0)
#define X_NEXT(st) do { if ((st) == 2 || (st) == 5 || (st) == 7) xs += K_WRAP(st) ? -1280 : 0; } while (0)
    const unsigned lw = smb + 1024 * (wave & 3);
    const unsigned lm = smb + (ROLE == 0 ? 3072 : 4096) * (wave & 3);
#define X_M0(st, i) G_M0(lm, BUF(st) + 1024 * (i) - 128 * (st))
#define X_LD(st, i) G_LDS(vox[i], xs, 128 * (st))
#define W_M0(st, j) G_M0(lw, BUF(st) + 16384 + 4096 * (j) - 128 * (st))
#define W_LD(st, j) G_LDV(wsr[j], 128 * (st))
#define M_M0(st, i) G_M0(lm, BUF(st) + 26624 + 1024 * (i))
#define M_LD(i) G_LDS(vox[i], w1s, 0)
    if constexpr (ROLE == 1) {
#pragma unroll
        for (int i = 0; i < 4; ++i) {
            const int p = 8 * i + l8;
            vox[i] = ((128 * rb + 32 * (wave & 3) + 8 * ((p & 15) >> 2) + 4 * (p >> 4) + (p & 3)) * DM + (((lane & 7) ^ ((p >> 1) & 7)) << 3)) * 2;
            if (i == 0) X_M0(0, 0); else if (i == 1) X_M0(0, 1); else if (i == 2) X_M0(0, 2); else X_M0(0, 3);
            G_NOP();
            G_LDS(vox[i], xs, 0);
            SCHED_FENCE();
        }
    } else {
#pragma unroll
        for (int j = 0; j < 3; ++j) {
            const int p = 8 * (4 * j + wave) + l8;
            const int ch = 40 * cb + 8 * (p >> 4) + ((p & 15) >> 1);
            wsr[j] = (const char*)(((p & 1) ? Bim : Bre) + ch * DM + 64 * rot + (((lane & 7) ^ ((p >> 1) & 7)) << 3));
            if (j < 2) {
                if (j == 0) W_M0(0, 0); else W_M0(0, 1);
                G_NOP();
                G_LDV(wsr[j], 0);
                SCHED_FENCE();
            }
        }
#pragma unroll
        for (int i = 0; i < 3; ++i) {
            const int gI = 64 * (3 * wave + i) + lane, row = gI / 12, cp = gI - 12 * row;
            int j = (cp >> 1) - ((row >> 3) & 1);
            j += j < 0 ? 6 : 0;
            vox[i] = (row * NF + 96 * cb + 16 * j + 8 * (cp & 1)) * 2;
            if (i == 0) M_M0(0, 0); else if (i == 1) M_M0(0, 1); else M_M0(0, 2);
            G_NOP();
            M_LD(i);
            SCHED_FENCE();
        }
        const u16* p = gw + lane * 8;
        if (wave == 3) {
            p = gw + 512 + t * 8;
            if (lane >= 16 && lane < 21) p = Lre + 40 * cb + (lane - 16) * 8;
            if (lane >= 21 && lane < 26) p = Lim + 40 * cb + (lane - 21) * 8;
            if (lane >= 26 && lane < 31) p = logdt + 40 * cb + (lane - 26) * 8;
        }
        glds16(wave < 2 ? wsr[2] : (const char*)p, wave < 2 ? lw + BUF(0) + 16384 + 8192 : smb + K1_GW + (wave - 2) * 1024);
    }
    const unsigned pu = 4 * bid + (wave & 3);
    char* ptl = sm + K1_PT + (wave & 3) * 4096;
    const int wc = wave & 3;
    const int xoff = (32 * wc + t) * 128;
    const int co0 = (q ^ ((t >> 1) & 7)) * 16, co1 = co0 ^ 64;
    char* a2 = ws + WS_A2 + (size_t)rb * NKS * 8192 + (2 * wc) * 1024 + lane * 16;
    const int rb2 = RG >> 1, hh = RG & 1;
    const int ks0 = ROLE == 0 ? 10 * wave : 40 + 12 * (wave - 4);
    const char* pa = ws + WS_A2 + ((size_t)rb2 * NKS + ks0) * 8192 + hh * 4096 + lane * 16;
    const char* pb = ws + WS_W2 + ((size_t)CB * NKS + ks0) * 5120 + 1024 + lane * 16;
    u64 tok;
    u32x4 ptf[4];
    const unsigned pkg = (pu * 1639u) >> 16, pv = pu - 400u, ptg = (pv * 1366u) >> 16;
    auto pretile_read = [&]() __attribute__((always_inline)) {
        if (pu < 400) {
#pragma unroll
            for (int i = 0; i < 4; ++i) {
                const char* pp = ptl + t * 128 + (((2 * i + (q >> 1)) ^ (t & 7)) << 4) + 8 * (q & 1);
                const u32x2 rr = *(const u32x2*)pp, ii = *(const u32x2*)(pp + 2048);
                ptf[i] = (u32x4){rr[0], rr[1], ii[0], ii[1]};
            }
        } else {
            const int r = 4 * q + (t >> 2);
#pragma unroll
            for (int i = 0; i < 4; ++i) {
                const char* pp = ptl + r * 128 + (((2 * i + ((lane >> 1) & 1)) ^ (r & 7)) << 4) + (lane & 1) * 8;
                const s16x4 lo = __builtin_amdgcn_ds_read_tr16_b64_v4i16((LAS s16x4*)pp);
                const s16x4 hi = __builtin_amdgcn_ds_read_tr16_b64_v4i16((LAS s16x4*)(pp + 2048));
                ptf[i] = __builtin_bit_cast(u32x4, __builtin_shufflevector(lo, hi, 0, 1, 2, 3, 4, 5, 6, 7));
            }
        }
    };
    auto pretile_mix = [&]() __attribute__((always_inline)) {
        if (pu < 400) {
#pragma unroll
            for (int i = 0; i < 4; ++i) {
                const unsigned r0 = ptf[i][0], r1 = ptf[i][1], i0 = ptf[i][2] ^ 0x80008000u, i1 = ptf[i][3] ^ 0x80008000u;
                ptf[i] = (u32x4){(r0 & 0xffffu) | (i0 << 16), (r0 >> 16) | (i0 & 0xffff0000u), (r1 & 0xffffu) | (i1 << 16), (r1 >> 16) | (i1 & 0xffff0000u)};
            }
        }
    };
    auto pretile_dst = [&](unsigned i) __attribute__((always_inline)) {
        const unsigned g = pu < 400 ? pu - 40 * pkg : 4 * ptg + i, KS = pu < 400 ? 4 * pkg + i : 40 + pv - 48 * ptg;
        const unsigned cB = (g * 13u) >> 6;
        unsigned o = (unsigned)WS_W2 + (cB * (5 * NKS - 5) + KS * 5 + g) * 1024u, j = (unsigned)WS_JUNK + (pu * 4 + i) * 1024u;
        asm("" : "+s"(o), "+s"(j));
        return pu < 880 ? o : j;
    };

    const int bco = wave == 6 ? 6 * 20480 + 4096 : wave * 20480;
    u32x4 Ab[4][4], Bb[3][5];
#define P2_L1(dst, P, OFF) asm volatile("global_load_dwordx4 %0, %1, off offset:" #OFF " sc1" : "=&v"(dst) : "v"(P) : "memory")
#define P2_LDB(slot, P)                                                                           \
    do { P2_L1(Bb[slot][0], P, -1024); P2_L1(Bb[slot][1], P, 0); P2_L1(Bb[slot][2], P, 1024); P2_L1(Bb[slot][3], P, 2048); P2_L1(Bb[slot][4], P, 3072); } while (0)
#define P2_LDA(slot, P)                                                                           \
    do { P2_L1(Ab[slot][0], P, 0); P2_L1(Ab[slot][1], P, 1024); P2_L1(Ab[slot][2], P, 2048); P2_L1(Ab[slot][3], P, 3072); } while (0)
    auto pt_check = [&]() __attribute__((always_inline)) {
        const u32x4 v0 = *(const u32x4*)(sm + K1_FM + lane * 16), v1 = *(const u32x4*)(sm + K1_FM + 1024 + lane * 16);
        const unsigned lo = (unsigned)tok, hi = (unsigned)(tok >> 32);
        if (!wave_all(v0[0] == lo && v0[1] == hi && v0[2] == lo && v0[3] == hi && v1[0] == lo && v1[1] == hi && v1[2] == lo && v1[3] == hi)) {
#pragma unroll 1
            for (int k = 0; k < 4; ++k) flag_wait(ws + WS_FPT + (size_t)(64 * k + lane) * 8, true, tok);
        }
    };
    const char* g3 = pb - 1024 + 3 * 5120;
#define P2_CACHE(i) glds16_c(g3 + (i) * 1024, smb + bco + (i) * 1024)
#define P2_M2(ua, ub, i)                                                                          \
    do {                                                                                          \
        ac2[(i) / 5][(i) % 5] = mfma16(__builtin_bit_cast(s16x8, Bb[ub][(i) % 5]), __builtin_bit_cast(s16x8, Ab[ua][(i) / 5]), ac2[(i) / 5][(i) % 5]); \
        ac2[((i) + 1) / 5][((i) + 1) % 5] = mfma16(__builtin_bit_cast(s16x8, Bb[ub][((i) + 1) % 5]), __builtin_bit_cast(s16x8, Ab[ua][((i) + 1) / 5]), ac2[((i) + 1) / 5][((i) + 1) % 5]); \
        SCHED_FENCE();                                                                            \
    } while (0)
#define P2_STEP(j, NS, WN, NL)                                                                    \
    do {                                                                                          \
        asm volatile("s_waitcnt vmcnt(%9)" : "+v"(Ab[(j) & 3][0]), "+v"(Ab[(j) & 3][1]), "+v"(Ab[(j) & 3][2]), "+v"(Ab[(j) & 3][3]), \
                     "+v"(Bb[(j) % 3][0]), "+v"(Bb[(j) % 3][1]), "+v"(Bb[(j) % 3][2]), "+v"(Bb[(j) % 3][3]), "+v"(Bb[(j) % 3][4]) : "n"(WN) : "memory"); \
        SCHED_FENCE();                                                                            \
        const char* pa_ = pa + ((j) + 3) * 8192;                                                  \
        const char* pb_ = pb + ((j) + 2) * 5120;                                                  \
        constexpr bool LA_ = (j) + 3 < (NS), LBG_ = (j) + 2 >= (NL) && (j) + 2 < (NS), LBL_ = (j) + 2 >= 3 && (j) + 2 < (NL); \
        P2_M2((j) & 3, (j) % 3, 0); if (LA_) { P2_L1(Ab[((j) + 3) & 3][0], pa_, 0); SCHED_FENCE(); }      \
        P2_M2((j) & 3, (j) % 3, 2); if (LA_) { P2_L1(Ab[((j) + 3) & 3][1], pa_, 1024); SCHED_FENCE(); }   \
        P2_M2((j) & 3, (j) % 3, 4); if (LA_) { P2_L1(Ab[((j) + 3) & 3][2], pa_, 2048); SCHED_FENCE(); }   \
        P2_M2((j) & 3, (j) % 3, 6); if (LA_) { P2_L1(Ab[((j) + 3) & 3][3], pa_, 3072); SCHED_FENCE(); }   \
        if (LBL_) { _Pragma("unroll") for (int n_ = 0; n_ < 5; ++n_) Bb[((j) + 2) % 3][n_] = *(const u32x4*)(sm + bco + (((j) + 2 - 3) * 5 + n_) * 1024 + lane * 16); SCHED_FENCE(); } \
        P2_M2((j) & 3, (j) % 3, 8); if (LBG_) { P2_L1(Bb[((j) + 2) % 3][0], pb_, -1024); SCHED_FENCE(); } \
        P2_M2((j) & 3, (j) % 3, 10); if (LBG_) { P2_L1(Bb[((j) + 2) % 3][1], pb_, 0); SCHED_FENCE(); }    \
        P2_M2((j) & 3, (j) % 3, 12); if (LBG_) { P2_L1(Bb[((j) + 2) % 3][2], pb_, 1024); SCHED_FENCE(); } \
        P2_M2((j) & 3, (j) % 3, 14); if (LBG_) { P2_L1(Bb[((j) + 2) % 3][3], pb_, 2048); SCHED_FENCE(); } \
        P2_M2((j) & 3, (j) % 3, 16); if (LBG_) { P2_L1(Bb[((j) + 2) % 3][4], pb_, 3072); SCHED_FENCE(); } \
        P2_M2((j) & 3, (j) % 3, 18);                                                              \
    } while (0)
    u64 pl[3];
#define PSL "4"
#define POLL1(i) "s_waitcnt vmcnt(2)\n\tv_cmp_ne_u64 vcc, %" #i ", %5\n\ts_cmp_eq_u64 vcc, 0\n\ts_cbranch_scc1 2f\n\t" \
                 "global_load_dwordx2 %" #i ", %4, off sc1\n\ts_sleep " PSL "\n\t"
#define P2_POLLED() asm volatile("" : "+v"(xv[0]), "+v"(xv[1]), "+v"(xv[2]), "+v"(dv[0]), "+v"(dv[1]), "+v"(dv[2]) : "v"(pl[0]), "v"(pl[1]), "v"(pl[2]))
#define P2_BEGIN(role)                                                                            \
    do {                                                                                          \
        WAIT_VM(0);                                                                               \
                                                                                           \
        if (lane == 0) flag_set(ws + WS_FP1 + (size_t)(((((role) * 16 + rb) * 2 + (wc >> 1)) * 16 + cb) * 2 + (wc & 1)) * 8, tok); \
        if ((role) == 0) { _Pragma("unroll") for (int i = 0; i < 5; ++i) glds16x4_c(g3 + i * 4096, __builtin_amdgcn_readfirstlane(smb + bco + i * 4096)); } \
        _Pragma("unroll") for (int m = 0; m < 3; ++m) {                                           \
            int tile = wave + 8 * m; tile = tile < 20 ? tile : wave;                              \
            const int r = tile / 5, n = tile - 5 * r;                                             \
            const int col = 80 * CB + 16 * n + 4 * q;                                             \
            oo[m] = (64 * RG + 32 * (r >> 1) + 8 * (t >> 2) + 4 * (r & 1) + (t & 3)) * DM + col;  \
            xv[m] = ld64_asm(x + oo[m]); dv[m] = ld64_asm(Dskip + col);                           \
        }                                                                                         \
        unsigned cnt_;                                         \
        asm volatile("global_load_dwordx2 %0, %4, off sc1\n\ts_sleep " PSL "\n\tglobal_load_dwordx2 %1, %4, off sc1\n\ts_sleep " PSL "\n\t" \
                     "global_load_dwordx2 %2, %4, off sc1\n\ts_mov_b32 %3, 0x80000\n"             \
                     "1:\n\t" POLL1(0) POLL1(1) POLL1(2)                                          \
                     "s_sub_u32 %3, %3, 1\n\ts_cbranch_scc0 1b\n"                                 \
                     "2:"                                                                         \
                     : "=&v"(pl[0]), "=&v"(pl[1]), "=&v"(pl[2]), "=&s"(cnt_)                      \
                     : "v"(ws + WS_FP1 + (size_t)(((role) * 32 + RG) * 32 + 8 * wc + (lane & 7)) * 8), "s"(tok) : "memory", "vcc", "scc"); \
        if ((role) == 0) P2_GATES();                                                              \
        P2_LDA(0, pa); P2_LDA(1, pa + 8192); P2_LDA(2, pa + 16384);                               \
        _Pragma("unroll") for (int r = 0; r < 4; ++r) _Pragma("unroll") for (int n = 0; n < 5; ++n) ac2[r][n] = (f32x4){0.f, 0.f, 0.f, 0.f}; \
    } while (0)
#define P2_GATES()                                                                                \
    _Pragma("unroll") for (int j_ = 0; j_ < 4; ++j_)                                              \
        asm volatile("global_load_dword %0, %1, off sc1" : "=&v"(gl[j_])                          \
                     : "v"(ws + WS_G + ((4 * wc + j_ < 10 ? 4 * wc + j_ : 9) * 2048 + 64 * RG + lane) * 4) : "memory")

    if constexpr (ROLE == 0) {
        f32x4 acc[6][2];
#pragma unroll
        for (int a = 0; a < 6; ++a) { acc[a][0] = (f32x4){0.f, 0.f, 0.f, 0.f}; acc[a][1] = (f32x4){0.f, 0.f, 0.f, 0.f}; }
        s16x8 fx[2][2], fw[2][5];
        const int woff = 16384 + t * 128;
        const int itg = cb >= 10 ? -1 : cb >= rot ? cb - rot : cb + 10 - rot;
#define S_RX(b, sb, co) do { fx[b][0] = *(const s16x8*)(sm + (sb) + xoff + (co)); fx[b][1] = *(const s16x8*)(sm + (sb) + xoff + 2048 + (co)); } while (0)
#define S_RW(b, sb, co, T) fw[b][T] = *(const s16x8*)(sm + (sb) + woff + (T) * 2048 + (co))
#define S_MM(b, T) do { acc[T][0] = mfma16(fx[b][0], fw[b][T], acc[T][0]); acc[T][1] = mfma16(fx[b][1], fw[b][T], acc[T][1]); SCHED_FENCE(); } while (0)
#define S_LB(slot, T) asm volatile("global_load_dwordx4 %0, %1, off sc1" : "=&v"(Bb[slot][T]) : "v"(pb + (slot) * 5120 + ((T) - 1) * 1024) : "memory")
#define S_PT(it, i) do { if ((it) == 1) asm volatile("global_store_dwordx4 %0, %1, %2 sc1\n\ts_nop 1" :: "v"(pto[i]), "v"(ptf[i]), "s"(ws) : "memory"); } while (0)
#define S_BODY(it, WN, XA0, XA1, XA2, XA3)                                                        \
    do {                                                                                          \
        constexpr bool RF_ = (it) < 7, RN_ = (it) < 9;                                            \
        constexpr int SB_ = (it) < 7 ? 0 : (it) - 7;                                              \
        S_MM(0, 0); S_RX(1, BUF(it), co1); S_RW(1, BUF(it), co1, 0); SCHED_FENCE();               \
        S_MM(0, 1); S_RW(1, BUF(it), co1, 1); S_RW(1, BUF(it), co1, 2); XA0; SCHED_FENCE();       \
        S_MM(0, 2); S_RW(1, BUF(it), co1, 3); S_RW(1, BUF(it), co1, 4); XA1; SCHED_FENCE();       \
        S_MM(0, 3); XA2; SCHED_FENCE();                                                           \
        S_MM(0, 4); XA3; SCHED_FENCE();                                                           \
        if ((it) == 0 && wave < 2) WAIT_VM(6); else if (RN_) WAIT_VM(WN);                         \
        WAIT_LGKM0();                                                                             \
        __builtin_amdgcn_s_barrier();                                                             \
        asm volatile("" ::: "memory");                                                            \
        SCHED_FENCE();                                                                            \
        S_MM(1, 0);                                                                               \
        if (__builtin_expect((it) == itg, 0)) {                                                   \
            const s16x8 g0_ = *(const s16x8*)(sm + K1_GW + (64 * cb + 8 * q) * 2), g1_ = *(const s16x8*)(sm + K1_GW + (64 * cb + 32 + 8 * q) * 2); \
            acc[5][0] = mfma16(fx[1][0], g1_, mfma16(fx[0][0], g0_, acc[5][0])); acc[5][1] = mfma16(fx[1][1], g1_, mfma16(fx[0][1], g0_, acc[5][1])); \
        }                                                                                         \
        if ((it) == 1) pretile_read();                                                            \
        if ((it) == 7) pt_check();                                                                \
        if (RF_) {                                                                                \
            W_NEXT((it) + 3);                                                                     \
            if (wave < 2) { W_M0((it) + 3, 2); G_NOP(); W_LD((it) + 3, 2); }                      \
            else if ((it) == 3) { if (lane == 0) flag_set(ws + WS_FPT + (size_t)bid * 8, tok); }    \
            else if ((it) == 6) glds16_c(ws + WS_FPT + (wave - 2) * 1024 + lane * 16, smb + K1_FM + (wave - 2) * 1024); \
            W_M0((it) + 3, 0);                                                                    \
        }                                                                                         \
        if (RN_) { S_RX(0, BUF((it) + 1), co0); S_RW(0, BUF((it) + 1), co0, 0); }                 \
        SCHED_FENCE();                                                                            \
        if (RF_) { W_LD((it) + 3, 0); W_M0((it) + 3, 1); } else S_LB(SB_, 0);                     \
        SCHED_FENCE();                                                                            \
        S_MM(1, 1); if (RN_) { S_RW(0, BUF((it) + 1), co0, 1); S_RW(0, BUF((it) + 1), co0, 2); }  \
        SCHED_FENCE();                                                                            \
        if (RF_) { W_LD((it) + 3, 1); M_M0((it) + 3, 0); } else S_LB(SB_, 1);                     \
        if ((it) == 1) pretile_mix();                                                             \
        S_PT(it, 0); SCHED_FENCE();                                                               \
        S_MM(1, 2); if (RN_) { S_RW(0, BUF((it) + 1), co0, 3); S_RW(0, BUF((it) + 1), co0, 4); }  \
        SCHED_FENCE();                                                                            \
        if (RF_) { M_LD(0); M_M0((it) + 3, 1); } else S_LB(SB_, 2);                               \
        S_PT(it, 1); SCHED_FENCE();                                                               \
        S_MM(1, 3);                                                                               \
        if (RF_) { M_LD(1); M_M0((it) + 3, 2); } else S_LB(SB_, 3);                               \
        S_PT(it, 2); SCHED_FENCE();                                                               \
        S_MM(1, 4);                                                                               \
        if (RF_) M_LD(2); else S_LB(SB_, 4);                                                      \
        S_PT(it, 3); SCHED_FENCE();                                                               \
    } while (0)
#define S_STAGE(st)                                                                               \
    do {                                                                                          \
        W_NEXT(st);                                                                               \
        if (wave < 2) { W_M0(st, 2); G_NOP(); W_LD(st, 2); }                                      \
        W_M0(st, 0); G_NOP(); W_LD(st, 0); W_M0(st, 1); G_NOP(); W_LD(st, 1);                     \
        M_M0(st, 0); G_NOP(); M_LD(0); M_M0(st, 1); G_NOP(); M_LD(1); M_M0(st, 2); G_NOP(); M_LD(2); \
    } while (0)
        S_STAGE(1);
        if (wave == 0 && lane == 0) *(u32x4*)(sm + K1_CNT) = (u32x4){0u, 0u, 0u, 0u};
        unsigned pto[4];
#pragma unroll
        for (int i = 0; i < 4; ++i) pto[i] = pretile_dst(i) + lane * 16;
        asm volatile("" : "+v"(a2), "+v"(pa), "+v"(pb), "+v"(pto[0]), "+v"(pto[1]), "+v"(pto[2]), "+v"(pto[3]));
        if (wave < 2) WAIT_VM(6); else WAIT_VM(5);
        asm volatile("" : "+v"(epv) :: "memory");
        WAIT_LGKM0();
        __builtin_amdgcn_s_barrier();
        asm volatile("" ::: "memory");
        S_RX(0, BUF(0), co0); S_RW(0, BUF(0), co0, 0); S_RW(0, BUF(0), co0, 1); S_RW(0, BUF(0), co0, 2); S_RW(0, BUF(0), co0, 3); S_RW(0, BUF(0), co0, 4);
        tok = (((u64)(unsigned)__builtin_amdgcn_readfirstlane(epv[1]) << 32) | (unsigned)__builtin_amdgcn_readfirstlane(epv[0])) + 1ull;
        const int cl = lane < 40 ? lane : 39;
        float lr, li, za, zb;
        f32x2 pA, pB;
#define TB0() do { lr = bflo(*(const u16*)(sm + K1_PLR + cl * 2)); li = bflo(*(const u16*)(sm + K1_PLI + cl * 2)); za = bflo(*(const u16*)(sm + K1_PLD + cl * 2)); } while (0)
#define TB1() do { const float dt = __builtin_amdgcn_exp2f(za * LOG2E); za = dt * lr * LOG2E; zb = dt * li * INV2PI;                   \
                   const float nA = wc == 1 ? 8.f : wc == 2 ? 24.f : 1.f;                                                             \
                   const float mA = __builtin_amdgcn_exp2f(nA * za), rA = __builtin_amdgcn_fractf(nA * zb);                           \
                   pA = (f32x2){mA * __builtin_amdgcn_cosf(rA), mA * __builtin_amdgcn_sinf(rA)}; } while (0)
#define TB2() do { const float nB = wc == 1 ? 16.f : wc == 2 ? 32.f : 0.f;                                                            \
                   const float mB = __builtin_amdgcn_exp2f(nB * za), rB = __builtin_amdgcn_fractf(nB * zb);                           \
                   pB = (f32x2){mB * __builtin_amdgcn_cosf(rB), mB * __builtin_amdgcn_sinf(rB)}; } while (0)
#define TB3() do { const float nr = pA.x - 1.0f, ni = pA.y, rden = 1.0f / (lr * lr + li * li);                                        \
                   const float z8 = __builtin_amdgcn_fractf(8.f * zb);                                                                \
                   if (wc == 3) pA = (f32x2){(nr * lr + ni * li) * rden, (ni * lr - nr * li) * rden};                 \
                   f32x4* tb = (f32x4*)(sm + K1_TAB + cl * 16);                                              \
                   tb[wc == 0 ? 0 : wc == 1 ? 120 : wc == 2 ? 200 : 40] = (f32x4){pA.x, -pA.y, pA.x, pA.y};                           \
                   tb[wc == 0 ? 80 : wc == 1 ? 160 : wc == 2 ? 240 : 280] = wc == 3 ? (f32x4){8.f * za, z8, 8.f * za, z8} : (f32x4){pB.x, -pB.y, pB.x, pB.y}; } while (0)
#define SA0() do { W_NEXT(2); if (wave < 2) { W_M0(2, 2); G_NOP(); W_LD(2, 2); } W_M0(2, 0); G_NOP(); W_LD(2, 0); W_M0(2, 1); } while (0)
#define SA1() do { SCHED_FENCE(); W_LD(2, 1); M_M0(2, 0); G_NOP(); M_LD(0); M_M0(2, 1); } while (0)
#define SA2() do { M_LD(1); M_M0(2, 2); } while (0)
        S_BODY(0, 5, SA0(), SA1(), SA2(), M_LD(2));   S_BODY(1, 5, , , , );   S_BODY(2, 9, , , , );   S_BODY(3, 5, TB0(), , TB1(), );
        S_BODY(4, 6, , TB2(), , );   S_BODY(5, 5, , TB3(), , );   S_BODY(6, 5, , , , );
        S_BODY(7, 5, , , , );   S_BODY(8, 5, , , , );
        const f32x2* tab = (const f32x2*)(sm + K1_TAB) + t;
        f32x2 Lc[5], Sc[5];
#pragma unroll
        for (int T = 0; T < 5; ++T) { Lc[T] = tab[16 * T]; Sc[T] = tab[(5 - q) * 80 + 16 * T]; }
        const f32x2 L32 = *(const f32x2*)(sm + K1_TAB + (6 * 80 + 2 * cl + 1) * 8);
        S_BODY(9, 0, , , , );
        __builtin_amdgcn_s_setprio(3);
        float ag8[5];
#pragma unroll
        for (int T = 0; T < 5; ++T) ag8[T] = acc[T][0][0];
#pragma unroll
        for (int j = 1; j < 8; ++j)
#pragma unroll
            for (int T = 0; T < 5; ++T) ag8[T] = fmaf(Lc[T].y, swp(ag8[T]), fmaf(Lc[T].x, ag8[T], acc[T][j >> 2][j & 3]));
        {
            unsigned sv[5];
#pragma unroll
            for (int T = 0; T < 5; ++T) sv[T] = f2u(fmaf(Sc[T].y, swp(ag8[T]), Sc[T].x * ag8[T]));
            const auto r01 = __builtin_amdgcn_permlane32_swap(sv[0], sv[1], false, false);
            const auto r23 = __builtin_amdgcn_permlane32_swap(sv[2], sv[3], false, false);
            const auto r44 = __builtin_amdgcn_permlane32_swap(sv[4], sv[4], false, false);
            const unsigned p4 = f2u(u2f(r44[0]) + u2f(r44[1]));
            const auto ra = __builtin_amdgcn_permlane16_swap(f2u(u2f(r01[0]) + u2f(r01[1])), f2u(u2f(r23[0]) + u2f(r23[1])), false, false);
            const auto rb4 = __builtin_amdgcn_permlane16_swap(p4, p4, false, false);
            float* xa = (float*)(sm + K1_XCH) + wc * 80 + t;
            xa[16 * (((q & 1) << 1) | (q >> 1))] = u2f(ra[0]) + u2f(ra[1]);
            xa[64] = u2f(rb4[0]) + u2f(rb4[1]);
        }
        asm volatile("" ::: "memory");
        unsigned old = 0;
        if (lane == 0) old = __hip_atomic_fetch_add((LAS unsigned*)(sm + K1_CNT), 1u, __ATOMIC_RELAXED, __HIP_MEMORY_SCOPE_WORKGROUP);
        old = __builtin_amdgcn_readfirstlane(old);
        asm volatile("" ::: "memory");
        if (old == 3) {
            const f32x2* xc = (const f32x2*)(sm + K1_XCH) + cl;
            const f32x2 s = cfma(L32, cfma(L32, cfma(L32, xc[0], xc[40]), xc[80]), xc[120]);
            if (lane < 40) st128_wt(ws + WS_AGG + (size_t)(cb * 16 + rb) * 640 + lane * 16,
                                    (u32x4){f2u(s.x), f2u(s.y), (unsigned)tok, (unsigned)(tok >> 32)});
        }
        SCHED_FENCE();
        char* own = sm + wave * 20480 + 2048;
#pragma unroll
        for (int T = 0; T < 5; ++T) *(float*)(own + 8192 + (16 * T + t) * 16 + q * 4) = ag8[T];
        if (cb < 10 && t < 2) {
            const f32x4 gv = t ? acc[5][1] : acc[5][0];
            st128_wt(ws + WS_G + (cb * 2048 + 128 * rb + 32 * wc + 8 * q + 4 * t) * 4, __builtin_bit_cast(u32x4, gv));
        }
        f32x2 Wj[4];
        {
            const f32x2 zz = *(const f32x2*)(sm + K1_TAB + (7 * 80 + 2 * cl) * 8);
            const float za = 16.f * zz.x, zb = __builtin_amdgcn_fractf(16.f * zz.y);
#pragma unroll
            for (int j = 0; j < 4; ++j) {
                const int n = rb - 1 - 4 * wc - j;
                const float fn = (float)(n < 0 ? 0 : n);
                const float mg = n < 0 ? 0.f : __builtin_amdgcn_exp2f(fn * za), rv = fn * zb;
                Wj[j] = (f32x2){mg * __builtin_amdgcn_cosf(rv), mg * __builtin_amdgcn_sinf(rv)};
            }
        }
        float pre[5], Ma[5], Mb[5];
        {
            f32x2 Cc[5], L8c[5];
            const float fs = (float)(4 * wc + q), qr = (t & 1) ? -0.25f : 0.f, sg = (t & 1) ? 1.f : -1.f;
#pragma unroll
            for (int T = 0; T < 5; ++T) {
                Cc[T] = tab[80 + 16 * T]; L8c[T] = tab[240 + 16 * T];
                const f32x2 zz = tab[560 + 16 * T];
                const float e = __builtin_amdgcn_exp2f(fs * zz.x) * __builtin_amdgcn_cosf(fmaf(fs, zz.y, qr));
                Ma[T] = fmaf(Cc[T].y, swp(e), Cc[T].x * e);
                Mb[T] = sg * swp(Ma[T]);
#pragma unroll
                for (int j = 0; j < 8; ++j) {
                    const float u = acc[T][j >> 2][j & 3];
                    acc[T][j >> 2][j & 3] = fmaf(Cc[T].y, swp(u), Cc[T].x * u);
                }
                pre[T] = 0.f;
            }
            {
                for (unsigned spins = 0; spins < (1u << 22); ++spins) {
                    if (__hip_atomic_load((LAS unsigned*)(sm + K1_CNT), __ATOMIC_RELAXED, __HIP_MEMORY_SCOPE_WORKGROUP) >= 4u) break;
                    __builtin_amdgcn_s_sleep(1);
                }
                asm volatile("" ::: "memory");
                const float* xa = (const float*)(sm + K1_XCH) + t;
#pragma unroll 1
                for (int w = 0; w < wc; ++w)
#pragma unroll
                    for (int T = 0; T < 5; ++T) {
                        const f32x2 z = tab[480 + 16 * T];
                        pre[T] = fmaf(z.y, swp(pre[T]), fmaf(z.x, pre[T], xa[w * 80 + 16 * T]));
                    }
            }
#pragma unroll
            for (int T = 0; T < 5; ++T) {
                const f32x4 xs = *(const f32x4*)(own + 8192 + (16 * T + t) * 16);
#pragma unroll
                for (int k = 0; k < 3; ++k) {
                    const float nx = fmaf(L8c[T].y, swp(pre[T]), fmaf(L8c[T].x, pre[T], xs[k]));
                    pre[T] = k < q ? nx : pre[T];
                }
                pre[T] = fmaf(Cc[T].y, swp(pre[T]), Cc[T].x * pre[T]);
            }
        }
#pragma unroll
        for (int T = 0; T < 5; ++T) asm volatile("" : "+v"(acc[T][0]), "+v"(acc[T][1]), "+v"(pre[T]), "+v"(Ma[T]), "+v"(Mb[T]));
        asm volatile("" : "+v"(Wj[0]), "+v"(Wj[1]), "+v"(Wj[2]), "+v"(Wj[3]));
        SCHED_FENCE();
        if (rb > 0) {
            f32x2 cy = {0.f, 0.f};
            if (4 * wc < rb) {
                const char* agp = ws + WS_AGG + (size_t)(cb * 16 + 4 * wc) * 640 + cl * 16;
                for (unsigned spins = 0; spins < (1u << 20); ++spins) {
                    u32x4 ag[4];
#pragma unroll
                    for (int j = 0; j < 4; ++j)
                        asm volatile("global_load_dwordx4 %0, %1, off sc1" : "=v"(ag[j]) : "v"(agp + (4 * wc + j < rb ? j : 0) * 640) : "memory");
                    asm volatile("s_waitcnt vmcnt(0)" : "+v"(ag[0]), "+v"(ag[1]), "+v"(ag[2]), "+v"(ag[3]) :: "memory");
                    bool ok = true;
#pragma unroll
                    for (int j = 0; j < 4; ++j) ok = ok && ag[j][2] == (unsigned)tok && ag[j][3] == (unsigned)(tok >> 32);
                    const f32x2 c0 = cfma(Wj[1], (f32x2){u2f(ag[1][0]), u2f(ag[1][1])}, cmul_pk(Wj[0], (f32x2){u2f(ag[0][0]), u2f(ag[0][1])}));
                    const f32x2 c1 = cfma(Wj[3], (f32x2){u2f(ag[3][0]), u2f(ag[3][1])}, cmul_pk(Wj[2], (f32x2){u2f(ag[2][0]), u2f(ag[2][1])}));
                    cy = (f32x2){c0.x + c1.x, c0.y + c1.y};
                    if (wave_all(ok)) break;
                    __builtin_amdgcn_s_sleep(4);
                }
            }
            if (lane < 40) *(f32x2*)(sm + K1_PRT + (lane * 4 + wc) * 8) = cy;
            asm volatile("" ::: "memory");
            if (lane == 0) __hip_atomic_fetch_add((LAS unsigned*)(sm + K1_CNT + 4), 1u, __ATOMIC_RELAXED, __HIP_MEMORY_SCOPE_WORKGROUP);
            for (unsigned spins = 0; spins < (1u << 22); ++spins) {
                if (__hip_atomic_load((LAS unsigned*)(sm + K1_CNT + 4), __ATOMIC_RELAXED, __HIP_MEMORY_SCOPE_WORKGROUP) >= 4u) break;
                __builtin_amdgcn_s_sleep(1);
            }
            asm volatile("" ::: "memory");
#pragma unroll
            for (int T = 0; T < 5; ++T) {
                const f32x4* pp = (const f32x4*)(sm + K1_PRT + (8 * T + (t >> 1)) * 32);
                const f32x4 p0 = pp[0], p1 = pp[1];
                pre[T] = fmaf(Mb[T], (p0[1] + p0[3]) + (p1[1] + p1[3]), fmaf(Ma[T], (p0[0] + p0[2]) + (p1[0] + p1[2]), pre[T]));
            }
        }
#pragma unroll
        for (int j = 0; j < 8; ++j)
#pragma unroll
            for (int T = 0; T < 5; ++T) {
                pre[T] = fmaf(Lc[T].y, swp(pre[T]), fmaf(Lc[T].x, pre[T], acc[T][j >> 2][j & 3]));
                acc[T][j >> 2][j & 3] = pre[T];
            }
#pragma unroll
        for (int T = 0; T < 5; ++T)
            *(u32x4*)(own + (16 * T + t) * 80 + q * 16) = (u32x4){pack2(acc[T][0][0], acc[T][0][1]), pack2(acc[T][0][2], acc[T][0][3]),
                                                                  pack2(acc[T][1][0], acc[T][1][1]), pack2(acc[T][1][2], acc[T][1][3])};
        asm volatile("" ::: "memory");
        {
            const int od = cb & 1;
            char* dst = a2 + (size_t)(((5 * cb) >> 1) + od) * 8192;
            const char* rp = own + (16 * od + 8 * q + (t >> 2)) * 80 + (t & 3) * 16;
#pragma unroll
            for (int m = 0; m < 2; ++m)
#pragma unroll
                for (int rt = 0; rt < 2; ++rt) {
                    const s16x4 lo = __builtin_amdgcn_ds_read_tr16_b64_v4i16((LAS s16x4*)(rp + m * 2560 + rt * 8));
                    const s16x4 hi = __builtin_amdgcn_ds_read_tr16_b64_v4i16((LAS s16x4*)(rp + m * 2560 + rt * 8 + 320));
                    st128_wt(dst + m * 8192 + rt * 1024, __builtin_bit_cast(u32x4, __builtin_shufflevector(lo, hi, 0, 1, 2, 3, 4, 5, 6, 7)));
                }
            const char* rl = own + ((od ? 0 : 64) + 8 * (q & 1) + (t >> 2)) * 80 + (t & 3) * 16 + (q >> 1) * 8;
            const s16x4 lo = __builtin_amdgcn_ds_read_tr16_b64_v4i16((LAS s16x4*)rl);
            const s16x4 hi = __builtin_amdgcn_ds_read_tr16_b64_v4i16((LAS s16x4*)(rl + 320));
            st128_wt(dst + (od ? -8192 + 512 : 16384) + (q >> 1) * 512, __builtin_bit_cast(u32x4, __builtin_shufflevector(lo, hi, 0, 1, 2, 3, 4, 5, 6, 7)));
        }
        WAIT_LGKM0();
        if (lane == 0) __hip_atomic_fetch_add((LAS unsigned*)(sm + K1_CNT + 8), 1u, __ATOMIC_RELAXED, __HIP_MEMORY_SCOPE_WORKGROUP);
        __builtin_amdgcn_s_setprio(0);
        P2_BEGIN(0);
        P2_STEP(0, 10, 8, 7); P2_POLLED(); P2_STEP(1, 10, 8, 7); P2_STEP(2, 10, 8, 7); P2_STEP(3, 10, 8, 7); P2_STEP(4, 10, 8, 7);
        P2_STEP(5, 10, 8, 7); P2_STEP(6, 10, 13, 7); P2_STEP(7, 10, 9, 7); P2_STEP(8, 10, 5, 7); P2_STEP(9, 10, 0, 7);
        asm volatile("" : "+v"(gl[0]), "+v"(gl[1]), "+v"(gl[2]), "+v"(gl[3]) :: "memory");
    } else {
        const int bb = q & 1;
        const int trb = 26624 + (8 * q + (t >> 2)) * 192 + (lane & 3) * 8;
        const int tra = trb + bb * 32;
        const int tr5 = trb + (bb ? 0 : 160);
        f32x4 acc[6][2];
        s16x8 fx[2][2], fw[2][6];
#define M_RW(b, sb, ks, j)                                                                        \
    do {                                                                                          \
        const char* pa_ = sm + (sb) + ((j) < 5 ? tra + (j) * 32 : tr5) + (ks) * 32 * 192;         \
        const s16x4 lo_ = __builtin_amdgcn_ds_read_tr16_b64_v4i16((LAS s16x4*)pa_);               \
        const s16x4 hi_ = __builtin_amdgcn_ds_read_tr16_b64_v4i16((LAS s16x4*)(pa_ + 4 * 192));   \
        fw[b][j] = __builtin_shufflevector(lo_, hi_, 0, 1, 2, 3, 4, 5, 6, 7);                     \
    } while (0)
#define M_MM(b, j) do { acc[j][0] = mfma16(fw[b][j], fx[b][0], acc[j][0]); acc[j][1] = mfma16(fw[b][j], fx[b][1], acc[j][1]); SCHED_FENCE(); } while (0)
#define M_BODY(it, WN)                                                                            \
    do {                                                                                          \
        constexpr bool RF_ = (it) < 8, RN_ = (it) < 9;                                            \
        constexpr int SB_ = (it) < 8 ? 0 : (it) - 7;                                              \
        M_MM(0, 0); if (RF_) { X_NEXT((it) + 2); X_M0((it) + 2, 0); } S_RX(1, BUF(it), co1); M_RW(1, BUF(it), 1, 0); SCHED_FENCE(); \
        if (RF_) { X_LD((it) + 2, 0); X_M0((it) + 2, 1); } else S_LB(SB_, 0);                     \
        SCHED_FENCE();                                                                            \
        M_MM(0, 1); M_RW(1, BUF(it), 1, 1); M_RW(1, BUF(it), 1, 2); SCHED_FENCE();                \
        if (RF_) { X_LD((it) + 2, 1); X_M0((it) + 2, 2); } else S_LB(SB_, 1);                     \
        SCHED_FENCE();                                                                            \
        M_MM(0, 2); M_RW(1, BUF(it), 1, 3); M_RW(1, BUF(it), 1, 4); SCHED_FENCE();                \
        if (RF_) { X_LD((it) + 2, 2); X_M0((it) + 2, 3); } else S_LB(SB_, 2);                     \
        SCHED_FENCE();                                                                            \
        M_MM(0, 3); M_RW(1, BUF(it), 1, 5); SCHED_FENCE();                                        \
        if (RF_) X_LD((it) + 2, 3); else S_LB(SB_, 3);                                            \
        SCHED_FENCE();                                                                            \
        M_MM(0, 4); if (!RF_) S_LB(SB_, 4);                                                       \
        SCHED_FENCE();                                                                            \
        M_MM(0, 5);                                                                               \
        if (RN_) WAIT_VM(WN);                                                                     \
        WAIT_LGKM0();                                                                             \
        __builtin_amdgcn_s_barrier();                                                             \
        asm volatile("" ::: "memory");                                                            \
        SCHED_FENCE();                                                                            \
        M_MM(1, 0); if (RN_) { S_RX(0, BUF((it) + 1), co0); M_RW(0, BUF((it) + 1), 0, 0); }       \
        if ((it) == 7) { pt_check(); S_LB(0, 0); }                                                \
        SCHED_FENCE();                                                                            \
        M_MM(1, 1); if (RN_) { M_RW(0, BUF((it) + 1), 0, 1); M_RW(0, BUF((it) + 1), 0, 2); } if ((it) == 7) S_LB(0, 1); SCHED_FENCE(); \
        M_MM(1, 2); if (RN_) { M_RW(0, BUF((it) + 1), 0, 3); M_RW(0, BUF((it) + 1), 0, 4); } if ((it) == 7) S_LB(0, 2); SCHED_FENCE(); \
        M_MM(1, 3); if (RN_) M_RW(0, BUF((it) + 1), 0, 5); if ((it) == 7) S_LB(0, 3); SCHED_FENCE(); \
        M_MM(1, 4); if ((it) == 7) S_LB(0, 4); SCHED_FENCE();                                     \
        M_MM(1, 5);                                                                               \
    } while (0)
        u32x2 bp[6];
        asm volatile("global_load_dwordx2 %0, %6, off\n\tglobal_load_dwordx2 %1, %6, off offset:32\n\tglobal_load_dwordx2 %2, %6, off offset:64\n\t"
                     "global_load_dwordx2 %3, %6, off offset:96\n\tglobal_load_dwordx2 %4, %6, off offset:128\n\tglobal_load_dwordx2 %5, %6, off offset:160"
                     : "=&v"(bp[0]), "=&v"(bp[1]), "=&v"(bp[2]), "=&v"(bp[3]), "=&v"(bp[4]), "=&v"(bp[5]) : "v"(b1 + 96 * cb + 4 * q) : "memory");
        X_M0(1, 0); G_NOP(); X_LD(1, 0); X_M0(1, 1); G_NOP(); X_LD(1, 1); X_M0(1, 2); G_NOP(); X_LD(1, 2); X_M0(1, 3); G_NOP(); X_LD(1, 3);
        {
            const unsigned u = pu < 880 ? pu : 0;
            const int cx = ((lane & 7) ^ l8) * 8;
#pragma unroll
            for (int i = 0; i < 4; ++i) {
                const u16* p0;
                if (u < 400) { const int kg = (u * 1639u) >> 16, nt = u - 40 * kg; p0 = ((i >> 1) ? Cim : Cre) + (16 * nt + 8 * (i & 1) + l8) * NP + 64 * kg + cx; }
                else { const int tg = ptg, ks = pv - 48 * tg; p0 = w2 + (32 * ks + 8 * i + l8) * DM + 64 * tg + cx; }
                glds16(p0, smb + K1_PT + wc * 4096 + i * 1024);
            }
        }
        asm volatile("" : "+v"(a2), "+v"(pa), "+v"(pb));
        asm volatile("s_waitcnt vmcnt(8)" : "+v"(epv), "+v"(bp[0]), "+v"(bp[1]), "+v"(bp[2]), "+v"(bp[3]), "+v"(bp[4]), "+v"(bp[5]) :: "memory");
#pragma unroll
        for (int j = 0; j < 6; ++j) {
            acc[j][0] = (f32x4){bflo(bp[j][0]), bfhi(bp[j][0]), bflo(bp[j][1]), bfhi(bp[j][1])};
            asm volatile("" : "+v"(acc[j][0]));
        }
        tok = (((u64)(unsigned)__builtin_amdgcn_readfirstlane(epv[1]) << 32) | (unsigned)__builtin_amdgcn_readfirstlane(epv[0])) + 1ull;
        __builtin_amdgcn_s_barrier();
        asm volatile("" ::: "memory");
        S_RX(0, BUF(0), co0); M_RW(0, BUF(0), 0, 0); M_RW(0, BUF(0), 0, 1); M_RW(0, BUF(0), 0, 2); M_RW(0, BUF(0), 0, 3); M_RW(0, BUF(0), 0, 4); M_RW(0, BUF(0), 0, 5);
#pragma unroll
        for (int j = 0; j < 6; ++j) acc[j][1] = acc[j][0];
        M_BODY(0, 8);   M_BODY(1, 4);   M_BODY(2, 4);   M_BODY(3, 4);   M_BODY(4, 4);   M_BODY(5, 4);   M_BODY(6, 4);
        M_BODY(7, 4);   M_BODY(8, 10);   M_BODY(9, 0);
#pragma unroll
        for (int v = 0; v < 3; ++v) {
#pragma unroll
            for (int rt = 0; rt < 2; ++rt) {
                u32x4 o;
                if (2 * v + rt < 5) { P2_CACHE(3 * (2 * v + rt)); P2_CACHE(3 * (2 * v + rt) + 1); P2_CACHE(3 * (2 * v + rt) + 2); }
#pragma unroll
                for (int jj = 0; jj < 2; ++jj) {
                    float r[4];
#pragma unroll
                    for (int i = 0; i < 4; ++i) {
                        const float h = acc[2 * v + jj][rt][i];
                        const float e = __builtin_amdgcn_exp2f(h * LOG2E);
                        const float d = fmaf(e, e + 2.0f, 2.0f);
                        r[i] = fmaf(-2.0f, h * __builtin_amdgcn_rcpf(d), h);
                    }
                    o[2 * jj] = pack2(r[0], r[1]);
                    o[2 * jj + 1] = pack2(r[2], r[3]);
                }
                st128_wt(a2 + (size_t)(40 + 3 * cb + v) * 8192 + rt * 1024, o);
            }
        }
        P2_BEGIN(1);
        P2_STEP(0, 12, 8, 6); P2_POLLED(); P2_STEP(1, 12, 8, 6); P2_STEP(2, 12, 8, 6); P2_STEP(3, 12, 8, 6); P2_STEP(4, 12, 8, 6); P2_STEP(5, 12, 13, 6);
        P2_STEP(6, 12, 9, 6); P2_STEP(7, 12, 9, 6); P2_STEP(8, 12, 9, 6); P2_STEP(9, 12, 9, 6); P2_STEP(10, 12, 5, 6); P2_STEP(11, 12, 0, 6);
        gl[0] = gl[1] = gl[2] = gl[3] = 0u;
        if (bid == 255 && wave == 7 && lane == 0) flag_set(ws + WS_EP, tok);
    }
    };
    if (wave < 4) run(IC<0>{}); else run(IC<1>{});
    if (wave == 5 || wave == 6) {
        for (unsigned spins = 0; spins < (1u << 22); ++spins) {
            if (__hip_atomic_load((LAS unsigned*)(sm + K1_CNT + 8), __ATOMIC_RELAXED, __HIP_MEMORY_SCOPE_WORKGROUP) >= 4u) break;
            __builtin_amdgcn_s_sleep(1);
        }
        if (lane == 0) __hip_atomic_fetch_add((LAS unsigned*)(sm + K1_CNT + 12), 1u, __ATOMIC_RELAXED, __HIP_MEMORY_SCOPE_WORKGROUP);
        if (wave == 6) {
            for (unsigned spins = 0; spins < (1u << 22); ++spins) {
                if (__hip_atomic_load((LAS unsigned*)(sm + K1_CNT + 12), __ATOMIC_RELAXED, __HIP_MEMORY_SCOPE_WORKGROUP) >= 2u) break;
                __builtin_amdgcn_s_sleep(1);
            }
        }
        asm volatile("" ::: "memory");
    }
#pragma unroll
    for (int r = 0; r < 4; ++r)
#pragma unroll
        for (int n = 0; n < 5; ++n) {
            if (r == 0 && n < 3 && wave == n) *(float*)(sm + wave * 21504 + lane * 4) = (u2f(gl[0]) + u2f(gl[1])) + (wave < 2 ? u2f(gl[2]) + u2f(gl[3]) : 0.f);
            else *(f32x4*)(sm + wave * 20480 + (r * 5 + n) * 1024 + lane * 16) = ac2[r][n];
        }
    wg_sync();
#pragma unroll
    for (int m = 0; m < 3; ++m) {
        const int tile = wave + 8 * m;
        if (tile < 20) {
            const char* rp = sm + tile * 1024 + lane * 16;
            f32x4 p0 = *(const f32x4*)rp, p1 = *(const f32x4*)(rp + 20480), p2 = *(const f32x4*)(rp + 40960);
            if (m == 0) { p0 = wave == 0 ? ac2[0][0] : p0; p1 = wave == 1 ? ac2[0][1] : p1; p2 = wave == 2 ? ac2[0][2] : p2; }
            const f32x4 vs = (p0 + p1) + (p2 + *(const f32x4*)(rp + 61440));
            const f32x4 vm = (*(const f32x4*)(rp + 81920) + *(const f32x4*)(rp + 102400)) + (*(const f32x4*)(rp + 122880) + *(const f32x4*)(rp + 143360));
            const int r = tile / 5;
            const float* gp = (const float*)sm + 32 * (r >> 1) + 8 * (t >> 2) + 4 * (r & 1) + (t & 3);
            const float g = sigmoidf_((gp[0] + gp[5376]) + gp[10752]);
            f32x4 v = vs * (2.0f * g) + vm * (1.0f - g);
            v[0] += g * bflo(xv[m][0]) * bflo(dv[m][0]);
            v[1] += g * bfhi(xv[m][0]) * bfhi(dv[m][0]);
            v[2] += g * bflo(xv[m][1]) * bflo(dv[m][1]);
            v[3] += g * bfhi(xv[m][1]) * bfhi(dv[m][1]);
            st128_wt(out + oo[m], __builtin_bit_cast(u32x4, v));
        }
    }
}

extern "C" void kernel_launch(void* const* d_in, const int* in_sizes, int n_in, void* d_out, int out_size, void* d_ws,
                              size_t ws_size, hipStream_t stream) {
    kf<<<256, 512, 0, stream>>>((const u16*)d_in[0], (const u16*)d_in[1], (const u16*)d_in[2], (const u16*)d_in[3], (const u16*)d_in[4],
                                (const u16*)d_in[5], (const u16*)d_in[6], (const u16*)d_in[7], (const u16*)d_in[8], (const u16*)d_in[10],
                                (const u16*)d_in[11], (const u16*)d_in[12], (const u16*)d_in[13], (char*)d_ws, (float*)d_out);
}
```

```cpp
#include <hip/hip_runtime.h>
#include <stdint.h>

typedef short s16x4 __attribute__((ext_vector_type(4)));
typedef short s16x8 __attribute__((ext_vector_type(8)));
typedef __bf16 bf16x2 __attribute__((ext_vector_type(2)));
typedef float f32x2 __attribute__((ext_vector_type(2)));
typedef float f32x4 __attribute__((ext_vector_type(4)));
typedef unsigned u32x2 __attribute__((ext_vector_type(2)));
typedef unsigned u32x4 __attribute__((ext_vector_type(4)));
typedef unsigned short u16;
#define LAS __attribute__((address_space(3)))
#define GAS __attribute__((address_space(1)))
#define DI __device__ __forceinline__

constexpr int SEQ = 2048, DM = 640, NP = 640, NF = 1536;
constexpr int NKS = 88;

constexpr size_t WS_A2 = 0;
constexpr size_t WS_W2 = WS_A2 + (size_t)16 * NKS * 8192;
constexpr size_t WS_G = WS_W2 + (size_t)8 * NKS * 5120;
constexpr size_t WS_AGG = WS_G + 10 * 2048 * 4;
constexpr size_t WS_EP = WS_AGG + 16 * 16 * 640;
constexpr size_t WS_FPT = WS_EP + 256;
constexpr size_t WS_FAG = WS_FPT + 2048;
constexpr size_t WS_FP1 = WS_FAG + 2048;
constexpr size_t WS_JUNK = WS_FP1 + 16384;

DI unsigned f2u(float v) { return __builtin_bit_cast(unsigned, v); }
DI float u2f(unsigned v) { return __builtin_bit_cast(float, v); }
DI bool wave_all(bool p) { return __builtin_amdgcn_ballot_w64(!p) == 0ull; }
DI void wg_sync() {
    __builtin_amdgcn_fence(__ATOMIC_RELEASE, "workgroup");
    __builtin_amdgcn_s_barrier();
    __builtin_amdgcn_fence(__ATOMIC_ACQUIRE, "workgroup");
}
DI float sigmoidf_(float v) { return 1.0f / (1.0f + __builtin_amdgcn_exp2f(v * -1.44269504088896341f)); }
DI float bflo(unsigned v) { return __uint_as_float(v << 16); }
DI float bfhi(unsigned v) { return __uint_as_float(v & 0xffff0000u); }
DI unsigned pack2(float a, float b) {
    f32x2 p = {a, b};
    return __builtin_bit_cast(unsigned, __builtin_convertvector(p, bf16x2));
}
DI f32x4 mfma16(s16x8 a, s16x8 b, f32x4 c) { return __builtin_amdgcn_mfma_f32_16x16x32_bf16(a, b, c, 0, 0, 0); }
template <int S> DI float shr_zero(float v) {
    return __builtin_bit_cast(float, __builtin_amdgcn_update_dpp(0, __builtin_bit_cast(int, v), 0x110 + S, 0xf, 0xf, true));
}
DI float swp(float v) {
    return __builtin_bit_cast(float, __builtin_amdgcn_update_dpp(0, __builtin_bit_cast(int, v), 0xB1, 0xf, 0xf, true));
}
DI f32x2 cmul(f32x2 a, f32x2 b) { return (f32x2){a.x * b.x - a.y * b.y, a.x * b.y + a.y * b.x}; }
DI f32x2 cfma(f32x2 a, f32x2 b, f32x2 c) {
    return (f32x2){fmaf(-a.y, b.y, fmaf(a.x, b.x, c.x)), fmaf(a.y, b.x, fmaf(a.x, b.y, c.y))};
}
DI f32x2 cmul_pk(f32x2 a, f32x2 b) { return (f32x2){fmaf(-a.y, b.y, a.x * b.x), fmaf(a.y, b.x, a.x * b.y)}; }
DI f32x2 cmul_sc(f32x2 a, f32x2 b) {
    float p = a.x * b.x, r = a.x * b.y;
    asm("" : "+v"(p));
    asm("" : "+v"(r));
    return (f32x2){fmaf(-a.y, b.y, p), fmaf(a.y, b.x, r)};
}
DI float dot2bf(unsigned a, unsigned b, float c) {
    return __builtin_amdgcn_fdot2_f32_bf16(__builtin_bit_cast(bf16x2, a), __builtin_bit_cast(bf16x2, b), c, false);
}
#define WAIT_VM(n) asm volatile("s_waitcnt vmcnt(%0)" ::"n"(n) : "memory")
#define WAIT_LGKM0() asm volatile("s_waitcnt lgkmcnt(0)" ::: "memory")
#define ACQUIRE() asm volatile("buffer_inv sc1" ::: "memory")
#define SCHED_FENCE() __builtin_amdgcn_sched_barrier(0)

DI void glds16(const void* gsrc, unsigned lds_dst) {
    unsigned keep;
    asm volatile("s_mov_b32 %0, m0\n\ts_mov_b32 m0, %2\n\ts_nop 0\n\tglobal_load_lds_dwordx4 %1, off\n\ts_mov_b32 m0, %0"
                 : "=&s"(keep) : "v"(gsrc), "s"(lds_dst) : "memory");
}
DI void glds4(const void* gsrc, unsigned lds_dst) {
    unsigned keep;
    asm volatile("s_mov_b32 %0, m0\n\ts_mov_b32 m0, %2\n\ts_nop 0\n\tglobal_load_lds_dword %1, off\n\ts_mov_b32 m0, %0"
                 : "=&s"(keep) : "v"(gsrc), "s"(lds_dst) : "memory");
}
DI void glds4_c(const void* gsrc, unsigned lds_dst) {
    unsigned keep;
    asm volatile("s_mov_b32 %0, m0\n\ts_mov_b32 m0, %2\n\ts_nop 0\n\tglobal_load_lds_dword %1, off sc1\n\ts_mov_b32 m0, %0"
                 : "=&s"(keep) : "v"(gsrc), "s"(lds_dst) : "memory");
}
DI void glds16x4(const void* gsrc, unsigned lds_dst) {
    unsigned keep;
    asm volatile("s_mov_b32 %0, m0\n\ts_mov_b32 m0, %2\n\ts_nop 0\n\tglobal_load_lds_dwordx4 %1, off\n\t"
                 "global_load_lds_dwordx4 %1, off offset:1024\n\tglobal_load_lds_dwordx4 %1, off offset:2048\n\t"
                 "global_load_lds_dwordx4 %1, off offset:3072\n\ts_mov_b32 m0, %0"
                 : "=&s"(keep) : "v"(gsrc), "s"(lds_dst) : "memory");
}
DI void st128_wt(void* p, u32x4 v) { asm volatile("global_store_dwordx4 %0, %1, off sc1\n\ts_nop 1" :: "v"(p), "v"(v) : "memory"); }
DI void st64_wt(void* p, u32x2 v) { asm volatile("global_store_dwordx2 %0, %1, off sc1\n\ts_nop 1" :: "v"(p), "v"(v) : "memory"); }
DI unsigned ld32_asm(const void* p) { unsigned r; asm volatile("global_load_dword %0, %1, off" : "=v"(r) : "v"(p) : "memory"); return r; }
DI unsigned ld16_asm(const void* p) { unsigned r; asm volatile("global_load_ushort %0, %1, off" : "=v"(r) : "v"(p) : "memory"); return r; }
DI u32x2 ld64_asm(const void* p) { u32x2 r; asm volatile("global_load_dwordx2 %0, %1, off" : "=v"(r) : "v"(p) : "memory"); return r; }
DI u32x4 ld128_asm(const void* p) { u32x4 r; asm volatile("global_load_dwordx4 %0, %1, off" : "=v"(r) : "v"(p) : "memory"); return r; }
constexpr float LOG2E = 1.44269504088896341f, INV2PI = 0.15915494309189535f;
DI f32x2 lam_bar(float lr, float li, float ld) {
    const float dt = __builtin_amdgcn_exp2f(ld * LOG2E);
    const float m = __builtin_amdgcn_exp2f(dt * lr * LOG2E);
    const float rv = __builtin_amdgcn_fractf(dt * li * INV2PI);
    return (f32x2){m * __builtin_amdgcn_cosf(rv), m * __builtin_amdgcn_sinf(rv)};
}

typedef unsigned long long u64;
DI void glds16_c(const void* gsrc, unsigned lds_dst) {
    unsigned keep;
    lds_dst = __builtin_amdgcn_readfirstlane(lds_dst);
    asm volatile("s_mov_b32 %0, m0\n\ts_mov_b32 m0, %2\n\ts_nop 0\n\tglobal_load_lds_dwordx4 %1, off sc1\n\ts_mov_b32 m0, %0"
                 : "=&s"(keep) : "v"(gsrc), "s"(lds_dst) : "memory");
}
DI void glds16x4_c(const void* gsrc, unsigned lds_dst) {
    unsigned keep;
    asm volatile("s_mov_b32 %0, m0\n\ts_mov_b32 m0, %2\n\ts_nop 0\n\tglobal_load_lds_dwordx4 %1, off sc1\n\t"
                 "global_load_lds_dwordx4 %1, off offset:1024 sc1\n\tglobal_load_lds_dwordx4 %1, off offset:2048 sc1\n\t"
                 "global_load_lds_dwordx4 %1, off offset:3072 sc1\n\ts_mov_b32 m0, %0"
                 : "=&s"(keep) : "v"(gsrc), "s"(lds_dst) : "memory");
}
DI void flag_set(char* p, u64 tok) { __hip_atomic_store((GAS u64*)p, tok, __ATOMIC_RELAXED, __HIP_MEMORY_SCOPE_AGENT); }
DI void flag_wait(const char* p, bool need, u64 tok) {
    for (unsigned spins = 0; spins < (1u << 20); ++spins) {
        u64 v = tok;
        if (need) v = __hip_atomic_load((const GAS u64*)p, __ATOMIC_RELAXED, __HIP_MEMORY_SCOPE_AGENT);
        if (__all(v == tok)) break;
        __builtin_amdgcn_s_sleep(8);
    }
    asm volatile("" ::: "memory");
}

template <int V> struct IC { static constexpr int value = V; };
#define ROTF ((((rb + cb) & 3) * 5 + 1) >> 1)
constexpr int K1_STAGE = 38 * 1024;
constexpr int K1_GW = 0;
constexpr int K1_PLR = K1_GW + 1280, K1_PLI = K1_GW + 1360, K1_PLD = K1_GW + 1440, K1_PB1 = K1_GW + 1536, K1_PEP = K1_GW + 1728;
constexpr int K1_RING = 2048;
#define BUF(it) (K1_RING + ((it) % 3) * K1_STAGE)
constexpr int K1_TAB = K1_RING + 3 * K1_STAGE;
constexpr int K1_XCH = K1_TAB + 5120;
constexpr int K1_CNT = K1_XCH + 1280;
constexpr int K1_PRT = K1_CNT + 16;
constexpr int K1_PT = 123 * 1024;
constexpr int K1_FM = 140 * 1024;
constexpr int KF_LDS = 160 * 1024;

__global__ __launch_bounds__(512, 2) __attribute__((target("no-packed-fp32-ops"))) void kf(const u16* __restrict__ x, const u16* __restrict__ Lre, const u16* __restrict__ Lim,
                                             const u16* __restrict__ Bre, const u16* __restrict__ Bim, const u16* __restrict__ Cre,
                                             const u16* __restrict__ Cim, const u16* __restrict__ logdt, const u16* __restrict__ Dskip,
                                             const u16* __restrict__ w1, const u16* __restrict__ b1, const u16* __restrict__ w2,
                                             const u16* __restrict__ gw, char* __restrict__ ws, float* __restrict__ out) {
    __shared__ __attribute__((aligned(16))) char sm[KF_LDS];
    const int tid = threadIdx.x, lane = tid & 63;
    const int wave = __builtin_amdgcn_readfirstlane(tid >> 6);
    const int bid = blockIdx.x, xcd = bid & 7, idx = bid >> 3;
    const int rb = 8 * (xcd & 1) + (idx & 7);
    const int cb = 4 * (xcd >> 1) + (idx >> 3);
    const int RG = 4 * xcd + (idx & 3);
    const int CB = idx >> 2;
    const int t = lane & 15, q = lane >> 4;
    const unsigned smb = (unsigned)(size_t)(LAS char*)sm;
    if (wave < 5) {
        u64 pc0;
        asm volatile("s_getpc_b64 %0" : "=s"(pc0));
        glds4((const char*)(pc0 & ~255ull) + wave * 8192 + lane * 128, smb + K1_FM);
    }
    asm volatile("" :: "s"(x), "s"(Lre), "s"(Lim), "s"(Bre), "s"(Bim), "s"(Cre), "s"(Cim), "s"(logdt), "s"(Dskip), "s"(w1), "s"(b1), "s"(w2),
                 "s"(gw), "s"(ws), "s"(out));
    if (wave == 7) {
        const char* tp = (const char*)(out + 64 * RG * DM);
        if (lane == 1) tp = ws + WS_A2 + (size_t)rb * NKS * 8192;
        if (lane == 2) tp = ws + WS_A2 + (size_t)(RG >> 1) * NKS * 8192;
        if (lane == 3) tp = ws + WS_W2 + (size_t)CB * NKS * 5120;
        if (lane == 4) tp = ws + WS_AGG;
        if (lane == 5) tp = (const char*)Dskip;
        if (lane == 6) tp = ws + WS_A2 + (size_t)rb * NKS * 8192 + NKS * 8192 - 128;
        if (lane == 7) tp = ws + WS_A2 + (size_t)(RG >> 1) * NKS * 8192 + NKS * 8192 - 128;
        if (lane == 8) tp = ws + WS_W2 + (size_t)CB * NKS * 5120 + NKS * 5120 - 128;
        if (lane == 9) tp = (const char*)(out + (64 * RG + 63) * DM);
        glds4_c(tp, smb + K1_FM);
    }
    f32x4 ac2[4][5];
    u32x2 xv[3], dv[3];
    unsigned gl[4];
    int oo[3];
    auto run = [&](auto ROLE_, auto PART_) __attribute__((always_inline)) {
    constexpr int ROLE = decltype(ROLE_)::value, PART = decltype(PART_)::value;
    int lane = tid & 63;
    asm volatile("" : "+v"(lane));
    const int t = lane & 15, q = lane >> 4;
#define G_M0(base, c) asm volatile("s_add_i32 m0, %0, %1" :: "s"(base), "n"(c) : "scc")
#define G_LDS(vo, sb, c) asm volatile("global_load_lds_dwordx4 %0, %1 offset:%2" :: "v"(vo), "s"(sb), "n"(c) : "memory")
#define G_LDV(va, c) asm volatile("global_load_lds_dwordx4 %0, off offset:%1" :: "v"(va), "n"(c) : "memory")
#define G_NOP() asm volatile("s_nop 0")
    const int l8 = lane >> 3;
    unsigned vox[4];
    const char* wsr[3];
    const int rot = ROTF;
    const char* w1s = (const char*)w1 + (size_t)rot * (64 * NF * 2);
    const char* xs = (const char*)x + rot * 128;
#define K_WRAP(st) (((st) == 2 || (st) == 5 || (st) == 7) && rot == 10 - (st))
#define W_NEXT(st)                                                                                \
    do {                                                                                          \
        w1s += K_WRAP(st) ? -9 * (64 * NF * 2) : 64 * NF * 2;                                     \
        if ((st) == 2 || (st) == 5 || (st) == 7) { const long d_ = K_WRAP(st) ? -1280 : 0; wsr[0] += d_; wsr[1] += d_; wsr[2] += d_; } \
    } while (0)
#define X_NEXT(st) do { if ((st) == 2 || (st) == 5 || (st) == 7) xs += K_WRAP(st) ? -1280 : 0; } while (0)
    const unsigned lw = smb + 1024 * (wave & 3);
    const unsigned lm = smb + (ROLE == 0 ? 3072 : 4096) * (wave & 3);
#define X_M0(st, i) G_M0(lm, BUF(st) + 1024 * (i) - 128 * (st))
#define X_LD(st, i) G_LDS(vox[i], xs, 128 * (st))
#define W_M0(st, j) G_M0(lw, BUF(st) + 16384 + 4096 * (j) - 128 * (st))
#define W_LD(st, j) G_LDV(wsr[j], 128 * (st))
#define M_M0(st, i) G_M0(lm, BUF(st) + 26624 + 1024 * (i))
#define M_LD(i) G_LDS(vox[i], w1s, 0)
    const unsigned pu = 4 * bid + (wave & 3);
    const unsigned pkg = (pu * 1639u) >> 16, pv = pu - 400u, ptg = (pv * 1366u) >> 16;
    if constexpr (ROLE == 1) {
#pragma unroll
        for (int i = 0; i < 4; ++i) {
            const int p = 8 * i + l8;
            vox[i] = ((128 * rb + 32 * (wave & 3) + 8 * ((p & 15) >> 2) + 4 * (p >> 4) + (p & 3)) * DM + (((lane & 7) ^ ((p >> 1) & 7)) << 3)) * 2;
            if constexpr (PART == 0) {
                if (i == 0) X_M0(0, 0); else if (i == 1) X_M0(0, 1); else if (i == 2) X_M0(0, 2); else X_M0(0, 3);
                G_NOP();
                G_LDS(vox[i], xs, 0);
                SCHED_FENCE();
            }
        }
        if constexpr (PART == 0) {
            X_M0(1, 0); G_NOP(); X_LD(1, 0); X_M0(1, 1); G_NOP(); X_LD(1, 1); X_M0(1, 2); G_NOP(); X_LD(1, 2); X_M0(1, 3); G_NOP(); X_LD(1, 3);
            const bool isc = pu < 400 || pu >= 880;
            const unsigned kg = pu < 400 ? pkg : 0, nt = pu < 400 ? pu - 40 * pkg : 0;
            const unsigned o = ((isc ? 16 * nt + l8 : 32 * (pv - 48 * ptg) + l8) * DM + 64 * (isc ? kg : ptg) + ((lane & 7) ^ l8) * 8) * 2;
#pragma unroll
            for (int i = 0; i < 4; ++i) {
                const u16* p0 = isc ? ((i >> 1) ? Cim : Cre) + 8 * (i & 1) * DM : w2 + 8 * i * DM;
                G_M0(lm, K1_PT + i * 1024); G_NOP(); G_LDS(o, p0, 0);
            }
            return;
        }
    } else {
#pragma unroll
        for (int j = 0; j < 3; ++j) {
            const int p = 8 * (4 * j + wave) + l8;
            const int ch = 40 * cb + 8 * (p >> 4) + ((p & 15) >> 1);
            wsr[j] = (const char*)(((p & 1) ? Bim : Bre) + ch * DM + 64 * rot + (((lane & 7) ^ ((p >> 1) & 7)) << 3));
            if (PART == 0 && j < 2) {
                if (j == 0) W_M0(0, 0); else W_M0(0, 1);
                G_NOP();
                G_LDV(wsr[j], 0);
                SCHED_FENCE();
            }
        }
#pragma unroll
        for (int i = 0; i < 3; ++i) {
            const int gI = 64 * (3 * wave + i) + lane, row = gI / 12, cp = gI - 12 * row;
            int j = (cp >> 1) - ((row >> 3) & 1);
            j += j < 0 ? 6 : 0;
            vox[i] = (row * NF + 96 * cb + 16 * j + 8 * (cp & 1)) * 2;
            if constexpr (PART == 0) {
                if (i == 0) M_M0(0, 0); else if (i == 1) M_M0(0, 1); else M_M0(0, 2);
                G_NOP();
                M_LD(i);
                SCHED_FENCE();
            }
        }
        W_NEXT(1);
        if constexpr (PART == 0) {
            const u16* p = gw + lane * 8;
            if (wave == 3) {
                p = gw + 512 + t * 8;
                if (lane >= 16 && lane < 21) p = Lre + 40 * cb + (lane - 16) * 8;
                if (lane >= 21 && lane < 26) p = Lim + 40 * cb + (lane - 21) * 8;
                if (lane >= 26 && lane < 31) p = logdt + 40 * cb + (lane - 26) * 8;
                if (lane >= 32 && lane < 44) p = b1 + 96 * cb + (lane - 32) * 8;
                if (lane == 44) p = (const u16*)(ws + WS_EP);
            }
            glds16_c(wave < 2 ? wsr[2] : (const char*)p, wave < 2 ? lw + BUF(0) + 16384 + 8192 : smb + K1_GW + (wave - 2) * 1024);
            if (wave < 2) { W_M0(1, 2); G_NOP(); W_LD(1, 2); }
            W_M0(1, 0); G_NOP(); W_LD(1, 0); W_M0(1, 1); G_NOP(); W_LD(1, 1);
            M_M0(1, 0); G_NOP(); M_LD(0); M_M0(1, 1); G_NOP(); M_LD(1); M_M0(1, 2); G_NOP(); M_LD(2);
            return;
        }
    }
    char* ptl = sm + K1_PT + (wave & 3) * 4096;
    const int wc = wave & 3;
    const int xoff = (32 * wc + t) * 128;
    const int co0 = (q ^ ((t >> 1) & 7)) * 16, co1 = co0 ^ 64;
    char* a2 = ws + WS_A2 + (size_t)rb * NKS * 8192 + (2 * wc) * 1024 + lane * 16;
    const int rb2 = RG >> 1, hh = RG & 1;
    const int ks0 = ROLE == 0 ? 10 * wave : 40 + 12 * (wave - 4);
    const char* pa = ws + WS_A2 + ((size_t)rb2 * NKS + ks0) * 8192 + hh * 4096 + lane * 16;
    const char* pb = ws + WS_W2 + ((size_t)CB * NKS + ks0) * 5120 + 1024 + lane * 16;
    u64 tok;
#define GET_TOK() do { const u32x2 ep_ = *(const u32x2*)(sm + K1_PEP); \
                       tok = (((u64)(unsigned)__builtin_amdgcn_readfirstlane(ep_[1]) << 32) | (unsigned)__builtin_amdgcn_readfirstlane(ep_[0])) + 1ull; } while (0)
    u32x4 ptf[4];
    auto pretile_read = [&]() __attribute__((always_inline)) {
        if (pu < 400) {
#pragma unroll
            for (int i = 0; i < 4; ++i) {
                const char* pp = ptl + t * 128 + (((2 * i + (q >> 1)) ^ (t & 7)) << 4) + 8 * (q & 1);
                const u32x2 rr = *(const u32x2*)pp, ii = *(const u32x2*)(pp + 2048);
                ptf[i] = (u32x4){rr[0], rr[1], ii[0], ii[1]};
            }
        } else {
            const int r = 4 * q + (t >> 2);
#pragma unroll
            for (int i = 0; i < 4; ++i) {
                const char* pp = ptl + r * 128 + (((2 * i + ((lane >> 1) & 1)) ^ (r & 7)) << 4) + (lane & 1) * 8;
                const s16x4 lo = __builtin_amdgcn_ds_read_tr16_b64_v4i16((LAS s16x4*)pp);
                const s16x4 hi = __builtin_amdgcn_ds_read_tr16_b64_v4i16((LAS s16x4*)(pp + 2048));
                ptf[i] = __builtin_bit_cast(u32x4, __builtin_shufflevector(lo, hi, 0, 1, 2, 3, 4, 5, 6, 7));
            }
        }
    };
    auto pretile_mix = [&]() __attribute__((always_inline)) {
        if (pu < 400) {
#pragma unroll
            for (int i = 0; i < 4; ++i) {
                const unsigned r0 = ptf[i][0], r1 = ptf[i][1], i0 = ptf[i][2] ^ 0x80008000u, i1 = ptf[i][3] ^ 0x80008000u;
                ptf[i] = (u32x4){(r0 & 0xffffu) | (i0 << 16), (r0 >> 16) | (i0 & 0xffff0000u), (r1 & 0xffffu) | (i1 << 16), (r1 >> 16) | (i1 & 0xffff0000u)};
            }
        }
    };
    auto pretile_dst = [&](unsigned i) __attribute__((always_inline)) {
        const unsigned g = pu < 400 ? pu - 40 * pkg : 4 * ptg + i, KS = pu < 400 ? 4 * pkg + i : 40 + pv - 48 * ptg;
        const unsigned cB = (g * 13u) >> 6;
        unsigned o = (unsigned)WS_W2 + (cB * (5 * NKS - 5) + KS * 5 + g) * 1024u, j = (unsigned)WS_JUNK + (pu * 4 + i) * 1024u;
        asm("" : "+s"(o), "+s"(j));
        return pu < 880 ? o : j;
    };

    const int bco = wave == 6 ? 6 * 20480 + 4096 : wave * 20480;
    u32x4 Ab[4][4], Bb[3][5];
#define P2_L1(dst, P, OFF) asm volatile("global_load_dwordx4 %0, %1, off offset:" #OFF " sc1" : "=&v"(dst) : "v"(P) : "memory")
#define P2_LDB(slot, P)                                                                           \
    do { P2_L1(Bb[slot][0], P, -1024); P2_L1(Bb[slot][1], P, 0); P2_L1(Bb[slot][2], P, 1024); P2_L1(Bb[slot][3], P, 2048); P2_L1(Bb[slot][4], P, 3072); } while (0)
#define P2_LDA(slot, P)                                                                           \
    do { P2_L1(Ab[slot][0], P, 0); P2_L1(Ab[slot][1], P, 1024); P2_L1(Ab[slot][2], P, 2048); P2_L1(Ab[slot][3], P, 3072); } while (0)
    auto pt_check = [&]() __attribute__((always_inline)) {
        const u32x4 v0 = *(const u32x4*)(sm + K1_FM + lane * 16), v1 = *(const u32x4*)(sm + K1_FM + 1024 + lane * 16);
        const unsigned lo = (unsigned)tok, hi = (unsigned)(tok >> 32);
        if (!wave_all(v0[0] == lo && v0[1] == hi && v0[2] == lo && v0[3] == hi && v1[0] == lo && v1[1] == hi && v1[2] == lo && v1[3] == hi)) {
#pragma unroll 1
            for (int k = 0; k < 4; ++k) flag_wait(ws + WS_FPT + (size_t)(64 * k + lane) * 8, true, tok);
        }
    };
    const char* g3 = pb - 1024 + 3 * 5120;
#define P2_CACHE(i) glds16_c(g3 + (i) * 1024, smb + bco + (i) * 1024)
#define P2_M2(ua, ub, i)                                                                          \
    do {                                                                                          \
        ac2[(i) / 5][(i) % 5] = mfma16(__builtin_bit_cast(s16x8, Bb[ub][(i) % 5]), __builtin_bit_cast(s16x8, Ab[ua][(i) / 5]), ac2[(i) / 5][(i) % 5]); \
        ac2[((i) + 1) / 5][((i) + 1) % 5] = mfma16(__builtin_bit_cast(s16x8, Bb[ub][((i) + 1) % 5]), __builtin_bit_cast(s16x8, Ab[ua][((i) + 1) / 5]), ac2[((i) + 1) / 5][((i) + 1) % 5]); \
        SCHED_FENCE();                                                                            \
    } while (0)
#define P2_STEP(j, NS, WN, NL)                                                                    \
    do {                                                                                          \
        asm volatile("s_waitcnt vmcnt(%9)" : "+v"(Ab[(j) & 3][0]), "+v"(Ab[(j) & 3][1]), "+v"(Ab[(j) & 3][2]), "+v"(Ab[(j) & 3][3]), \
                     "+v"(Bb[(j) % 3][0]), "+v"(Bb[(j) % 3][1]), "+v"(Bb[(j) % 3][2]), "+v"(Bb[(j) % 3][3]), "+v"(Bb[(j) % 3][4]) : "n"(WN) : "memory"); \
        SCHED_FENCE();                                                                            \
        const char* pa_ = pa + ((j) + 3) * 8192;                                                  \
        const char* pb_ = pb + ((j) + 2) * 5120;                                                  \
        constexpr bool LA_ = (j) + 3 < (NS), LBG_ = (j) + 2 >= (NL) && (j) + 2 < (NS), LBL_ = (j) + 2 >= 3 && (j) + 2 < (NL); \
        P2_M2((j) & 3, (j) % 3, 0); if (LA_) { P2_L1(Ab[((j) + 3) & 3][0], pa_, 0); SCHED_FENCE(); }      \
        P2_M2((j) & 3, (j) % 3, 2); if (LA_) { P2_L1(Ab[((j) + 3) & 3][1], pa_, 1024); SCHED_FENCE(); }   \
        P2_M2((j) & 3, (j) % 3, 4); if (LA_) { P2_L1(Ab[((j) + 3) & 3][2], pa_, 2048); SCHED_FENCE(); }   \
        P2_M2((j) & 3, (j) % 3, 6); if (LA_) { P2_L1(Ab[((j) + 3) & 3][3], pa_, 3072); SCHED_FENCE(); }   \
        if (LBL_) { _Pragma("unroll") for (int n_ = 0; n_ < 5; ++n_) Bb[((j) + 2) % 3][n_] = *(const u32x4*)(sm + bco + (((j) + 2 - 3) * 5 + n_) * 1024 + lane * 16); SCHED_FENCE(); } \
        P2_M2((j) & 3, (j) % 3, 8); if (LBG_) { P2_L1(Bb[((j) + 2) % 3][0], pb_, -1024); SCHED_FENCE(); } \
        P2_M2((j) & 3, (j) % 3, 10); if (LBG_) { P2_L1(Bb[((j) + 2) % 3][1], pb_, 0); SCHED_FENCE(); }    \
        P2_M2((j) & 3, (j) % 3, 12); if (LBG_) { P2_L1(Bb[((j) + 2) % 3][2], pb_, 1024); SCHED_FENCE(); } \
        P2_M2((j) & 3, (j) % 3, 14); if (LBG_) { P2_L1(Bb[((j) + 2) % 3][3], pb_, 2048); SCHED_FENCE(); } \
        P2_M2((j) & 3, (j) % 3, 16); if (LBG_) { P2_L1(Bb[((j) + 2) % 3][4], pb_, 3072); SCHED_FENCE(); } \
        P2_M2((j) & 3, (j) % 3, 18);                                                              \
    } while (0)
    u64 pl[3];
#define PSL "4"
#define POLL1(i) "s_waitcnt vmcnt(2)\n\tv_cmp_ne_u64 vcc, %" #i ", %5\n\ts_cmp_eq_u64 vcc, 0\n\ts_cbranch_scc1 2f\n\t" \
                 "global_load_dwordx2 %" #i ", %4, off sc1\n\ts_sleep " PSL "\n\t"
#define P2_POLLED() asm volatile("" : "+v"(xv[0]), "+v"(xv[1]), "+v"(xv[2]), "+v"(dv[0]), "+v"(dv[1]), "+v"(dv[2]) : "v"(pl[0]), "v"(pl[1]), "v"(pl[2]))
#define P2_BEGIN(role)                                                                            \
    do {                                                                                          \
        WAIT_VM(0);                                                                               \
                                                                                           \
        if (lane == 0) flag_set(ws + WS_FP1 + (size_t)(((((role) * 16 + rb) * 2 + (wc >> 1)) * 16 + cb) * 2 + (wc & 1)) * 8, tok); \
        if ((role) == 0) { _Pragma("unroll") for (int i = 0; i < 5; ++i) glds16x4_c(g3 + i * 4096, __builtin_amdgcn_readfirstlane(smb + bco + i * 4096)); } \
        _Pragma("unroll") for (int m = 0; m < 3; ++m) {                                           \
            int tile = wave + 8 * m; tile = tile < 20 ? tile : wave;                              \
            const int r = tile / 5, n = tile - 5 * r;                                             \
            const int col = 80 * CB + 16 * n + 4 * q;                                             \
            oo[m] = (64 * RG + 32 * (r >> 1) + 8 * (t >> 2) + 4 * (r & 1) + (t & 3)) * DM + col;  \
            xv[m] = ld64_asm(x + oo[m]); dv[m] = ld64_asm(Dskip + col);                           \
        }                                                                                         \
        unsigned cnt_;                                         \
        asm volatile("global_load_dwordx2 %0, %4, off sc1\n\ts_sleep " PSL "\n\tglobal_load_dwordx2 %1, %4, off sc1\n\ts_sleep " PSL "\n\t" \
                     "global_load_dwordx2 %2, %4, off sc1\n\ts_mov_b32 %3, 0x80000\n"             \
                     "1:\n\t" POLL1(0) POLL1(1) POLL1(2)                                          \
                     "s_sub_u32 %3, %3, 1\n\ts_cbranch_scc0 1b\n"                                 \
                     "2:"                                                                         \
                     : "=&v"(pl[0]), "=&v"(pl[1]), "=&v"(pl[2]), "=&s"(cnt_)                      \
                     : "v"(ws + WS_FP1 + (size_t)(((role) * 32 + RG) * 32 + 8 * wc + (lane & 7)) * 8), "s"(tok) : "memory", "vcc", "scc"); \
        if ((role) == 0) P2_GATES();                                                              \
        P2_LDA(0, pa); P2_LDA(1, pa + 8192); P2_LDA(2, pa + 16384);                               \
        _Pragma("unroll") for (int r = 0; r < 4; ++r) _Pragma("unroll") for (int n = 0; n < 5; ++n) ac2[r][n] = (f32x4){0.f, 0.f, 0.f, 0.f}; \
    } while (0)
#define P2_GATES()                                                                                \
    _Pragma("unroll") for (int j_ = 0; j_ < 4; ++j_)                                              \
        asm volatile("global_load_dword %0, %1, off sc1" : "=&v"(gl[j_])                          \
                     : "v"(ws + WS_G + ((4 * wc + j_ < 10 ? 4 * wc + j_ : 9) * 2048 + 64 * RG + lane) * 4) : "memory")

    if constexpr (ROLE == 0) {
        f32x4 acc[6][2];
#pragma unroll
        for (int a = 0; a < 6; ++a) { acc[a][0] = (f32x4){0.f, 0.f, 0.f, 0.f}; acc[a][1] = (f32x4){0.f, 0.f, 0.f, 0.f}; }
        s16x8 fx[2][2], fw[2][5];
        const int woff = 16384 + t * 128;
        const int itg = cb >= 10 ? -1 : cb >= rot ? cb - rot : cb + 10 - rot;
#define S_RX(b, sb, co) do { fx[b][0] = *(const s16x8*)(sm + (sb) + xoff + (co)); fx[b][1] = *(const s16x8*)(sm + (sb) + xoff + 2048 + (co)); } while (0)
#define S_RW(b, sb, co, T) fw[b][T] = *(const s16x8*)(sm + (sb) + woff + (T) * 2048 + (co))
#define S_MM(b, T) do { acc[T][0] = mfma16(fx[b][0], fw[b][T], acc[T][0]); acc[T][1] = mfma16(fx[b][1], fw[b][T], acc[T][1]); SCHED_FENCE(); } while (0)
#define S_LB(slot, T) asm volatile("global_load_dwordx4 %0, %1, off sc1" : "=&v"(Bb[slot][T]) : "v"(pb + (slot) * 5120 + ((T) - 1) * 1024) : "memory")
#define S_PT(it, i) do { if ((it) == 1) asm volatile("global_store_dwordx4 %0, %1, %2 sc1\n\ts_nop 1" :: "v"(pto[i]), "v"(ptf[i]), "s"(ws) : "memory"); } while (0)
#define S_BODY(it, WN, XA0, XA1, XA2, XA3)                                                        \
    do {                                                                                          \
        constexpr bool RF_ = (it) < 7, RN_ = (it) < 9;                                            \
        constexpr int SB_ = (it) < 7 ? 0 : (it) - 7;                                              \
        S_MM(0, 0); S_RX(1, BUF(it), co1); S_RW(1, BUF(it), co1, 0); SCHED_FENCE();               \
        S_MM(0, 1); S_RW(1, BUF(it), co1, 1); S_RW(1, BUF(it), co1, 2); XA0; SCHED_FENCE();       \
        S_MM(0, 2); S_RW(1, BUF(it), co1, 3); S_RW(1, BUF(it), co1, 4); XA1; SCHED_FENCE();       \
        S_MM(0, 3); XA2; SCHED_FENCE();                                                           \
        S_MM(0, 4); XA3; SCHED_FENCE();                                                           \
        if ((it) == 0 && wave < 2) WAIT_VM(6); else if (RN_) WAIT_VM(WN);                         \
        WAIT_LGKM0();                                                                             \
        __builtin_amdgcn_s_barrier();                                                             \
        asm volatile("" ::: "memory");                                                            \
        SCHED_FENCE();                                                                            \
        S_MM(1, 0);                                                                               \
        if (__builtin_expect((it) == itg, 0)) {                                                   \
            const s16x8 g0_ = *(const s16x8*)(sm + K1_GW + (64 * cb + 8 * q) * 2), g1_ = *(const s16x8*)(sm + K1_GW + (64 * cb + 32 + 8 * q) * 2); \
            acc[5][0] = mfma16(fx[1][0], g1_, mfma16(fx[0][0], g0_, acc[5][0])); acc[5][1] = mfma16(fx[1][1], g1_, mfma16(fx[0][1], g0_, acc[5][1])); \
        }                                                                                         \
        if ((it) == 1) pretile_read();                                                            \
        if ((it) == 7) pt_check();                                                                \
        if (RF_) {                                                                                \
            W_NEXT((it) + 3);                                                                     \
            if (wave < 2) { W_M0((it) + 3, 2); G_NOP(); W_LD((it) + 3, 2); }                      \
            else if ((it) == 3) { if (lane == 0) flag_set(ws + WS_FPT + (size_t)bid * 8, tok); }    \
            else if ((it) == 6) glds16_c(ws + WS_FPT + (wave - 2) * 1024 + lane * 16, smb + K1_FM + (wave - 2) * 1024); \
            W_M0((it) + 3, 0);                                                                    \
        }                                                                                         \
        if (RN_) { S_RX(0, BUF((it) + 1), co0); S_RW(0, BUF((it) + 1), co0, 0); }                 \
        SCHED_FENCE();                                                                            \
        if (RF_) { W_LD((it) + 3, 0); W_M0((it) + 3, 1); } else S_LB(SB_, 0);                     \
        SCHED_FENCE();                                                                            \
        S_MM(1, 1); if (RN_) { S_RW(0, BUF((it) + 1), co0, 1); S_RW(0, BUF((it) + 1), co0, 2); }  \
        SCHED_FENCE();                                                                            \
        if (RF_) { W_LD((it) + 3, 1); M_M0((it) + 3, 0); } else S_LB(SB_, 1);                     \
        if ((it) == 1) pretile_mix();                                                             \
        S_PT(it, 0); SCHED_FENCE();                                                               \
        S_MM(1, 2); if (RN_) { S_RW(0, BUF((it) + 1), co0, 3); S_RW(0, BUF((it) + 1), co0, 4); }  \
        SCHED_FENCE();                                                                            \
        if (RF_) { M_LD(0); M_M0((it) + 3, 1); } else S_LB(SB_, 2);                               \
        S_PT(it, 1); SCHED_FENCE();                                                               \
        S_MM(1, 3);                                                                               \
        if (RF_) { M_LD(1); M_M0((it) + 3, 2); } else S_LB(SB_, 3);                               \
        S_PT(it, 2); SCHED_FENCE();                                                               \
        S_MM(1, 4);                                                                               \
        if (RF_) M_LD(2); else S_LB(SB_, 4);                                                      \
        S_PT(it, 3); SCHED_FENCE();                                                               \
    } while (0)
        if (wave == 0 && lane == 0) *(u32x4*)(sm + K1_CNT) = (u32x4){0u, 0u, 0u, 0u};
        unsigned pto[4];
#pragma unroll
        for (int i = 0; i < 4; ++i) pto[i] = pretile_dst(i) + lane * 16;
        asm volatile("" : "+v"(a2), "+v"(pa), "+v"(pb), "+v"(pto[0]), "+v"(pto[1]), "+v"(pto[2]), "+v"(pto[3]));
        asm volatile("" : "+v"(wsr[0]), "+v"(wsr[1]), "+v"(wsr[2]), "+v"(vox[0]), "+v"(vox[1]), "+v"(vox[2]));
        if (wave < 2) WAIT_VM(6); else WAIT_VM(5);
        WAIT_LGKM0();
        __builtin_amdgcn_s_barrier();
        asm volatile("" ::: "memory");
        S_RX(0, BUF(0), co0); S_RW(0, BUF(0), co0, 0); S_RW(0, BUF(0), co0, 1); S_RW(0, BUF(0), co0, 2); S_RW(0, BUF(0), co0, 3); S_RW(0, BUF(0), co0, 4);
        const int cl = lane < 40 ? lane : 39;
        float lr, li, za, zb;
        f32x2 pA, pB;
#define TB0() do { lr = bflo(*(const u16*)(sm + K1_PLR + cl * 2)); li = bflo(*(const u16*)(sm + K1_PLI + cl * 2)); za = bflo(*(const u16*)(sm + K1_PLD + cl * 2)); } while (0)
#define TB1() do { const float dt = __builtin_amdgcn_exp2f(za * LOG2E); za = dt * lr * LOG2E; zb = dt * li * INV2PI;                   \
                   const float nA = wc == 1 ? 8.f : wc == 2 ? 24.f : 1.f;                                                             \
                   const float mA = __builtin_amdgcn_exp2f(nA * za), rA = __builtin_amdgcn_fractf(nA * zb);                           \
                   pA = (f32x2){mA * __builtin_amdgcn_cosf(rA), mA * __builtin_amdgcn_sinf(rA)}; } while (0)
#define TB2() do { const float nB = wc == 1 ? 16.f : wc == 2 ? 32.f : 0.f;                                                            \
                   const float mB = __builtin_amdgcn_exp2f(nB * za), rB = __builtin_amdgcn_fractf(nB * zb);                           \
                   pB = (f32x2){mB * __builtin_amdgcn_cosf(rB), mB * __builtin_amdgcn_sinf(rB)}; } while (0)
#define TB3() do { const float nr = pA.x - 1.0f, ni = pA.y, rden = 1.0f / (lr * lr + li * li);                                        \
                   const float z8 = __builtin_amdgcn_fractf(8.f * zb);                                                                \
                   if (wc == 3) pA = (f32x2){(nr * lr + ni * li) * rden, (ni * lr - nr * li) * rden};                 \
                   f32x4* tb = (f32x4*)(sm + K1_TAB + cl * 16);                                              \
                   tb[wc == 0 ? 0 : wc == 1 ? 120 : wc == 2 ? 200 : 40] = (f32x4){pA.x, -pA.y, pA.x, pA.y};                           \
                   tb[wc == 0 ? 80 : wc == 1 ? 160 : wc == 2 ? 240 : 280] = wc == 3 ? (f32x4){8.f * za, z8, 8.f * za, z8} : (f32x4){pB.x, -pB.y, pB.x, pB.y}; } while (0)
#define SA0() do { W_NEXT(2); if (wave < 2) { W_M0(2, 2); G_NOP(); W_LD(2, 2); } W_M0(2, 0); G_NOP(); W_LD(2, 0); W_M0(2, 1); } while (0)
#define SA1() do { SCHED_FENCE(); W_LD(2, 1); M_M0(2, 0); G_NOP(); M_LD(0); M_M0(2, 1); } while (0)
#define SA2() do { M_LD(1); M_M0(2, 2); } while (0)
        S_BODY(0, 5, SA0(), SA1(), SA2(), M_LD(2));   S_BODY(1, 5, , , , );   S_BODY(2, 9, , , GET_TOK(), );   S_BODY(3, 5, TB0(), , TB1(), );
        S_BODY(4, 6, , TB2(), , );   S_BODY(5, 5, , TB3(), , );   S_BODY(6, 5, , , , );
        S_BODY(7, 5, , , , );   S_BODY(8, 5, , , , );
        const f32x2* tab = (const f32x2*)(sm + K1_TAB) + t;
        f32x2 Lc[5], Sc[5];
#pragma unroll
        for (int T = 0; T < 5; ++T) { Lc[T] = tab[16 * T]; Sc[T] = tab[(5 - q) * 80 + 16 * T]; }
        const f32x2 L32 = *(const f32x2*)(sm + K1_TAB + (6 * 80 + 2 * cl + 1) * 8);
        S_BODY(9, 0, , , , );
        __builtin_amdgcn_s_setprio(3);
        float ag8[5];
#pragma unroll
        for (int T = 0; T < 5; ++T) ag8[T] = acc[T][0][0];
#pragma unroll
        for (int j = 1; j < 8; ++j)
#pragma unroll
            for (int T = 0; T < 5; ++T) ag8[T] = fmaf(Lc[T].y, swp(ag8[T]), fmaf(Lc[T].x, ag8[T], acc[T][j >> 2][j & 3]));
        {
            unsigned sv[5];
#pragma unroll
            for (int T = 0; T < 5; ++T) sv[T] = f2u(fmaf(Sc[T].y, swp(ag8[T]), Sc[T].x * ag8[T]));
            const auto r01 = __builtin_amdgcn_permlane32_swap(sv[0], sv[1], false, false);
            const auto r23 = __builtin_amdgcn_permlane32_swap(sv[2], sv[3], false, false);
            const auto r44 = __builtin_amdgcn_permlane32_swap(sv[4], sv[4], false, false);
            const unsigned p4 = f2u(u2f(r44[0]) + u2f(r44[1]));
            const auto ra = __builtin_amdgcn_permlane16_swap(f2u(u2f(r01[0]) + u2f(r01[1])), f2u(u2f(r23[0]) + u2f(r23[1])), false, false);
            const auto rb4 = __builtin_amdgcn_permlane16_swap(p4, p4, false, false);
            float* xa = (float*)(sm + K1_XCH) + wc * 80 + t;
            xa[16 * (((q & 1) << 1) | (q >> 1))] = u2f(ra[0]) + u2f(ra[1]);
            xa[64] = u2f(rb4[0]) + u2f(rb4[1]);
        }
        asm volatile("" ::: "memory");
        unsigned old = 0;
        if (lane == 0) old = __hip_atomic_fetch_add((LAS unsigned*)(sm + K1_CNT), 1u, __ATOMIC_RELAXED, __HIP_MEMORY_SCOPE_WORKGROUP);
        old = __builtin_amdgcn_readfirstlane(old);
        asm volatile("" ::: "memory");
        if (old == 3) {
            const f32x2* xc = (const f32x2*)(sm + K1_XCH) + cl;
            const f32x2 s = cfma(L32, cfma(L32, cfma(L32, xc[0], xc[40]), xc[80]), xc[120]);
            if (lane < 40) st128_wt(ws + WS_AGG + (size_t)(cb * 16 + rb) * 640 + lane * 16,
                                    (u32x4){f2u(s.x), f2u(s.y), (unsigned)tok, (unsigned)(tok >> 32)});
        }
        SCHED_FENCE();
        char* own = sm + wave * 20480 + 2048;
#pragma unroll
        for (int T = 0; T < 5; ++T) *(float*)(own + 8192 + (16 * T + t) * 16 + q * 4) = ag8[T];
        if (cb < 10 && t < 2) {
            const f32x4 gv = t ? acc[5][1] : acc[5][0];
            st128_wt(ws + WS_G + (cb * 2048 + 128 * rb + 32 * wc + 8 * q + 4 * t) * 4, __builtin_bit_cast(u32x4, gv));
        }
        f32x2 Wj[4];
        {
            const f32x2 zz = *(const f32x2*)(sm + K1_TAB + (7 * 80 + 2 * cl) * 8);
            const float za = 16.f * zz.x, zb = __builtin_amdgcn_fractf(16.f * zz.y);
#pragma unroll
            for (int j = 0; j < 4; ++j) {
                const int n = rb - 1 - 4 * wc - j;
                const float fn = (float)(n < 0 ? 0 : n);
                const float mg = n < 0 ? 0.f : __builtin_amdgcn_exp2f(fn * za), rv = fn * zb;
                Wj[j] = (f32x2){mg * __builtin_amdgcn_cosf(rv), mg * __builtin_amdgcn_sinf(rv)};
            }
        }
        float pre[5], Ma[5], Mb[5];
        {
            f32x2 Cc[5], L8c[5];
            const float fs = (float)(4 * wc + q), qr = (t & 1) ? -0.25f : 0.f, sg = (t & 1) ? 1.f : -1.f;
#pragma unroll
            for (int T = 0; T < 5; ++T) {
                Cc[T] = tab[80 + 16 * T]; L8c[T] = tab[240 + 16 * T];
                const f32x2 zz = tab[560 + 16 * T];
                const float e = __builtin_amdgcn_exp2f(fs * zz.x) * __builtin_amdgcn_cosf(fmaf(fs, zz.y, qr));
                Ma[T] = fmaf(Cc[T].y, swp(e), Cc[T].x * e);
                Mb[T] = sg * swp(Ma[T]);
#pragma unroll
                for (int j = 0; j < 8; ++j) {
                    const float u = acc[T][j >> 2][j & 3];
                    acc[T][j >> 2][j & 3] = fmaf(Cc[T].y, swp(u), Cc[T].x * u);
                }
                pre[T] = 0.f;
            }
            {
                for (unsigned spins = 0; spins < (1u << 22); ++spins) {
                    if (__hip_atomic_load((LAS unsigned*)(sm + K1_CNT), __ATOMIC_RELAXED, __HIP_MEMORY_SCOPE_WORKGROUP) >= 4u) break;
                    __builtin_amdgcn_s_sleep(1);
                }
                asm volatile("" ::: "memory");
                const float* xa = (const float*)(sm + K1_XCH) + t;
#pragma unroll 1
                for (int w = 0; w < wc; ++w)
#pragma unroll
                    for (int T = 0; T < 5; ++T) {
                        const f32x2 z = tab[480 + 16 * T];
                        pre[T] = fmaf(z.y, swp(pre[T]), fmaf(z.x, pre[T], xa[w * 80 + 16 * T]));
                    }
            }
#pragma unroll
            for (int T = 0; T < 5; ++T) {
                const f32x4 xs = *(const f32x4*)(own + 8192 + (16 * T + t) * 16);
#pragma unroll
                for (int k = 0; k < 3; ++k) {
                    const float nx = fmaf(L8c[T].y, swp(pre[T]), fmaf(L8c[T].x, pre[T], xs[k]));
                    pre[T] = k < q ? nx : pre[T];
                }
                pre[T] = fmaf(Cc[T].y, swp(pre[T]), Cc[T].x * pre[T]);
            }
        }
#pragma unroll
        for (int T = 0; T < 5; ++T) asm volatile("" : "+v"(acc[T][0]), "+v"(acc[T][1]), "+v"(pre[T]), "+v"(Ma[T]), "+v"(Mb[T]));
        asm volatile("" : "+v"(Wj[0]), "+v"(Wj[1]), "+v"(Wj[2]), "+v"(Wj[3]));
        SCHED_FENCE();
        if (rb > 0) {
            f32x2 cy = {0.f, 0.f};
            if (4 * wc < rb) {
                const char* agp = ws + WS_AGG + (size_t)(cb * 16 + 4 * wc) * 640 + cl * 16;
                for (unsigned spins = 0; spins < (1u << 20); ++spins) {
                    u32x4 ag[4];
#pragma unroll
                    for (int j = 0; j < 4; ++j)
                        asm volatile("global_load_dwordx4 %0, %1, off sc1" : "=v"(ag[j]) : "v"(agp + (4 * wc + j < rb ? j : 0) * 640) : "memory");
                    asm volatile("s_waitcnt vmcnt(0)" : "+v"(ag[0]), "+v"(ag[1]), "+v"(ag[2]), "+v"(ag[3]) :: "memory");
                    bool ok = true;
#pragma unroll
                    for (int j = 0; j < 4; ++j) ok = ok && ag[j][2] == (unsigned)tok && ag[j][3] == (unsigned)(tok >> 32);
                    const f32x2 c0 = cfma(Wj[1], (f32x2){u2f(ag[1][0]), u2f(ag[1][1])}, cmul_pk(Wj[0], (f32x2){u2f(ag[0][0]), u2f(ag[0][1])}));
                    const f32x2 c1 = cfma(Wj[3], (f32x2){u2f(ag[3][0]), u2f(ag[3][1])}, cmul_pk(Wj[2], (f32x2){u2f(ag[2][0]), u2f(ag[2][1])}));
                    cy = (f32x2){c0.x + c1.x, c0.y + c1.y};
                    if (wave_all(ok)) break;
                    __builtin_amdgcn_s_sleep(4);
                }
            }
            if (lane < 40) *(f32x2*)(sm + K1_PRT + (lane * 4 + wc) * 8) = cy;
            asm volatile("" ::: "memory");
            if (lane == 0) __hip_atomic_fetch_add((LAS unsigned*)(sm + K1_CNT + 4), 1u, __ATOMIC_RELAXED, __HIP_MEMORY_SCOPE_WORKGROUP);
            for (unsigned spins = 0; spins < (1u << 22); ++spins) {
                if (__hip_atomic_load((LAS unsigned*)(sm + K1_CNT + 4), __ATOMIC_RELAXED, __HIP_MEMORY_SCOPE_WORKGROUP) >= 4u) break;
                __builtin_amdgcn_s_sleep(1);
            }
            asm volatile("" ::: "memory");
#pragma unroll
            for (int T = 0; T < 5; ++T) {
                const f32x4* pp = (const f32x4*)(sm + K1_PRT + (8 * T + (t >> 1)) * 32);
                const f32x4 p0 = pp[0], p1 = pp[1];
                pre[T] = fmaf(Mb[T], (p0[1] + p0[3]) + (p1[1] + p1[3]), fmaf(Ma[T], (p0[0] + p0[2]) + (p1[0] + p1[2]), pre[T]));
            }
        }
#pragma unroll
        for (int j = 0; j < 8; ++j)
#pragma unroll
            for (int T = 0; T < 5; ++T) {
                pre[T] = fmaf(Lc[T].y, swp(pre[T]), fmaf(Lc[T].x, pre[T], acc[T][j >> 2][j & 3]));
                acc[T][j >> 2][j & 3] = pre[T];
            }
#pragma unroll
        for (int T = 0; T < 5; ++T)
            *(u32x4*)(own + (16 * T + t) * 80 + q * 16) = (u32x4){pack2(acc[T][0][0], acc[T][0][1]), pack2(acc[T][0][2], acc[T][0][3]),
                                                                  pack2(acc[T][1][0], acc[T][1][1]), pack2(acc[T][1][2], acc[T][1][3])};
        asm volatile("" ::: "memory");
        {
            const int od = cb & 1;
            char* dst = a2 + (size_t)(((5 * cb) >> 1) + od) * 8192;
            const char* rp = own + (16 * od + 8 * q + (t >> 2)) * 80 + (t & 3) * 16;
#pragma unroll
            for (int m = 0; m < 2; ++m)
#pragma unroll
                for (int rt = 0; rt < 2; ++rt) {
                    const s16x4 lo = __builtin_amdgcn_ds_read_tr16_b64_v4i16((LAS s16x4*)(rp + m * 2560 + rt * 8));
                    const s16x4 hi = __builtin_amdgcn_ds_read_tr16_b64_v4i16((LAS s16x4*)(rp + m * 2560 + rt * 8 + 320));
                    st128_wt(dst + m * 8192 + rt * 1024, __builtin_bit_cast(u32x4, __builtin_shufflevector(lo, hi, 0, 1, 2, 3, 4, 5, 6, 7)));
                }
            const char* rl = own + ((od ? 0 : 64) + 8 * (q & 1) + (t >> 2)) * 80 + (t & 3) * 16 + (q >> 1) * 8;
            const s16x4 lo = __builtin_amdgcn_ds_read_tr16_b64_v4i16((LAS s16x4*)rl);
            const s16x4 hi = __builtin_amdgcn_ds_read_tr16_b64_v4i16((LAS s16x4*)(rl + 320));
            st128_wt(dst + (od ? -8192 + 512 : 16384) + (q >> 1) * 512, __builtin_bit_cast(u32x4, __builtin_shufflevector(lo, hi, 0, 1, 2, 3, 4, 5, 6, 7)));
        }
        WAIT_LGKM0();
        if (lane == 0) __hip_atomic_fetch_add((LAS unsigned*)(sm + K1_CNT + 8), 1u, __ATOMIC_RELAXED, __HIP_MEMORY_SCOPE_WORKGROUP);
        __builtin_amdgcn_s_setprio(0);
        P2_BEGIN(0);
        P2_STEP(0, 10, 8, 7); P2_POLLED(); P2_STEP(1, 10, 8, 7); P2_STEP(2, 10, 8, 7); P2_STEP(3, 10, 8, 7); P2_STEP(4, 10, 8, 7);
        P2_STEP(5, 10, 8, 7); P2_STEP(6, 10, 13, 7); P2_STEP(7, 10, 9, 7); P2_STEP(8, 10, 5, 7); P2_STEP(9, 10, 0, 7);
        asm volatile("" : "+v"(gl[0]), "+v"(gl[1]), "+v"(gl[2]), "+v"(gl[3]) :: "memory");
    } else {
        const int bb = q & 1;
        const int trb = 26624 + (8 * q + (t >> 2)) * 192 + (lane & 3) * 8;
        const int tra = trb + bb * 32;
        const int tr5 = trb + (bb ? 0 : 160);
        f32x4 acc[6][2];
        s16x8 fx[2][2], fw[2][6];
#define M_RW(b, sb, ks, j)                                                                        \
    do {                                                                                          \
        const char* pa_ = sm + (sb) + ((j) < 5 ? tra + (j) * 32 : tr5) + (ks) * 32 * 192;         \
        const s16x4 lo_ = __builtin_amdgcn_ds_read_tr16_b64_v4i16((LAS s16x4*)pa_);               \
        const s16x4 hi_ = __builtin_amdgcn_ds_read_tr16_b64_v4i16((LAS s16x4*)(pa_ + 4 * 192));   \
        fw[b][j] = __builtin_shufflevector(lo_, hi_, 0, 1, 2, 3, 4, 5, 6, 7);                     \
    } while (0)
#define M_BIAS(j)                                                                                 \
    do {                                                                                          \
        const u32x2 bp_ = *(const u32x2*)(sm + K1_PB1 + (16 * (j) + 4 * q) * 2);                  \
        const f32x4 bv_ = {bflo(bp_[0]), bfhi(bp_[0]), bflo(bp_[1]), bfhi(bp_[1])};               \
        acc[j][0] += bv_; acc[j][1] += bv_;                                                       \
    } while (0)
#define M_MM(b, j) do { acc[j][0] = mfma16(fw[b][j], fx[b][0], acc[j][0]); acc[j][1] = mfma16(fw[b][j], fx[b][1], acc[j][1]); SCHED_FENCE(); } while (0)
#define M_BODY(it, WN)                                                                            \
    do {                                                                                          \
        constexpr bool RF_ = (it) < 8, RN_ = (it) < 9;                                            \
        constexpr int SB_ = (it) < 8 ? 0 : (it) - 7;                                              \
        M_MM(0, 0); if (RF_) { X_NEXT((it) + 2); X_M0((it) + 2, 0); } S_RX(1, BUF(it), co1); M_RW(1, BUF(it), 1, 0); SCHED_FENCE(); \
        if (RF_) { X_LD((it) + 2, 0); X_M0((it) + 2, 1); } else S_LB(SB_, 0);                     \
        SCHED_FENCE();                                                                            \
        M_MM(0, 1); M_RW(1, BUF(it), 1, 1); M_RW(1, BUF(it), 1, 2); SCHED_FENCE();                \
        if (RF_) { X_LD((it) + 2, 1); X_M0((it) + 2, 2); } else S_LB(SB_, 1);                     \
        SCHED_FENCE();                                                                            \
        M_MM(0, 2); M_RW(1, BUF(it), 1, 3); M_RW(1, BUF(it), 1, 4); SCHED_FENCE();                \
        if (RF_) { X_LD((it) + 2, 2); X_M0((it) + 2, 3); } else S_LB(SB_, 2);                     \
        SCHED_FENCE();                                                                            \
        M_MM(0, 3); M_RW(1, BUF(it), 1, 5); SCHED_FENCE();                                        \
        if (RF_) X_LD((it) + 2, 3); else S_LB(SB_, 3);                                            \
        SCHED_FENCE();                                                                            \
        M_MM(0, 4); if (!RF_) S_LB(SB_, 4);                                                       \
        SCHED_FENCE();                                                                            \
        M_MM(0, 5);                                                                               \
        if (RN_) WAIT_VM(WN);                                                                     \
        WAIT_LGKM0();                                                                             \
        __builtin_amdgcn_s_barrier();                                                             \
        asm volatile("" ::: "memory");                                                            \
        SCHED_FENCE();                                                                            \
        M_MM(1, 0); if (RN_) { S_RX(0, BUF((it) + 1), co0); M_RW(0, BUF((it) + 1), 0, 0); }       \
        if ((it) == 7) { pt_check(); S_LB(0, 0); }                                                \
        SCHED_FENCE();                                                                            \
        M_MM(1, 1); if (RN_) { M_RW(0, BUF((it) + 1), 0, 1); M_RW(0, BUF((it) + 1), 0, 2); } if ((it) == 7) S_LB(0, 1); \
        if ((it) == 4) { M_BIAS(4); M_BIAS(5); }                                                  \
        SCHED_FENCE();                                                                            \
        M_MM(1, 2); if (RN_) { M_RW(0, BUF((it) + 1), 0, 3); M_RW(0, BUF((it) + 1), 0, 4); } if ((it) == 7) S_LB(0, 2); SCHED_FENCE(); \
        M_MM(1, 3); if (RN_) M_RW(0, BUF((it) + 1), 0, 5); if ((it) == 7) S_LB(0, 3); SCHED_FENCE(); \
        M_MM(1, 4); if ((it) == 7) S_LB(0, 4);                                                    \
        if ((it) == 2 || (it) == 3) { M_BIAS(2 * (it) - 4); M_BIAS(2 * (it) - 3); }               \
        if ((it) == 5) GET_TOK();                                                                 \
        SCHED_FENCE();                                                                            \
        M_MM(1, 5);                                                                               \
    } while (0)
        asm volatile("" : "+v"(a2), "+v"(pa), "+v"(pb), "+v"(vox[0]), "+v"(vox[1]), "+v"(vox[2]), "+v"(vox[3]));
        WAIT_VM(8);
        __builtin_amdgcn_s_barrier();
        asm volatile("" ::: "memory");
        S_RX(0, BUF(0), co0); M_RW(0, BUF(0), 0, 0); M_RW(0, BUF(0), 0, 1); M_RW(0, BUF(0), 0, 2); M_RW(0, BUF(0), 0, 3); M_RW(0, BUF(0), 0, 4); M_RW(0, BUF(0), 0, 5);
#pragma unroll
        for (int j = 0; j < 6; ++j) { acc[j][0] = (f32x4){0.f, 0.f, 0.f, 0.f}; acc[j][1] = (f32x4){0.f, 0.f, 0.f, 0.f}; }
        M_BODY(0, 8);   M_BODY(1, 4);   M_BODY(2, 4);   M_BODY(3, 4);   M_BODY(4, 4);   M_BODY(5, 4);   M_BODY(6, 4);
        M_BODY(7, 4);   M_BODY(8, 10);   M_BODY(9, 0);
#pragma unroll
        for (int v = 0; v < 3; ++v) {
#pragma unroll
            for (int rt = 0; rt < 2; ++rt) {
                u32x4 o;
                if (2 * v + rt < 5) { P2_CACHE(3 * (2 * v + rt)); P2_CACHE(3 * (2 * v + rt) + 1); P2_CACHE(3 * (2 * v + rt) + 2); }
#pragma unroll
                for (int jj = 0; jj < 2; ++jj) {
                    float r[4];
#pragma unroll
                    for (int i = 0; i < 4; ++i) {
                        const float h = acc[2 * v + jj][rt][i];
                        const float e = __builtin_amdgcn_exp2f(h * LOG2E);
                        const float d = fmaf(e, e + 2.0f, 2.0f);
                        r[i] = fmaf(-2.0f, h * __builtin_amdgcn_rcpf(d), h);
                    }
                    o[2 * jj] = pack2(r[0], r[1]);
                    o[2 * jj + 1] = pack2(r[2], r[3]);
                }
                st128_wt(a2 + (size_t)(40 + 3 * cb + v) * 8192 + rt * 1024, o);
            }
        }
        P2_BEGIN(1);
        P2_STEP(0, 12, 8, 6); P2_POLLED(); P2_STEP(1, 12, 8, 6); P2_STEP(2, 12, 8, 6); P2_STEP(3, 12, 8, 6); P2_STEP(4, 12, 8, 6); P2_STEP(5, 12, 13, 6);
        P2_STEP(6, 12, 9, 6); P2_STEP(7, 12, 9, 6); P2_STEP(8, 12, 9, 6); P2_STEP(9, 12, 9, 6); P2_STEP(10, 12, 5, 6); P2_STEP(11, 12, 0, 6);
        gl[0] = gl[1] = gl[2] = gl[3] = 0u;
        if (bid == 255 && wave == 7 && lane == 0) flag_set(ws + WS_EP, tok);
    }
    };
    if (wave < 4) run(IC<0>{}, IC<0>{}); else run(IC<1>{}, IC<0>{});
    int role = wave >> 2;
    asm volatile("" : "+s"(role));
    if (role == 0) run(IC<0>{}, IC<1>{}); else run(IC<1>{}, IC<1>{});
    if (wave == 5 || wave == 6) {
        for (unsigned spins = 0; spins < (1u << 22); ++spins) {
            if (__hip_atomic_load((LAS unsigned*)(sm + K1_CNT + 8), __ATOMIC_RELAXED, __HIP_MEMORY_SCOPE_WORKGROUP) >= 4u) break;
            __builtin_amdgcn_s_sleep(1);
        }
        if (lane == 0) __hip_atomic_fetch_add((LAS unsigned*)(sm + K1_CNT + 12), 1u, __ATOMIC_RELAXED, __HIP_MEMORY_SCOPE_WORKGROUP);
        if (wave == 6) {
            for (unsigned spins = 0; spins < (1u << 22); ++spins) {
                if (__hip_atomic_load((LAS unsigned*)(sm + K1_CNT + 12), __ATOMIC_RELAXED, __HIP_MEMORY_SCOPE_WORKGROUP) >= 2u) break;
                __builtin_amdgcn_s_sleep(1);
            }
        }
        asm volatile("" ::: "memory");
    }
#pragma unroll
    for (int r = 0; r < 4; ++r)
#pragma unroll
        for (int n = 0; n < 5; ++n) {
            if (r == 0 && n < 3 && wave == n) *(float*)(sm + wave * 21504 + lane * 4) = (u2f(gl[0]) + u2f(gl[1])) + (wave < 2 ? u2f(gl[2]) + u2f(gl[3]) : 0.f);
            else *(f32x4*)(sm + wave * 20480 + (r * 5 + n) * 1024 + lane * 16) = ac2[r][n];
        }
    wg_sync();
#pragma unroll
    for (int m = 0; m < 3; ++m) {
        const int tile = wave + 8 * m;
        if (tile < 20) {
            const char* rp = sm + tile * 1024 + lane * 16;
            f32x4 p0 = *(const f32x4*)rp, p1 = *(const f32x4*)(rp + 20480), p2 = *(const f32x4*)(rp + 40960);
            if (m == 0) { p0 = wave == 0 ? ac2[0][0] : p0; p1 = wave == 1 ? ac2[0][1] : p1; p2 = wave == 2 ? ac2[0][2] : p2; }
            const f32x4 vs = (p0 + p1) + (p2 + *(const f32x4*)(rp + 61440));
            const f32x4 vm = (*(const f32x4*)(rp + 81920) + *(const f32x4*)(rp + 102400)) + (*(const f32x4*)(rp + 122880) + *(const f32x4*)(rp + 143360));
            const int r = tile / 5;
            const float* gp = (const float*)sm + 32 * (r >> 1) + 8 * (t >> 2) + 4 * (r & 1) + (t & 3);
            const float g = sigmoidf_((gp[0] + gp[5376]) + gp[10752]);
            f32x4 v = vs * (2.0f * g) + vm * (1.0f - g);
            v[0] += g * bflo(xv[m][0]) * bflo(dv[m][0]);
            v[1] += g * bfhi(xv[m][0]) * bfhi(dv[m][0]);
            v[2] += g * bflo(xv[m][1]) * bflo(dv[m][1]);
            v[3] += g * bfhi(xv[m][1]) * bfhi(dv[m][1]);
            st128_wt(out + oo[m], __builtin_bit_cast(u32x4, v));
        }
    }
}

extern "C" void kernel_launch(void* const* d_in, const int* in_sizes, int n_in, void* d_out, int out_size, void* d_ws,
                              size_t ws_size, hipStream_t stream) {
    kf<<<256, 512, 0, stream>>>((const u16*)d_in[0], (const u16*)d_in[1], (const u16*)d_in[2], (const u16*)d_in[3], (const u16*)d_in[4],
                                (const u16*)d_in[5], (const u16*)d_in[6], (const u16*)d_in[7], (const u16*)d_in[8], (const u16*)d_in[10],
                                (const u16*)d_in[11], (const u16*)d_in[12], (const u16*)d_in[13], (char*)d_ws, (float*)d_out);
}
```

```cpp
#include <hip/hip_runtime.h>
#include <stdint.h>

typedef short s16x4 __attribute__((ext_vector_type(4)));
typedef short s16x8 __attribute__((ext_vector_type(8)));
typedef __bf16 bf16x2 __attribute__((ext_vector_type(2)));
typedef float f32x2 __attribute__((ext_vector_type(2)));
typedef float f32x4 __attribute__((ext_vector_type(4)));
typedef unsigned u32x2 __attribute__((ext_vector_type(2)));
typedef unsigned u32x4 __attribute__((ext_vector_type(4)));
typedef unsigned short u16;
#define LAS __attribute__((address_space(3)))
#define GAS __attribute__((address_space(1)))
#define DI __device__ __forceinline__

constexpr int SEQ = 2048, DM = 640, NP = 640, NF = 1536;
constexpr int NKS = 88;

constexpr size_t WS_A2 = 0;
constexpr size_t WS_W2 = WS_A2 + (size_t)16 * NKS * 8192;
constexpr size_t WS_G = WS_W2 + (size_t)8 * NKS * 5120;
constexpr size_t WS_AGG = WS_G + 10 * 2048 * 4;
constexpr size_t WS_EP = WS_AGG + 16 * 16 * 640;
constexpr size_t WS_FPT = WS_EP + 256;
constexpr size_t WS_FAG = WS_FPT + 2048;
constexpr size_t WS_FP1 = WS_FAG + 2048;
constexpr size_t WS_JUNK = WS_FP1 + 16384;

DI unsigned f2u(float v) { return __builtin_bit_cast(unsigned, v); }
DI float u2f(unsigned v) { return __builtin_bit_cast(float, v); }
DI bool wave_all(bool p) { return __builtin_amdgcn_ballot_w64(!p) == 0ull; }
DI void wg_sync() {
    __builtin_amdgcn_fence(__ATOMIC_RELEASE, "workgroup");
    __builtin_amdgcn_s_barrier();
    __builtin_amdgcn_fence(__ATOMIC_ACQUIRE, "workgroup");
}
DI float sigmoidf_(float v) { return 1.0f / (1.0f + __builtin_amdgcn_exp2f(v * -1.44269504088896341f)); }
DI float bflo(unsigned v) { return __uint_as_float(v << 16); }
DI float bfhi(unsigned v) { return __uint_as_float(v & 0xffff0000u); }
DI unsigned pack2(float a, float b) {
    f32x2 p = {a, b};
    return __builtin_bit_cast(unsigned, __builtin_convertvector(p, bf16x2));
}
DI f32x4 mfma16(s16x8 a, s16x8 b, f32x4 c) { return __builtin_amdgcn_mfma_f32_16x16x32_bf16(a, b, c, 0, 0, 0); }
template <int S> DI float shr_zero(float v) {
    return __builtin_bit_cast(float, __builtin_amdgcn_update_dpp(0, __builtin_bit_cast(int, v), 0x110 + S, 0xf, 0xf, true));
}
DI float swp(float v) {
    return __builtin_bit_cast(float, __builtin_amdgcn_update_dpp(0, __builtin_bit_cast(int, v), 0xB1, 0xf, 0xf, true));
}
DI f32x2 cmul(f32x2 a, f32x2 b) { return (f32x2){a.x * b.x - a.y * b.y, a.x * b.y + a.y * b.x}; }
DI f32x2 cfma(f32x2 a, f32x2 b, f32x2 c) {
    return (f32x2){fmaf(-a.y, b.y, fmaf(a.x, b.x, c.x)), fmaf(a.y, b.x, fmaf(a.x, b.y, c.y))};
}
DI f32x2 cmul_pk(f32x2 a, f32x2 b) { return (f32x2){fmaf(-a.y, b.y, a.x * b.x), fmaf(a.y, b.x, a.x * b.y)}; }
DI f32x2 cmul_sc(f32x2 a, f32x2 b) {
    float p = a.x * b.x, r = a.x * b.y;
    asm("" : "+v"(p));
    asm("" : "+v"(r));
    return (f32x2){fmaf(-a.y, b.y, p), fmaf(a.y, b.x, r)};
}
DI float dot2bf(unsigned a, unsigned b, float c) {
    return __builtin_amdgcn_fdot2_f32_bf16(__builtin_bit_cast(bf16x2, a), __builtin_bit_cast(bf16x2, b), c, false);
}
#define WAIT_VM(n) asm volatile("s_waitcnt vmcnt(%0)" ::"n"(n) : "memory")
#define WAIT_LGKM0() asm volatile("s_waitcnt lgkmcnt(0)" ::: "memory")
#define ACQUIRE() asm volatile("buffer_inv sc1" ::: "memory")
#define SCHED_FENCE() __builtin_amdgcn_sched_barrier(0)

DI void glds16(const void* gsrc, unsigned lds_dst) {
    unsigned keep;
    asm volatile("s_mov_b32 %0, m0\n\ts_mov_b32 m0, %2\n\ts_nop 0\n\tglobal_load_lds_dwordx4 %1, off\n\ts_mov_b32 m0, %0"
                 : "=&s"(keep) : "v"(gsrc), "s"(lds_dst) : "memory");
}
DI void glds4(const void* gsrc, unsigned lds_dst) {
    unsigned keep;
    asm volatile("s_mov_b32 %0, m0\n\ts_mov_b32 m0, %2\n\ts_nop 0\n\tglobal_load_lds_dword %1, off\n\ts_mov_b32 m0, %0"
                 : "=&s"(keep) : "v"(gsrc), "s"(lds_dst) : "memory");
}
DI void glds4_c(const void* gsrc, unsigned lds_dst) {
    unsigned keep;
    asm volatile("s_mov_b32 %0, m0\n\ts_mov_b32 m0, %2\n\ts_nop 0\n\tglobal_load_lds_dword %1, off sc1\n\ts_mov_b32 m0, %0"
                 : "=&s"(keep) : "v"(gsrc), "s"(lds_dst) : "memory");
}
DI void glds16x4(const void* gsrc, unsigned lds_dst) {
    unsigned keep;
    asm volatile("s_mov_b32 %0, m0\n\ts_mov_b32 m0, %2\n\ts_nop 0\n\tglobal_load_lds_dwordx4 %1, off\n\t"
                 "global_load_lds_dwordx4 %1, off offset:1024\n\tglobal_load_lds_dwordx4 %1, off offset:2048\n\t"
                 "global_load_lds_dwordx4 %1, off offset:3072\n\ts_mov_b32 m0, %0"
                 : "=&s"(keep) : "v"(gsrc), "s"(lds_dst) : "memory");
}
DI void st128_wt(void* p, u32x4 v) { asm volatile("global_store_dwordx4 %0, %1, off sc1\n\ts_nop 1" :: "v"(p), "v"(v) : "memory"); }
DI void st64_wt(void* p, u32x2 v) { asm volatile("global_store_dwordx2 %0, %1, off sc1\n\ts_nop 1" :: "v"(p), "v"(v) : "memory"); }
DI unsigned ld32_asm(const void* p) { unsigned r; asm volatile("global_load_dword %0, %1, off" : "=v"(r) : "v"(p) : "memory"); return r; }
DI unsigned ld16_asm(const void* p) { unsigned r; asm volatile("global_load_ushort %0, %1, off" : "=v"(r) : "v"(p) : "memory"); return r; }
DI u32x2 ld64_asm(const void* p) { u32x2 r; asm volatile("global_load_dwordx2 %0, %1, off" : "=v"(r) : "v"(p) : "memory"); return r; }
DI u32x4 ld128_asm(const void* p) { u32x4 r; asm volatile("global_load_dwordx4 %0, %1, off" : "=v"(r) : "v"(p) : "memory"); return r; }
constexpr float LOG2E = 1.44269504088896341f, INV2PI = 0.15915494309189535f;
DI f32x2 lam_bar(float lr, float li, float ld) {
    const float dt = __builtin_amdgcn_exp2f(ld * LOG2E);
    const float m = __builtin_amdgcn_exp2f(dt * lr * LOG2E);
    const float rv = __builtin_amdgcn_fractf(dt * li * INV2PI);
    return (f32x2){m * __builtin_amdgcn_cosf(rv), m * __builtin_amdgcn_sinf(rv)};
}

typedef unsigned long long u64;
DI void glds16_c(const void* gsrc, unsigned lds_dst) {
    unsigned keep;
    lds_dst = __builtin_amdgcn_readfirstlane(lds_dst);
    asm volatile("s_mov_b32 %0, m0\n\ts_mov_b32 m0, %2\n\ts_nop 0\n\tglobal_load_lds_dwordx4 %1, off sc1\n\ts_mov_b32 m0, %0"
                 : "=&s"(keep) : "v"(gsrc), "s"(lds_dst) : "memory");
}
DI void glds16x4_c(const void* gsrc, unsigned lds_dst) {
    unsigned keep;
    asm volatile("s_mov_b32 %0, m0\n\ts_mov_b32 m0, %2\n\ts_nop 0\n\tglobal_load_lds_dwordx4 %1, off sc1\n\t"
                 "global_load_lds_dwordx4 %1, off offset:1024 sc1\n\tglobal_load_lds_dwordx4 %1, off offset:2048 sc1\n\t"
                 "global_load_lds_dwordx4 %1, off offset:3072 sc1\n\ts_mov_b32 m0, %0"
                 : "=&s"(keep) : "v"(gsrc), "s"(lds_dst) : "memory");
}
DI void flag_set(char* p, u64 tok) { __hip_atomic_store((GAS u64*)p, tok, __ATOMIC_RELAXED, __HIP_MEMORY_SCOPE_AGENT); }
DI void flag_wait(const char* p, bool need, u64 tok) {
    for (unsigned spins = 0; spins < (1u << 20); ++spins) {
        u64 v = tok;
        if (need) v = __hip_atomic_load((const GAS u64*)p, __ATOMIC_RELAXED, __HIP_MEMORY_SCOPE_AGENT);
        if (__all(v == tok)) break;
        __builtin_amdgcn_s_sleep(8);
    }
    asm volatile("" ::: "memory");
}

#define PRIO_OFF 1
DI void lds_arrive(char* p, int lane) {
    asm volatile("" ::: "memory");
    if (lane == 0) __hip_atomic_fetch_add((LAS unsigned*)p, 1u, __ATOMIC_RELAXED, __HIP_MEMORY_SCOPE_WORKGROUP);
    asm volatile("" ::: "memory");
}
DI void lds_wait(const char* p, unsigned n) {
    for (unsigned spins = 0; spins < (1u << 22); ++spins) {
        if (__hip_atomic_load((const LAS unsigned*)p, __ATOMIC_RELAXED, __HIP_MEMORY_SCOPE_WORKGROUP) >= n) break;
        __builtin_amdgcn_s_sleep(1);
    }
    asm volatile("" ::: "memory");
}
#define SSL 40
#define PSL0 "16"
#define PSL1 "8"
template <int V> struct IC { static constexpr int value = V; };
#define ROTF ((((rb + cb) & 3) * 5 + 1) >> 1)
constexpr int K1_STAGE = 38 * 1024;
constexpr int K1_GW = 0;
constexpr int K1_PLR = K1_GW + 1280, K1_PLI = K1_GW + 1360, K1_PLD = K1_GW + 1440, K1_PB1 = K1_GW + 1536, K1_PEP = K1_GW + 1728;
constexpr int K1_RING = 2048;
#define BUF(it) (K1_RING + ((it) % 3) * K1_STAGE)
constexpr int K1_TAB = K1_RING + 3 * K1_STAGE;
constexpr int K1_XCH = K1_TAB + 5120;
constexpr int K1_CNT = K1_XCH + 1280;
constexpr int K1_PRT = K1_CNT + 32;
constexpr int K1_PT = 123 * 1024;
constexpr int K1_FM = 140 * 1024;
constexpr int KF_LDS = 160 * 1024;

__global__ __launch_bounds__(512, 2) __attribute__((target("no-packed-fp32-ops"))) void kf(const u16* __restrict__ x, const u16* __restrict__ Lre, const u16* __restrict__ Lim,
                                             const u16* __restrict__ Bre, const u16* __restrict__ Bim, const u16* __restrict__ Cre,
                                             const u16* __restrict__ Cim, const u16* __restrict__ logdt, const u16* __restrict__ Dskip,
                                             const u16* __restrict__ w1, const u16* __restrict__ b1, const u16* __restrict__ w2,
                                             const u16* __restrict__ gw, char* __restrict__ ws, float* __restrict__ out) {
    __shared__ __attribute__((aligned(16))) char sm[KF_LDS];
    const int tid = threadIdx.x, lane = tid & 63;
    const int wave = __builtin_amdgcn_readfirstlane(tid >> 6);
    const int bid = blockIdx.x, xcd = bid & 7, idx = bid >> 3;
    const int rb = 8 * (xcd & 1) + (idx & 7);
    const int cb = 4 * (xcd >> 1) + (idx >> 3);
    const int RG = 4 * xcd + (idx & 3);
    const int CB = idx >> 2;
    const int t = lane & 15, q = lane >> 4;
    const unsigned smb = (unsigned)(size_t)(LAS char*)sm;
    if (wave < 5) {
        u64 pc0;
        asm volatile("s_getpc_b64 %0" : "=s"(pc0));
        glds4((const char*)(pc0 & ~255ull) + wave * 8192 + lane * 128, smb + K1_FM);
    }
    asm volatile("" :: "s"(x), "s"(Lre), "s"(Lim), "s"(Bre), "s"(Bim), "s"(Cre), "s"(Cim), "s"(logdt), "s"(Dskip), "s"(w1), "s"(b1), "s"(w2),
                 "s"(gw), "s"(ws), "s"(out));
    if (wave == 7) {
        const char* tp = (const char*)(out + 64 * RG * DM);
        if (lane == 1) tp = ws + WS_A2 + (size_t)rb * NKS * 8192;
        if (lane == 2) tp = ws + WS_A2 + (size_t)(RG >> 1) * NKS * 8192;
        if (lane == 3) tp = ws + WS_W2 + (size_t)CB * NKS * 5120;
        if (lane == 4) tp = ws + WS_AGG;
        if (lane == 5) tp = (const char*)Dskip;
        if (lane == 6) tp = ws + WS_A2 + (size_t)rb * NKS * 8192 + NKS * 8192 - 128;
        if (lane == 7) tp = ws + WS_A2 + (size_t)(RG >> 1) * NKS * 8192 + NKS * 8192 - 128;
        if (lane == 8) tp = ws + WS_W2 + (size_t)CB * NKS * 5120 + NKS * 5120 - 128;
        if (lane == 9) tp = (const char*)(out + (64 * RG + 63) * DM);
        glds4_c(tp, smb + K1_FM);
    }
    f32x4 ac2[4][5];
    u32x2 xv[3], dv[3];
    unsigned gl[4];
    int oo[3];
    auto run = [&](auto ROLE_, auto PART_) __attribute__((always_inline)) {
    constexpr int ROLE = decltype(ROLE_)::value, PART = decltype(PART_)::value;
    int lane = tid & 63;
    asm volatile("" : "+v"(lane));
    const int t = lane & 15, q = lane >> 4;
#define G_M0(base, c) asm volatile("s_add_i32 m0, %0, %1" :: "s"(base), "n"(c) : "scc")
#define G_LDS(vo, sb, c) asm volatile("global_load_lds_dwordx4 %0, %1 offset:%2" :: "v"(vo), "s"(sb), "n"(c) : "memory")
#define G_LDV(va, c) asm volatile("global_load_lds_dwordx4 %0, off offset:%1" :: "v"(va), "n"(c) : "memory")
#define G_NOP() asm volatile("s_nop 0")
    const int l8 = lane >> 3;
    unsigned vox[4];
    const char* wsr[3];
    const int rot = ROTF;
    const char* w1s = (const char*)w1 + (size_t)rot * (64 * NF * 2);
    const char* xs = (const char*)x + rot * 128;
#define K_WRAP(st) (((st) == 2 || (st) == 5 || (st) == 7) && rot == 10 - (st))
#define W_NEXT(st)                                                                                \
    do {                                                                                          \
        w1s += K_WRAP(st) ? -9 * (64 * NF * 2) : 64 * NF * 2;                                     \
        if ((st) == 2 || (st) == 5 || (st) == 7) { const long d_ = K_WRAP(st) ? -1280 : 0; wsr[0] += d_; wsr[1] += d_; wsr[2] += d_; } \
    } while (0)
#define X_NEXT(st) do { if ((st) == 2 || (st) == 5 || (st) == 7) xs += K_WRAP(st) ? -1280 : 0; } while (0)
    const unsigned lw = smb + 1024 * (wave & 3);
    const unsigned lm = smb + (ROLE == 0 ? 3072 : 4096) * (wave & 3);
#define X_M0(st, i) G_M0(lm, BUF(st) + 1024 * (i) - 128 * (st))
#define X_LD(st, i) G_LDS(vox[i], xs, 128 * (st))
#define W_M0(st, j) G_M0(lw, BUF(st) + 16384 + 4096 * (j) - 128 * (st))
#define W_LD(st, j) G_LDV(wsr[j], 128 * (st))
#define M_M0(st, i) G_M0(lm, BUF(st) + 26624 + 1024 * (i))
#define M_LD(i) G_LDS(vox[i], w1s, 0)
    const unsigned pu = 4 * bid + (wave & 3);
    const unsigned pkg = (pu * 1639u) >> 16, pv = pu - 400u, ptg = (pv * 1366u) >> 16;
    if constexpr (ROLE == 1) {
#pragma unroll
        for (int i = 0; i < 4; ++i) {
            const int p = 8 * i + l8;
            vox[i] = ((128 * rb + 32 * (wave & 3) + 8 * ((p & 15) >> 2) + 4 * (p >> 4) + (p & 3)) * DM + (((lane & 7) ^ ((p >> 1) & 7)) << 3)) * 2;
            if constexpr (PART == 0) {
                if (i == 0) X_M0(0, 0); else if (i == 1) X_M0(0, 1); else if (i == 2) X_M0(0, 2); else X_M0(0, 3);
                G_NOP();
                G_LDS(vox[i], xs, 0);
                SCHED_FENCE();
            }
        }
        if constexpr (PART == 0) {
            X_M0(1, 0); G_NOP(); X_LD(1, 0); X_M0(1, 1); G_NOP(); X_LD(1, 1); X_M0(1, 2); G_NOP(); X_LD(1, 2); X_M0(1, 3); G_NOP(); X_LD(1, 3);
            const bool isc = pu < 400 || pu >= 880;
            const unsigned kg = pu < 400 ? pkg : 0, nt = pu < 400 ? pu - 40 * pkg : 0;
            const unsigned o = ((isc ? 16 * nt + l8 : 32 * (pv - 48 * ptg) + l8) * DM + 64 * (isc ? kg : ptg) + ((lane & 7) ^ l8) * 8) * 2;
#pragma unroll
            for (int i = 0; i < 4; ++i) {
                const u16* p0 = isc ? ((i >> 1) ? Cim : Cre) + 8 * (i & 1) * DM : w2 + 8 * i * DM;
                G_M0(lm, K1_PT + i * 1024); G_NOP(); G_LDS(o, p0, 0);
            }
            return;
        }
    } else {
#pragma unroll
        for (int j = 0; j < 3; ++j) {
            const int p = 8 * (4 * j + wave) + l8;
            const int ch = 40 * cb + 8 * (p >> 4) + ((p & 15) >> 1);
            wsr[j] = (const char*)(((p & 1) ? Bim : Bre) + ch * DM + 64 * rot + (((lane & 7) ^ ((p >> 1) & 7)) << 3));
            if (PART == 0 && j < 2) {
                if (j == 0) W_M0(0, 0); else W_M0(0, 1);
                G_NOP();
                G_LDV(wsr[j], 0);
                SCHED_FENCE();
            }
        }
#pragma unroll
        for (int i = 0; i < 3; ++i) {
            const int gI = 64 * (3 * wave + i) + lane, row = gI / 12, cp = gI - 12 * row;
            int j = (cp >> 1) - ((row >> 3) & 1);
            j += j < 0 ? 6 : 0;
            vox[i] = (row * NF + 96 * cb + 16 * j + 8 * (cp & 1)) * 2;
            if constexpr (PART == 0) {
                if (i == 0) M_M0(0, 0); else if (i == 1) M_M0(0, 1); else M_M0(0, 2);
                G_NOP();
                M_LD(i);
                SCHED_FENCE();
            }
        }
        W_NEXT(1);
        if constexpr (PART == 0) {
            const u16* p = gw + lane * 8;
            if (wave == 3) {
                p = gw + 512 + t * 8;
                if (lane >= 16 && lane < 21) p = Lre + 40 * cb + (lane - 16) * 8;
                if (lane >= 21 && lane < 26) p = Lim + 40 * cb + (lane - 21) * 8;
                if (lane >= 26 && lane < 31) p = logdt + 40 * cb + (lane - 26) * 8;
                if (lane >= 32 && lane < 44) p = b1 + 96 * cb + (lane - 32) * 8;
                if (lane == 44) p = (const u16*)(ws + WS_EP);
            }
            glds16_c(wave < 2 ? wsr[2] : (const char*)p, wave < 2 ? lw + BUF(0) + 16384 + 8192 : smb + K1_GW + (wave - 2) * 1024);
            if (wave < 2) { W_M0(1, 2); G_NOP(); W_LD(1, 2); }
            W_M0(1, 0); G_NOP(); W_LD(1, 0); W_M0(1, 1); G_NOP(); W_LD(1, 1);
            M_M0(1, 0); G_NOP(); M_LD(0); M_M0(1, 1); G_NOP(); M_LD(1); M_M0(1, 2); G_NOP(); M_LD(2);
            return;
        }
    }
    char* ptl = sm + K1_PT + (wave & 3) * 4096;
    const int wc = wave & 3;
    const int xoff = (32 * wc + t) * 128;
    const int co0 = (q ^ ((t >> 1) & 7)) * 16, co1 = co0 ^ 64;
    char* a2 = ws + WS_A2 + (size_t)rb * NKS * 8192 + (2 * wc) * 1024 + lane * 16;
    const int rb2 = RG >> 1, hh = RG & 1;
    const int ks0 = ROLE == 0 ? 10 * wave : 40 + 12 * (wave - 4);
    const char* pa = ws + WS_A2 + ((size_t)rb2 * NKS + ks0) * 8192 + hh * 4096 + lane * 16;
    const char* pb = ws + WS_W2 + ((size_t)CB * NKS + ks0) * 5120 + 1024 + lane * 16;
    u64 tok;
#define GET_TOK() do { const u32x2 ep_ = *(const u32x2*)(sm + K1_PEP); \
                       tok = (((u64)(unsigned)__builtin_amdgcn_readfirstlane(ep_[1]) << 32) | (unsigned)__builtin_amdgcn_readfirstlane(ep_[0])) + 1ull; } while (0)
    u32x4 ptf[4];
    auto pretile_read = [&]() __attribute__((always_inline)) {
        if (pu < 400) {
#pragma unroll
            for (int i = 0; i < 4; ++i) {
                const char* pp = ptl + t * 128 + (((2 * i + (q >> 1)) ^ (t & 7)) << 4) + 8 * (q & 1);
                const u32x2 rr = *(const u32x2*)pp, ii = *(const u32x2*)(pp + 2048);
                ptf[i] = (u32x4){rr[0], rr[1], ii[0], ii[1]};
            }
        } else {
            const int r = 4 * q + (t >> 2);
#pragma unroll
            for (int i = 0; i < 4; ++i) {
                const char* pp = ptl + r * 128 + (((2 * i + ((lane >> 1) & 1)) ^ (r & 7)) << 4) + (lane & 1) * 8;
                const s16x4 lo = __builtin_amdgcn_ds_read_tr16_b64_v4i16((LAS s16x4*)pp);
                const s16x4 hi = __builtin_amdgcn_ds_read_tr16_b64_v4i16((LAS s16x4*)(pp + 2048));
                ptf[i] = __builtin_bit_cast(u32x4, __builtin_shufflevector(lo, hi, 0, 1, 2, 3, 4, 5, 6, 7));
            }
        }
    };
    auto pretile_mix = [&]() __attribute__((always_inline)) {
        if (pu < 400) {
#pragma unroll
            for (int i = 0; i < 4; ++i) {
                const unsigned r0 = ptf[i][0], r1 = ptf[i][1], i0 = ptf[i][2] ^ 0x80008000u, i1 = ptf[i][3] ^ 0x80008000u;
                ptf[i] = (u32x4){(r0 & 0xffffu) | (i0 << 16), (r0 >> 16) | (i0 & 0xffff0000u), (r1 & 0xffffu) | (i1 << 16), (r1 >> 16) | (i1 & 0xffff0000u)};
            }
        }
    };
    auto pretile_dst = [&](unsigned i) __attribute__((always_inline)) {
        const unsigned g = pu < 400 ? pu - 40 * pkg : 4 * ptg + i, KS = pu < 400 ? 4 * pkg + i : 40 + pv - 48 * ptg;
        const unsigned cB = (g * 13u) >> 6;
        unsigned o = (unsigned)WS_W2 + (cB * (5 * NKS - 5) + KS * 5 + g) * 1024u, j = (unsigned)WS_JUNK + (pu * 4 + i) * 1024u;
        asm("" : "+s"(o), "+s"(j));
        return pu < 880 ? o : j;
    };

    const int bco = wave == 6 ? 6 * 20480 + 4096 : wave * 20480;
    u32x4 Ab[4][4], Bb[3][5];
#define P2_L1(dst, P, OFF) asm volatile("global_load_dwordx4 %0, %1, off offset:" #OFF " sc1" : "=&v"(dst) : "v"(P) : "memory")
#define P2_LDB(slot, P)                                                                           \
    do { P2_L1(Bb[slot][0], P, -1024); P2_L1(Bb[slot][1], P, 0); P2_L1(Bb[slot][2], P, 1024); P2_L1(Bb[slot][3], P, 2048); P2_L1(Bb[slot][4], P, 3072); } while (0)
#define P2_LDA(slot, P)                                                                           \
    do { P2_L1(Ab[slot][0], P, 0); P2_L1(Ab[slot][1], P, 1024); P2_L1(Ab[slot][2], P, 2048); P2_L1(Ab[slot][3], P, 3072); } while (0)
    auto pt_check = [&]() __attribute__((always_inline)) {
        const u32x4 v0 = *(const u32x4*)(sm + K1_FM + lane * 16), v1 = *(const u32x4*)(sm + K1_FM + 1024 + lane * 16);
        const unsigned lo = (unsigned)tok, hi = (unsigned)(tok >> 32);
        if (!wave_all(v0[0] == lo && v0[1] == hi && v0[2] == lo && v0[3] == hi && v1[0] == lo && v1[1] == hi && v1[2] == lo && v1[3] == hi)) {
#pragma unroll 1
            for (int k = 0; k < 4; ++k) flag_wait(ws + WS_FPT + (size_t)(64 * k + lane) * 8, true, tok);
        }
    };
    const char* g3 = pb - 1024 + 3 * 5120;
#define P2_CACHE(i) glds16_c(g3 + (i) * 1024, smb + bco + (i) * 1024)
#define P2_M2(ua, ub, i)                                                                          \
    do {                                                                                          \
        ac2[(i) / 5][(i) % 5] = mfma16(__builtin_bit_cast(s16x8, Bb[ub][(i) % 5]), __builtin_bit_cast(s16x8, Ab[ua][(i) / 5]), ac2[(i) / 5][(i) % 5]); \
        ac2[((i) + 1) / 5][((i) + 1) % 5] = mfma16(__builtin_bit_cast(s16x8, Bb[ub][((i) + 1) % 5]), __builtin_bit_cast(s16x8, Ab[ua][((i) + 1) / 5]), ac2[((i) + 1) / 5][((i) + 1) % 5]); \
        SCHED_FENCE();                                                                            \
    } while (0)
#define P2_STEP(j, NS, WN, NL)                                                                    \
    do {                                                                                          \
        asm volatile("s_waitcnt vmcnt(%9)" : "+v"(Ab[(j) & 3][0]), "+v"(Ab[(j) & 3][1]), "+v"(Ab[(j) & 3][2]), "+v"(Ab[(j) & 3][3]), \
                     "+v"(Bb[(j) % 3][0]), "+v"(Bb[(j) % 3][1]), "+v"(Bb[(j) % 3][2]), "+v"(Bb[(j) % 3][3]), "+v"(Bb[(j) % 3][4]) : "n"(WN) : "memory"); \
        SCHED_FENCE();                                                                            \
        const char* pa_ = pa + ((j) + 3) * 8192;                                                  \
        const char* pb_ = pb + ((j) + 2) * 5120;                                                  \
        constexpr bool LA_ = (j) + 3 < (NS), LBG_ = (j) + 2 >= (NL) && (j) + 2 < (NS), LBL_ = (j) + 2 >= 3 && (j) + 2 < (NL); \
        P2_M2((j) & 3, (j) % 3, 0); if (LA_) { P2_L1(Ab[((j) + 3) & 3][0], pa_, 0); SCHED_FENCE(); }      \
        P2_M2((j) & 3, (j) % 3, 2); if (LA_) { P2_L1(Ab[((j) + 3) & 3][1], pa_, 1024); SCHED_FENCE(); }   \
        P2_M2((j) & 3, (j) % 3, 4); if (LA_) { P2_L1(Ab[((j) + 3) & 3][2], pa_, 2048); SCHED_FENCE(); }   \
        P2_M2((j) & 3, (j) % 3, 6); if (LA_) { P2_L1(Ab[((j) + 3) & 3][3], pa_, 3072); SCHED_FENCE(); }   \
        if (LBL_) { _Pragma("unroll") for (int n_ = 0; n_ < 5; ++n_) Bb[((j) + 2) % 3][n_] = *(const u32x4*)(sm + bco + (((j) + 2 - 3) * 5 + n_) * 1024 + lane * 16); SCHED_FENCE(); } \
        P2_M2((j) & 3, (j) % 3, 8); if (LBG_) { P2_L1(Bb[((j) + 2) % 3][0], pb_, -1024); SCHED_FENCE(); } \
        P2_M2((j) & 3, (j) % 3, 10); if (LBG_) { P2_L1(Bb[((j) + 2) % 3][1], pb_, 0); SCHED_FENCE(); }    \
        P2_M2((j) & 3, (j) % 3, 12); if (LBG_) { P2_L1(Bb[((j) + 2) % 3][2], pb_, 1024); SCHED_FENCE(); } \
        P2_M2((j) & 3, (j) % 3, 14); if (LBG_) { P2_L1(Bb[((j) + 2) % 3][3], pb_, 2048); SCHED_FENCE(); } \
        P2_M2((j) & 3, (j) % 3, 16); if (LBG_) { P2_L1(Bb[((j) + 2) % 3][4], pb_, 3072); SCHED_FENCE(); } \
        P2_M2((j) & 3, (j) % 3, 18);                                                              \
    } while (0)
    u64 pl[3];
#define POLL1(i, PSL) "s_waitcnt vmcnt(2)\n\tv_cmp_ne_u64 vcc, %" #i ", %5\n\ts_cmp_eq_u64 vcc, 0\n\ts_cbranch_scc1 2f\n\t" \
                 "global_load_dwordx2 %" #i ", %4, off sc1\n\ts_sleep " PSL "\n\t"
#define P2_POLLED() asm volatile("" : "+v"(xv[0]), "+v"(xv[1]), "+v"(xv[2]), "+v"(dv[0]), "+v"(dv[1]), "+v"(dv[2]) : "v"(pl[0]), "v"(pl[1]), "v"(pl[2]))
#define P2_BEGIN(role, PSL)                                                                        \
    do {                                                                                          \
        if ((role) == 1) { _Pragma("unroll") for (int i = 0; i < 15; ++i) P2_CACHE(i); WAIT_VM(15); } else WAIT_VM(0); \
                                                                                           \
        if (lane == 0) flag_set(ws + WS_FP1 + (size_t)(((((role) * 16 + rb) * 2 + (wc >> 1)) * 16 + cb) * 2 + (wc & 1)) * 8, tok); \
        if ((role) == 1) {         \
            lds_wait(sm + K1_CNT, 4u);                                                            \
            const char* gs_ = ws + WS_W2 + ((size_t)CB * NKS + 10 * wc + 3) * 5120 + lane * 16;   \
            _Pragma("unroll") for (int i = 0; i < 5; ++i) glds16x4_c(gs_ + i * 4096, __builtin_amdgcn_readfirstlane(smb + wc * 20480 + i * 4096)); \
        }                                                                                         \
        _Pragma("unroll") for (int m = 0; m < 3; ++m) {                                           \
            int tile = wave + 8 * m; tile = tile < 20 ? tile : wave;                              \
            const int r = tile / 5, n = tile - 5 * r;                                             \
            const int col = 80 * CB + 16 * n + 4 * q;                                             \
            oo[m] = (64 * RG + 32 * (r >> 1) + 8 * (t >> 2) + 4 * (r & 1) + (t & 3)) * DM + col;  \
            xv[m] = ld64_asm(x + oo[m]); dv[m] = ld64_asm(Dskip + col);                           \
        }                                                                                         \
        if ((role) == 0) __builtin_amdgcn_s_sleep(SSL);         \
        unsigned cnt_;                                         \
        asm volatile("global_load_dwordx2 %0, %4, off sc1\n\ts_sleep " PSL "\n\tglobal_load_dwordx2 %1, %4, off sc1\n\ts_sleep " PSL "\n\t" \
                     "global_load_dwordx2 %2, %4, off sc1\n\ts_mov_b32 %3, 0x80000\n"             \
                     "1:\n\t" POLL1(0, PSL) POLL1(1, PSL) POLL1(2, PSL)                                 \
                     "s_sub_u32 %3, %3, 1\n\ts_cbranch_scc0 1b\n"                                 \
                     "2:"                                                                         \
                     : "=&v"(pl[0]), "=&v"(pl[1]), "=&v"(pl[2]), "=&s"(cnt_)                      \
                     : "v"(ws + WS_FP1 + (size_t)(((role) * 32 + RG) * 32 + 8 * wc + (lane & 7)) * 8), "s"(tok) : "memory", "vcc", "scc"); \
        if ((role) == 0) P2_GATES();                                                              \
        P2_LDA(0, pa); P2_LDA(1, pa + 8192); P2_LDA(2, pa + 16384);                               \
        _Pragma("unroll") for (int r = 0; r < 4; ++r) _Pragma("unroll") for (int n = 0; n < 5; ++n) ac2[r][n] = (f32x4){0.f, 0.f, 0.f, 0.f}; \
    } while (0)
#define P2_GATES()                                                                                \
    _Pragma("unroll") for (int j_ = 0; j_ < 4; ++j_)                                              \
        asm volatile("global_load_dword %0, %1, off sc1" : "=&v"(gl[j_])                          \
                     : "v"(ws + WS_G + ((4 * wc + j_ < 10 ? 4 * wc + j_ : 9) * 2048 + 64 * RG + lane) * 4) : "memory")

    if constexpr (ROLE == 0) {
        f32x4 acc[6][2];
#pragma unroll
        for (int a = 0; a < 6; ++a) { acc[a][0] = (f32x4){0.f, 0.f, 0.f, 0.f}; acc[a][1] = (f32x4){0.f, 0.f, 0.f, 0.f}; }
        s16x8 fx[2][2], fw[2][5];
        const int woff = 16384 + t * 128;
        const int itg = cb >= 10 ? -1 : cb >= rot ? cb - rot : cb + 10 - rot;
#define S_RX(b, sb, co) do { fx[b][0] = *(const s16x8*)(sm + (sb) + xoff + (co)); fx[b][1] = *(const s16x8*)(sm + (sb) + xoff + 2048 + (co)); } while (0)
#define S_RW(b, sb, co, T) fw[b][T] = *(const s16x8*)(sm + (sb) + woff + (T) * 2048 + (co))
#define S_MM(b, T) do { acc[T][0] = mfma16(fx[b][0], fw[b][T], acc[T][0]); acc[T][1] = mfma16(fx[b][1], fw[b][T], acc[T][1]); SCHED_FENCE(); } while (0)
#define S_LB(slot, T) asm volatile("global_load_dwordx4 %0, %1, off sc1" : "=&v"(Bb[slot][T]) : "v"(pb + (slot) * 5120 + ((T) - 1) * 1024) : "memory")
#define S_PT(it, i) do { if ((it) == 1) asm volatile("global_store_dwordx4 %0, %1, %2 sc1\n\ts_nop 1" :: "v"(pto[i]), "v"(ptf[i]), "s"(ws) : "memory"); } while (0)
#define S_BODY(it, WN, XA0, XA1, XA2, XA3)                                                        \
    do {                                                                                          \
        constexpr bool RF_ = (it) < 7, RN_ = (it) < 9;                                            \
        constexpr int SB_ = (it) < 7 ? 0 : (it) - 7;                                              \
        S_MM(0, 0); S_RX(1, BUF(it), co1); S_RW(1, BUF(it), co1, 0); SCHED_FENCE();               \
        S_MM(0, 1); S_RW(1, BUF(it), co1, 1); S_RW(1, BUF(it), co1, 2); XA0; SCHED_FENCE();       \
        S_MM(0, 2); S_RW(1, BUF(it), co1, 3); S_RW(1, BUF(it), co1, 4); XA1; SCHED_FENCE();       \
        S_MM(0, 3); XA2; SCHED_FENCE();                                                           \
        S_MM(0, 4); XA3; SCHED_FENCE();                                                           \
        if ((it) == 0 && wave < 2) WAIT_VM(6); else if (RN_) WAIT_VM(WN);                         \
        WAIT_LGKM0();                                                                             \
        __builtin_amdgcn_s_barrier();                                                             \
        asm volatile("" ::: "memory");                                                            \
        SCHED_FENCE();                                                                            \
        S_MM(1, 0);                                                                               \
        if (__builtin_expect((it) == itg, 0)) {                                                   \
            const s16x8 g0_ = *(const s16x8*)(sm + K1_GW + (64 * cb + 8 * q) * 2), g1_ = *(const s16x8*)(sm + K1_GW + (64 * cb + 32 + 8 * q) * 2); \
            acc[5][0] = mfma16(fx[1][0], g1_, mfma16(fx[0][0], g0_, acc[5][0])); acc[5][1] = mfma16(fx[1][1], g1_, mfma16(fx[0][1], g0_, acc[5][1])); \
        }                                                                                         \
        if ((it) == 1) pretile_read();                                                            \
        if ((it) == 7) pt_check();                                                                \
        if (RF_) {                                                                                \
            W_NEXT((it) + 3);                                                                     \
            if (wave < 2) { W_M0((it) + 3, 2); G_NOP(); W_LD((it) + 3, 2); }                      \
            else if ((it) == 3) { if (lane == 0) flag_set(ws + WS_FPT + (size_t)bid * 8, tok); }    \
            else if ((it) == 6) glds16_c(ws + WS_FPT + (wave - 2) * 1024 + lane * 16, smb + K1_FM + (wave - 2) * 1024); \
            W_M0((it) + 3, 0);                                                                    \
        }                                                                                         \
        if (RN_) { S_RX(0, BUF((it) + 1), co0); S_RW(0, BUF((it) + 1), co0, 0); }                 \
        SCHED_FENCE();                                                                            \
        if (RF_) { W_LD((it) + 3, 0); W_M0((it) + 3, 1); } else S_LB(SB_, 0);                     \
        SCHED_FENCE();                                                                            \
        S_MM(1, 1); if (RN_) { S_RW(0, BUF((it) + 1), co0, 1); S_RW(0, BUF((it) + 1), co0, 2); }  \
        SCHED_FENCE();                                                                            \
        if (RF_) { W_LD((it) + 3, 1); M_M0((it) + 3, 0); } else S_LB(SB_, 1);                     \
        if ((it) == 1) pretile_mix();                                                             \
        S_PT(it, 0); SCHED_FENCE();                                                               \
        S_MM(1, 2); if (RN_) { S_RW(0, BUF((it) + 1), co0, 3); S_RW(0, BUF((it) + 1), co0, 4); }  \
        SCHED_FENCE();                                                                            \
        if (RF_) { M_LD(0); M_M0((it) + 3, 1); } else S_LB(SB_, 2);                               \
        S_PT(it, 1); SCHED_FENCE();                                                               \
        S_MM(1, 3);                                                                               \
        if (RF_) { M_LD(1); M_M0((it) + 3, 2); } else S_LB(SB_, 3);                               \
        S_PT(it, 2); SCHED_FENCE();                                                               \
        S_MM(1, 4);                                                                               \
        if (RF_) M_LD(2); else S_LB(SB_, 4);                                                      \
        S_PT(it, 3); SCHED_FENCE();                                                               \
    } while (0)
        if (wave == 0 && lane < 2) *(u32x4*)(sm + K1_CNT + lane * 16) = (u32x4){0u, 0u, 0u, 0u};
        unsigned pto[4];
#pragma unroll
        for (int i = 0; i < 4; ++i) pto[i] = pretile_dst(i) + lane * 16;
        asm volatile("" : "+v"(a2), "+v"(pa), "+v"(pb), "+v"(pto[0]), "+v"(pto[1]), "+v"(pto[2]), "+v"(pto[3]));
        asm volatile("" : "+v"(wsr[0]), "+v"(wsr[1]), "+v"(wsr[2]), "+v"(vox[0]), "+v"(vox[1]), "+v"(vox[2]));
        if (wave < 2) WAIT_VM(6); else WAIT_VM(5);
        WAIT_LGKM0();
        __builtin_amdgcn_s_barrier();
        asm volatile("" ::: "memory");
        S_RX(0, BUF(0), co0); S_RW(0, BUF(0), co0, 0); S_RW(0, BUF(0), co0, 1); S_RW(0, BUF(0), co0, 2); S_RW(0, BUF(0), co0, 3); S_RW(0, BUF(0), co0, 4);
        const int cl = lane < 40 ? lane : 39;
        float lr, li, za, zb;
        f32x2 pA, pB;
#define TB0() do { lr = bflo(*(const u16*)(sm + K1_PLR + cl * 2)); li = bflo(*(const u16*)(sm + K1_PLI + cl * 2)); za = bflo(*(const u16*)(sm + K1_PLD + cl * 2)); } while (0)
#define TB1() do { const float dt = __builtin_amdgcn_exp2f(za * LOG2E); za = dt * lr * LOG2E; zb = dt * li * INV2PI;                   \
                   const float nA = wc == 1 ? 8.f : wc == 2 ? 24.f : 1.f;                                                             \
                   const float mA = __builtin_amdgcn_exp2f(nA * za), rA = __builtin_amdgcn_fractf(nA * zb);                           \
                   pA = (f32x2){mA * __builtin_amdgcn_cosf(rA), mA * __builtin_amdgcn_sinf(rA)}; } while (0)
#define TB2() do { const float nB = wc == 1 ? 16.f : wc == 2 ? 32.f : 0.f;                                                            \
                   const float mB = __builtin_amdgcn_exp2f(nB * za), rB = __builtin_amdgcn_fractf(nB * zb);                           \
                   pB = (f32x2){mB * __builtin_amdgcn_cosf(rB), mB * __builtin_amdgcn_sinf(rB)}; } while (0)
#define TB3() do { const float nr = pA.x - 1.0f, ni = pA.y, rden = 1.0f / (lr * lr + li * li);                                        \
                   const float z8 = __builtin_amdgcn_fractf(8.f * zb);                                                                \
                   if (wc == 3) pA = (f32x2){(nr * lr + ni * li) * rden, (ni * lr - nr * li) * rden};                 \
                   f32x4* tb = (f32x4*)(sm + K1_TAB + cl * 16);                                              \
                   tb[wc == 0 ? 0 : wc == 1 ? 120 : wc == 2 ? 200 : 40] = (f32x4){pA.x, -pA.y, pA.x, pA.y};                           \
                   tb[wc == 0 ? 80 : wc == 1 ? 160 : wc == 2 ? 240 : 280] = wc == 3 ? (f32x4){8.f * za, z8, 8.f * za, z8} : (f32x4){pB.x, -pB.y, pB.x, pB.y}; } while (0)
        const f32x2* tab = (const f32x2*)(sm + K1_TAB) + t;
        f32x2 Wj[4];
        float Ma[5], Mb[5];
#define PRE_W(j0)                                                                                 \
    do {                                                                                          \
        const f32x2 zz = *(const f32x2*)(sm + K1_TAB + (7 * 80 + 2 * cl) * 8);                    \
        const float za = 16.f * zz.x, zb = __builtin_amdgcn_fractf(16.f * zz.y);                  \
        _Pragma("unroll") for (int j = (j0); j < (j0) + 2; ++j) {                                 \
            const int n = rb - 1 - 4 * wc - j;                                                    \
            const float fn = (float)(n < 0 ? 0 : n);                                              \
            const float mg = n < 0 ? 0.f : __builtin_amdgcn_exp2f(fn * za), rv = fn * zb;         \
            Wj[j] = (f32x2){mg * __builtin_amdgcn_cosf(rv), mg * __builtin_amdgcn_sinf(rv)};      \
            asm volatile("" : "+v"(Wj[j]));                                                       \
        }                                                                                         \
    } while (0)
#define PRE_M(T0, T1)                                                                             \
    do {                                                                                          \
        const float fs = (float)(4 * wc + q), qr = (t & 1) ? -0.25f : 0.f, sg = (t & 1) ? 1.f : -1.f; \
        _Pragma("unroll") for (int T = (T0); T < (T1); ++T) {                                     \
            const f32x2 cc = tab[80 + 16 * T], zz = tab[560 + 16 * T];                            \
            const float e = __builtin_amdgcn_exp2f(fs * zz.x) * __builtin_amdgcn_cosf(fmaf(fs, zz.y, qr)); \
            Ma[T] = fmaf(cc.y, swp(e), cc.x * e);                                                 \
            Mb[T] = sg * swp(Ma[T]);                                                              \
            asm volatile("" : "+v"(Ma[T]), "+v"(Mb[T]));                                          \
        }                                                                                         \
    } while (0)
#define SA0() do { W_NEXT(2); if (wave < 2) { W_M0(2, 2); G_NOP(); W_LD(2, 2); } W_M0(2, 0); G_NOP(); W_LD(2, 0); W_M0(2, 1); } while (0)
#define SA1() do { SCHED_FENCE(); W_LD(2, 1); M_M0(2, 0); G_NOP(); M_LD(0); M_M0(2, 1); } while (0)
#define SA2() do { M_LD(1); M_M0(2, 2); } while (0)
        S_BODY(0, 5, SA0(), SA1(), SA2(), M_LD(2));   S_BODY(1, 5, TB0(), , TB1(), );   S_BODY(2, 9, , TB2(), GET_TOK(), );   S_BODY(3, 5, , TB3(), , );
        S_BODY(4, 6, , , PRE_W(0), );   S_BODY(5, 5, , , PRE_W(2), );   S_BODY(6, 5, , , PRE_M(0, 2), );
        S_BODY(7, 5, , , PRE_M(2, 4), );   S_BODY(8, 5, , , PRE_M(4, 5), );
        f32x2 Lc[5], Sc[5];
#pragma unroll
        for (int T = 0; T < 5; ++T) { Lc[T] = tab[16 * T]; Sc[T] = tab[(5 - q) * 80 + 16 * T]; }
        const f32x2 L32 = *(const f32x2*)(sm + K1_TAB + (6 * 80 + 2 * cl + 1) * 8);
        S_BODY(9, 0, , , , );
        __builtin_amdgcn_s_setprio(3);
        float ag8[5];
#pragma unroll
        for (int T = 0; T < 5; ++T) ag8[T] = acc[T][0][0];
#pragma unroll
        for (int j = 1; j < 8; ++j)
#pragma unroll
            for (int T = 0; T < 5; ++T) ag8[T] = fmaf(Lc[T].y, swp(ag8[T]), fmaf(Lc[T].x, ag8[T], acc[T][j >> 2][j & 3]));
        {
            unsigned sv[5];
#pragma unroll
            for (int T = 0; T < 5; ++T) sv[T] = f2u(fmaf(Sc[T].y, swp(ag8[T]), Sc[T].x * ag8[T]));
            const auto r01 = __builtin_amdgcn_permlane32_swap(sv[0], sv[1], false, false);
            const auto r23 = __builtin_amdgcn_permlane32_swap(sv[2], sv[3], false, false);
            const auto r44 = __builtin_amdgcn_permlane32_swap(sv[4], sv[4], false, false);
            const unsigned p4 = f2u(u2f(r44[0]) + u2f(r44[1]));
            const auto ra = __builtin_amdgcn_permlane16_swap(f2u(u2f(r01[0]) + u2f(r01[1])), f2u(u2f(r23[0]) + u2f(r23[1])), false, false);
            const auto rb4 = __builtin_amdgcn_permlane16_swap(p4, p4, false, false);
            float* xa = (float*)(sm + K1_XCH) + wc * 80 + t;
            xa[16 * (((q & 1) << 1) | (q >> 1))] = u2f(ra[0]) + u2f(ra[1]);
            xa[64] = u2f(rb4[0]) + u2f(rb4[1]);
        }
        asm volatile("" ::: "memory");
        unsigned old = 0;
        if (lane == 0) old = __hip_atomic_fetch_add((LAS unsigned*)(sm + K1_CNT), 1u, __ATOMIC_RELAXED, __HIP_MEMORY_SCOPE_WORKGROUP);
        old = __builtin_amdgcn_readfirstlane(old);
        asm volatile("" ::: "memory");
        if (old == 3) {
            const f32x2* xc = (const f32x2*)(sm + K1_XCH) + cl;
            const f32x2 s = cfma(L32, cfma(L32, cfma(L32, xc[0], xc[40]), xc[80]), xc[120]);
            if (lane < 40) st128_wt(ws + WS_AGG + (size_t)(cb * 16 + rb) * 640 + lane * 16,
                                    (u32x4){f2u(s.x), f2u(s.y), (unsigned)tok, (unsigned)(tok >> 32)});
        }
        SCHED_FENCE();
        __builtin_amdgcn_s_setprio(PRIO_OFF);
        char* own = sm + (wc < 2 ? 5 * 20480 : 8 * 20480) - 5120 + (wc & 1) * 2560;
#pragma unroll
        for (int T = 0; T < 5; ++T) *(float*)(own + (16 * T + t) * 16 + q * 4) = ag8[T];
        if (cb < 10 && t < 2) {
            const f32x4 gv = t ? acc[5][1] : acc[5][0];
            st128_wt(ws + WS_G + (cb * 2048 + 128 * rb + 32 * wc + 8 * q + 4 * t) * 4, __builtin_bit_cast(u32x4, gv));
        }
        float pre[5];
        {
            f32x2 Cc[5], L8c[5];
#pragma unroll
            for (int T = 0; T < 5; ++T) {
                Cc[T] = tab[80 + 16 * T]; L8c[T] = tab[240 + 16 * T];
#pragma unroll
                for (int j = 0; j < 8; ++j) {
                    const float u = acc[T][j >> 2][j & 3];
                    acc[T][j >> 2][j & 3] = fmaf(Cc[T].y, swp(u), Cc[T].x * u);
                }
                pre[T] = 0.f;
            }
            {
                for (unsigned spins = 0; spins < (1u << 22); ++spins) {
                    if (__hip_atomic_load((LAS unsigned*)(sm + K1_CNT), __ATOMIC_RELAXED, __HIP_MEMORY_SCOPE_WORKGROUP) >= 4u) break;
                    __builtin_amdgcn_s_sleep(1);
                }
                asm volatile("" ::: "memory");
                const float* xa = (const float*)(sm + K1_XCH) + t;
#pragma unroll 1
                for (int w = 0; w < wc; ++w)
#pragma unroll
                    for (int T = 0; T < 5; ++T) {
                        const f32x2 z = tab[480 + 16 * T];
                        pre[T] = fmaf(z.y, swp(pre[T]), fmaf(z.x, pre[T], xa[w * 80 + 16 * T]));
                    }
            }
#pragma unroll
            for (int T = 0; T < 5; ++T) {
                const f32x4 xs = *(const f32x4*)(own + (16 * T + t) * 16);
#pragma unroll
                for (int k = 0; k < 3; ++k) {
                    const float nx = fmaf(L8c[T].y, swp(pre[T]), fmaf(L8c[T].x, pre[T], xs[k]));
                    pre[T] = k < q ? nx : pre[T];
                }
                pre[T] = fmaf(Cc[T].y, swp(pre[T]), Cc[T].x * pre[T]);
            }
        }
#pragma unroll
        for (int T = 0; T < 5; ++T) asm volatile("" : "+v"(acc[T][0]), "+v"(acc[T][1]), "+v"(pre[T]));
        SCHED_FENCE();
        __builtin_amdgcn_s_setprio(3);
        if (rb > 0) {
            f32x2 cy = {0.f, 0.f};
            if (4 * wc < rb) {
                const char* agp = ws + WS_AGG + (size_t)(cb * 16 + 4 * wc) * 640 + cl * 16;
                for (unsigned spins = 0; spins < (1u << 20); ++spins) {
                    u32x4 ag[4];
#pragma unroll
                    for (int j = 0; j < 4; ++j)
                        asm volatile("global_load_dwordx4 %0, %1, off sc1" : "=v"(ag[j]) : "v"(agp + (4 * wc + j < rb ? j : 0) * 640) : "memory");
                    asm volatile("s_waitcnt vmcnt(0)" : "+v"(ag[0]), "+v"(ag[1]), "+v"(ag[2]), "+v"(ag[3]) :: "memory");
                    bool ok = true;
#pragma unroll
                    for (int j = 0; j < 4; ++j) ok = ok && ag[j][2] == (unsigned)tok && ag[j][3] == (unsigned)(tok >> 32);
                    const f32x2 c0 = cfma(Wj[1], (f32x2){u2f(ag[1][0]), u2f(ag[1][1])}, cmul_pk(Wj[0], (f32x2){u2f(ag[0][0]), u2f(ag[0][1])}));
                    const f32x2 c1 = cfma(Wj[3], (f32x2){u2f(ag[3][0]), u2f(ag[3][1])}, cmul_pk(Wj[2], (f32x2){u2f(ag[2][0]), u2f(ag[2][1])}));
                    cy = (f32x2){c0.x + c1.x, c0.y + c1.y};
                    if (wave_all(ok)) break;
                    __builtin_amdgcn_s_sleep(4);
                }
            }
            if (lane < 40) *(f32x2*)(sm + K1_PRT + (lane * 4 + wc) * 8) = cy;
            asm volatile("" ::: "memory");
            if (lane == 0) __hip_atomic_fetch_add((LAS unsigned*)(sm + K1_CNT + 4), 1u, __ATOMIC_RELAXED, __HIP_MEMORY_SCOPE_WORKGROUP);
            for (unsigned spins = 0; spins < (1u << 22); ++spins) {
                if (__hip_atomic_load((LAS unsigned*)(sm + K1_CNT + 4), __ATOMIC_RELAXED, __HIP_MEMORY_SCOPE_WORKGROUP) >= 4u) break;
                __builtin_amdgcn_s_sleep(1);
            }
            asm volatile("" ::: "memory");
#pragma unroll
            for (int T = 0; T < 5; ++T) {
                const f32x4* pp = (const f32x4*)(sm + K1_PRT + (8 * T + (t >> 1)) * 32);
                const f32x4 p0 = pp[0], p1 = pp[1];
                pre[T] = fmaf(Mb[T], (p0[1] + p0[3]) + (p1[1] + p1[3]), fmaf(Ma[T], (p0[0] + p0[2]) + (p1[0] + p1[2]), pre[T]));
            }
        }
#pragma unroll
        for (int j = 0; j < 8; ++j)
#pragma unroll
            for (int T = 0; T < 5; ++T) {
                pre[T] = fmaf(Lc[T].y, swp(pre[T]), fmaf(Lc[T].x, pre[T], acc[T][j >> 2][j & 3]));
                acc[T][j >> 2][j & 3] = pre[T];
            }
        {
            const int od = cb & 1;
            char* dst = a2 + (size_t)((5 * cb) >> 1) * 8192;
            char* wp0 = own + (16 * od + t) * 80 + q * 16;
            char* wp1 = own + (16 * (od ^ 1) + t) * 80 + q * 16;
            const char* rp = own + (8 * q + (t >> 2)) * 80 + (t & 3) * 16;
            const char* rl = own + (16 * od + 8 * (q & 1) + (t >> 2)) * 80 + (t & 3) * 16 + (q >> 1) * 8;
#define H_WR(T) *(u32x4*)(((T) & 1) ? wp1 : wp0) = (u32x4){pack2(acc[T][0][0], acc[T][0][1]), pack2(acc[T][0][2], acc[T][0][3]), \
                                                           pack2(acc[T][1][0], acc[T][1][1]), pack2(acc[T][1][2], acc[T][1][3])}
#define H_RD(p) __builtin_bit_cast(u32x4, __builtin_shufflevector(__builtin_amdgcn_ds_read_tr16_b64_v4i16((LAS s16x4*)(p)), \
                                                                  __builtin_amdgcn_ds_read_tr16_b64_v4i16((LAS s16x4*)((p) + 320)), 0, 1, 2, 3, 4, 5, 6, 7))
#define H_PAIR(ks) do { asm volatile("" ::: "memory"); hf[2 * (ks)] = H_RD(rp); hf[2 * (ks) + 1] = H_RD(rp + 8); asm volatile("" ::: "memory"); } while (0)
#define H_LONE() do { asm volatile("" ::: "memory"); hf[4] = H_RD(rl); asm volatile("" ::: "memory"); } while (0)
            u32x4 hf[5];
            if (od) { H_WR(0); H_LONE(); H_WR(1); H_WR(2); H_PAIR(0); H_WR(3); H_WR(4); H_PAIR(1); }
            else { H_WR(0); H_WR(1); H_PAIR(0); H_WR(2); H_WR(3); H_PAIR(1); H_WR(4); H_LONE(); }
            st128_wt(dst + od * 8192, hf[0]); st128_wt(dst + od * 8192 + 1024, hf[1]);
            st128_wt(dst + od * 8192 + 8192, hf[2]); st128_wt(dst + od * 8192 + 9216, hf[3]);
            st128_wt(dst + (od ? 512 : 16384) + (q >> 1) * 512, hf[4]);
        }
        WAIT_LGKM0();
        if (lane == 0) __hip_atomic_fetch_add((LAS unsigned*)(sm + K1_CNT + 8), 1u, __ATOMIC_RELAXED, __HIP_MEMORY_SCOPE_WORKGROUP);
        __builtin_amdgcn_s_setprio(0);
        P2_BEGIN(0, PSL0);
        P2_STEP(0, 10, 8, 7); P2_POLLED();
        lds_wait(sm + K1_CNT + 16 + 4 * wc, 1u);
        lds_arrive(sm + K1_CNT + 12, lane);
        P2_STEP(1, 10, 8, 7); P2_STEP(2, 10, 8, 7); P2_STEP(3, 10, 8, 7); P2_STEP(4, 10, 8, 7);
        P2_STEP(5, 10, 8, 7); P2_STEP(6, 10, 13, 7); P2_STEP(7, 10, 9, 7); P2_STEP(8, 10, 5, 7); P2_STEP(9, 10, 0, 7);
        asm volatile("" : "+v"(gl[0]), "+v"(gl[1]), "+v"(gl[2]), "+v"(gl[3]) :: "memory");
    } else {
        const int bb = q & 1;
        const int trb = 26624 + (8 * q + (t >> 2)) * 192 + (lane & 3) * 8;
        const int tra = trb + bb * 32;
        const int tr5 = trb + (bb ? 0 : 160);
        f32x4 acc[6][2];
        s16x8 fx[2][2], fw[2][6];
#define M_RW(b, sb, ks, j)                                                                        \
    do {                                                                                          \
        const char* pa_ = sm + (sb) + ((j) < 5 ? tra + (j) * 32 : tr5) + (ks) * 32 * 192;         \
        const s16x4 lo_ = __builtin_amdgcn_ds_read_tr16_b64_v4i16((LAS s16x4*)pa_);               \
        const s16x4 hi_ = __builtin_amdgcn_ds_read_tr16_b64_v4i16((LAS s16x4*)(pa_ + 4 * 192));   \
        fw[b][j] = __builtin_shufflevector(lo_, hi_, 0, 1, 2, 3, 4, 5, 6, 7);                     \
    } while (0)
#define M_BIAS(j)                                                                                 \
    do {                                                                                          \
        const u32x2 bp_ = *(const u32x2*)(sm + K1_PB1 + (16 * (j) + 4 * q) * 2);                  \
        const f32x4 bv_ = {bflo(bp_[0]), bfhi(bp_[0]), bflo(bp_[1]), bfhi(bp_[1])};               \
        acc[j][0] += bv_; acc[j][1] += bv_;                                                       \
    } while (0)
#define M_MM(b, j) do { acc[j][0] = mfma16(fw[b][j], fx[b][0], acc[j][0]); acc[j][1] = mfma16(fw[b][j], fx[b][1], acc[j][1]); SCHED_FENCE(); } while (0)
#define M_BODY(it, WN)                                                                            \
    do {                                                                                          \
        constexpr bool RF_ = (it) < 8, RN_ = (it) < 9;                                            \
        constexpr int SB_ = (it) < 8 ? 0 : (it) - 7;                                              \
        M_MM(0, 0); if (RF_) { X_NEXT((it) + 2); X_M0((it) + 2, 0); } S_RX(1, BUF(it), co1); M_RW(1, BUF(it), 1, 0); SCHED_FENCE(); \
        if (RF_) { X_LD((it) + 2, 0); X_M0((it) + 2, 1); } else S_LB(SB_, 0);                     \
        SCHED_FENCE();                                                                            \
        M_MM(0, 1); M_RW(1, BUF(it), 1, 1); M_RW(1, BUF(it), 1, 2); SCHED_FENCE();                \
        if (RF_) { X_LD((it) + 2, 1); X_M0((it) + 2, 2); } else S_LB(SB_, 1);                     \
        SCHED_FENCE();                                                                            \
        M_MM(0, 2); M_RW(1, BUF(it), 1, 3); M_RW(1, BUF(it), 1, 4); SCHED_FENCE();                \
        if (RF_) { X_LD((it) + 2, 2); X_M0((it) + 2, 3); } else S_LB(SB_, 2);                     \
        SCHED_FENCE();                                                                            \
        M_MM(0, 3); M_RW(1, BUF(it), 1, 5); SCHED_FENCE();                                        \
        if (RF_) X_LD((it) + 2, 3); else S_LB(SB_, 3);                                            \
        SCHED_FENCE();                                                                            \
        M_MM(0, 4); if (!RF_) S_LB(SB_, 4);                                                       \
        SCHED_FENCE();                                                                            \
        M_MM(0, 5);                                                                               \
        if (RN_) WAIT_VM(WN);                                                                     \
        WAIT_LGKM0();                                                                             \
        __builtin_amdgcn_s_barrier();                                                             \
        asm volatile("" ::: "memory");                                                            \
        SCHED_FENCE();                                                                            \
        M_MM(1, 0); if (RN_) { S_RX(0, BUF((it) + 1), co0); M_RW(0, BUF((it) + 1), 0, 0); }       \
        if ((it) == 7) { pt_check(); S_LB(0, 0); }                                                \
        SCHED_FENCE();                                                                            \
        M_MM(1, 1); if (RN_) { M_RW(0, BUF((it) + 1), 0, 1); M_RW(0, BUF((it) + 1), 0, 2); } if ((it) == 7) S_LB(0, 1); \
        if ((it) == 4) { M_BIAS(4); M_BIAS(5); }                                                  \
        SCHED_FENCE();                                                                            \
        M_MM(1, 2); if (RN_) { M_RW(0, BUF((it) + 1), 0, 3); M_RW(0, BUF((it) + 1), 0, 4); } if ((it) == 7) S_LB(0, 2); SCHED_FENCE(); \
        M_MM(1, 3); if (RN_) M_RW(0, BUF((it) + 1), 0, 5); if ((it) == 7) S_LB(0, 3); SCHED_FENCE(); \
        M_MM(1, 4); if ((it) == 7) S_LB(0, 4);                                                    \
        if ((it) == 2 || (it) == 3) { M_BIAS(2 * (it) - 4); M_BIAS(2 * (it) - 3); }               \
        if ((it) == 5) GET_TOK();                                                                 \
        SCHED_FENCE();                                                                            \
        M_MM(1, 5);                                                                               \
    } while (0)
        asm volatile("" : "+v"(a2), "+v"(pa), "+v"(pb), "+v"(vox[0]), "+v"(vox[1]), "+v"(vox[2]), "+v"(vox[3]));
        WAIT_VM(8);
        __builtin_amdgcn_s_barrier();
        asm volatile("" ::: "memory");
        S_RX(0, BUF(0), co0); M_RW(0, BUF(0), 0, 0); M_RW(0, BUF(0), 0, 1); M_RW(0, BUF(0), 0, 2); M_RW(0, BUF(0), 0, 3); M_RW(0, BUF(0), 0, 4); M_RW(0, BUF(0), 0, 5);
#pragma unroll
        for (int j = 0; j < 6; ++j) { acc[j][0] = (f32x4){0.f, 0.f, 0.f, 0.f}; acc[j][1] = (f32x4){0.f, 0.f, 0.f, 0.f}; }
        M_BODY(0, 8);   M_BODY(1, 4);   M_BODY(2, 4);   M_BODY(3, 4);   M_BODY(4, 4);   M_BODY(5, 4);   M_BODY(6, 4);
        M_BODY(7, 4);   M_BODY(8, 10);   M_BODY(9, 0);
        __builtin_amdgcn_s_setprio(2);
#pragma unroll
        for (int v = 0; v < 3; ++v) {
#pragma unroll
            for (int rt = 0; rt < 2; ++rt) {
                u32x4 o;
#pragma unroll
                for (int jj = 0; jj < 2; ++jj) {
                    float r[4];
#pragma unroll
                    for (int i = 0; i < 4; ++i) {
                        const float h = acc[2 * v + jj][rt][i];
                        const float e = __builtin_amdgcn_exp2f(h * LOG2E);
                        const float d = fmaf(e, fmaf(0.5f, e, 1.0f), 1.0f);
                        r[i] = fmaf(-h, __builtin_amdgcn_rcpf(d), h);
                    }
                    o[2 * jj] = pack2(r[0], r[1]);
                    o[2 * jj + 1] = pack2(r[2], r[3]);
                }
                st128_wt(a2 + (size_t)(40 + 3 * cb + v) * 8192 + rt * 1024, o);
            }
        }
        __builtin_amdgcn_s_setprio(0);
        P2_BEGIN(1, PSL1);
        P2_STEP(0, 12, 8, 6); P2_POLLED();
        lds_arrive(sm + K1_CNT + 16 + 4 * wc, lane);
        P2_STEP(1, 12, 8, 6); P2_STEP(2, 12, 8, 6); P2_STEP(3, 12, 8, 6); P2_STEP(4, 12, 8, 6); P2_STEP(5, 12, 13, 6);
        P2_STEP(6, 12, 9, 6); P2_STEP(7, 12, 9, 6); P2_STEP(8, 12, 9, 6);
        lds_wait(sm + K1_CNT + 8, 4u);
        lds_arrive(sm + K1_CNT + 12, lane);
        P2_STEP(9, 12, 9, 6); P2_STEP(10, 12, 5, 6); P2_STEP(11, 12, 0, 6);
        gl[0] = gl[1] = gl[2] = gl[3] = 0u;
        if (bid == 255 && wave == 7 && lane == 0) flag_set(ws + WS_EP, tok);
    }
    };
    if (wave < 4) run(IC<0>{}, IC<0>{}); else run(IC<1>{}, IC<0>{});
    int role = wave >> 2;
    asm volatile("" : "+s"(role));
    if (role == 0) run(IC<0>{}, IC<1>{}); else run(IC<1>{}, IC<1>{});
    if (wave == 6) lds_wait(sm + K1_CNT + 12, 8u);
#pragma unroll
    for (int r = 0; r < 4; ++r)
#pragma unroll
        for (int n = 0; n < 5; ++n) {
            if (r == 0 && n < 3 && wave == n) *(float*)(sm + wave * 21504 + lane * 4) = (u2f(gl[0]) + u2f(gl[1])) + (wave < 2 ? u2f(gl[2]) + u2f(gl[3]) : 0.f);
            else *(f32x4*)(sm + wave * 20480 + (r * 5 + n) * 1024 + lane * 16) = ac2[r][n];
        }
    wg_sync();
#pragma unroll
    for (int m = 0; m < 3; ++m) {
        const int tile = wave + 8 * m;
        if (tile < 20) {
            const char* rp = sm + tile * 1024 + lane * 16;
            f32x4 p0 = *(const f32x4*)rp, p1 = *(const f32x4*)(rp + 20480), p2 = *(const f32x4*)(rp + 40960);
            if (m == 0) { p0 = wave == 0 ? ac2[0][0] : p0; p1 = wave == 1 ? ac2[0][1] : p1; p2 = wave == 2 ? ac2[0][2] : p2; }
            const f32x4 vs = (p0 + p1) + (p2 + *(const f32x4*)(rp + 61440));
            const f32x4 vm = (*(const f32x4*)(rp + 81920) + *(const f32x4*)(rp + 102400)) + (*(const f32x4*)(rp + 122880) + *(const f32x4*)(rp + 143360));
            const int r = tile / 5;
            const float* gp = (const float*)sm + 32 * (r >> 1) + 8 * (t >> 2) + 4 * (r & 1) + (t & 3);
            const float g = sigmoidf_((gp[0] + gp[5376]) + gp[10752]);
            f32x4 v = vs * (2.0f * g) + vm * (1.0f - g);
            v[0] += g * bflo(xv[m][0]) * bflo(dv[m][0]);
            v[1] += g * bfhi(xv[m][0]) * bfhi(dv[m][0]);
            v[2] += g * bflo(xv[m][1]) * bflo(dv[m][1]);
            v[3] += g * bfhi(xv[m][1]) * bfhi(dv[m][1]);
            st128_wt(out + oo[m], __builtin_bit_cast(u32x4, v));
        }
    }
}

extern "C" void kernel_launch(void* const* d_in, const int* in_sizes, int n_in, void* d_out, int out_size, void* d_ws,
                              size_t ws_size, hipStream_t stream) {
    kf<<<256, 512, 0, stream>>>((const u16*)d_in[0], (const u16*)d_in[1], (const u16*)d_in[2], (const u16*)d_in[3], (const u16*)d_in[4],
                                (const u16*)d_in[5], (const u16*)d_in[6], (const u16*)d_in[7], (const u16*)d_in[8], (const u16*)d_in[10],
                                (const u16*)d_in[11], (const u16*)d_in[12], (const u16*)d_in[13], (char*)d_ws, (float*)d_out);
}
```

```cpp
#include <hip/hip_runtime.h>
#include <stdint.h>

typedef short s16x4 __attribute__((ext_vector_type(4)));
typedef short s16x8 __attribute__((ext_vector_type(8)));
typedef __bf16 bf16x2 __attribute__((ext_vector_type(2)));
typedef float f32x2 __attribute__((ext_vector_type(2)));
typedef float f32x4 __attribute__((ext_vector_type(4)));
typedef unsigned u32x2 __attribute__((ext_vector_type(2)));
typedef unsigned u32x4 __attribute__((ext_vector_type(4)));
typedef unsigned short u16;
#define LAS __attribute__((address_space(3)))
#define GAS __attribute__((address_space(1)))
#define DI __device__ __forceinline__

constexpr int SEQ = 2048, DM = 640, NP = 640, NF = 1536;
constexpr int NKS = 88;

constexpr size_t WS_A2 = 0;
constexpr size_t WS_W2 = WS_A2 + (size_t)16 * NKS * 8192;
constexpr size_t WS_G = WS_W2 + (size_t)8 * NKS * 5120;
constexpr size_t WS_AGG = WS_G + 10 * 2048 * 4;
constexpr size_t WS_EP = WS_AGG + 16 * 16 * 640;
constexpr size_t WS_FPT = WS_EP + 256;
constexpr size_t WS_FAG = WS_FPT + 2048;
constexpr size_t WS_FP1 = WS_FAG + 2048;
constexpr size_t WS_JUNK = WS_FP1 + 16384;

DI unsigned f2u(float v) { return __builtin_bit_cast(unsigned, v); }
DI float u2f(unsigned v) { return __builtin_bit_cast(float, v); }
DI bool wave_all(bool p) { return __builtin_amdgcn_ballot_w64(!p) == 0ull; }
DI void wg_sync() {
    __builtin_amdgcn_fence(__ATOMIC_RELEASE, "workgroup");
    __builtin_amdgcn_s_barrier();
    __builtin_amdgcn_fence(__ATOMIC_ACQUIRE, "workgroup");
}
DI float sigmoidf_(float v) { return 1.0f / (1.0f + __builtin_amdgcn_exp2f(v * -1.44269504088896341f)); }
DI float bflo(unsigned v) { return __uint_as_float(v << 16); }
DI float bfhi(unsigned v) { return __uint_as_float(v & 0xffff0000u); }
DI unsigned pack2(float a, float b) {
    f32x2 p = {a, b};
    return __builtin_bit_cast(unsigned, __builtin_convertvector(p, bf16x2));
}
DI f32x4 mfma16(s16x8 a, s16x8 b, f32x4 c) { return __builtin_amdgcn_mfma_f32_16x16x32_bf16(a, b, c, 0, 0, 0); }
template <int S> DI float shr_zero(float v) {
    return __builtin_bit_cast(float, __builtin_amdgcn_update_dpp(0, __builtin_bit_cast(int, v), 0x110 + S, 0xf, 0xf, true));
}
DI float swp(float v) {
    return __builtin_bit_cast(float, __builtin_amdgcn_update_dpp(0, __builtin_bit_cast(int, v), 0xB1, 0xf, 0xf, true));
}
DI f32x2 cmul(f32x2 a, f32x2 b) { return (f32x2){a.x * b.x - a.y * b.y, a.x * b.y + a.y * b.x}; }
DI f32x2 cfma(f32x2 a, f32x2 b, f32x2 c) {
    return (f32x2){fmaf(-a.y, b.y, fmaf(a.x, b.x, c.x)), fmaf(a.y, b.x, fmaf(a.x, b.y, c.y))};
}
DI f32x2 cmul_pk(f32x2 a, f32x2 b) { return (f32x2){fmaf(-a.y, b.y, a.x * b.x), fmaf(a.y, b.x, a.x * b.y)}; }
DI f32x2 cmul_sc(f32x2 a, f32x2 b) {
    float p = a.x * b.x, r = a.x * b.y;
    asm("" : "+v"(p));
    asm("" : "+v"(r));
    return (f32x2){fmaf(-a.y, b.y, p), fmaf(a.y, b.x, r)};
}
DI float dot2bf(unsigned a, unsigned b, float c) {
    return __builtin_amdgcn_fdot2_f32_bf16(__builtin_bit_cast(bf16x2, a), __builtin_bit_cast(bf16x2, b), c, false);
}
#define WAIT_VM(n) asm volatile("s_waitcnt vmcnt(%0)" ::"n"(n) : "memory")
#define WAIT_LGKM0() asm volatile("s_waitcnt lgkmcnt(0)" ::: "memory")
#define ACQUIRE() asm volatile("buffer_inv sc1" ::: "memory")
#define SCHED_FENCE() __builtin_amdgcn_sched_barrier(0)

DI void glds16(const void* gsrc, unsigned lds_dst) {
    unsigned keep;
    asm volatile("s_mov_b32 %0, m0\n\ts_mov_b32 m0, %2\n\ts_nop 0\n\tglobal_load_lds_dwordx4 %1, off\n\ts_mov_b32 m0, %0"
                 : "=&s"(keep) : "v"(gsrc), "s"(lds_dst) : "memory");
}
DI void glds4(const void* gsrc, unsigned lds_dst) {
    unsigned keep;
    asm volatile("s_mov_b32 %0, m0\n\ts_mov_b32 m0, %2\n\ts_nop 0\n\tglobal_load_lds_dword %1, off\n\ts_mov_b32 m0, %0"
                 : "=&s"(keep) : "v"(gsrc), "s"(lds_dst) : "memory");
}
DI void glds4_c(const void* gsrc, unsigned lds_dst) {
    unsigned keep;
    asm volatile("s_mov_b32 %0, m0\n\ts_mov_b32 m0, %2\n\ts_nop 0\n\tglobal_load_lds_dword %1, off sc1\n\ts_mov_b32 m0, %0"
                 : "=&s"(keep) : "v"(gsrc), "s"(lds_dst) : "memory");
}
DI void glds16x4(const void* gsrc, unsigned lds_dst) {
    unsigned keep;
    asm volatile("s_mov_b32 %0, m0\n\ts_mov_b32 m0, %2\n\ts_nop 0\n\tglobal_load_lds_dwordx4 %1, off\n\t"
                 "global_load_lds_dwordx4 %1, off offset:1024\n\tglobal_load_lds_dwordx4 %1, off offset:2048\n\t"
                 "global_load_lds_dwordx4 %1, off offset:3072\n\ts_mov_b32 m0, %0"
                 : "=&s"(keep) : "v"(gsrc), "s"(lds_dst) : "memory");
}
DI void st128_wt(void* p, u32x4 v) { asm volatile("global_store_dwordx4 %0, %1, off sc1\n\ts_nop 1" :: "v"(p), "v"(v) : "memory"); }
DI void st64_wt(void* p, u32x2 v) { asm volatile("global_store_dwordx2 %0, %1, off sc1\n\ts_nop 1" :: "v"(p), "v"(v) : "memory"); }
DI unsigned ld32_asm(const void* p) { unsigned r; asm volatile("global_load_dword %0, %1, off" : "=v"(r) : "v"(p) : "memory"); return r; }
DI unsigned ld16_asm(const void* p) { unsigned r; asm volatile("global_load_ushort %0, %1, off" : "=v"(r) : "v"(p) : "memory"); return r; }
DI u32x2 ld64_asm(const void* p) { u32x2 r; asm volatile("global_load_dwordx2 %0, %1, off" : "=v"(r) : "v"(p) : "memory"); return r; }
DI u32x4 ld128_asm(const void* p) { u32x4 r; asm volatile("global_load_dwordx4 %0, %1, off" : "=v"(r) : "v"(p) : "memory"); return r; }
constexpr float LOG2E = 1.44269504088896341f, INV2PI = 0.15915494309189535f;
DI f32x2 lam_bar(float lr, float li, float ld) {
    const float dt = __builtin_amdgcn_exp2f(ld * LOG2E);
    const float m = __builtin_amdgcn_exp2f(dt * lr * LOG2E);
    const float rv = __builtin_amdgcn_fractf(dt * li * INV2PI);
    return (f32x2){m * __builtin_amdgcn_cosf(rv), m * __builtin_amdgcn_sinf(rv)};
}

typedef unsigned long long u64;
DI void glds16_c(const void* gsrc, unsigned lds_dst) {
    unsigned keep;
    lds_dst = __builtin_amdgcn_readfirstlane(lds_dst);
    asm volatile("s_mov_b32 %0, m0\n\ts_mov_b32 m0, %2\n\ts_nop 0\n\tglobal_load_lds_dwordx4 %1, off sc1\n\ts_mov_b32 m0, %0"
                 : "=&s"(keep) : "v"(gsrc), "s"(lds_dst) : "memory");
}
DI void glds16x4_c(const void* gsrc, unsigned lds_dst) {
    unsigned keep;
    asm volatile("s_mov_b32 %0, m0\n\ts_mov_b32 m0, %2\n\ts_nop 0\n\tglobal_load_lds_dwordx4 %1, off sc1\n\t"
                 "global_load_lds_dwordx4 %1, off offset:1024 sc1\n\tglobal_load_lds_dwordx4 %1, off offset:2048 sc1\n\t"
                 "global_load_lds_dwordx4 %1, off offset:3072 sc1\n\ts_mov_b32 m0, %0"
                 : "=&s"(keep) : "v"(gsrc), "s"(lds_dst) : "memory");
}
DI void flag_set(char* p, u64 tok) { __hip_atomic_store((GAS u64*)p, tok, __ATOMIC_RELAXED, __HIP_MEMORY_SCOPE_AGENT); }
DI void flag_wait(const char* p, bool need, u64 tok) {
    for (unsigned spins = 0; spins < (1u << 20); ++spins) {
        u64 v = tok;
        if (need) v = __hip_atomic_load((const GAS u64*)p, __ATOMIC_RELAXED, __HIP_MEMORY_SCOPE_AGENT);
        if (__all(v == tok)) break;
        __builtin_amdgcn_s_sleep(8);
    }
    asm volatile("" ::: "memory");
}

#define PRIO_OFF 1
DI void lds_arrive(char* p, int) {
    asm volatile("s_mov_b64 exec, 1\n\tds_add_u32 %0, %1\n\ts_mov_b64 exec, -1" :: "v"((unsigned)(size_t)(LAS char*)p), "v"(1u) : "memory");
}
DI void lds_wait(const char* p, unsigned n) {
    unsigned v, sv, cnt;
    asm volatile("s_mov_b32 %2, 0x400000\n"
                 "1:\n\tds_read_b32 %0, %3\n\ts_waitcnt lgkmcnt(0)\n\tv_readfirstlane_b32 %1, %0\n\ts_cmp_ge_u32 %1, %4\n\ts_cbranch_scc1 2f\n\t"
                 "s_sleep 1\n\ts_sub_u32 %2, %2, 1\n\ts_cbranch_scc0 1b\n"
                 "2:" : "=&v"(v), "=&s"(sv), "=&s"(cnt) : "v"((unsigned)(size_t)(LAS const char*)p), "s"(n) : "memory", "scc");
}
#define SSL 40
#define PSL0 "8"
#define PSL1 "4"
template <int V> struct IC { static constexpr int value = V; };
#define ROTF ((((rb + cb) & 3) * 5 + 1) >> 1)
constexpr int K1_STAGE = 38 * 1024;
constexpr int K1_GW = 0;
constexpr int K1_PLR = K1_GW + 1280, K1_PLI = K1_GW + 1360, K1_PLD = K1_GW + 1440, K1_PB1 = K1_GW + 1536, K1_PEP = K1_GW + 1728;
constexpr int K1_RING = 2048;
#define BUF(it) (K1_RING + ((it) % 3) * K1_STAGE)
constexpr int K1_TAB = K1_RING + 3 * K1_STAGE;
constexpr int K1_XCH = K1_TAB + 5120;
constexpr int K1_CNT = K1_XCH + 1280;
constexpr int K1_PRT = K1_CNT + 32;
constexpr int K1_PT = 123 * 1024;
constexpr int K1_FM = 140 * 1024;
constexpr int KF_LDS = 160 * 1024;

__global__ __launch_bounds__(512, 2) __attribute__((target("no-packed-fp32-ops"))) void kf(const u16* __restrict__ x, const u16* __restrict__ Lre, const u16* __restrict__ Lim,
                                             const u16* __restrict__ Bre, const u16* __restrict__ Bim, const u16* __restrict__ Cre,
                                             const u16* __restrict__ Cim, const u16* __restrict__ logdt, const u16* __restrict__ Dskip,
                                             const u16* __restrict__ w1, const u16* __restrict__ b1, const u16* __restrict__ w2,
                                             const u16* __restrict__ gw, char* __restrict__ ws, float* __restrict__ out) {
    __shared__ __attribute__((aligned(16))) char sm[KF_LDS];
    const int tid = threadIdx.x, lane = tid & 63;
    const int wave = __builtin_amdgcn_readfirstlane(tid >> 6);
    const int bid = blockIdx.x, xcd = bid & 7, idx = bid >> 3;
    const int rb = 8 * (xcd & 1) + (idx & 7);
    const int cb = 4 * (xcd >> 1) + (idx >> 3);
    const int RG = 4 * xcd + (idx & 3);
    const int CB = idx >> 2;
    const int t = lane & 15, q = lane >> 4;
    const unsigned smb = (unsigned)(size_t)(LAS char*)sm;
    if (wave < 2) {
        u64 pc0;
        asm volatile("s_getpc_b64 %0" : "=s"(pc0));
        glds4((const char*)(pc0 & ~255ull) + ((idx + 2 * wave) % 5) * 8192 + lane * 128, smb + K1_FM);
    }
    asm volatile("" :: "s"(x), "s"(Lre), "s"(Lim), "s"(Bre), "s"(Bim), "s"(Cre), "s"(Cim), "s"(logdt), "s"(Dskip), "s"(w1), "s"(b1), "s"(w2),
                 "s"(gw), "s"(ws), "s"(out));
    if (wave == 7) {
        const char* tp = (const char*)(out + 64 * RG * DM);
        if (lane == 1) tp = ws + WS_A2 + (size_t)rb * NKS * 8192;
        if (lane == 2) tp = ws + WS_A2 + (size_t)(RG >> 1) * NKS * 8192;
        if (lane == 3) tp = ws + WS_W2 + (size_t)CB * NKS * 5120;
        if (lane == 4) tp = ws + WS_AGG;
        if (lane == 5) tp = (const char*)Dskip;
        if (lane == 6) tp = ws + WS_A2 + (size_t)rb * NKS * 8192 + NKS * 8192 - 128;
        if (lane == 7) tp = ws + WS_A2 + (size_t)(RG >> 1) * NKS * 8192 + NKS * 8192 - 128;
        if (lane == 8) tp = ws + WS_W2 + (size_t)CB * NKS * 5120 + NKS * 5120 - 128;
        if (lane == 9) tp = (const char*)(out + (64 * RG + 63) * DM);
        glds4_c(tp, smb + K1_FM);
    }
    f32x4 ac2[4][5];
    u32x2 xv[3], dv[3];
    unsigned gl[4];
    int oo[3];
    auto run = [&](auto ROLE_, auto PART_) __attribute__((always_inline)) {
    constexpr int ROLE = decltype(ROLE_)::value, PART = decltype(PART_)::value;
    int lane = tid & 63;
    asm volatile("" : "+v"(lane));
    const int t = lane & 15, q = lane >> 4;
#define G_M0(base, c) asm volatile("s_add_i32 m0, %0, %1" :: "s"(base), "n"(c) : "scc")
#define G_LDS(vo, sb, c) asm volatile("global_load_lds_dwordx4 %0, %1 offset:%2" :: "v"(vo), "s"(sb), "n"(c) : "memory")
#define G_LDV(va, c) asm volatile("global_load_lds_dwordx4 %0, off offset:%1" :: "v"(va), "n"(c) : "memory")
#define G_NOP() asm volatile("s_nop 0")
    const int l8 = lane >> 3;
    unsigned vox[4];
    const char* wsr[3];
    const int rot = ROTF;
    const char* w1s = (const char*)w1 + (size_t)rot * (64 * NF * 2);
    const char* xs = (const char*)x + rot * 128;
#define K_WRAP(st) (((st) == 2 || (st) == 5 || (st) == 7) && rot == 10 - (st))
#define W_NEXT(st)                                                                                \
    do {                                                                                          \
        w1s += K_WRAP(st) ? -9 * (64 * NF * 2) : 64 * NF * 2;                                     \
        if ((st) == 2 || (st) == 5 || (st) == 7) { const long d_ = K_WRAP(st) ? -1280 : 0; wsr[0] += d_; wsr[1] += d_; wsr[2] += d_; } \
    } while (0)
#define X_NEXT(st) do { if ((st) == 2 || (st) == 5 || (st) == 7) xs += K_WRAP(st) ? -1280 : 0; } while (0)
    const unsigned lw = smb + 1024 * (wave & 3);
    const unsigned lm = smb + (ROLE == 0 ? 3072 : 4096) * (wave & 3);
#define X_M0(st, i) G_M0(lm, BUF(st) + 1024 * (i) - 128 * (st))
#define X_LD(st, i) G_LDS(vox[i], xs, 128 * (st))
#define W_M0(st, j) G_M0(lw, BUF(st) + 16384 + 4096 * (j) - 128 * (st))
#define W_LD(st, j) G_LDV(wsr[j], 128 * (st))
#define M_M0(st, i) G_M0(lm, BUF(st) + 26624 + 1024 * (i))
#define M_LD(i) G_LDS(vox[i], w1s, 0)
    const unsigned pu = 4 * bid + (wave & 3);
    const unsigned pkg = (pu * 1639u) >> 16, pv = pu - 400u, ptg = (pv * 1366u) >> 16;
    if constexpr (ROLE == 1) {
#pragma unroll
        for (int i = 0; i < 4; ++i) {
            const int p = 8 * i + l8;
            vox[i] = ((128 * rb + 32 * (wave & 3) + 8 * ((p & 15) >> 2) + 4 * (p >> 4) + (p & 3)) * DM + (((lane & 7) ^ ((p >> 1) & 7)) << 3)) * 2;
            if constexpr (PART == 0) {
                if (i == 0) X_M0(0, 0); else if (i == 1) X_M0(0, 1); else if (i == 2) X_M0(0, 2); else X_M0(0, 3);
                G_NOP();
                G_LDS(vox[i], xs, 0);
                SCHED_FENCE();
            }
        }
        if constexpr (PART == 0) {
            X_M0(1, 0); G_NOP(); X_LD(1, 0); X_M0(1, 1); G_NOP(); X_LD(1, 1); X_M0(1, 2); G_NOP(); X_LD(1, 2); X_M0(1, 3); G_NOP(); X_LD(1, 3);
            const bool isc = pu < 400 || pu >= 880;
            const unsigned kg = pu < 400 ? pkg : 0, nt = pu < 400 ? pu - 40 * pkg : 0;
            const unsigned o = ((isc ? 16 * nt + l8 : 32 * (pv - 48 * ptg) + l8) * DM + 64 * (isc ? kg : ptg) + ((lane & 7) ^ l8) * 8) * 2;
#pragma unroll
            for (int i = 0; i < 4; ++i) {
                const u16* p0 = isc ? ((i >> 1) ? Cim : Cre) + 8 * (i & 1) * DM : w2 + 8 * i * DM;
                G_M0(lm, K1_PT + i * 1024); G_NOP(); G_LDS(o, p0, 0);
            }
            return;
        }
    } else {
#pragma unroll
        for (int j = 0; j < 3; ++j) {
            const int p = 8 * (4 * j + wave) + l8;
            const int ch = 40 * cb + 8 * (p >> 4) + ((p & 15) >> 1);
            wsr[j] = (const char*)(((p & 1) ? Bim : Bre) + ch * DM + 64 * rot + (((lane & 7) ^ ((p >> 1) & 7)) << 3));
            if (PART == 0 && j < 2) {
                if (j == 0) W_M0(0, 0); else W_M0(0, 1);
                G_NOP();
                G_LDV(wsr[j], 0);
                SCHED_FENCE();
            }
        }
#pragma unroll
        for (int i = 0; i < 3; ++i) {
            const int gI = 64 * (3 * wave + i) + lane, row = gI / 12, cp = gI - 12 * row;
            int j = (cp >> 1) - ((row >> 3) & 1);
            j += j < 0 ? 6 : 0;
            vox[i] = (row * NF + 96 * cb + 16 * j + 8 * (cp & 1)) * 2;
            if constexpr (PART == 0) {
                if (i == 0) M_M0(0, 0); else if (i == 1) M_M0(0, 1); else M_M0(0, 2);
                G_NOP();
                M_LD(i);
                SCHED_FENCE();
            }
        }
        W_NEXT(1);
        if constexpr (PART == 0) {
            const u16* p = gw + lane * 8;
            if (wave == 3) {
                p = gw + 512 + t * 8;
                if (lane >= 16 && lane < 21) p = Lre + 40 * cb + (lane - 16) * 8;
                if (lane >= 21 && lane < 26) p = Lim + 40 * cb + (lane - 21) * 8;
                if (lane >= 26 && lane < 31) p = logdt + 40 * cb + (lane - 26) * 8;
                if (lane >= 32 && lane < 44) p = b1 + 96 * cb + (lane - 32) * 8;
                if (lane == 44) p = (const u16*)(ws + WS_EP);
            }
            glds16_c(wave < 2 ? wsr[2] : (const char*)p, wave < 2 ? lw + BUF(0) + 16384 + 8192 : smb + K1_GW + (wave - 2) * 1024);
            if (wave < 2) { W_M0(1, 2); G_NOP(); W_LD(1, 2); }
            W_M0(1, 0); G_NOP(); W_LD(1, 0); W_M0(1, 1); G_NOP(); W_LD(1, 1);
            M_M0(1, 0); G_NOP(); M_LD(0); M_M0(1, 1); G_NOP(); M_LD(1); M_M0(1, 2); G_NOP(); M_LD(2);
            return;
        }
    }
    char* ptl = sm + K1_PT + (wave & 3) * 4096;
    const int wc = wave & 3;
    const int xoff = (32 * wc + t) * 128;
    const int co0 = (q ^ ((t >> 1) & 7)) * 16, co1 = co0 ^ 64;
    char* a2 = ws + WS_A2 + (size_t)rb * NKS * 8192 + (2 * wc) * 1024 + lane * 16;
    const int rb2 = RG >> 1, hh = RG & 1;
    const int ks0 = ROLE == 0 ? 10 * wave : 40 + 12 * (wave - 4);
    const char* pa = ws + WS_A2 + ((size_t)rb2 * NKS + ks0) * 8192 + hh * 4096 + lane * 16;
    const char* pb = ws + WS_W2 + ((size_t)CB * NKS + ks0) * 5120 + 1024 + lane * 16;
    u64 tok;
#define GET_TOK() do { const u32x2 ep_ = *(const u32x2*)(sm + K1_PEP); \
                       tok = (((u64)(unsigned)__builtin_amdgcn_readfirstlane(ep_[1]) << 32) | (unsigned)__builtin_amdgcn_readfirstlane(ep_[0])) + 1ull; } while (0)
    u32x4 ptf[4];
    auto pretile_read = [&]() __attribute__((always_inline)) {
        if (pu < 400) {
#pragma unroll
            for (int i = 0; i < 4; ++i) {
                const char* pp = ptl + t * 128 + (((2 * i + (q >> 1)) ^ (t & 7)) << 4) + 8 * (q & 1);
                const u32x2 rr = *(const u32x2*)pp, ii = *(const u32x2*)(pp + 2048);
                ptf[i] = (u32x4){rr[0], rr[1], ii[0], ii[1]};
            }
        } else {
            const int r = 4 * q + (t >> 2);
#pragma unroll
            for (int i = 0; i < 4; ++i) {
                const char* pp = ptl + r * 128 + (((2 * i + ((lane >> 1) & 1)) ^ (r & 7)) << 4) + (lane & 1) * 8;
                const s16x4 lo = __builtin_amdgcn_ds_read_tr16_b64_v4i16((LAS s16x4*)pp);
                const s16x4 hi = __builtin_amdgcn_ds_read_tr16_b64_v4i16((LAS s16x4*)(pp + 2048));
                ptf[i] = __builtin_bit_cast(u32x4, __builtin_shufflevector(lo, hi, 0, 1, 2, 3, 4, 5, 6, 7));
            }
        }
    };
    auto pretile_mix = [&]() __attribute__((always_inline)) {
        if (pu < 400) {
#pragma unroll
            for (int i = 0; i < 4; ++i) {
                const unsigned r0 = ptf[i][0], r1 = ptf[i][1], i0 = ptf[i][2] ^ 0x80008000u, i1 = ptf[i][3] ^ 0x80008000u;
                ptf[i] = (u32x4){(r0 & 0xffffu) | (i0 << 16), (r0 >> 16) | (i0 & 0xffff0000u), (r1 & 0xffffu) | (i1 << 16), (r1 >> 16) | (i1 & 0xffff0000u)};
            }
        }
    };
    auto pretile_dst = [&](unsigned i) __attribute__((always_inline)) {
        const unsigned g = pu < 400 ? pu - 40 * pkg : 4 * ptg + i, KS = pu < 400 ? 4 * pkg + i : 40 + pv - 48 * ptg;
        const unsigned cB = (g * 13u) >> 6;
        unsigned o = (unsigned)WS_W2 + (cB * (5 * NKS - 5) + KS * 5 + g) * 1024u, j = (unsigned)WS_JUNK + (pu * 4 + i) * 1024u;
        asm("" : "+s"(o), "+s"(j));
        return pu < 880 ? o : j;
    };

    const int bco = wave == 6 ? 6 * 20480 + 4096 : wave * 20480;
    u32x4 Ab[4][4], Bb[4][5];
#define P2_L1(dst, P, OFF) asm volatile("global_load_dwordx4 %0, %1, off offset:" #OFF " sc1" : "=&v"(dst) : "v"(P) : "memory")
#define P2_LDB(slot, P)                                                                           \
    do { P2_L1(Bb[slot][0], P, -1024); P2_L1(Bb[slot][1], P, 0); P2_L1(Bb[slot][2], P, 1024); P2_L1(Bb[slot][3], P, 2048); P2_L1(Bb[slot][4], P, 3072); } while (0)
#define P2_LDA(slot, P)                                                                           \
    do { P2_L1(Ab[slot][0], P, 0); P2_L1(Ab[slot][1], P, 1024); P2_L1(Ab[slot][2], P, 2048); P2_L1(Ab[slot][3], P, 3072); } while (0)
    auto pt_check = [&]() __attribute__((always_inline)) {
        const u32x4 v0 = *(const u32x4*)(sm + K1_FM + lane * 16), v1 = *(const u32x4*)(sm + K1_FM + 1024 + lane * 16);
        const unsigned lo = (unsigned)tok, hi = (unsigned)(tok >> 32);
        if (!wave_all(v0[0] == lo && v0[1] == hi && v0[2] == lo && v0[3] == hi && v1[0] == lo && v1[1] == hi && v1[2] == lo && v1[3] == hi)) {
#pragma unroll 1
            for (int k = 0; k < 4; ++k) flag_wait(ws + WS_FPT + (size_t)(64 * k + lane) * 8, true, tok);
        }
    };
    const char* g3 = pb - 1024 + 4 * 5120;
#define P2_CACHE(i) glds16_c(g3 + (i) * 1024, smb + bco + (i) * 1024)
#define P2_M2(ua, ub, i)                                                                          \
    do {                                                                                          \
        ac2[(i) / 5][(i) % 5] = mfma16(__builtin_bit_cast(s16x8, Bb[ub][(i) % 5]), __builtin_bit_cast(s16x8, Ab[ua][(i) / 5]), ac2[(i) / 5][(i) % 5]); \
        ac2[((i) + 1) / 5][((i) + 1) % 5] = mfma16(__builtin_bit_cast(s16x8, Bb[ub][((i) + 1) % 5]), __builtin_bit_cast(s16x8, Ab[ua][((i) + 1) / 5]), ac2[((i) + 1) / 5][((i) + 1) % 5]); \
        SCHED_FENCE();                                                                            \
    } while (0)
#define P2_STEP(j, NS, WN, NL)                                                                    \
    do {                                                                                          \
        asm volatile("s_waitcnt vmcnt(%9)" : "+v"(Ab[(j) & 3][0]), "+v"(Ab[(j) & 3][1]), "+v"(Ab[(j) & 3][2]), "+v"(Ab[(j) & 3][3]), \
                     "+v"(Bb[(j) & 3][0]), "+v"(Bb[(j) & 3][1]), "+v"(Bb[(j) & 3][2]), "+v"(Bb[(j) & 3][3]), "+v"(Bb[(j) & 3][4]) : "n"(WN) : "memory"); \
        SCHED_FENCE();                                                                            \
        const char* pa_ = pa + ((j) + 3) * 8192;                                                  \
        const char* pb_ = pb + ((j) + 3) * 5120;                                                  \
        constexpr bool LA_ = (j) + 3 < (NS), LBG_ = (j) + 3 >= (NL) && (j) + 3 < (NS), LBL_ = (j) + 3 >= 4 && (j) + 3 < (NL); \
        P2_M2((j) & 3, (j) & 3, 0); if (LA_) { P2_L1(Ab[((j) + 3) & 3][0], pa_, 0); SCHED_FENCE(); }      \
        P2_M2((j) & 3, (j) & 3, 2); if (LA_) { P2_L1(Ab[((j) + 3) & 3][1], pa_, 1024); SCHED_FENCE(); }   \
        P2_M2((j) & 3, (j) & 3, 4); if (LA_) { P2_L1(Ab[((j) + 3) & 3][2], pa_, 2048); SCHED_FENCE(); }   \
        P2_M2((j) & 3, (j) & 3, 6); if (LA_) { P2_L1(Ab[((j) + 3) & 3][3], pa_, 3072); SCHED_FENCE(); }   \
        if (LBL_) { _Pragma("unroll") for (int n_ = 0; n_ < 5; ++n_) Bb[((j) + 3) & 3][n_] = *(const u32x4*)(sm + bco + (((j) + 3 - 4) * 5 + n_) * 1024 + lane * 16); SCHED_FENCE(); } \
        P2_M2((j) & 3, (j) & 3, 8); if (LBG_) { P2_L1(Bb[((j) + 3) & 3][0], pb_, -1024); SCHED_FENCE(); } \
        P2_M2((j) & 3, (j) & 3, 10); if (LBG_) { P2_L1(Bb[((j) + 3) & 3][1], pb_, 0); SCHED_FENCE(); }    \
        P2_M2((j) & 3, (j) & 3, 12); if (LBG_) { P2_L1(Bb[((j) + 3) & 3][2], pb_, 1024); SCHED_FENCE(); } \
        P2_M2((j) & 3, (j) & 3, 14); if (LBG_) { P2_L1(Bb[((j) + 3) & 3][3], pb_, 2048); SCHED_FENCE(); } \
        P2_M2((j) & 3, (j) & 3, 16); if (LBG_) { P2_L1(Bb[((j) + 3) & 3][4], pb_, 3072); SCHED_FENCE(); } \
        P2_M2((j) & 3, (j) & 3, 18);                                                              \
    } while (0)
    u64 pl[3];
#define POLL1(i, PSL) "s_waitcnt vmcnt(2)\n\tv_cmp_ne_u64 vcc, %" #i ", %5\n\ts_cmp_eq_u64 vcc, 0\n\ts_cbranch_scc1 2f\n\t" \
                 "global_load_dwordx2 %" #i ", %4, off sc1\n\ts_sleep " PSL "\n\t"
#define P2_POLLED() asm volatile("" : "+v"(xv[0]), "+v"(xv[1]), "+v"(xv[2]), "+v"(dv[0]), "+v"(dv[1]), "+v"(dv[2]) : "v"(pl[0]), "v"(pl[1]), "v"(pl[2]))
#define P2_BEGIN(role, PSL)                                                                        \
    do {                                                                                          \
        if ((role) == 1) { _Pragma("unroll") for (int i = 0; i < 15; ++i) P2_CACHE(i); WAIT_VM(15); } else WAIT_VM(0); \
                                                                                           \
        if (lane == 0) flag_set(ws + WS_FP1 + (size_t)(((((role) * 16 + rb) * 2 + (wc >> 1)) * 16 + cb) * 2 + (wc & 1)) * 8, tok); \
        if ((role) == 1) {         \
            lds_wait(sm + K1_CNT, 4u);                                                            \
            const char* gs_ = ws + WS_W2 + ((size_t)CB * NKS + 10 * wc + 4) * 5120 + lane * 16;   \
            _Pragma("unroll") for (int i = 0; i < 5; ++i) glds16x4_c(gs_ + i * 4096, __builtin_amdgcn_readfirstlane(smb + wc * 20480 + i * 4096)); \
        }                                                                                         \
        _Pragma("unroll") for (int m = 0; m < 3; ++m) {                                           \
            int tile = wave + 8 * m; tile = tile < 20 ? tile : wave;                              \
            const int r = tile / 5, n = tile - 5 * r;                                             \
            const int col = 80 * CB + 16 * n + 4 * q;                                             \
            oo[m] = (64 * RG + 32 * (r >> 1) + 8 * (t >> 2) + 4 * (r & 1) + (t & 3)) * DM + col;  \
            xv[m] = ld64_asm(x + oo[m]); dv[m] = ld64_asm(Dskip + col);                           \
        }                                                                                         \
        if ((role) == 0) lds_wait(sm + K1_CNT + 16 + 4 * wc, 1u);     \
        unsigned cnt_;                                         \
        asm volatile("global_load_dwordx2 %0, %4, off sc1\n\ts_sleep " PSL "\n\tglobal_load_dwordx2 %1, %4, off sc1\n\ts_sleep " PSL "\n\t" \
                     "global_load_dwordx2 %2, %4, off sc1\n\ts_mov_b32 %3, 0x80000\n"             \
                     "1:\n\t" POLL1(0, PSL) POLL1(1, PSL) POLL1(2, PSL)                                 \
                     "s_sub_u32 %3, %3, 1\n\ts_cbranch_scc0 1b\n"                                 \
                     "2:"                                                                         \
                     : "=&v"(pl[0]), "=&v"(pl[1]), "=&v"(pl[2]), "=&s"(cnt_)                      \
                     : "v"(ws + WS_FP1 + (size_t)(((role) * 32 + RG) * 32 + 8 * wc + (lane & 7)) * 8), "s"(tok) : "memory", "vcc", "scc"); \
        if ((role) == 0) P2_GATES();                                                              \
        P2_LDA(0, pa); P2_LDA(1, pa + 8192); P2_LDA(2, pa + 16384);                               \
        _Pragma("unroll") for (int r = 0; r < 4; ++r) _Pragma("unroll") for (int n = 0; n < 5; ++n) ac2[r][n] = (f32x4){0.f, 0.f, 0.f, 0.f}; \
    } while (0)
#define P2_GATES()                                                                                \
    _Pragma("unroll") for (int j_ = 0; j_ < 4; ++j_)                                              \
        asm volatile("global_load_dword %0, %1, off sc1" : "=&v"(gl[j_])                          \
                     : "v"(ws + WS_G + ((4 * wc + j_ < 10 ? 4 * wc + j_ : 9) * 2048 + 64 * RG + lane) * 4) : "memory")

    if constexpr (ROLE == 0) {
        f32x4 acc[6][2];
#pragma unroll
        for (int a = 0; a < 6; ++a) { acc[a][0] = (f32x4){0.f, 0.f, 0.f, 0.f}; acc[a][1] = (f32x4){0.f, 0.f, 0.f, 0.f}; }
        s16x8 fx[2][2], fw[2][5];
        const int woff = 16384 + t * 128;
        const int itg = cb >= 10 ? -1 : cb >= rot ? cb - rot : cb + 10 - rot;
#define S_RX(b, sb, co) do { fx[b][0] = *(const s16x8*)(sm + (sb) + xoff + (co)); fx[b][1] = *(const s16x8*)(sm + (sb) + xoff + 2048 + (co)); } while (0)
#define S_RW(b, sb, co, T) fw[b][T] = *(const s16x8*)(sm + (sb) + woff + (T) * 2048 + (co))
#define S_MM(b, T) do { acc[T][0] = mfma16(fx[b][0], fw[b][T], acc[T][0]); acc[T][1] = mfma16(fx[b][1], fw[b][T], acc[T][1]); SCHED_FENCE(); } while (0)
#define S_LB(slot, T) asm volatile("global_load_dwordx4 %0, %1, off sc1" : "=&v"(Bb[slot][T]) : "v"(pb + (slot) * 5120 + ((T) - 1) * 1024) : "memory")
#define S_PT(it, i) do { if ((it) == 1) asm volatile("global_store_dwordx4 %0, %1, %2 sc1\n\ts_nop 1" :: "v"(pto[i]), "v"(ptf[i]), "s"(ws) : "memory"); } while (0)
#define S_BODY(it, WN, XA0, XA1, XA2, XA3)                                                        \
    do {                                                                                          \
        constexpr bool RF_ = (it) < 7, RN_ = (it) < 9;                                            \
        constexpr int SB_ = (it) < 7 ? 0 : (it) - 7;                                              \
        S_MM(0, 0); S_RX(1, BUF(it), co1); S_RW(1, BUF(it), co1, 0); SCHED_FENCE();               \
        S_MM(0, 1); S_RW(1, BUF(it), co1, 1); S_RW(1, BUF(it), co1, 2); XA0; SCHED_FENCE();       \
        S_MM(0, 2); S_RW(1, BUF(it), co1, 3); S_RW(1, BUF(it), co1, 4); XA1; SCHED_FENCE();       \
        S_MM(0, 3); XA2; SCHED_FENCE();                                                           \
        S_MM(0, 4); XA3; SCHED_FENCE();                                                           \
        if ((it) == 0 && wave < 2) WAIT_VM(6); else if (RN_) WAIT_VM(WN);                         \
        WAIT_LGKM0();                                                                             \
        __builtin_amdgcn_s_barrier();                                                             \
        asm volatile("" ::: "memory");                                                            \
        SCHED_FENCE();                                                                            \
        S_MM(1, 0);                                                                               \
        if (__builtin_expect((it) == itg, 0)) {                                                   \
            const s16x8 g0_ = *(const s16x8*)(sm + K1_GW + (64 * cb + 8 * q) * 2), g1_ = *(const s16x8*)(sm + K1_GW + (64 * cb + 32 + 8 * q) * 2); \
            acc[5][0] = mfma16(fx[1][0], g1_, mfma16(fx[0][0], g0_, acc[5][0])); acc[5][1] = mfma16(fx[1][1], g1_, mfma16(fx[0][1], g0_, acc[5][1])); \
        }                                                                                         \
        if ((it) == 1) pretile_read();                                                            \
        if ((it) == 7) pt_check();                                                                \
        if (RF_) {                                                                                \
            W_NEXT((it) + 3);                                                                     \
            if (wave < 2) { W_M0((it) + 3, 2); G_NOP(); W_LD((it) + 3, 2); }                      \
            else if ((it) == 3) { if (lane == 0) flag_set(ws + WS_FPT + (size_t)bid * 8, tok); }    \
            else if ((it) == 6) glds16_c(ws + WS_FPT + (wave - 2) * 1024 + lane * 16, smb + K1_FM + (wave - 2) * 1024); \
            W_M0((it) + 3, 0);                                                                    \
        }                                                                                         \
        if (RN_) { S_RX(0, BUF((it) + 1), co0); S_RW(0, BUF((it) + 1), co0, 0); }                 \
        SCHED_FENCE();                                                                            \
        if (RF_) { W_LD((it) + 3, 0); W_M0((it) + 3, 1); } else S_LB(SB_, 0);                     \
        SCHED_FENCE();                                                                            \
        S_MM(1, 1); if (RN_) { S_RW(0, BUF((it) + 1), co0, 1); S_RW(0, BUF((it) + 1), co0, 2); }  \
        SCHED_FENCE();                                                                            \
        if (RF_) { W_LD((it) + 3, 1); M_M0((it) + 3, 0); } else S_LB(SB_, 1);                     \
        if ((it) == 1) pretile_mix();                                                             \
        S_PT(it, 0); SCHED_FENCE();                                                               \
        S_MM(1, 2); if (RN_) { S_RW(0, BUF((it) + 1), co0, 3); S_RW(0, BUF((it) + 1), co0, 4); }  \
        SCHED_FENCE();                                                                            \
        if (RF_) { M_LD(0); M_M0((it) + 3, 1); } else S_LB(SB_, 2);                               \
        S_PT(it, 1); SCHED_FENCE();                                                               \
        S_MM(1, 3);                                                                               \
        if (RF_) { M_LD(1); M_M0((it) + 3, 2); } else S_LB(SB_, 3);                               \
        S_PT(it, 2); SCHED_FENCE();                                                               \
        S_MM(1, 4);                                                                               \
        if (RF_) M_LD(2); else S_LB(SB_, 4);                                                      \
        S_PT(it, 3); SCHED_FENCE();                                                               \
    } while (0)
        if (wave == 0 && lane < 2) *(u32x4*)(sm + K1_CNT + lane * 16) = (u32x4){0u, 0u, 0u, 0u};
        unsigned pto[4];
#pragma unroll
        for (int i = 0; i < 4; ++i) pto[i] = pretile_dst(i) + lane * 16;
        asm volatile("" : "+v"(a2), "+v"(pa), "+v"(pb), "+v"(pto[0]), "+v"(pto[1]), "+v"(pto[2]), "+v"(pto[3]));
        asm volatile("" : "+v"(wsr[0]), "+v"(wsr[1]), "+v"(wsr[2]), "+v"(vox[0]), "+v"(vox[1]), "+v"(vox[2]));
        if (wave < 2) WAIT_VM(6); else WAIT_VM(5);
        WAIT_LGKM0();
        __builtin_amdgcn_s_barrier();
        asm volatile("" ::: "memory");
        S_RX(0, BUF(0), co0); S_RW(0, BUF(0), co0, 0); S_RW(0, BUF(0), co0, 1); S_RW(0, BUF(0), co0, 2); S_RW(0, BUF(0), co0, 3); S_RW(0, BUF(0), co0, 4);
        const int cl = lane < 40 ? lane : 39;
        float lr, li, za, zb;
        f32x2 pA, pB;
#define TB0() do { lr = bflo(*(const u16*)(sm + K1_PLR + cl * 2)); li = bflo(*(const u16*)(sm + K1_PLI + cl * 2)); za = bflo(*(const u16*)(sm + K1_PLD + cl * 2)); } while (0)
#define TB1() do { const float dt = __builtin_amdgcn_exp2f(za * LOG2E); za = dt * lr * LOG2E; zb = dt * li * INV2PI;                   \
                   const float nA = wc == 1 ? 8.f : wc == 2 ? 24.f : 1.f;                                                             \
                   const float mA = __builtin_amdgcn_exp2f(nA * za), rA = __builtin_amdgcn_fractf(nA * zb);                           \
                   pA = (f32x2){mA * __builtin_amdgcn_cosf(rA), mA * __builtin_amdgcn_sinf(rA)}; } while (0)
#define TB2() do { const float nB = wc == 1 ? 16.f : wc == 2 ? 32.f : 0.f;                                                            \
                   const float mB = __builtin_amdgcn_exp2f(nB * za), rB = __builtin_amdgcn_fractf(nB * zb);                           \
                   pB = (f32x2){mB * __builtin_amdgcn_cosf(rB), mB * __builtin_amdgcn_sinf(rB)}; } while (0)
#define TB3() do { const float nr = pA.x - 1.0f, ni = pA.y, rden = 1.0f / (lr * lr + li * li);                                        \
                   const float z8 = __builtin_amdgcn_fractf(8.f * zb);                                                                \
                   if (wc == 3) pA = (f32x2){(nr * lr + ni * li) * rden, (ni * lr - nr * li) * rden};                 \
                   f32x4* tb = (f32x4*)(sm + K1_TAB + cl * 16);                                              \
                   tb[wc == 0 ? 0 : wc == 1 ? 120 : wc == 2 ? 200 : 40] = (f32x4){pA.x, -pA.y, pA.x, pA.y};                           \
                   tb[wc == 0 ? 80 : wc == 1 ? 160 : wc == 2 ? 240 : 280] = wc == 3 ? (f32x4){8.f * za, z8, 8.f * za, z8} : (f32x4){pB.x, -pB.y, pB.x, pB.y}; } while (0)
        const f32x2* tab = (const f32x2*)(sm + K1_TAB) + t;
        f32x2 Wj[4];
        float Ma[5], Mb[5];
#define PRE_W(j0)                                                                                 \
    do {                                                                                          \
        const f32x2 zz = *(const f32x2*)(sm + K1_TAB + (7 * 80 + 2 * cl) * 8);                    \
        const float za = 16.f * zz.x, zb = __builtin_amdgcn_fractf(16.f * zz.y);                  \
        _Pragma("unroll") for (int j = (j0); j < (j0) + 2; ++j) {                                 \
            const int n = rb - 1 - 4 * wc - j;                                                    \
            const float fn = (float)(n < 0 ? 0 : n);                                              \
            const float mg = n < 0 ? 0.f : __builtin_amdgcn_exp2f(fn * za), rv = fn * zb;         \
            Wj[j] = (f32x2){mg * __builtin_amdgcn_cosf(rv), mg * __builtin_amdgcn_sinf(rv)};      \
            asm volatile("" : "+v"(Wj[j]));                                                       \
        }                                                                                         \
    } while (0)
#define PRE_M(T0, T1)                                                                             \
    do {                                                                                          \
        const float fs = (float)(4 * wc + q), qr = (t & 1) ? -0.25f : 0.f, sg = (t & 1) ? 1.f : -1.f; \
        _Pragma("unroll") for (int T = (T0); T < (T1); ++T) {                                     \
            const f32x2 cc = tab[80 + 16 * T], zz = tab[560 + 16 * T];                            \
            const float e = __builtin_amdgcn_exp2f(fs * zz.x) * __builtin_amdgcn_cosf(fmaf(fs, zz.y, qr)); \
            Ma[T] = fmaf(cc.y, swp(e), cc.x * e);                                                 \
            Mb[T] = sg * swp(Ma[T]);                                                              \
            asm volatile("" : "+v"(Ma[T]), "+v"(Mb[T]));                                          \
        }                                                                                         \
    } while (0)
#define SA0() do { W_NEXT(2); if (wave < 2) { W_M0(2, 2); G_NOP(); W_LD(2, 2); } W_M0(2, 0); G_NOP(); W_LD(2, 0); W_M0(2, 1); } while (0)
#define SA1() do { SCHED_FENCE(); W_LD(2, 1); M_M0(2, 0); G_NOP(); M_LD(0); M_M0(2, 1); } while (0)
#define SA2() do { M_LD(1); M_M0(2, 2); } while (0)
        S_BODY(0, 5, SA0(), SA1(), SA2(), M_LD(2));   S_BODY(1, 5, TB0(), , TB1(), );   S_BODY(2, 9, , TB2(), GET_TOK(), );   S_BODY(3, 5, , TB3(), , );
        S_BODY(4, 6, , , PRE_W(0), );   S_BODY(5, 5, , , PRE_W(2), );   S_BODY(6, 5, , , PRE_M(0, 2), );
        S_BODY(7, 5, , , PRE_M(2, 4), );   S_BODY(8, 5, , , PRE_M(4, 5), );
        f32x2 Lc[5], Sc[5];
#pragma unroll
        for (int T = 0; T < 5; ++T) { Lc[T] = tab[16 * T]; Sc[T] = tab[(5 - q) * 80 + 16 * T]; }
        const f32x2 L32 = *(const f32x2*)(sm + K1_TAB + (6 * 80 + 2 * cl + 1) * 8);
        S_BODY(9, 0, S_LB(3, 0); S_LB(3, 1), S_LB(3, 2), S_LB(3, 3), S_LB(3, 4));
        __builtin_amdgcn_s_setprio(3);
        float ag8[5];
#pragma unroll
        for (int T = 0; T < 5; ++T) ag8[T] = acc[T][0][0];
#pragma unroll
        for (int j = 1; j < 8; ++j)
#pragma unroll
            for (int T = 0; T < 5; ++T) ag8[T] = fmaf(Lc[T].y, swp(ag8[T]), fmaf(Lc[T].x, ag8[T], acc[T][j >> 2][j & 3]));
        {
            unsigned sv[5];
#pragma unroll
            for (int T = 0; T < 5; ++T) sv[T] = f2u(fmaf(Sc[T].y, swp(ag8[T]), Sc[T].x * ag8[T]));
            const auto r01 = __builtin_amdgcn_permlane32_swap(sv[0], sv[1], false, false);
            const auto r23 = __builtin_amdgcn_permlane32_swap(sv[2], sv[3], false, false);
            const auto r44 = __builtin_amdgcn_permlane32_swap(sv[4], sv[4], false, false);
            const unsigned p4 = f2u(u2f(r44[0]) + u2f(r44[1]));
            const auto ra = __builtin_amdgcn_permlane16_swap(f2u(u2f(r01[0]) + u2f(r01[1])), f2u(u2f(r23[0]) + u2f(r23[1])), false, false);
            const auto rb4 = __builtin_amdgcn_permlane16_swap(p4, p4, false, false);
            float* xa = (float*)(sm + K1_XCH) + wc * 80 + t;
            xa[16 * (((q & 1) << 1) | (q >> 1))] = u2f(ra[0]) + u2f(ra[1]);
            xa[64] = u2f(rb4[0]) + u2f(rb4[1]);
        }
        asm volatile("" ::: "memory");
        unsigned old = 0;
        if (lane == 0) old = __hip_atomic_fetch_add((LAS unsigned*)(sm + K1_CNT), 1u, __ATOMIC_RELAXED, __HIP_MEMORY_SCOPE_WORKGROUP);
        old = __builtin_amdgcn_readfirstlane(old);
        asm volatile("" ::: "memory");
        if (old == 3) {
            const f32x2* xc = (const f32x2*)(sm + K1_XCH) + cl;
            const f32x2 s = cfma(L32, cfma(L32, cfma(L32, xc[0], xc[40]), xc[80]), xc[120]);
            if (lane < 40) st128_wt(ws + WS_AGG + (size_t)(cb * 16 + rb) * 640 + lane * 16,
                                    (u32x4){f2u(s.x), f2u(s.y), (unsigned)tok, (unsigned)(tok >> 32)});
        }
        SCHED_FENCE();
        __builtin_amdgcn_s_setprio(PRIO_OFF);
        char* own = sm + (wc < 2 ? 5 * 20480 : 8 * 20480) - 5120 + (wc & 1) * 2560;
#pragma unroll
        for (int T = 0; T < 5; ++T) *(float*)(own + (16 * T + t) * 16 + q * 4) = ag8[T];
        if (cb < 10 && t < 2) {
            const f32x4 gv = t ? acc[5][1] : acc[5][0];
            st128_wt(ws + WS_G + (cb * 2048 + 128 * rb + 32 * wc + 8 * q + 4 * t) * 4, __builtin_bit_cast(u32x4, gv));
        }
        float pre[5];
        {
            f32x2 Cc[5], L8c[5];
#pragma unroll
            for (int T = 0; T < 5; ++T) {
                Cc[T] = tab[80 + 16 * T]; L8c[T] = tab[240 + 16 * T];
#pragma unroll
                for (int j = 0; j < 8; ++j) {
                    const float u = acc[T][j >> 2][j & 3];
                    acc[T][j >> 2][j & 3] = fmaf(Cc[T].y, swp(u), Cc[T].x * u);
                }
                pre[T] = 0.f;
            }
            {
                for (unsigned spins = 0; spins < (1u << 22); ++spins) {
                    if (__hip_atomic_load((LAS unsigned*)(sm + K1_CNT), __ATOMIC_RELAXED, __HIP_MEMORY_SCOPE_WORKGROUP) >= 4u) break;
                    __builtin_amdgcn_s_sleep(1);
                }
                asm volatile("" ::: "memory");
                const float* xa = (const float*)(sm + K1_XCH) + t;
#pragma unroll 1
                for (int w = 0; w < wc; ++w)
#pragma unroll
                    for (int T = 0; T < 5; ++T) {
                        const f32x2 z = tab[480 + 16 * T];
                        pre[T] = fmaf(z.y, swp(pre[T]), fmaf(z.x, pre[T], xa[w * 80 + 16 * T]));
                    }
            }
#pragma unroll
            for (int T = 0; T < 5; ++T) {
                const f32x4 xs = *(const f32x4*)(own + (16 * T + t) * 16);
#pragma unroll
                for (int k = 0; k < 3; ++k) {
                    const float nx = fmaf(L8c[T].y, swp(pre[T]), fmaf(L8c[T].x, pre[T], xs[k]));
                    pre[T] = k < q ? nx : pre[T];
                }
                pre[T] = fmaf(Cc[T].y, swp(pre[T]), Cc[T].x * pre[T]);
            }
        }
#pragma unroll
        for (int T = 0; T < 5; ++T) asm volatile("" : "+v"(acc[T][0]), "+v"(acc[T][1]), "+v"(pre[T]));
        SCHED_FENCE();
        __builtin_amdgcn_s_setprio(3);
        if (rb > 0) {
            f32x2 cy = {0.f, 0.f};
            if (4 * wc < rb) {
                const char* agp = ws + WS_AGG + (size_t)(cb * 16 + 4 * wc) * 640 + cl * 16;
                for (unsigned spins = 0; spins < (1u << 20); ++spins) {
                    u32x4 ag[4];
#pragma unroll
                    for (int j = 0; j < 4; ++j)
                        asm volatile("global_load_dwordx4 %0, %1, off sc1" : "=v"(ag[j]) : "v"(agp + (4 * wc + j < rb ? j : 0) * 640) : "memory");
                    asm volatile("s_waitcnt vmcnt(0)" : "+v"(ag[0]), "+v"(ag[1]), "+v"(ag[2]), "+v"(ag[3]) :: "memory");
                    bool ok = true;
#pragma unroll
                    for (int j = 0; j < 4; ++j) ok = ok && ag[j][2] == (unsigned)tok && ag[j][3] == (unsigned)(tok >> 32);
                    const f32x2 c0 = cfma(Wj[1], (f32x2){u2f(ag[1][0]), u2f(ag[1][1])}, cmul_pk(Wj[0], (f32x2){u2f(ag[0][0]), u2f(ag[0][1])}));
                    const f32x2 c1 = cfma(Wj[3], (f32x2){u2f(ag[3][0]), u2f(ag[3][1])}, cmul_pk(Wj[2], (f32x2){u2f(ag[2][0]), u2f(ag[2][1])}));
                    cy = (f32x2){c0.x + c1.x, c0.y + c1.y};
                    if (wave_all(ok)) break;
                    __builtin_amdgcn_s_sleep(4);
                }
            }
            if (lane < 40) *(f32x2*)(sm + K1_PRT + (lane * 4 + wc) * 8) = cy;
            asm volatile("" ::: "memory");
            if (lane == 0) __hip_atomic_fetch_add((LAS unsigned*)(sm + K1_CNT + 4), 1u, __ATOMIC_RELAXED, __HIP_MEMORY_SCOPE_WORKGROUP);
            for (unsigned spins = 0; spins < (1u << 22); ++spins) {
                if (__hip_atomic_load((LAS unsigned*)(sm + K1_CNT + 4), __ATOMIC_RELAXED, __HIP_MEMORY_SCOPE_WORKGROUP) >= 4u) break;
                __builtin_amdgcn_s_sleep(1);
            }
            asm volatile("" ::: "memory");
#pragma unroll
            for (int T = 0; T < 5; ++T) {
                const f32x4* pp = (const f32x4*)(sm + K1_PRT + (8 * T + (t >> 1)) * 32);
                const f32x4 p0 = pp[0], p1 = pp[1];
                pre[T] = fmaf(Mb[T], (p0[1] + p0[3]) + (p1[1] + p1[3]), fmaf(Ma[T], (p0[0] + p0[2]) + (p1[0] + p1[2]), pre[T]));
            }
        }
#pragma unroll
        for (int j = 0; j < 8; ++j)
#pragma unroll
            for (int T = 0; T < 5; ++T) {
                pre[T] = fmaf(Lc[T].y, swp(pre[T]), fmaf(Lc[T].x, pre[T], acc[T][j >> 2][j & 3]));
                acc[T][j >> 2][j & 3] = pre[T];
            }
        {
            const int od = cb & 1;
            char* dst = a2 + (size_t)((5 * cb) >> 1) * 8192;
            char* wp0 = own + (16 * od + t) * 80 + q * 16;
            char* wp1 = own + (16 * (od ^ 1) + t) * 80 + q * 16;
            const char* rp = own + (8 * q + (t >> 2)) * 80 + (t & 3) * 16;
            const char* rl = own + (16 * od + 8 * (q & 1) + (t >> 2)) * 80 + (t & 3) * 16 + (q >> 1) * 8;
#define H_WR(T) *(u32x4*)(((T) & 1) ? wp1 : wp0) = (u32x4){pack2(acc[T][0][0], acc[T][0][1]), pack2(acc[T][0][2], acc[T][0][3]), \
                                                           pack2(acc[T][1][0], acc[T][1][1]), pack2(acc[T][1][2], acc[T][1][3])}
#define H_RD(p) __builtin_bit_cast(u32x4, __builtin_shufflevector(__builtin_amdgcn_ds_read_tr16_b64_v4i16((LAS s16x4*)(p)), \
                                                                  __builtin_amdgcn_ds_read_tr16_b64_v4i16((LAS s16x4*)((p) + 320)), 0, 1, 2, 3, 4, 5, 6, 7))
#define H_PAIR(ks) do { asm volatile("" ::: "memory"); hf[2 * (ks)] = H_RD(rp); hf[2 * (ks) + 1] = H_RD(rp + 8); asm volatile("" ::: "memory"); } while (0)
#define H_LONE() do { asm volatile("" ::: "memory"); hf[4] = H_RD(rl); asm volatile("" ::: "memory"); } while (0)
            u32x4 hf[5];
            if (od) { H_WR(0); H_LONE(); H_WR(1); H_WR(2); H_PAIR(0); H_WR(3); H_WR(4); H_PAIR(1); }
            else { H_WR(0); H_WR(1); H_PAIR(0); H_WR(2); H_WR(3); H_PAIR(1); H_WR(4); H_LONE(); }
            st128_wt(dst + od * 8192, hf[0]); st128_wt(dst + od * 8192 + 1024, hf[1]);
            st128_wt(dst + od * 8192 + 8192, hf[2]); st128_wt(dst + od * 8192 + 9216, hf[3]);
            st128_wt(dst + (od ? 512 : 16384) + (q >> 1) * 512, hf[4]);
        }
        WAIT_LGKM0();
        if (lane == 0) __hip_atomic_fetch_add((LAS unsigned*)(sm + K1_CNT + 8), 1u, __ATOMIC_RELAXED, __HIP_MEMORY_SCOPE_WORKGROUP);
        __builtin_amdgcn_s_setprio(0);
        P2_BEGIN(0, PSL0);
        P2_STEP(0, 10, 8, 8); P2_POLLED();
        lds_arrive(sm + K1_CNT + 12, lane);
        P2_STEP(1, 10, 8, 8); P2_STEP(2, 10, 8, 8); P2_STEP(3, 10, 8, 8); P2_STEP(4, 10, 8, 8);
        P2_STEP(5, 10, 8, 8); P2_STEP(6, 10, 13, 8); P2_STEP(7, 10, 18, 8); P2_STEP(8, 10, 9, 8); P2_STEP(9, 10, 0, 8);
        asm volatile("" : "+v"(gl[0]), "+v"(gl[1]), "+v"(gl[2]), "+v"(gl[3]) :: "memory");
    } else {
        const int bb = q & 1;
        const int trb = 26624 + (8 * q + (t >> 2)) * 192 + (lane & 3) * 8;
        const int tra = trb + bb * 32;
        const int tr5 = trb + (bb ? 0 : 160);
        f32x4 acc[6][2];
        s16x8 fx[2][2], fw[2][6];
#define M_RW(b, sb, ks, j)                                                                        \
    do {                                                                                          \
        const char* pa_ = sm + (sb) + ((j) < 5 ? tra + (j) * 32 : tr5) + (ks) * 32 * 192;         \
        const s16x4 lo_ = __builtin_amdgcn_ds_read_tr16_b64_v4i16((LAS s16x4*)pa_);               \
        const s16x4 hi_ = __builtin_amdgcn_ds_read_tr16_b64_v4i16((LAS s16x4*)(pa_ + 4 * 192));   \
        fw[b][j] = __builtin_shufflevector(lo_, hi_, 0, 1, 2, 3, 4, 5, 6, 7);                     \
    } while (0)
#define M_BIAS(j)                                                                                 \
    do {                                                                                          \
        const u32x2 bp_ = *(const u32x2*)(sm + K1_PB1 + (16 * (j) + 4 * q) * 2);                  \
        const f32x4 bv_ = {bflo(bp_[0]), bfhi(bp_[0]), bflo(bp_[1]), bfhi(bp_[1])};               \
        acc[j][0] += bv_; acc[j][1] += bv_;                                                       \
    } while (0)
#define M_MM(b, j) do { acc[j][0] = mfma16(fw[b][j], fx[b][0], acc[j][0]); acc[j][1] = mfma16(fw[b][j], fx[b][1], acc[j][1]); SCHED_FENCE(); } while (0)
#define M_BODY(it, WN)                                                                            \
    do {                                                                                          \
        constexpr bool RF_ = (it) < 8, RN_ = (it) < 9;                                            \
        constexpr int SB_ = (it) < 8 ? 0 : (it) - 7;                                              \
        M_MM(0, 0); if (RF_) { X_NEXT((it) + 2); X_M0((it) + 2, 0); } S_RX(1, BUF(it), co1); M_RW(1, BUF(it), 1, 0); SCHED_FENCE(); \
        if (RF_) { X_LD((it) + 2, 0); X_M0((it) + 2, 1); } else S_LB(SB_, 0);                     \
        SCHED_FENCE();                                                                            \
        M_MM(0, 1); M_RW(1, BUF(it), 1, 1); M_RW(1, BUF(it), 1, 2); SCHED_FENCE();                \
        if (RF_) { X_LD((it) + 2, 1); X_M0((it) + 2, 2); } else S_LB(SB_, 1);                     \
        SCHED_FENCE();                                                                            \
        M_MM(0, 2); M_RW(1, BUF(it), 1, 3); M_RW(1, BUF(it), 1, 4); SCHED_FENCE();                \
        if (RF_) { X_LD((it) + 2, 2); X_M0((it) + 2, 3); } else S_LB(SB_, 2);                     \
        SCHED_FENCE();                                                                            \
        M_MM(0, 3); M_RW(1, BUF(it), 1, 5); SCHED_FENCE();                                        \
        if (RF_) X_LD((it) + 2, 3); else S_LB(SB_, 3);                                            \
        SCHED_FENCE();                                                                            \
        M_MM(0, 4); if (!RF_) S_LB(SB_, 4);                                                       \
        SCHED_FENCE();                                                                            \
        M_MM(0, 5);                                                                               \
        if (RN_) WAIT_VM(WN);                                                                     \
        WAIT_LGKM0();                                                                             \
        __builtin_amdgcn_s_barrier();                                                             \
        asm volatile("" ::: "memory");                                                            \
        SCHED_FENCE();                                                                            \
        M_MM(1, 0); if (RN_) { S_RX(0, BUF((it) + 1), co0); M_RW(0, BUF((it) + 1), 0, 0); }       \
        if ((it) == 7) { pt_check(); S_LB(0, 0); } if ((it) == 8) S_LB(3, 0);                                            \
        SCHED_FENCE();                                                                            \
        M_MM(1, 1); if (RN_) { M_RW(0, BUF((it) + 1), 0, 1); M_RW(0, BUF((it) + 1), 0, 2); } if ((it) == 7) S_LB(0, 1); if ((it) == 8) S_LB(3, 1); \
        if ((it) == 4) { M_BIAS(4); M_BIAS(5); }                                                  \
        SCHED_FENCE();                                                                            \
        M_MM(1, 2); if (RN_) { M_RW(0, BUF((it) + 1), 0, 3); M_RW(0, BUF((it) + 1), 0, 4); } if ((it) == 7) S_LB(0, 2); if ((it) == 8) S_LB(3, 2); SCHED_FENCE(); \
        M_MM(1, 3); if (RN_) M_RW(0, BUF((it) + 1), 0, 5); if ((it) == 7) S_LB(0, 3); if ((it) == 8) S_LB(3, 3); SCHED_FENCE(); \
        M_MM(1, 4); if ((it) == 7) S_LB(0, 4); if ((it) == 8) S_LB(3, 4);                                                    \
        if ((it) == 2 || (it) == 3) { M_BIAS(2 * (it) - 4); M_BIAS(2 * (it) - 3); }               \
        if ((it) == 5) GET_TOK();                                                                 \
        SCHED_FENCE();                                                                            \
        M_MM(1, 5);                                                                               \
    } while (0)
        asm volatile("" : "+v"(a2), "+v"(pa), "+v"(pb), "+v"(vox[0]), "+v"(vox[1]), "+v"(vox[2]), "+v"(vox[3]));
        WAIT_VM(8);
        __builtin_amdgcn_s_barrier();
        asm volatile("" ::: "memory");
        S_RX(0, BUF(0), co0); M_RW(0, BUF(0), 0, 0); M_RW(0, BUF(0), 0, 1); M_RW(0, BUF(0), 0, 2); M_RW(0, BUF(0), 0, 3); M_RW(0, BUF(0), 0, 4); M_RW(0, BUF(0), 0, 5);
#pragma unroll
        for (int j = 0; j < 6; ++j) { acc[j][0] = (f32x4){0.f, 0.f, 0.f, 0.f}; acc[j][1] = (f32x4){0.f, 0.f, 0.f, 0.f}; }
        M_BODY(0, 8);   M_BODY(1, 4);   M_BODY(2, 4);   M_BODY(3, 4);   M_BODY(4, 4);   M_BODY(5, 4);   M_BODY(6, 4);
        M_BODY(7, 4);   M_BODY(8, 10);   M_BODY(9, 0);
        __builtin_amdgcn_s_setprio(2);
#pragma unroll
        for (int v = 0; v < 3; ++v) {
#pragma unroll
            for (int rt = 0; rt < 2; ++rt) {
                u32x4 o;
#pragma unroll
                for (int jj = 0; jj < 2; ++jj) {
                    float r[4];
#pragma unroll
                    for (int i = 0; i < 4; ++i) {
                        const float h = acc[2 * v + jj][rt][i];
                        const float e = __builtin_amdgcn_exp2f(h * LOG2E);
                        const float d = fmaf(e, fmaf(0.5f, e, 1.0f), 1.0f);
                        r[i] = fmaf(-h, __builtin_amdgcn_rcpf(d), h);
                    }
                    o[2 * jj] = pack2(r[0], r[1]);
                    o[2 * jj + 1] = pack2(r[2], r[3]);
                }
                st128_wt(a2 + (size_t)(40 + 3 * cb + v) * 8192 + rt * 1024, o);
            }
        }
        P2_BEGIN(1, PSL1);
        P2_STEP(0, 12, 8, 7); P2_POLLED();
        P2_STEP(1, 12, 8, 7); P2_STEP(2, 12, 8, 7); P2_STEP(3, 12, 8, 7); P2_STEP(4, 12, 8, 7); P2_STEP(5, 12, 13, 7); lds_arrive(sm + K1_CNT + 16 + 4 * wc, lane);
        P2_STEP(6, 12, 18, 7); P2_STEP(7, 12, 18, 7); P2_STEP(8, 12, 18, 7);
        lds_wait(sm + K1_CNT + 8, 4u);
        lds_arrive(sm + K1_CNT + 12, lane);
        P2_STEP(9, 12, 18, 7); P2_STEP(10, 12, 9, 7); P2_STEP(11, 12, 0, 7);
        gl[0] = gl[1] = gl[2] = gl[3] = 0u;
        __builtin_amdgcn_s_setprio(0);
        if (bid == 255 && wave == 7 && lane == 0) flag_set(ws + WS_EP, tok);
    }
    };
    if (wave < 4) run(IC<0>{}, IC<0>{}); else run(IC<1>{}, IC<0>{});
    int role = wave >> 2;
    asm volatile("" : "+s"(role));
    if (role == 0) run(IC<0>{}, IC<1>{}); else run(IC<1>{}, IC<1>{});
    if (wave == 6) lds_wait(sm + K1_CNT + 12, 8u);
#pragma unroll
    for (int r = 0; r < 4; ++r)
#pragma unroll
        for (int n = 0; n < 5; ++n) {
            if (r == 0 && n < 3 && wave == n) *(float*)(sm + wave * 21504 + lane * 4) = (u2f(gl[0]) + u2f(gl[1])) + (wave < 2 ? u2f(gl[2]) + u2f(gl[3]) : 0.f);
            else *(f32x4*)(sm + wave * 20480 + (r * 5 + n) * 1024 + lane * 16) = ac2[r][n];
        }
    wg_sync();
#pragma unroll
    for (int m = 0; m < 3; ++m) {
        const int tile = wave + 8 * m;
        if (tile < 20) {
            const char* rp = sm + tile * 1024 + lane * 16;
            f32x4 p0 = *(const f32x4*)rp, p1 = *(const f32x4*)(rp + 20480), p2 = *(const f32x4*)(rp + 40960);
            if (m == 0) { p0 = wave == 0 ? ac2[0][0] : p0; p1 = wave == 1 ? ac2[0][1] : p1; p2 = wave == 2 ? ac2[0][2] : p2; }
            const f32x4 vs = (p0 + p1) + (p2 + *(const f32x4*)(rp + 61440));
            const f32x4 vm = (*(const f32x4*)(rp + 81920) + *(const f32x4*)(rp + 102400)) + (*(const f32x4*)(rp + 122880) + *(const f32x4*)(rp + 143360));
            const int r = tile / 5;
            const float* gp = (const float*)sm + 32 * (r >> 1) + 8 * (t >> 2) + 4 * (r & 1) + (t & 3);
            const float g = sigmoidf_((gp[0] + gp[5376]) + gp[10752]);
            f32x4 v = vs * (2.0f * g) + vm * (1.0f - g);
            v[0] += g * bflo(xv[m][0]) * bflo(dv[m][0]);
            v[1] += g * bfhi(xv[m][0]) * bfhi(dv[m][0]);
            v[2] += g * bflo(xv[m][1]) * bflo(dv[m][1]);
            v[3] += g * bfhi(xv[m][1]) * bfhi(dv[m][1]);
            st128_wt(out + oo[m], __builtin_bit_cast(u32x4, v));
        }
    }
}

extern "C" void kernel_launch(void* const* d_in, const int* in_sizes, int n_in, void* d_out, int out_size, void* d_ws,
                              size_t ws_size, hipStream_t stream) {
    kf<<<256, 512, 0, stream>>>((const u16*)d_in[0], (const u16*)d_in[1], (const u16*)d_in[2], (const u16*)d_in[3], (const u16*)d_in[4],
                                (const u16*)d_in[5], (const u16*)d_in[6], (const u16*)d_in[7], (const u16*)d_in[8], (const u16*)d_in[10],
                                (const u16*)d_in[11], (const u16*)d_in[12], (const u16*)d_in[13], (char*)d_ws, (float*)d_out);
}
```

```cpp
#include <hip/hip_runtime.h>
#include <stdint.h>

typedef short s16x4 __attribute__((ext_vector_type(4)));
typedef short s16x8 __attribute__((ext_vector_type(8)));
typedef __bf16 bf16x2 __attribute__((ext_vector_type(2)));
typedef float f32x2 __attribute__((ext_vector_type(2)));
typedef float f32x4 __attribute__((ext_vector_type(4)));
typedef unsigned u32x2 __attribute__((ext_vector_type(2)));
typedef unsigned u32x4 __attribute__((ext_vector_type(4)));
typedef unsigned short u16;
#define LAS __attribute__((address_space(3)))
#define GAS __attribute__((address_space(1)))
#define DI __device__ __forceinline__

constexpr int SEQ = 2048, DM = 640, NP = 640, NF = 1536;
constexpr int NKS = 88;

constexpr size_t WS_A2 = 0;
constexpr size_t WS_W2 = WS_A2 + (size_t)16 * NKS * 8192;
constexpr size_t WS_G = WS_W2 + (size_t)8 * NKS * 5120;
constexpr size_t WS_AGG = WS_G + 10 * 2048 * 4;
constexpr size_t WS_EP = WS_AGG + 16 * 16 * 640;
constexpr size_t WS_FPT = WS_EP + 256;
constexpr size_t WS_FAG = WS_FPT + 2048;
constexpr size_t WS_FP1 = WS_FAG + 2048;
constexpr size_t WS_JUNK = WS_FP1 + 16384;

DI unsigned f2u(float v) { return __builtin_bit_cast(unsigned, v); }
DI float u2f(unsigned v) { return __builtin_bit_cast(float, v); }
DI bool wave_all(bool p) { return __builtin_amdgcn_ballot_w64(!p) == 0ull; }
DI void wg_sync() {
    __builtin_amdgcn_fence(__ATOMIC_RELEASE, "workgroup");
    __builtin_amdgcn_s_barrier();
    __builtin_amdgcn_fence(__ATOMIC_ACQUIRE, "workgroup");
}
DI float sigmoidf_(float v) { return 1.0f / (1.0f + __builtin_amdgcn_exp2f(v * -1.44269504088896341f)); }
DI float bflo(unsigned v) { return __uint_as_float(v << 16); }
DI float bfhi(unsigned v) { return __uint_as_float(v & 0xffff0000u); }
DI unsigned pack2(float a, float b) {
    f32x2 p = {a, b};
    return __builtin_bit_cast(unsigned, __builtin_convertvector(p, bf16x2));
}
DI f32x4 mfma16(s16x8 a, s16x8 b, f32x4 c) { return __builtin_amdgcn_mfma_f32_16x16x32_bf16(a, b, c, 0, 0, 0); }
template <int S> DI float shr_zero(float v) {
    return __builtin_bit_cast(float, __builtin_amdgcn_update_dpp(0, __builtin_bit_cast(int, v), 0x110 + S, 0xf, 0xf, true));
}
DI float swp(float v) {
    return __builtin_bit_cast(float, __builtin_amdgcn_update_dpp(0, __builtin_bit_cast(int, v), 0xB1, 0xf, 0xf, true));
}
DI f32x2 cmul(f32x2 a, f32x2 b) { return (f32x2){a.x * b.x - a.y * b.y, a.x * b.y + a.y * b.x}; }
DI f32x2 cfma(f32x2 a, f32x2 b, f32x2 c) {
    return (f32x2){fmaf(-a.y, b.y, fmaf(a.x, b.x, c.x)), fmaf(a.y, b.x, fmaf(a.x, b.y, c.y))};
}
DI f32x2 cmul_pk(f32x2 a, f32x2 b) { return (f32x2){fmaf(-a.y, b.y, a.x * b.x), fmaf(a.y, b.x, a.x * b.y)}; }
DI f32x2 cmul_sc(f32x2 a, f32x2 b) {
    float p = a.x * b.x, r = a.x * b.y;
    asm("" : "+v"(p));
    asm("" : "+v"(r));
    return (f32x2){fmaf(-a.y, b.y, p), fmaf(a.y, b.x, r)};
}
DI float dot2bf(unsigned a, unsigned b, float c) {
    return __builtin_amdgcn_fdot2_f32_bf16(__builtin_bit_cast(bf16x2, a), __builtin_bit_cast(bf16x2, b), c, false);
}
#define WAIT_VM(n) asm volatile("s_waitcnt vmcnt(%0)" ::"n"(n) : "memory")
#define WAIT_LGKM0() asm volatile("s_waitcnt lgkmcnt(0)" ::: "memory")
#define ACQUIRE() asm volatile("buffer_inv sc1" ::: "memory")
#define SCHED_FENCE() __builtin_amdgcn_sched_barrier(0)

DI void glds16(const void* gsrc, unsigned lds_dst) {
    unsigned keep;
    asm volatile("s_mov_b32 %0, m0\n\ts_mov_b32 m0, %2\n\ts_nop 0\n\tglobal_load_lds_dwordx4 %1, off\n\ts_mov_b32 m0, %0"
                 : "=&s"(keep) : "v"(gsrc), "s"(lds_dst) : "memory");
}
DI void glds4(const void* gsrc, unsigned lds_dst) {
    unsigned keep;
    asm volatile("s_mov_b32 %0, m0\n\ts_mov_b32 m0, %2\n\ts_nop 0\n\tglobal_load_lds_dword %1, off\n\ts_mov_b32 m0, %0"
                 : "=&s"(keep) : "v"(gsrc), "s"(lds_dst) : "memory");
}
DI void glds4_c(const void* gsrc, unsigned lds_dst) {
    unsigned keep;
    asm volatile("s_mov_b32 %0, m0\n\ts_mov_b32 m0, %2\n\ts_nop 0\n\tglobal_load_lds_dword %1, off sc1\n\ts_mov_b32 m0, %0"
                 : "=&s"(keep) : "v"(gsrc), "s"(lds_dst) : "memory");
}
DI void glds16x4(const void* gsrc, unsigned lds_dst) {
    unsigned keep;
    asm volatile("s_mov_b32 %0, m0\n\ts_mov_b32 m0, %2\n\ts_nop 0\n\tglobal_load_lds_dwordx4 %1, off\n\t"
                 "global_load_lds_dwordx4 %1, off offset:1024\n\tglobal_load_lds_dwordx4 %1, off offset:2048\n\t"
                 "global_load_lds_dwordx4 %1, off offset:3072\n\ts_mov_b32 m0, %0"
                 : "=&s"(keep) : "v"(gsrc), "s"(lds_dst) : "memory");
}
DI void st128_wt(void* p, u32x4 v) { asm volatile("global_store_dwordx4 %0, %1, off sc1\n\ts_nop 1" :: "v"(p), "v"(v) : "memory"); }
DI void st64_wt(void* p, u32x2 v) { asm volatile("global_store_dwordx2 %0, %1, off sc1\n\ts_nop 1" :: "v"(p), "v"(v) : "memory"); }
DI unsigned ld32_asm(const void* p) { unsigned r; asm volatile("global_load_dword %0, %1, off" : "=v"(r) : "v"(p) : "memory"); return r; }
DI unsigned ld16_asm(const void* p) { unsigned r; asm volatile("global_load_ushort %0, %1, off" : "=v"(r) : "v"(p) : "memory"); return r; }
DI u32x2 ld64_asm(const void* p) { u32x2 r; asm volatile("global_load_dwordx2 %0, %1, off" : "=v"(r) : "v"(p) : "memory"); return r; }
DI u32x4 ld128_asm(const void* p) { u32x4 r; asm volatile("global_load_dwordx4 %0, %1, off" : "=v"(r) : "v"(p) : "memory"); return r; }
constexpr float LOG2E = 1.44269504088896341f, INV2PI = 0.15915494309189535f;
DI f32x2 lam_bar(float lr, float li, float ld) {
    const float dt = __builtin_amdgcn_exp2f(ld * LOG2E);
    const float m = __builtin_amdgcn_exp2f(dt * lr * LOG2E);
    const float rv = __builtin_amdgcn_fractf(dt * li * INV2PI);
    return (f32x2){m * __builtin_amdgcn_cosf(rv), m * __builtin_amdgcn_sinf(rv)};
}

typedef unsigned long long u64;
DI void glds16_c(const void* gsrc, unsigned lds_dst) {
    unsigned keep;
    lds_dst = __builtin_amdgcn_readfirstlane(lds_dst);
    asm volatile("s_mov_b32 %0, m0\n\ts_mov_b32 m0, %2\n\ts_nop 0\n\tglobal_load_lds_dwordx4 %1, off sc1\n\ts_mov_b32 m0, %0"
                 : "=&s"(keep) : "v"(gsrc), "s"(lds_dst) : "memory");
}
DI void glds16x4_c(const void* gsrc, unsigned lds_dst) {
    unsigned keep;
    asm volatile("s_mov_b32 %0, m0\n\ts_mov_b32 m0, %2\n\ts_nop 0\n\tglobal_load_lds_dwordx4 %1, off sc1\n\t"
                 "global_load_lds_dwordx4 %1, off offset:1024 sc1\n\tglobal_load_lds_dwordx4 %1, off offset:2048 sc1\n\t"
                 "global_load_lds_dwordx4 %1, off offset:3072 sc1\n\ts_mov_b32 m0, %0"
                 : "=&s"(keep) : "v"(gsrc), "s"(lds_dst) : "memory");
}
DI void flag_set(char* p, u64 tok) { __hip_atomic_store((GAS u64*)p, tok, __ATOMIC_RELAXED, __HIP_MEMORY_SCOPE_AGENT); }
DI void flag_wait(const char* p, bool need, u64 tok) {
    for (unsigned spins = 0; spins < (1u << 20); ++spins) {
        u64 v = tok;
        if (need) v = __hip_atomic_load((const GAS u64*)p, __ATOMIC_RELAXED, __HIP_MEMORY_SCOPE_AGENT);
        if (__all(v == tok)) break;
        __builtin_amdgcn_s_sleep(8);
    }
    asm volatile("" ::: "memory");
}

#define PRIO_OFF 1
DI void lds_arrive(char* p, int) {
    asm volatile("s_mov_b64 exec, 1\n\tds_add_u32 %0, %1\n\ts_mov_b64 exec, -1" :: "v"((unsigned)(size_t)(LAS char*)p), "v"(1u) : "memory");
}
DI void lds_wait(const char* p, unsigned n) {
    unsigned v, sv, cnt;
    asm volatile("s_mov_b32 %2, 0x400000\n"
                 "1:\n\tds_read_b32 %0, %3\n\ts_waitcnt lgkmcnt(0)\n\tv_readfirstlane_b32 %1, %0\n\ts_cmp_ge_u32 %1, %4\n\ts_cbranch_scc1 2f\n\t"
                 "s_sleep 1\n\ts_sub_u32 %2, %2, 1\n\ts_cbranch_scc0 1b\n"
                 "2:" : "=&v"(v), "=&s"(sv), "=&s"(cnt) : "v"((unsigned)(size_t)(LAS const char*)p), "s"(n) : "memory", "scc");
}
#define SSL 40
#define PSL0 "8"
#define PSL1 "4"
template <int V> struct IC { static constexpr int value = V; };
#define ROTF ((((rb + cb) & 3) * 5 + 1) >> 1)
constexpr int K1_STAGE = 38 * 1024;
constexpr int K1_GW = 0;
constexpr int K1_PLR = K1_GW + 1280, K1_PLI = K1_GW + 1360, K1_PLD = K1_GW + 1440, K1_PB1 = K1_GW + 1536, K1_PEP = K1_GW + 1728;
constexpr int K1_RING = 2048;
#define BUF(it) (K1_RING + ((it) % 3) * K1_STAGE)
constexpr int K1_TAB = K1_RING + 3 * K1_STAGE;
constexpr int K1_XCH = K1_TAB + 5120;
constexpr int K1_CNT = K1_XCH + 1280;
constexpr int K1_PRT = K1_CNT + 32;
constexpr int K1_PT = 123 * 1024;
constexpr int K1_FM = 140 * 1024;
constexpr int KF_LDS = 160 * 1024;

__global__ __launch_bounds__(512, 2) __attribute__((target("no-packed-fp32-ops"))) void kf(const u16* __restrict__ x, const u16* __restrict__ Lre, const u16* __restrict__ Lim,
                                             const u16* __restrict__ Bre, const u16* __restrict__ Bim, const u16* __restrict__ Cre,
                                             const u16* __restrict__ Cim, const u16* __restrict__ logdt, const u16* __restrict__ Dskip,
                                             const u16* __restrict__ w1, const u16* __restrict__ b1, const u16* __restrict__ w2,
                                             const u16* __restrict__ gw, char* __restrict__ ws, float* __restrict__ out) {
    __shared__ __attribute__((aligned(16))) char sm[KF_LDS];
    const int tid = threadIdx.x, lane = tid & 63;
    const int wave = __builtin_amdgcn_readfirstlane(tid >> 6);
    const int bid = blockIdx.x, xcd = bid & 7, idx = bid >> 3;
    const int rb = 8 * (xcd & 1) + (idx & 7);
    const int cb = 4 * (xcd >> 1) + (idx >> 3);
    const int RG = 4 * xcd + (idx & 3);
    const int CB = idx >> 2;
    const int t = lane & 15, q = lane >> 4;
    const unsigned smb = (unsigned)(size_t)(LAS char*)sm;
    if (wave < 2) {
        u64 pc0;
        asm volatile("s_getpc_b64 %0" : "=s"(pc0));
        glds4((const char*)(pc0 & ~255ull) + ((idx + 2 * wave) % 5) * 8192 + lane * 128, smb + K1_FM);
    }
    asm volatile("" :: "s"(x), "s"(Lre), "s"(Lim), "s"(Bre), "s"(Bim), "s"(Cre), "s"(Cim), "s"(logdt), "s"(Dskip), "s"(w1), "s"(b1), "s"(w2),
                 "s"(gw), "s"(ws), "s"(out));
    if (wave == 7) {
        const char* tp = (const char*)(out + 64 * RG * DM);
        if (lane == 1) tp = ws + WS_A2 + (size_t)rb * NKS * 8192;
        if (lane == 2) tp = ws + WS_A2 + (size_t)(RG >> 1) * NKS * 8192;
        if (lane == 3) tp = ws + WS_W2 + (size_t)CB * NKS * 5120;
        if (lane == 4) tp = ws + WS_AGG;
        if (lane == 5) tp = (const char*)Dskip;
        if (lane == 6) tp = ws + WS_A2 + (size_t)rb * NKS * 8192 + NKS * 8192 - 128;
        if (lane == 7) tp = ws + WS_A2 + (size_t)(RG >> 1) * NKS * 8192 + NKS * 8192 - 128;
        if (lane == 8) tp = ws + WS_W2 + (size_t)CB * NKS * 5120 + NKS * 5120 - 128;
        if (lane == 9) tp = (const char*)(out + (64 * RG + 63) * DM);
        glds4_c(tp, smb + K1_FM);
    }
    float keep = 0.f;
    u32x2 xv[3], dv[3];
    int oo[3];
    auto run = [&](auto ROLE_, auto PART_) __attribute__((always_inline)) {
    constexpr int ROLE = decltype(ROLE_)::value, PART = decltype(PART_)::value;
    int lane = tid & 63;
    asm volatile("" : "+v"(lane));
    const int t = lane & 15, q = lane >> 4;
#define G_M0(base, c) asm volatile("s_add_i32 m0, %0, %1" :: "s"(base), "n"(c) : "scc")
#define G_LDS(vo, sb, c) asm volatile("global_load_lds_dwordx4 %0, %1 offset:%2" :: "v"(vo), "s"(sb), "n"(c) : "memory")
#define G_LDV(va, c) asm volatile("global_load_lds_dwordx4 %0, off offset:%1" :: "v"(va), "n"(c) : "memory")
#define G_NOP() asm volatile("s_nop 0")
    const int l8 = lane >> 3;
    unsigned vox[4];
    const char* wsr[3];
    const int rot = ROTF;
    const char* w1s = (const char*)w1 + (size_t)rot * (64 * NF * 2);
    const char* xs = (const char*)x + rot * 128;
#define K_WRAP(st) (((st) == 2 || (st) == 5 || (st) == 7) && rot == 10 - (st))
#define W_NEXT(st)                                                                                \
    do {                                                                                          \
        w1s += K_WRAP(st) ? -9 * (64 * NF * 2) : 64 * NF * 2;                                     \
        if ((st) == 2 || (st) == 5 || (st) == 7) { const long d_ = K_WRAP(st) ? -1280 : 0; wsr[0] += d_; wsr[1] += d_; wsr[2] += d_; } \
    } while (0)
#define X_NEXT(st) do { if ((st) == 2 || (st) == 5 || (st) == 7) xs += K_WRAP(st) ? -1280 : 0; } while (0)
    const unsigned lw = smb + 1024 * (wave & 3);
    const unsigned lm = smb + (ROLE == 0 ? 3072 : 4096) * (wave & 3);
#define X_M0(st, i) G_M0(lm, BUF(st) + 1024 * (i) - 128 * (st))
#define X_LD(st, i) G_LDS(vox[i], xs, 128 * (st))
#define W_M0(st, j) G_M0(lw, BUF(st) + 16384 + 4096 * (j) - 128 * (st))
#define W_LD(st, j) G_LDV(wsr[j], 128 * (st))
#define M_M0(st, i) G_M0(lm, BUF(st) + 26624 + 1024 * (i))
#define M_LD(i) G_LDS(vox[i], w1s, 0)
    const unsigned pu = 4 * bid + (wave & 3);
    const unsigned pkg = (pu * 1639u) >> 16, pv = pu - 400u, ptg = (pv * 1366u) >> 16;
    if constexpr (ROLE == 1) {
#pragma unroll
        for (int i = 0; i < 4; ++i) {
            const int p = 8 * i + l8;
            vox[i] = ((128 * rb + 32 * (wave & 3) + 8 * ((p & 15) >> 2) + 4 * (p >> 4) + (p & 3)) * DM + (((lane & 7) ^ ((p >> 1) & 7)) << 3)) * 2;
            if constexpr (PART == 0) {
                if (i == 0) X_M0(0, 0); else if (i == 1) X_M0(0, 1); else if (i == 2) X_M0(0, 2); else X_M0(0, 3);
                G_NOP();
                G_LDS(vox[i], xs, 0);
                SCHED_FENCE();
            }
        }
        if constexpr (PART == 0) {
            X_M0(1, 0); G_NOP(); X_LD(1, 0); X_M0(1, 1); G_NOP(); X_LD(1, 1); X_M0(1, 2); G_NOP(); X_LD(1, 2); X_M0(1, 3); G_NOP(); X_LD(1, 3);
            const bool isc = pu < 400 || pu >= 880;
            const unsigned kg = pu < 400 ? pkg : 0, nt = pu < 400 ? pu - 40 * pkg : 0;
            const unsigned o = ((isc ? 16 * nt + l8 : 32 * (pv - 48 * ptg) + l8) * DM + 64 * (isc ? kg : ptg) + ((lane & 7) ^ l8) * 8) * 2;
#pragma unroll
            for (int i = 0; i < 4; ++i) {
                const u16* p0 = isc ? ((i >> 1) ? Cim : Cre) + 8 * (i & 1) * DM : w2 + 8 * i * DM;
                G_M0(lm, K1_PT + i * 1024); G_NOP(); G_LDS(o, p0, 0);
            }
            return;
        }
    } else {
#pragma unroll
        for (int j = 0; j < 3; ++j) {
            const int p = 8 * (4 * j + wave) + l8;
            const int ch = 40 * cb + 8 * (p >> 4) + ((p & 15) >> 1);
            wsr[j] = (const char*)(((p & 1) ? Bim : Bre) + ch * DM + 64 * rot + (((lane & 7) ^ ((p >> 1) & 7)) << 3));
            if (PART == 0 && j < 2) {
                if (j == 0) W_M0(0, 0); else W_M0(0, 1);
                G_NOP();
                G_LDV(wsr[j], 0);
                SCHED_FENCE();
            }
        }
#pragma unroll
        for (int i = 0; i < 3; ++i) {
            const int gI = 64 * (3 * wave + i) + lane, row = gI / 12, cp = gI - 12 * row;
            int j = (cp >> 1) - ((row >> 3) & 1);
            j += j < 0 ? 6 : 0;
            vox[i] = (row * NF + 96 * cb + 16 * j + 8 * (cp & 1)) * 2;
            if constexpr (PART == 0) {
                if (i == 0) M_M0(0, 0); else if (i == 1) M_M0(0, 1); else M_M0(0, 2);
                G_NOP();
                M_LD(i);
                SCHED_FENCE();
            }
        }
        W_NEXT(1);
        if constexpr (PART == 0) {
            const u16* p = gw + lane * 8;
            if (wave == 3) {
                p = gw + 512 + t * 8;
                if (lane >= 16 && lane < 21) p = Lre + 40 * cb + (lane - 16) * 8;
                if (lane >= 21 && lane < 26) p = Lim + 40 * cb + (lane - 21) * 8;
                if (lane >= 26 && lane < 31) p = logdt + 40 * cb + (lane - 26) * 8;
                if (lane >= 32 && lane < 44) p = b1 + 96 * cb + (lane - 32) * 8;
                if (lane == 44) p = (const u16*)(ws + WS_EP);
            }
            glds16_c(wave < 2 ? wsr[2] : (const char*)p, wave < 2 ? lw + BUF(0) + 16384 + 8192 : smb + K1_GW + (wave - 2) * 1024);
            if (wave < 2) { W_M0(1, 2); G_NOP(); W_LD(1, 2); }
            W_M0(1, 0); G_NOP(); W_LD(1, 0); W_M0(1, 1); G_NOP(); W_LD(1, 1);
            M_M0(1, 0); G_NOP(); M_LD(0); M_M0(1, 1); G_NOP(); M_LD(1); M_M0(1, 2); G_NOP(); M_LD(2);
            return;
        }
    }
    char* ptl = sm + K1_PT + (wave & 3) * 4096;
    const int wc = wave & 3;
    const int xoff = (32 * wc + t) * 128;
    const int co0 = (q ^ ((t >> 1) & 7)) * 16, co1 = co0 ^ 64;
    char* a2 = ws + WS_A2 + (size_t)rb * NKS * 8192 + (2 * wc) * 1024 + lane * 16;
    const int rb2 = RG >> 1, hh = RG & 1;
    const int ks0 = ROLE == 0 ? 10 * wave : 40 + 12 * (wave - 4);
    const char* pa = ws + WS_A2 + ((size_t)rb2 * NKS + ks0) * 8192 + hh * 4096 + lane * 16;
    const char* pb = ws + WS_W2 + ((size_t)CB * NKS + ks0) * 5120 + 1024 + lane * 16;
    u64 tok;
#define GET_TOK() do { const u32x2 ep_ = *(const u32x2*)(sm + K1_PEP); \
                       tok = (((u64)(unsigned)__builtin_amdgcn_readfirstlane(ep_[1]) << 32) | (unsigned)__builtin_amdgcn_readfirstlane(ep_[0])) + 1ull; } while (0)
    u32x4 ptf[4];
    auto pretile_read = [&]() __attribute__((always_inline)) {
        if (pu < 400) {
#pragma unroll
            for (int i = 0; i < 4; ++i) {
                const char* pp = ptl + t * 128 + (((2 * i + (q >> 1)) ^ (t & 7)) << 4) + 8 * (q & 1);
                const u32x2 rr = *(const u32x2*)pp, ii = *(const u32x2*)(pp + 2048);
                ptf[i] = (u32x4){rr[0], rr[1], ii[0], ii[1]};
            }
        } else {
            const int r = 4 * q + (t >> 2);
#pragma unroll
            for (int i = 0; i < 4; ++i) {
                const char* pp = ptl + r * 128 + (((2 * i + ((lane >> 1) & 1)) ^ (r & 7)) << 4) + (lane & 1) * 8;
                const s16x4 lo = __builtin_amdgcn_ds_read_tr16_b64_v4i16((LAS s16x4*)pp);
                const s16x4 hi = __builtin_amdgcn_ds_read_tr16_b64_v4i16((LAS s16x4*)(pp + 2048));
                ptf[i] = __builtin_bit_cast(u32x4, __builtin_shufflevector(lo, hi, 0, 1, 2, 3, 4, 5, 6, 7));
            }
        }
    };
    auto pretile_mix = [&]() __attribute__((always_inline)) {
        if (pu < 400) {
#pragma unroll
            for (int i = 0; i < 4; ++i) {
                const unsigned r0 = ptf[i][0], r1 = ptf[i][1], i0 = ptf[i][2] ^ 0x80008000u, i1 = ptf[i][3] ^ 0x80008000u;
                ptf[i] = (u32x4){(r0 & 0xffffu) | (i0 << 16), (r0 >> 16) | (i0 & 0xffff0000u), (r1 & 0xffffu) | (i1 << 16), (r1 >> 16) | (i1 & 0xffff0000u)};
            }
        }
    };
    auto pretile_dst = [&](unsigned i) __attribute__((always_inline)) {
        const unsigned g = pu < 400 ? pu - 40 * pkg : 4 * ptg + i, KS = pu < 400 ? 4 * pkg + i : 40 + pv - 48 * ptg;
        const unsigned cB = (g * 13u) >> 6;
        unsigned o = (unsigned)WS_W2 + (cB * (5 * NKS - 5) + KS * 5 + g) * 1024u, j = (unsigned)WS_JUNK + (pu * 4 + i) * 1024u;
        asm("" : "+s"(o), "+s"(j));
        return pu < 880 ? o : j;
    };

    const int bco = wave == 6 ? 6 * 20480 + 4096 : wave * 20480;
    u32x4 Ab[4][4], Bb[4][5];
    f32x4 ac2[4][5];
    unsigned gl[4];
#define P2_L1(dst, P, OFF) asm volatile("global_load_dwordx4 %0, %1, off offset:" #OFF " sc1" : "=&v"(dst) : "v"(P) : "memory")
#define P2_LDB(slot, P)                                                                           \
    do { P2_L1(Bb[slot][0], P, -1024); P2_L1(Bb[slot][1], P, 0); P2_L1(Bb[slot][2], P, 1024); P2_L1(Bb[slot][3], P, 2048); P2_L1(Bb[slot][4], P, 3072); } while (0)
#define P2_LDA(slot, P)                                                                           \
    do { P2_L1(Ab[slot][0], P, 0); P2_L1(Ab[slot][1], P, 1024); P2_L1(Ab[slot][2], P, 2048); P2_L1(Ab[slot][3], P, 3072); } while (0)
    auto pt_check = [&]() __attribute__((always_inline)) {
        const u32x4 v0 = *(const u32x4*)(sm + K1_FM + lane * 16), v1 = *(const u32x4*)(sm + K1_FM + 1024 + lane * 16);
        const unsigned lo = (unsigned)tok, hi = (unsigned)(tok >> 32);
        if (!wave_all(v0[0] == lo && v0[1] == hi && v0[2] == lo && v0[3] == hi && v1[0] == lo && v1[1] == hi && v1[2] == lo && v1[3] == hi)) {
#pragma unroll 1
            for (int k = 0; k < 4; ++k) flag_wait(ws + WS_FPT + (size_t)(64 * k + lane) * 8, true, tok);
        }
    };
    const char* g3 = pb - 1024 + 4 * 5120;
#define P2_CACHE(i) glds16_c(g3 + (i) * 1024, smb + bco + (i) * 1024)
#define P2_M2(ua, ub, i)                                                                          \
    do {                                                                                          \
        ac2[(i) / 5][(i) % 5] = mfma16(__builtin_bit_cast(s16x8, Bb[ub][(i) % 5]), __builtin_bit_cast(s16x8, Ab[ua][(i) / 5]), ac2[(i) / 5][(i) % 5]); \
        ac2[((i) + 1) / 5][((i) + 1) % 5] = mfma16(__builtin_bit_cast(s16x8, Bb[ub][((i) + 1) % 5]), __builtin_bit_cast(s16x8, Ab[ua][((i) + 1) / 5]), ac2[((i) + 1) / 5][((i) + 1) % 5]); \
        SCHED_FENCE();                                                                            \
    } while (0)
#define P2_STEP(j, NS, WN, NL)                                                                    \
    do {                                                                                          \
        asm volatile("s_waitcnt vmcnt(%9)" : "+v"(Ab[(j) & 3][0]), "+v"(Ab[(j) & 3][1]), "+v"(Ab[(j) & 3][2]), "+v"(Ab[(j) & 3][3]), \
                     "+v"(Bb[(j) & 3][0]), "+v"(Bb[(j) & 3][1]), "+v"(Bb[(j) & 3][2]), "+v"(Bb[(j) & 3][3]), "+v"(Bb[(j) & 3][4]) : "n"(WN) : "memory"); \
        SCHED_FENCE();                                                                            \
        const char* pa_ = pa + ((j) + 3) * 8192;                                                  \
        const char* pb_ = pb + ((j) + 3) * 5120;                                                  \
        constexpr bool LA_ = (j) + 3 < (NS), LBG_ = (j) + 3 >= (NL) && (j) + 3 < (NS), LBL_ = (j) + 3 >= 4 && (j) + 3 < (NL); \
        P2_M2((j) & 3, (j) & 3, 0); if (LA_) { P2_L1(Ab[((j) + 3) & 3][0], pa_, 0); SCHED_FENCE(); }      \
        P2_M2((j) & 3, (j) & 3, 2); if (LA_) { P2_L1(Ab[((j) + 3) & 3][1], pa_, 1024); SCHED_FENCE(); }   \
        P2_M2((j) & 3, (j) & 3, 4); if (LA_) { P2_L1(Ab[((j) + 3) & 3][2], pa_, 2048); SCHED_FENCE(); }   \
        P2_M2((j) & 3, (j) & 3, 6); if (LA_) { P2_L1(Ab[((j) + 3) & 3][3], pa_, 3072); SCHED_FENCE(); }   \
        if (LBL_) { _Pragma("unroll") for (int n_ = 0; n_ < 5; ++n_) Bb[((j) + 3) & 3][n_] = *(const u32x4*)(sm + bco + (((j) + 3 - 4) * 5 + n_) * 1024 + lane * 16); SCHED_FENCE(); } \
        P2_M2((j) & 3, (j) & 3, 8); if (LBG_) { P2_L1(Bb[((j) + 3) & 3][0], pb_, -1024); SCHED_FENCE(); } \
        P2_M2((j) & 3, (j) & 3, 10); if (LBG_) { P2_L1(Bb[((j) + 3) & 3][1], pb_, 0); SCHED_FENCE(); }    \
        P2_M2((j) & 3, (j) & 3, 12); if (LBG_) { P2_L1(Bb[((j) + 3) & 3][2], pb_, 1024); SCHED_FENCE(); } \
        P2_M2((j) & 3, (j) & 3, 14); if (LBG_) { P2_L1(Bb[((j) + 3) & 3][3], pb_, 2048); SCHED_FENCE(); } \
        P2_M2((j) & 3, (j) & 3, 16); if (LBG_) { P2_L1(Bb[((j) + 3) & 3][4], pb_, 3072); SCHED_FENCE(); } \
        P2_M2((j) & 3, (j) & 3, 18);                                                              \
    } while (0)
    u64 pl[3];
#define POLL1(i, PSL) "s_waitcnt vmcnt(2)\n\tv_cmp_ne_u64 vcc, %" #i ", %5\n\ts_cmp_eq_u64 vcc, 0\n\ts_cbranch_scc1 2f\n\t" \
                 "global_load_dwordx2 %" #i ", %4, off sc1\n\ts_sleep " PSL "\n\t"
#define P2_POLLED() asm volatile("" : "+v"(xv[0]), "+v"(xv[1]), "+v"(xv[2]), "+v"(dv[0]), "+v"(dv[1]), "+v"(dv[2]) : "v"(pl[0]), "v"(pl[1]), "v"(pl[2]))
#define P2_BEGIN(role, PSL)                                                                        \
    do {                                                                                          \
        if ((role) == 1) { _Pragma("unroll") for (int i = 0; i < 15; ++i) P2_CACHE(i); WAIT_VM(15); } else WAIT_VM(0); \
                                                                                           \
        if (lane == 0) flag_set(ws + WS_FP1 + (size_t)(((((role) * 16 + rb) * 2 + (wc >> 1)) * 16 + cb) * 2 + (wc & 1)) * 8, tok); \
        if ((role) == 1) {         \
            lds_wait(sm + K1_CNT, 4u);                                                            \
            const char* gs_ = ws + WS_W2 + ((size_t)CB * NKS + 10 * wc + 4) * 5120 + lane * 16;   \
            _Pragma("unroll") for (int i = 0; i < 5; ++i) glds16x4_c(gs_ + i * 4096, __builtin_amdgcn_readfirstlane(smb + wc * 20480 + i * 4096)); \
        }                                                                                         \
        _Pragma("unroll") for (int m = 0; m < 3; ++m) {                                           \
            int tile = wave + 8 * m; tile = tile < 20 ? tile : wave;                              \
            const int r = tile / 5, n = tile - 5 * r;                                             \
            const int col = 80 * CB + 16 * n + 4 * q;                                             \
            oo[m] = (64 * RG + 32 * (r >> 1) + 8 * (t >> 2) + 4 * (r & 1) + (t & 3)) * DM + col;  \
            xv[m] = ld64_asm(x + oo[m]); dv[m] = ld64_asm(Dskip + col);                           \
        }                                                                                         \
        if ((role) == 0) lds_wait(sm + K1_CNT + 16 + 4 * wc, 1u);     \
        unsigned cnt_;                                         \
        asm volatile("global_load_dwordx2 %0, %4, off sc1\n\ts_sleep " PSL "\n\tglobal_load_dwordx2 %1, %4, off sc1\n\ts_sleep " PSL "\n\t" \
                     "global_load_dwordx2 %2, %4, off sc1\n\ts_mov_b32 %3, 0x80000\n"             \
                     "1:\n\t" POLL1(0, PSL) POLL1(1, PSL) POLL1(2, PSL)                                 \
                     "s_sub_u32 %3, %3, 1\n\ts_cbranch_scc0 1b\n"                                 \
                     "2:"                                                                         \
                     : "=&v"(pl[0]), "=&v"(pl[1]), "=&v"(pl[2]), "=&s"(cnt_)                      \
                     : "v"(ws + WS_FP1 + (size_t)(((role) * 32 + RG) * 32 + 8 * wc + (lane & 7)) * 8), "s"(tok) : "memory", "vcc", "scc"); \
        if ((role) == 0) P2_GATES();                                                              \
        P2_LDA(0, pa); P2_LDA(1, pa + 8192); P2_LDA(2, pa + 16384);                               \
        _Pragma("unroll") for (int r = 0; r < 4; ++r) _Pragma("unroll") for (int n = 0; n < 5; ++n) ac2[r][n] = (f32x4){0.f, 0.f, 0.f, 0.f}; \
    } while (0)
#define P2_GATES()                                                                                \
    _Pragma("unroll") for (int j_ = 0; j_ < 4; ++j_)                                              \
        asm volatile("global_load_dword %0, %1, off sc1" : "=&v"(gl[j_])                          \
                     : "v"(ws + WS_G + ((4 * wc + j_ < 10 ? 4 * wc + j_ : 9) * 2048 + 64 * RG + lane) * 4) : "memory")

    if constexpr (ROLE == 0) {
        f32x4 acc[6][2];
#pragma unroll
        for (int a = 0; a < 6; ++a) { acc[a][0] = (f32x4){0.f, 0.f, 0.f, 0.f}; acc[a][1] = (f32x4){0.f, 0.f, 0.f, 0.f}; }
        s16x8 fx[2][2], fw[2][5];
        const int woff = 16384 + t * 128;
        const int itg = cb >= 10 ? -1 : cb >= rot ? cb - rot : cb + 10 - rot;
#define S_RX(b, sb, co) do { fx[b][0] = *(const s16x8*)(sm + (sb) + xoff + (co)); fx[b][1] = *(const s16x8*)(sm + (sb) + xoff + 2048 + (co)); } while (0)
#define S_RW(b, sb, co, T) fw[b][T] = *(const s16x8*)(sm + (sb) + woff + (T) * 2048 + (co))
#define S_MM(b, T) do { acc[T][0] = mfma16(fx[b][0], fw[b][T], acc[T][0]); acc[T][1] = mfma16(fx[b][1], fw[b][T], acc[T][1]); SCHED_FENCE(); } while (0)
#define S_LB(slot, T) asm volatile("global_load_dwordx4 %0, %1, off sc1" : "=&v"(Bb[slot][T]) : "v"(pb + (slot) * 5120 + ((T) - 1) * 1024) : "memory")
#define S_PT(it, i) do { if ((it) == 1) asm volatile("global_store_dwordx4 %0, %1, %2 sc1\n\ts_nop 1" :: "v"(pto[i]), "v"(ptf[i]), "s"(ws) : "memory"); } while (0)
#define S_BODY(it, WN, XA0, XA1, XA2, XA3)                                                        \
    do {                                                                                          \
        constexpr bool RF_ = (it) < 7, RN_ = (it) < 9;                                            \
        constexpr int SB_ = (it) < 7 ? 0 : (it) - 7;                                              \
        S_MM(0, 0); S_RX(1, BUF(it), co1); S_RW(1, BUF(it), co1, 0); SCHED_FENCE();               \
        S_MM(0, 1); S_RW(1, BUF(it), co1, 1); S_RW(1, BUF(it), co1, 2); XA0; SCHED_FENCE();       \
        S_MM(0, 2); S_RW(1, BUF(it), co1, 3); S_RW(1, BUF(it), co1, 4); XA1; SCHED_FENCE();       \
        S_MM(0, 3); XA2; SCHED_FENCE();                                                           \
        S_MM(0, 4); XA3; SCHED_FENCE();                                                           \
        if ((it) == 0 && wave < 2) WAIT_VM(6); else if (RN_) WAIT_VM(WN);                         \
        WAIT_LGKM0();                                                                             \
        __builtin_amdgcn_s_barrier();                                                             \
        asm volatile("" ::: "memory");                                                            \
        SCHED_FENCE();                                                                            \
        S_MM(1, 0);                                                                               \
        if (__builtin_expect((it) == itg, 0)) {                                                   \
            const s16x8 g0_ = *(const s16x8*)(sm + K1_GW + (64 * cb + 8 * q) * 2), g1_ = *(const s16x8*)(sm + K1_GW + (64 * cb + 32 + 8 * q) * 2); \
            acc[5][0] = mfma16(fx[1][0], g1_, mfma16(fx[0][0], g0_, acc[5][0])); acc[5][1] = mfma16(fx[1][1], g1_, mfma16(fx[0][1], g0_, acc[5][1])); \
        }                                                                                         \
        if ((it) == 1) pretile_read();                                                            \
        if ((it) == 7) pt_check();                                                                \
        if (RF_) {                                                                                \
            W_NEXT((it) + 3);                                                                     \
            if (wave < 2) { W_M0((it) + 3, 2); G_NOP(); W_LD((it) + 3, 2); }                      \
            else if ((it) == 3) { if (lane == 0) flag_set(ws + WS_FPT + (size_t)bid * 8, tok); }    \
            else if ((it) == 6) glds16_c(ws + WS_FPT + (wave - 2) * 1024 + lane * 16, smb + K1_FM + (wave - 2) * 1024); \
            W_M0((it) + 3, 0);                                                                    \
        }                                                                                         \
        if (RN_) { S_RX(0, BUF((it) + 1), co0); S_RW(0, BUF((it) + 1), co0, 0); }                 \
        SCHED_FENCE();                                                                            \
        if (RF_) { W_LD((it) + 3, 0); W_M0((it) + 3, 1); } else S_LB(SB_, 0);                     \
        SCHED_FENCE();                                                                            \
        S_MM(1, 1); if (RN_) { S_RW(0, BUF((it) + 1), co0, 1); S_RW(0, BUF((it) + 1), co0, 2); }  \
        SCHED_FENCE();                                                                            \
        if (RF_) { W_LD((it) + 3, 1); M_M0((it) + 3, 0); } else S_LB(SB_, 1);                     \
        if ((it) == 1) pretile_mix();                                                             \
        S_PT(it, 0); SCHED_FENCE();                                                               \
        S_MM(1, 2); if (RN_) { S_RW(0, BUF((it) + 1), co0, 3); S_RW(0, BUF((it) + 1), co0, 4); }  \
        SCHED_FENCE();                                                                            \
        if (RF_) { M_LD(0); M_M0((it) + 3, 1); } else S_LB(SB_, 2);                               \
        S_PT(it, 1); SCHED_FENCE();                                                               \
        S_MM(1, 3);                                                                               \
        if (RF_) { M_LD(1); M_M0((it) + 3, 2); } else S_LB(SB_, 3);                               \
        S_PT(it, 2); SCHED_FENCE();                                                               \
        S_MM(1, 4);                                                                               \
        if (RF_) M_LD(2); else S_LB(SB_, 4);                                                      \
        S_PT(it, 3); SCHED_FENCE();                                                               \
    } while (0)
        if (wave == 0 && lane < 2) *(u32x4*)(sm + K1_CNT + lane * 16) = (u32x4){0u, 0u, 0u, 0u};
        unsigned pto[4];
#pragma unroll
        for (int i = 0; i < 4; ++i) pto[i] = pretile_dst(i) + lane * 16;
        asm volatile("" : "+v"(a2), "+v"(pa), "+v"(pb), "+v"(pto[0]), "+v"(pto[1]), "+v"(pto[2]), "+v"(pto[3]));
        asm volatile("" : "+v"(wsr[0]), "+v"(wsr[1]), "+v"(wsr[2]), "+v"(vox[0]), "+v"(vox[1]), "+v"(vox[2]));
        if (wave < 2) WAIT_VM(6); else WAIT_VM(5);
        WAIT_LGKM0();
        __builtin_amdgcn_s_barrier();
        asm volatile("" ::: "memory");
        S_RX(0, BUF(0), co0); S_RW(0, BUF(0), co0, 0); S_RW(0, BUF(0), co0, 1); S_RW(0, BUF(0), co0, 2); S_RW(0, BUF(0), co0, 3); S_RW(0, BUF(0), co0, 4);
        const int cl = lane < 40 ? lane : 39;
        float lr, li, za, zb;
        f32x2 pA, pB;
#define TB0() do { lr = bflo(*(const u16*)(sm + K1_PLR + cl * 2)); li = bflo(*(const u16*)(sm + K1_PLI + cl * 2)); za = bflo(*(const u16*)(sm + K1_PLD + cl * 2)); } while (0)
#define TB1() do { const float dt = __builtin_amdgcn_exp2f(za * LOG2E); za = dt * lr * LOG2E; zb = dt * li * INV2PI;                   \
                   const float nA = wc == 1 ? 8.f : wc == 2 ? 24.f : 1.f;                                                             \
                   const float mA = __builtin_amdgcn_exp2f(nA * za), rA = __builtin_amdgcn_fractf(nA * zb);                           \
                   pA = (f32x2){mA * __builtin_amdgcn_cosf(rA), mA * __builtin_amdgcn_sinf(rA)}; } while (0)
#define TB2() do { const float nB = wc == 1 ? 16.f : wc == 2 ? 32.f : 0.f;                                                            \
                   const float mB = __builtin_amdgcn_exp2f(nB * za), rB = __builtin_amdgcn_fractf(nB * zb);                           \
                   pB = (f32x2){mB * __builtin_amdgcn_cosf(rB), mB * __builtin_amdgcn_sinf(rB)}; } while (0)
#define TB3() do { const float nr = pA.x - 1.0f, ni = pA.y, rden = 1.0f / (lr * lr + li * li);                                        \
                   const float z8 = __builtin_amdgcn_fractf(8.f * zb);                                                                \
                   if (wc == 3) pA = (f32x2){(nr * lr + ni * li) * rden, (ni * lr - nr * li) * rden};                 \
                   f32x4* tb = (f32x4*)(sm + K1_TAB + cl * 16);                                              \
                   tb[wc == 0 ? 0 : wc == 1 ? 120 : wc == 2 ? 200 : 40] = (f32x4){pA.x, -pA.y, pA.x, pA.y};                           \
                   tb[wc == 0 ? 80 : wc == 1 ? 160 : wc == 2 ? 240 : 280] = wc == 3 ? (f32x4){8.f * za, z8, 8.f * za, z8} : (f32x4){pB.x, -pB.y, pB.x, pB.y}; } while (0)
        const f32x2* tab = (const f32x2*)(sm + K1_TAB) + t;
        f32x2 Wj[4];
        float Ma[5], Mb[5];
#define PRE_W(j0)                                                                                 \
    do {                                                                                          \
        const f32x2 zz = *(const f32x2*)(sm + K1_TAB + (7 * 80 + 2 * cl) * 8);                    \
        const float za = 16.f * zz.x, zb = __builtin_amdgcn_fractf(16.f * zz.y);                  \
        _Pragma("unroll") for (int j = (j0); j < (j0) + 2; ++j) {                                 \
            const int n = rb - 1 - 4 * wc - j;                                                    \
            const float fn = (float)(n < 0 ? 0 : n);                                              \
            const float mg = n < 0 ? 0.f : __builtin_amdgcn_exp2f(fn * za), rv = fn * zb;         \
            Wj[j] = (f32x2){mg * __builtin_amdgcn_cosf(rv), mg * __builtin_amdgcn_sinf(rv)};      \
            asm volatile("" : "+v"(Wj[j]));                                                       \
        }                                                                                         \
    } while (0)
#define PRE_M(T0, T1)                                                                             \
    do {                                                                                          \
        const float fs = (float)(4 * wc + q), qr = (t & 1) ? -0.25f : 0.f, sg = (t & 1) ? 1.f : -1.f; \
        _Pragma("unroll") for (int T = (T0); T < (T1); ++T) {                                     \
            const f32x2 cc = tab[80 + 16 * T], zz = tab[560 + 16 * T];                            \
            const float e = __builtin_amdgcn_exp2f(fs * zz.x) * __builtin_amdgcn_cosf(fmaf(fs, zz.y, qr)); \
            Ma[T] = fmaf(cc.y, swp(e), cc.x * e);                                                 \
            Mb[T] = sg * swp(Ma[T]);                                                              \
            asm volatile("" : "+v"(Ma[T]), "+v"(Mb[T]));                                          \
        }                                                                                         \
    } while (0)
#define SA0() do { W_NEXT(2); if (wave < 2) { W_M0(2, 2); G_NOP(); W_LD(2, 2); } W_M0(2, 0); G_NOP(); W_LD(2, 0); W_M0(2, 1); } while (0)
#define SA1() do { SCHED_FENCE(); W_LD(2, 1); M_M0(2, 0); G_NOP(); M_LD(0); M_M0(2, 1); } while (0)
#define SA2() do { M_LD(1); M_M0(2, 2); } while (0)
        S_BODY(0, 5, SA0(), SA1(), SA2(), M_LD(2));   S_BODY(1, 5, TB0(), , TB1(), );   S_BODY(2, 9, , TB2(), GET_TOK(), );   S_BODY(3, 5, , TB3(), , );
        S_BODY(4, 6, , , PRE_W(0), );   S_BODY(5, 5, , , PRE_W(2), );   S_BODY(6, 5, , , PRE_M(0, 2), );
        S_BODY(7, 5, , , PRE_M(2, 4), );   S_BODY(8, 5, , , PRE_M(4, 5), );
        f32x2 Lc[5], Sc[5];
#pragma unroll
        for (int T = 0; T < 5; ++T) { Lc[T] = tab[16 * T]; Sc[T] = tab[(5 - q) * 80 + 16 * T]; }
        const f32x2 L32 = *(const f32x2*)(sm + K1_TAB + (6 * 80 + 2 * cl + 1) * 8);
        S_BODY(9, 0, S_LB(3, 0); S_LB(3, 1), S_LB(3, 2), S_LB(3, 3), S_LB(3, 4));
        __builtin_amdgcn_s_setprio(3);
        float ag8[5];
#pragma unroll
        for (int T = 0; T < 5; ++T) ag8[T] = acc[T][0][0];
#pragma unroll
        for (int j = 1; j < 8; ++j)
#pragma unroll
            for (int T = 0; T < 5; ++T) ag8[T] = fmaf(Lc[T].y, swp(ag8[T]), fmaf(Lc[T].x, ag8[T], acc[T][j >> 2][j & 3]));
        {
            unsigned sv[5];
#pragma unroll
            for (int T = 0; T < 5; ++T) sv[T] = f2u(fmaf(Sc[T].y, swp(ag8[T]), Sc[T].x * ag8[T]));
            const auto r01 = __builtin_amdgcn_permlane32_swap(sv[0], sv[1], false, false);
            const auto r23 = __builtin_amdgcn_permlane32_swap(sv[2], sv[3], false, false);
            const auto r44 = __builtin_amdgcn_permlane32_swap(sv[4], sv[4], false, false);
            const unsigned p4 = f2u(u2f(r44[0]) + u2f(r44[1]));
            const auto ra = __builtin_amdgcn_permlane16_swap(f2u(u2f(r01[0]) + u2f(r01[1])), f2u(u2f(r23[0]) + u2f(r23[1])), false, false);
            const auto rb4 = __builtin_amdgcn_permlane16_swap(p4, p4, false, false);
            float* xa = (float*)(sm + K1_XCH) + wc * 80 + t;
            xa[16 * (((q & 1) << 1) | (q >> 1))] = u2f(ra[0]) + u2f(ra[1]);
            xa[64] = u2f(rb4[0]) + u2f(rb4[1]);
        }
        asm volatile("" ::: "memory");
        unsigned old = 0;
        if (lane == 0) old = __hip_atomic_fetch_add((LAS unsigned*)(sm + K1_CNT), 1u, __ATOMIC_RELAXED, __HIP_MEMORY_SCOPE_WORKGROUP);
        old = __builtin_amdgcn_readfirstlane(old);
        asm volatile("" ::: "memory");
        if (old == 3) {
            const f32x2* xc = (const f32x2*)(sm + K1_XCH) + cl;
            const f32x2 s = cfma(L32, cfma(L32, cfma(L32, xc[0], xc[40]), xc[80]), xc[120]);
            if (lane < 40) st128_wt(ws + WS_AGG + (size_t)(cb * 16 + rb) * 640 + lane * 16,
                                    (u32x4){f2u(s.x), f2u(s.y), (unsigned)tok, (unsigned)(tok >> 32)});
        }
        SCHED_FENCE();
        __builtin_amdgcn_s_setprio(PRIO_OFF);
        char* own = sm + (wc < 2 ? 5 * 20480 : 8 * 20480) - 5120 + (wc & 1) * 2560;
#pragma unroll
        for (int T = 0; T < 5; ++T) *(float*)(own + (16 * T + t) * 16 + q * 4) = ag8[T];
        if (cb < 10 && t < 2) {
            const f32x4 gv = t ? acc[5][1] : acc[5][0];
            st128_wt(ws + WS_G + (cb * 2048 + 128 * rb + 32 * wc + 8 * q + 4 * t) * 4, __builtin_bit_cast(u32x4, gv));
        }
        float pre[5];
        {
            f32x2 Cc[5], L8c[5];
#pragma unroll
            for (int T = 0; T < 5; ++T) {
                Cc[T] = tab[80 + 16 * T]; L8c[T] = tab[240 + 16 * T];
#pragma unroll
                for (int j = 0; j < 8; ++j) {
                    const float u = acc[T][j >> 2][j & 3];
                    acc[T][j >> 2][j & 3] = fmaf(Cc[T].y, swp(u), Cc[T].x * u);
                }
                pre[T] = 0.f;
            }
            {
                for (unsigned spins = 0; spins < (1u << 22); ++spins) {
                    if (__hip_atomic_load((LAS unsigned*)(sm + K1_CNT), __ATOMIC_RELAXED, __HIP_MEMORY_SCOPE_WORKGROUP) >= 4u) break;
                    __builtin_amdgcn_s_sleep(1);
                }
                asm volatile("" ::: "memory");
                const float* xa = (const float*)(sm + K1_XCH) + t;
#pragma unroll 1
                for (int w = 0; w < wc; ++w)
#pragma unroll
                    for (int T = 0; T < 5; ++T) {
                        const f32x2 z = tab[480 + 16 * T];
                        pre[T] = fmaf(z.y, swp(pre[T]), fmaf(z.x, pre[T], xa[w * 80 + 16 * T]));
                    }
            }
#pragma unroll
            for (int T = 0; T < 5; ++T) {
                const f32x4 xs = *(const f32x4*)(own + (16 * T + t) * 16);
#pragma unroll
                for (int k = 0; k < 3; ++k) {
                    const float nx = fmaf(L8c[T].y, swp(pre[T]), fmaf(L8c[T].x, pre[T], xs[k]));
                    pre[T] = k < q ? nx : pre[T];
                }
                pre[T] = fmaf(Cc[T].y, swp(pre[T]), Cc[T].x * pre[T]);
            }
        }
#pragma unroll
        for (int T = 0; T < 5; ++T) asm volatile("" : "+v"(acc[T][0]), "+v"(acc[T][1]), "+v"(pre[T]));
        SCHED_FENCE();
        __builtin_amdgcn_s_setprio(3);
        if (rb > 0) {
            f32x2 cy = {0.f, 0.f};
            if (4 * wc < rb) {
                const char* agp = ws + WS_AGG + (size_t)(cb * 16 + 4 * wc) * 640 + cl * 16;
                for (unsigned spins = 0; spins < (1u << 20); ++spins) {
                    u32x4 ag[4];
#pragma unroll
                    for (int j = 0; j < 4; ++j)
                        asm volatile("global_load_dwordx4 %0, %1, off sc1" : "=v"(ag[j]) : "v"(agp + (4 * wc + j < rb ? j : 0) * 640) : "memory");
                    asm volatile("s_waitcnt vmcnt(0)" : "+v"(ag[0]), "+v"(ag[1]), "+v"(ag[2]), "+v"(ag[3]) :: "memory");
                    bool ok = true;
#pragma unroll
                    for (int j = 0; j < 4; ++j) ok = ok && ag[j][2] == (unsigned)tok && ag[j][3] == (unsigned)(tok >> 32);
                    const f32x2 c0 = cfma(Wj[1], (f32x2){u2f(ag[1][0]), u2f(ag[1][1])}, cmul_pk(Wj[0], (f32x2){u2f(ag[0][0]), u2f(ag[0][1])}));
                    const f32x2 c1 = cfma(Wj[3], (f32x2){u2f(ag[3][0]), u2f(ag[3][1])}, cmul_pk(Wj[2], (f32x2){u2f(ag[2][0]), u2f(ag[2][1])}));
                    cy = (f32x2){c0.x + c1.x, c0.y + c1.y};
                    if (wave_all(ok)) break;
                    __builtin_amdgcn_s_sleep(4);
                }
            }
            if (lane < 40) *(f32x2*)(sm + K1_PRT + (lane * 4 + wc) * 8) = cy;
            asm volatile("" ::: "memory");
            if (lane == 0) __hip_atomic_fetch_add((LAS unsigned*)(sm + K1_CNT + 4), 1u, __ATOMIC_RELAXED, __HIP_MEMORY_SCOPE_WORKGROUP);
            for (unsigned spins = 0; spins < (1u << 22); ++spins) {
                if (__hip_atomic_load((LAS unsigned*)(sm + K1_CNT + 4), __ATOMIC_RELAXED, __HIP_MEMORY_SCOPE_WORKGROUP) >= 4u) break;
                __builtin_amdgcn_s_sleep(1);
            }
            asm volatile("" ::: "memory");
#pragma unroll
            for (int T = 0; T < 5; ++T) {
                const f32x4* pp = (const f32x4*)(sm + K1_PRT + (8 * T + (t >> 1)) * 32);
                const f32x4 p0 = pp[0], p1 = pp[1];
                pre[T] = fmaf(Mb[T], (p0[1] + p0[3]) + (p1[1] + p1[3]), fmaf(Ma[T], (p0[0] + p0[2]) + (p1[0] + p1[2]), pre[T]));
            }
        }
#pragma unroll
        for (int j = 0; j < 8; ++j)
#pragma unroll
            for (int T = 0; T < 5; ++T) {
                pre[T] = fmaf(Lc[T].y, swp(pre[T]), fmaf(Lc[T].x, pre[T], acc[T][j >> 2][j & 3]));
                acc[T][j >> 2][j & 3] = pre[T];
            }
        {
            const int od = cb & 1;
            char* dst = a2 + (size_t)((5 * cb) >> 1) * 8192;
            char* wp0 = own + (16 * od + t) * 80 + q * 16;
            char* wp1 = own + (16 * (od ^ 1) + t) * 80 + q * 16;
            const char* rp = own + (8 * q + (t >> 2)) * 80 + (t & 3) * 16;
            const char* rl = own + (16 * od + 8 * (q & 1) + (t >> 2)) * 80 + (t & 3) * 16 + (q >> 1) * 8;
#define H_WR(T) *(u32x4*)(((T) & 1) ? wp1 : wp0) = (u32x4){pack2(acc[T][0][0], acc[T][0][1]), pack2(acc[T][0][2], acc[T][0][3]), \
                                                           pack2(acc[T][1][0], acc[T][1][1]), pack2(acc[T][1][2], acc[T][1][3])}
#define H_RD(p) __builtin_bit_cast(u32x4, __builtin_shufflevector(__builtin_amdgcn_ds_read_tr16_b64_v4i16((LAS s16x4*)(p)), \
                                                                  __builtin_amdgcn_ds_read_tr16_b64_v4i16((LAS s16x4*)((p) + 320)), 0, 1, 2, 3, 4, 5, 6, 7))
#define H_PAIR(ks) do { asm volatile("" ::: "memory"); hf[2 * (ks)] = H_RD(rp); hf[2 * (ks) + 1] = H_RD(rp + 8); asm volatile("" ::: "memory"); } while (0)
#define H_LONE() do { asm volatile("" ::: "memory"); hf[4] = H_RD(rl); asm volatile("" ::: "memory"); } while (0)
            u32x4 hf[5];
            if (od) { H_WR(0); H_LONE(); H_WR(1); H_WR(2); H_PAIR(0); H_WR(3); H_WR(4); H_PAIR(1); }
            else { H_WR(0); H_WR(1); H_PAIR(0); H_WR(2); H_WR(3); H_PAIR(1); H_WR(4); H_LONE(); }
            st128_wt(dst + od * 8192, hf[0]); st128_wt(dst + od * 8192 + 1024, hf[1]);
            st128_wt(dst + od * 8192 + 8192, hf[2]); st128_wt(dst + od * 8192 + 9216, hf[3]);
            st128_wt(dst + (od ? 512 : 16384) + (q >> 1) * 512, hf[4]);
        }
        WAIT_LGKM0();
        if (lane == 0) __hip_atomic_fetch_add((LAS unsigned*)(sm + K1_CNT + 8), 1u, __ATOMIC_RELAXED, __HIP_MEMORY_SCOPE_WORKGROUP);
        __builtin_amdgcn_s_setprio(0);
        P2_BEGIN(0, PSL0);
        P2_STEP(0, 10, 8, 8); P2_POLLED();
        P2_STEP(1, 10, 8, 8); P2_STEP(2, 10, 8, 8); P2_STEP(3, 10, 8, 8); P2_STEP(4, 10, 8, 8);
        P2_STEP(5, 10, 8, 8); P2_STEP(6, 10, 13, 8); P2_STEP(7, 10, 18, 8); P2_STEP(8, 10, 9, 8); P2_STEP(9, 10, 0, 8);
        asm volatile("" : "+v"(gl[0]), "+v"(gl[1]), "+v"(gl[2]), "+v"(gl[3]) :: "memory");
        {
            const float gs = (u2f(gl[0]) + u2f(gl[1])) + (wave < 2 ? u2f(gl[2]) + u2f(gl[3]) : 0.f);
            keep = wave == 0 ? ac2[0][0][0] : wave == 1 ? ac2[0][1][0] : ac2[0][2][0];
#pragma unroll
            for (int n = 0; n < 3; ++n) ac2[0][n][0] = wave == n ? gs : ac2[0][n][0];
#pragma unroll
            for (int i = 0; i < 20; ++i) *(f32x4*)(sm + wave * 20480 + i * 1024 + lane * 16) = ac2[i / 5][i % 5];
        }
    } else {
        const int bb = q & 1;
        const int trb = 26624 + (8 * q + (t >> 2)) * 192 + (lane & 3) * 8;
        const int tra = trb + bb * 32;
        const int tr5 = trb + (bb ? 0 : 160);
        f32x4 acc[6][2];
        s16x8 fx[2][2], fw[2][6];
#define M_RW(b, sb, ks, j)                                                                        \
    do {                                                                                          \
        const char* pa_ = sm + (sb) + ((j) < 5 ? tra + (j) * 32 : tr5) + (ks) * 32 * 192;         \
        const s16x4 lo_ = __builtin_amdgcn_ds_read_tr16_b64_v4i16((LAS s16x4*)pa_);               \
        const s16x4 hi_ = __builtin_amdgcn_ds_read_tr16_b64_v4i16((LAS s16x4*)(pa_ + 4 * 192));   \
        fw[b][j] = __builtin_shufflevector(lo_, hi_, 0, 1, 2, 3, 4, 5, 6, 7);                     \
    } while (0)
#define M_BIAS(j)                                                                                 \
    do {                                                                                          \
        const u32x2 bp_ = *(const u32x2*)(sm + K1_PB1 + (16 * (j) + 4 * q) * 2);                  \
        const f32x4 bv_ = {bflo(bp_[0]), bfhi(bp_[0]), bflo(bp_[1]), bfhi(bp_[1])};               \
        acc[j][0] += bv_; acc[j][1] += bv_;                                                       \
    } while (0)
#define M_MM(b, j) do { acc[j][0] = mfma16(fw[b][j], fx[b][0], acc[j][0]); acc[j][1] = mfma16(fw[b][j], fx[b][1], acc[j][1]); SCHED_FENCE(); } while (0)
#define M_BODY(it, WN)                                                                            \
    do {                                                                                          \
        constexpr bool RF_ = (it) < 8, RN_ = (it) < 9;                                            \
        constexpr int SB_ = (it) < 8 ? 0 : (it) - 7;                                              \
        M_MM(0, 0); if (RF_) { X_NEXT((it) + 2); X_M0((it) + 2, 0); } S_RX(1, BUF(it), co1); M_RW(1, BUF(it), 1, 0); SCHED_FENCE(); \
        if (RF_) { X_LD((it) + 2, 0); X_M0((it) + 2, 1); } else S_LB(SB_, 0);                     \
        SCHED_FENCE();                                                                            \
        M_MM(0, 1); M_RW(1, BUF(it), 1, 1); M_RW(1, BUF(it), 1, 2); SCHED_FENCE();                \
        if (RF_) { X_LD((it) + 2, 1); X_M0((it) + 2, 2); } else S_LB(SB_, 1);                     \
        SCHED_FENCE();                                                                            \
        M_MM(0, 2); M_RW(1, BUF(it), 1, 3); M_RW(1, BUF(it), 1, 4); SCHED_FENCE();                \
        if (RF_) { X_LD((it) + 2, 2); X_M0((it) + 2, 3); } else S_LB(SB_, 2);                     \
        SCHED_FENCE();                                                                            \
        M_MM(0, 3); M_RW(1, BUF(it), 1, 5); SCHED_FENCE();                                        \
        if (RF_) X_LD((it) + 2, 3); else S_LB(SB_, 3);                                            \
        SCHED_FENCE();                                                                            \
        M_MM(0, 4); if (!RF_) S_LB(SB_, 4);                                                       \
        SCHED_FENCE();                                                                            \
        M_MM(0, 5);                                                                               \
        if (RN_) WAIT_VM(WN);                                                                     \
        WAIT_LGKM0();                                                                             \
        __builtin_amdgcn_s_barrier();                                                             \
        asm volatile("" ::: "memory");                                                            \
        SCHED_FENCE();                                                                            \
        M_MM(1, 0); if (RN_) { S_RX(0, BUF((it) + 1), co0); M_RW(0, BUF((it) + 1), 0, 0); }       \
        if ((it) == 7) { pt_check(); S_LB(0, 0); } if ((it) == 8) S_LB(3, 0);                                            \
        SCHED_FENCE();                                                                            \
        M_MM(1, 1); if (RN_) { M_RW(0, BUF((it) + 1), 0, 1); M_RW(0, BUF((it) + 1), 0, 2); } if ((it) == 7) S_LB(0, 1); if ((it) == 8) S_LB(3, 1); \
        if ((it) == 4) { M_BIAS(4); M_BIAS(5); }                                                  \
        SCHED_FENCE();                                                                            \
        M_MM(1, 2); if (RN_) { M_RW(0, BUF((it) + 1), 0, 3); M_RW(0, BUF((it) + 1), 0, 4); } if ((it) == 7) S_LB(0, 2); if ((it) == 8) S_LB(3, 2); SCHED_FENCE(); \
        M_MM(1, 3); if (RN_) M_RW(0, BUF((it) + 1), 0, 5); if ((it) == 7) S_LB(0, 3); if ((it) == 8) S_LB(3, 3); SCHED_FENCE(); \
        M_MM(1, 4); if ((it) == 7) S_LB(0, 4); if ((it) == 8) S_LB(3, 4);                                                    \
        if ((it) == 2 || (it) == 3) { M_BIAS(2 * (it) - 4); M_BIAS(2 * (it) - 3); }               \
        if ((it) == 5) GET_TOK();                                                                 \
        SCHED_FENCE();                                                                            \
        M_MM(1, 5);                                                                               \
    } while (0)
        asm volatile("" : "+v"(a2), "+v"(pa), "+v"(pb), "+v"(vox[0]), "+v"(vox[1]), "+v"(vox[2]), "+v"(vox[3]));
        WAIT_VM(8);
        __builtin_amdgcn_s_barrier();
        asm volatile("" ::: "memory");
        S_RX(0, BUF(0), co0); M_RW(0, BUF(0), 0, 0); M_RW(0, BUF(0), 0, 1); M_RW(0, BUF(0), 0, 2); M_RW(0, BUF(0), 0, 3); M_RW(0, BUF(0), 0, 4); M_RW(0, BUF(0), 0, 5);
#pragma unroll
        for (int j = 0; j < 6; ++j) { acc[j][0] = (f32x4){0.f, 0.f, 0.f, 0.f}; acc[j][1] = (f32x4){0.f, 0.f, 0.f, 0.f}; }
        M_BODY(0, 8);   M_BODY(1, 4);   M_BODY(2, 4);   M_BODY(3, 4);   M_BODY(4, 4);   M_BODY(5, 4);   M_BODY(6, 4);
        M_BODY(7, 4);   M_BODY(8, 10);   M_BODY(9, 0);
        __builtin_amdgcn_s_setprio(2);
#pragma unroll
        for (int v = 0; v < 3; ++v) {
#pragma unroll
            for (int rt = 0; rt < 2; ++rt) {
                u32x4 o;
#pragma unroll
                for (int jj = 0; jj < 2; ++jj) {
                    float r[4];
#pragma unroll
                    for (int i = 0; i < 4; ++i) {
                        const float h = acc[2 * v + jj][rt][i];
                        const float e = __builtin_amdgcn_exp2f(h * LOG2E);
                        const float d = fmaf(e, fmaf(0.5f, e, 1.0f), 1.0f);
                        r[i] = fmaf(-h, __builtin_amdgcn_rcpf(d), h);
                    }
                    o[2 * jj] = pack2(r[0], r[1]);
                    o[2 * jj + 1] = pack2(r[2], r[3]);
                }
                st128_wt(a2 + (size_t)(40 + 3 * cb + v) * 8192 + rt * 1024, o);
            }
        }
        P2_BEGIN(1, PSL1);
        P2_STEP(0, 12, 8, 7); P2_POLLED();
        P2_STEP(1, 12, 8, 7); P2_STEP(2, 12, 8, 7); P2_STEP(3, 12, 8, 7); P2_STEP(4, 12, 8, 7); P2_STEP(5, 12, 13, 7); lds_arrive(sm + K1_CNT + 16 + 4 * wc, lane);
        P2_STEP(6, 12, 18, 7); P2_STEP(7, 12, 18, 7); P2_STEP(8, 12, 18, 7);
        lds_wait(sm + K1_CNT + 8, 4u);
        P2_STEP(9, 12, 18, 7); P2_STEP(10, 12, 9, 7); P2_STEP(11, 12, 0, 7);
        __builtin_amdgcn_s_setprio(0);
        {
            const int rr = (wc + 2) & 3;
            f32x4 o5[5];
#pragma unroll
            for (int r = 0; r < 4; ++r) {
                if (r == rr) {
#pragma unroll
                    for (int n = 0; n < 5; ++n) o5[n] = ac2[r][n];
                } else {
#pragma unroll
                    for (int n = 0; n < 5; ++n) *(f32x4*)(sm + wave * 20480 + (r * 5 + n) * 1024 + lane * 16) = ac2[r][n];
                }
            }
            lds_arrive(sm + K1_CNT + 12, lane);
            lds_wait(sm + K1_CNT + 12, 4u);
            char* rp = sm + 81920 + rr * 5120 + lane * 16;
            char* r1 = rp + ((wc + 1) & 3) * 20480;
            const char* r2 = rp + ((wc + 2) & 3) * 20480;
            const char* r3 = rp + ((wc + 3) & 3) * 20480;
#pragma unroll
            for (int n = 0; n < 5; ++n)
                *(f32x4*)(r1 + n * 1024) = (o5[n] + *(const f32x4*)(r1 + n * 1024)) + (*(const f32x4*)(r2 + n * 1024) + *(const f32x4*)(r3 + n * 1024));
        }
        if (bid == 255 && wave == 7 && lane == 0) flag_set(ws + WS_EP, tok);
    }
    };
    if (wave < 4) run(IC<0>{}, IC<0>{}); else run(IC<1>{}, IC<0>{});
    int role = wave >> 2;
    asm volatile("" : "+s"(role));
    if (role == 0) run(IC<0>{}, IC<1>{}); else run(IC<1>{}, IC<1>{});
    f32x4 xd[3];
    const char *rp[3], *rm[3], *gp[3];
#pragma unroll
    for (int m = 0; m < 3; ++m) {
        xd[m] = (f32x4){bflo(xv[m][0]) * bflo(dv[m][0]), bfhi(xv[m][0]) * bfhi(dv[m][0]), bflo(xv[m][1]) * bflo(dv[m][1]), bfhi(xv[m][1]) * bfhi(dv[m][1])};
        const int tile = wave + 8 * m < 20 ? wave + 8 * m : wave, r = tile / 5;
        rp[m] = sm + tile * 1024 + lane * 16;
        rm[m] = rp[m] + 81920 + ((r + 3) & 3) * 20480;
        gp[m] = sm + (32 * (r >> 1) + 8 * (t >> 2) + 4 * (r & 1) + (t & 3)) * 16;
        asm volatile("" : "+v"(xd[m]));
    }
    wg_sync();
#pragma unroll
    for (int m = 0; m < 3; ++m) {
        if (m < 2 || wave < 4) {
            const float lg = (*(const float*)gp[m] + *(const float*)(gp[m] + 21504)) + *(const float*)(gp[m] + 43008);
            f32x4 p0 = *(const f32x4*)rp[m], p1 = *(const f32x4*)(rp[m] + 20480), p2 = *(const f32x4*)(rp[m] + 40960);
            const f32x4 p3 = *(const f32x4*)(rp[m] + 61440), vm = *(const f32x4*)rm[m];
            if (m == 0) { p0[0] = wave == 0 ? keep : p0[0]; p1[0] = wave == 1 ? keep : p1[0]; p2[0] = wave == 2 ? keep : p2[0]; }
            const float g = __builtin_amdgcn_rcpf(1.0f + __builtin_amdgcn_exp2f(lg * -LOG2E));
            const f32x4 vs = ((p0 + p1) + (p2 + p3)) * 2.0f + xd[m];
            st128_wt(out + oo[m], __builtin_bit_cast(u32x4, (vs - vm) * g + vm));
        }
    }
}

extern "C" void kernel_launch(void* const* d_in, const int* in_sizes, int n_in, void* d_out, int out_size, void* d_ws,
                              size_t ws_size, hipStream_t stream) {
    kf<<<256, 512, 0, stream>>>((const u16*)d_in[0], (const u16*)d_in[1], (const u16*)d_in[2], (const u16*)d_in[3], (const u16*)d_in[4],
                                (const u16*)d_in[5], (const u16*)d_in[6], (const u16*)d_in[7], (const u16*)d_in[8], (const u16*)d_in[10],
                                (const u16*)d_in[11], (const u16*)d_in[12], (const u16*)d_in[13], (char*)d_ws, (float*)d_out);
}
```

```cpp
#include <hip/hip_runtime.h>
#include <stdint.h>

typedef short s16x4 __attribute__((ext_vector_type(4)));
typedef short s16x8 __attribute__((ext_vector_type(8)));
typedef __bf16 bf16x2 __attribute__((ext_vector_type(2)));
typedef float f32x2 __attribute__((ext_vector_type(2)));
typedef float f32x4 __attribute__((ext_vector_type(4)));
typedef unsigned u32x2 __attribute__((ext_vector_type(2)));
typedef unsigned u32x4 __attribute__((ext_vector_type(4)));
typedef unsigned short u16;
#define LAS __attribute__((address_space(3)))
#define GAS __attribute__((address_space(1)))
#define DI __device__ __forceinline__

constexpr int DM = 640, NF = 1536;
constexpr int NKS = 88;

constexpr size_t WS_A2 = 0;
constexpr size_t WS_W2 = WS_A2 + (size_t)16 * NKS * 8192;
constexpr size_t WS_G = WS_W2 + (size_t)8 * NKS * 5120;
constexpr size_t WS_AGG = WS_G + 10 * 2048 * 4;
constexpr size_t WS_EP = WS_AGG + 16 * 16 * 640;
constexpr size_t WS_FPT = WS_EP + 256;
constexpr size_t WS_FP1 = WS_FPT + 4096;
constexpr size_t WS_JUNK = WS_FP1 + 16384;

DI unsigned f2u(float v) { return __builtin_bit_cast(unsigned, v); }
DI float u2f(unsigned v) { return __builtin_bit_cast(float, v); }
DI bool wave_all(bool p) { return __builtin_amdgcn_ballot_w64(!p) == 0ull; }
DI void wg_sync() {
    __builtin_amdgcn_fence(__ATOMIC_RELEASE, "workgroup");
    __builtin_amdgcn_s_barrier();
    __builtin_amdgcn_fence(__ATOMIC_ACQUIRE, "workgroup");
}
DI float bflo(unsigned v) { return __uint_as_float(v << 16); }
DI float bfhi(unsigned v) { return __uint_as_float(v & 0xffff0000u); }
DI unsigned pack2(float a, float b) {
    f32x2 p = {a, b};
    return __builtin_bit_cast(unsigned, __builtin_convertvector(p, bf16x2));
}
DI f32x4 mfma16(s16x8 a, s16x8 b, f32x4 c) { return __builtin_amdgcn_mfma_f32_16x16x32_bf16(a, b, c, 0, 0, 0); }
DI float swp(float v) {
    return __builtin_bit_cast(float, __builtin_amdgcn_update_dpp(0, __builtin_bit_cast(int, v), 0xB1, 0xf, 0xf, true));
}
DI f32x2 cfma(f32x2 a, f32x2 b, f32x2 c) {
    return (f32x2){fmaf(-a.y, b.y, fmaf(a.x, b.x, c.x)), fmaf(a.y, b.x, fmaf(a.x, b.y, c.y))};
}
DI f32x2 cmul_pk(f32x2 a, f32x2 b) { return (f32x2){fmaf(-a.y, b.y, a.x * b.x), fmaf(a.y, b.x, a.x * b.y)}; }
#define WAIT_VM(n) asm volatile("s_waitcnt vmcnt(%0)" ::"n"(n) : "memory")
#define WAIT_LGKM0() asm volatile("s_waitcnt lgkmcnt(0)" ::: "memory")
#define SCHED_FENCE() __builtin_amdgcn_sched_barrier(0)

DI void glds4(const void* gsrc, unsigned lds_dst) {
    unsigned keep;
    asm volatile("s_mov_b32 %0, m0\n\ts_mov_b32 m0, %2\n\ts_nop 0\n\tglobal_load_lds_dword %1, off\n\ts_mov_b32 m0, %0"
                 : "=&s"(keep) : "v"(gsrc), "s"(lds_dst) : "memory");
}
DI void glds4_c(const void* gsrc, unsigned lds_dst) {
    unsigned keep;
    asm volatile("s_mov_b32 %0, m0\n\ts_mov_b32 m0, %2\n\ts_nop 0\n\tglobal_load_lds_dword %1, off sc1\n\ts_mov_b32 m0, %0"
                 : "=&s"(keep) : "v"(gsrc), "s"(lds_dst) : "memory");
}
DI void st128_wt(void* p, u32x4 v) { asm volatile("global_store_dwordx4 %0, %1, off sc1\n\ts_nop 1" :: "v"(p), "v"(v) : "memory"); }
DI u32x2 ld64_asm(const void* p) { u32x2 r; asm volatile("global_load_dwordx2 %0, %1, off" : "=v"(r) : "v"(p) : "memory"); return r; }
constexpr float LOG2E = 1.44269504088896341f, INV2PI = 0.15915494309189535f;
typedef unsigned long long u64;
DI void glds16_c(const void* gsrc, unsigned lds_dst) {
    unsigned keep;
    lds_dst = __builtin_amdgcn_readfirstlane(lds_dst);
    asm volatile("s_mov_b32 %0, m0\n\ts_mov_b32 m0, %2\n\ts_nop 0\n\tglobal_load_lds_dwordx4 %1, off sc1\n\ts_mov_b32 m0, %0"
                 : "=&s"(keep) : "v"(gsrc), "s"(lds_dst) : "memory");
}
DI void glds16x4_c(const void* gsrc, unsigned lds_dst) {
    unsigned keep;
    asm volatile("s_mov_b32 %0, m0\n\ts_mov_b32 m0, %2\n\ts_nop 0\n\tglobal_load_lds_dwordx4 %1, off sc1\n\t"
                 "global_load_lds_dwordx4 %1, off offset:1024 sc1\n\tglobal_load_lds_dwordx4 %1, off offset:2048 sc1\n\t"
                 "global_load_lds_dwordx4 %1, off offset:3072 sc1\n\ts_mov_b32 m0, %0"
                 : "=&s"(keep) : "v"(gsrc), "s"(lds_dst) : "memory");
}
DI void flag_set(char* p, u64 tok) { __hip_atomic_store((GAS u64*)p, tok, __ATOMIC_RELAXED, __HIP_MEMORY_SCOPE_AGENT); }
DI void flag_wait(const char* p, bool need, u64 tok) {
    for (unsigned spins = 0; spins < (1u << 20); ++spins) {
        u64 v = tok;
        if (need) v = __hip_atomic_load((const GAS u64*)p, __ATOMIC_RELAXED, __HIP_MEMORY_SCOPE_AGENT);
        if (__all(v == tok)) break;
        __builtin_amdgcn_s_sleep(8);
    }
    asm volatile("" ::: "memory");
}

#define PRIO_OFF 1
DI void lds_arrive(char* p, int) {
    asm volatile("s_mov_b64 exec, 1\n\tds_add_u32 %0, %1\n\ts_mov_b64 exec, -1" :: "v"((unsigned)(size_t)(LAS char*)p), "v"(1u) : "memory");
}
DI void lds_wait(const char* p, unsigned n) {
    unsigned v, sv, cnt;
    asm volatile("s_mov_b32 %2, 0x400000\n"
                 "1:\n\tds_read_b32 %0, %3\n\ts_waitcnt lgkmcnt(0)\n\tv_readfirstlane_b32 %1, %0\n\ts_cmp_ge_u32 %1, %4\n\ts_cbranch_scc1 2f\n\t"
                 "s_sleep 1\n\ts_sub_u32 %2, %2, 1\n\ts_cbranch_scc0 1b\n"
                 "2:" : "=&v"(v), "=&s"(sv), "=&s"(cnt) : "v"((unsigned)(size_t)(LAS const char*)p), "s"(n) : "memory", "scc");
}
#define PSL0 "8"
#define PSL1 "4"
template <int V> struct IC { static constexpr int value = V; };
#define ROTF ((((rb + cb) & 3) * 5 + 1) >> 1)
constexpr int K1_STAGE = 38 * 1024;
constexpr int K1_GW = 0;
constexpr int K1_PLR = K1_GW + 1280, K1_PLI = K1_GW + 1360, K1_PLD = K1_GW + 1440, K1_PB1 = K1_GW + 1536, K1_PEP = K1_GW + 1728;
constexpr int K1_RING = 2048;
#define BUF(it) (K1_RING + ((it) % 3) * K1_STAGE)
constexpr int K1_TAB = K1_RING + 3 * K1_STAGE;
constexpr int K1_XCH = K1_TAB + 5120;
constexpr int K1_CNT = K1_XCH + 1280;
constexpr int K1_PRT = K1_CNT + 32;
constexpr int K1_PT = 123 * 1024;
constexpr int K1_FM = 140 * 1024;
constexpr int KF_LDS = 160 * 1024;

__global__ __launch_bounds__(512, 2) __attribute__((target("no-packed-fp32-ops"))) void kf(const u16* __restrict__ x, const u16* __restrict__ Lre, const u16* __restrict__ Lim,
                                             const u16* __restrict__ Bre, const u16* __restrict__ Bim, const u16* __restrict__ Cre,
                                             const u16* __restrict__ Cim, const u16* __restrict__ logdt, const u16* __restrict__ Dskip,
                                             const u16* __restrict__ w1, const u16* __restrict__ b1, const u16* __restrict__ w2,
                                             const u16* __restrict__ gw, char* __restrict__ ws, float* __restrict__ out) {
    __shared__ __attribute__((aligned(16))) char sm[KF_LDS];
    const int tid = threadIdx.x, lane = tid & 63;
    const int wave = __builtin_amdgcn_readfirstlane(tid >> 6);
    const int bid = blockIdx.x, xcd = bid & 7, idx = bid >> 3;
    const int rb = 8 * (xcd & 1) + (idx & 7);
    const int cb = 4 * (xcd >> 1) + (idx >> 3);
    const int RG = 4 * xcd + (idx & 3);
    const int CB = idx >> 2;
    const int t = lane & 15, q = lane >> 4;
    const unsigned smb = (unsigned)(size_t)(LAS char*)sm;

    if (wave < 2) {
        u64 pc0;
        asm volatile("s_getpc_b64 %0" : "=s"(pc0));
        glds4((const char*)(pc0 & ~255ull) + ((idx + 2 * wave) % 5) * 8192 + lane * 128, smb + K1_FM);
    }
    asm volatile("" :: "s"(x), "s"(Lre), "s"(Lim), "s"(Bre), "s"(Bim), "s"(Cre), "s"(Cim), "s"(logdt), "s"(Dskip), "s"(w1), "s"(b1), "s"(w2),
                 "s"(gw), "s"(ws), "s"(out));
    if (wave == 7) {
        const char* tp = (const char*)(out + 64 * RG * DM);
        if (lane == 1) tp = ws + WS_A2 + (size_t)rb * NKS * 8192;
        if (lane == 2) tp = ws + WS_A2 + (size_t)(RG >> 1) * NKS * 8192;
        if (lane == 3) tp = ws + WS_W2 + (size_t)CB * NKS * 5120;
        if (lane == 4) tp = ws + WS_AGG;
        if (lane == 5) tp = (const char*)Dskip;
        if (lane == 6) tp = ws + WS_A2 + (size_t)rb * NKS * 8192 + NKS * 8192 - 128;
        if (lane == 7) tp = ws + WS_A2 + (size_t)(RG >> 1) * NKS * 8192 + NKS * 8192 - 128;
        if (lane == 8) tp = ws + WS_W2 + (size_t)CB * NKS * 5120 + NKS * 5120 - 128;
        if (lane == 9) tp = (const char*)(out + (64 * RG + 63) * DM);
        glds4_c(tp, smb + K1_FM);
    }
    float keep = 0.f;
    u32x2 xv[3], dv[3];
    int oo[3];
    auto run = [&](auto ROLE_, auto PART_) __attribute__((always_inline)) {
    constexpr int ROLE = decltype(ROLE_)::value, PART = decltype(PART_)::value;
    int lane = tid & 63;
    asm volatile("" : "+v"(lane));
    const int t = lane & 15, q = lane >> 4;
#define G_M0(base, c) asm volatile("s_add_i32 m0, %0, %1" :: "s"(base), "n"(c) : "scc")
#define G_LDS(vo, sb, c) asm volatile("global_load_lds_dwordx4 %0, %1 offset:%2" :: "v"(vo), "s"(sb), "n"(c) : "memory")
#define G_LDV(va, c) asm volatile("global_load_lds_dwordx4 %0, off offset:%1" :: "v"(va), "n"(c) : "memory")
#define G_NOP() asm volatile("s_nop 0")
    const int l8 = lane >> 3;
    unsigned vox[4];
    const char* wsr[3];
    const int rot = ROTF;
    const char* w1s = (const char*)w1 + (size_t)rot * (64 * NF * 2);
    const char* xs = (const char*)x + rot * 128;
#define K_WRAP(st) (((st) == 2 || (st) == 5 || (st) == 7) && rot == 10 - (st))
#define W_NEXT(st)                                                                                \
    do {                                                                                          \
        w1s += K_WRAP(st) ? -9 * (64 * NF * 2) : 64 * NF * 2;                                     \
        if ((st) == 2 || (st) == 5 || (st) == 7) { const long d_ = K_WRAP(st) ? -1280 : 0; wsr[0] += d_; wsr[1] += d_; wsr[2] += d_; } \
    } while (0)
#define X_NEXT(st) do { if ((st) == 2 || (st) == 5 || (st) == 7) xs += K_WRAP(st) ? -1280 : 0; } while (0)
    const unsigned lw = smb + 1024 * (wave & 3);
    const unsigned lm = smb + (ROLE == 0 ? 3072 : 4096) * (wave & 3);
#define X_M0(st, i) G_M0(lm, BUF(st) + 1024 * (i) - 128 * (st))
#define X_LD(st, i) G_LDS(vox[i], xs, 128 * (st))
#define W_M0(st, j) G_M0(lw, BUF(st) + 16384 + 4096 * (j) - 128 * (st))
#define W_LD(st, j) G_LDV(wsr[j], 128 * (st))
#define M_M0(st, i) G_M0(lm, BUF(st) + 26624 + 1024 * (i))
#define M_LD(i) G_LDS(vox[i], w1s, 0)
    const unsigned pu = 4 * bid + (wave & 3);
    const unsigned pkg = (pu * 1639u) >> 16, pv = pu - 400u, ptg = (pv * 1366u) >> 16;
    if constexpr (ROLE == 1) {
#pragma unroll
        for (int i = 0; i < 4; ++i) {
            const int p = 8 * i + l8;
            vox[i] = ((128 * rb + 32 * (wave & 3) + 8 * ((p & 15) >> 2) + 4 * (p >> 4) + (p & 3)) * DM + (((lane & 7) ^ ((p >> 1) & 7)) << 3)) * 2;
            if constexpr (PART == 0) {
                if (i == 0) X_M0(0, 0); else if (i == 1) X_M0(0, 1); else if (i == 2) X_M0(0, 2); else X_M0(0, 3);
                G_NOP();
                G_LDS(vox[i], xs, 0);
                SCHED_FENCE();
            }
        }
        if constexpr (PART == 0) {
            X_M0(1, 0); G_NOP(); X_LD(1, 0); X_M0(1, 1); G_NOP(); X_LD(1, 1); X_M0(1, 2); G_NOP(); X_LD(1, 2); X_M0(1, 3); G_NOP(); X_LD(1, 3);
            const bool isc = pu < 400 || pu >= 880;
            const unsigned kg = pu < 400 ? pkg : 0, nt = pu < 400 ? pu - 40 * pkg : 0;
            const unsigned o = ((isc ? 16 * nt + l8 : 32 * (pv - 48 * ptg) + l8) * DM + 64 * (isc ? kg : ptg) + ((lane & 7) ^ l8) * 8) * 2;
#pragma unroll
            for (int i = 0; i < 4; ++i) {
                const u16* p0 = isc ? ((i >> 1) ? Cim : Cre) + 8 * (i & 1) * DM : w2 + 8 * i * DM;
                G_M0(lm, K1_PT + i * 1024); G_NOP(); G_LDS(o, p0, 0);
            }
            return;
        }
    } else {
#pragma unroll
        for (int j = 0; j < 3; ++j) {
            const int p = 8 * (4 * j + wave) + l8;
            const int ch = 40 * cb + 8 * (p >> 4) + ((p & 15) >> 1);
            wsr[j] = (const char*)(((p & 1) ? Bim : Bre) + ch * DM + 64 * rot + (((lane & 7) ^ ((p >> 1) & 7)) << 3));
            if (PART == 0 && j < 2) {
                if (j == 0) W_M0(0, 0); else W_M0(0, 1);
                G_NOP();
                G_LDV(wsr[j], 0);
                SCHED_FENCE();
            }
        }
#pragma unroll
        for (int i = 0; i < 3; ++i) {
            const int gI = 64 * (3 * wave + i) + lane, row = gI / 12, cp = gI - 12 * row;
            int j = (cp >> 1) - ((row >> 3) & 1);
            j += j < 0 ? 6 : 0;
            vox[i] = (row * NF + 96 * cb + 16 * j + 8 * (cp & 1)) * 2;
            if constexpr (PART == 0) {
                if (i == 0) M_M0(0, 0); else if (i == 1) M_M0(0, 1); else M_M0(0, 2);
                G_NOP();
                M_LD(i);
                SCHED_FENCE();
            }
        }
        W_NEXT(1);
        if constexpr (PART == 0) {
            const u16* p = gw + lane * 8;
            if (wave == 3) {
                p = gw + 512 + t * 8;
                if (lane >= 16 && lane < 21) p = Lre + 40 * cb + (lane - 16) * 8;
                if (lane >= 21 && lane < 26) p = Lim + 40 * cb + (lane - 21) * 8;
                if (lane >= 26 && lane < 31) p = logdt + 40 * cb + (lane - 26) * 8;
                if (lane >= 32 && lane < 44) p = b1 + 96 * cb + (lane - 32) * 8;
                if (lane == 44) p = (const u16*)(ws + WS_EP);
            }
            glds16_c(wave < 2 ? wsr[2] : (const char*)p, wave < 2 ? lw + BUF(0) + 16384 + 8192 : smb + K1_GW + (wave - 2) * 1024);
            if (wave < 2) { W_M0(1, 2); G_NOP(); W_LD(1, 2); }
            W_M0(1, 0); G_NOP(); W_LD(1, 0); W_M0(1, 1); G_NOP(); W_LD(1, 1);
            M_M0(1, 0); G_NOP(); M_LD(0); M_M0(1, 1); G_NOP(); M_LD(1); M_M0(1, 2); G_NOP(); M_LD(2);
            return;
        }
    }

    char* ptl = sm + K1_PT + (wave & 3) * 4096;
    const int wc = wave & 3;
    const int xoff = (32 * wc + t) * 128;
    const int co0 = (q ^ ((t >> 1) & 7)) * 16, co1 = co0 ^ 64;
    char* a2 = ws + WS_A2 + (size_t)rb * NKS * 8192 + (2 * wc) * 1024 + lane * 16;
    const int rb2 = RG >> 1, hh = RG & 1;
    const int ks0 = ROLE == 0 ? 10 * wave : 40 + 12 * (wave - 4);
    const char* pa = ws + WS_A2 + ((size_t)rb2 * NKS + ks0) * 8192 + hh * 4096 + lane * 16;
    const char* pb = ws + WS_W2 + ((size_t)CB * NKS + ks0) * 5120 + 1024 + lane * 16;
    u64 tok;
#define GET_TOK() do { const u32x2 ep_ = *(const u32x2*)(sm + K1_PEP); \
                       tok = (((u64)(unsigned)__builtin_amdgcn_readfirstlane(ep_[1]) << 32) | (unsigned)__builtin_amdgcn_readfirstlane(ep_[0])) + 1ull; } while (0)
    u32x4 ptf[4];
    auto pretile_read = [&]() __attribute__((always_inline)) {
        if (pu < 400) {
#pragma unroll
            for (int i = 0; i < 4; ++i) {
                const char* pp = ptl + t * 128 + (((2 * i + (q >> 1)) ^ (t & 7)) << 4) + 8 * (q & 1);
                const u32x2 rr = *(const u32x2*)pp, ii = *(const u32x2*)(pp + 2048);
                ptf[i] = (u32x4){rr[0], rr[1], ii[0], ii[1]};
            }
        } else {
            const int r = 4 * q + (t >> 2);
#pragma unroll
            for (int i = 0; i < 4; ++i) {
                const char* pp = ptl + r * 128 + (((2 * i + ((lane >> 1) & 1)) ^ (r & 7)) << 4) + (lane & 1) * 8;
                const s16x4 lo = __builtin_amdgcn_ds_read_tr16_b64_v4i16((LAS s16x4*)pp);
                const s16x4 hi = __builtin_amdgcn_ds_read_tr16_b64_v4i16((LAS s16x4*)(pp + 2048));
                ptf[i] = __builtin_bit_cast(u32x4, __builtin_shufflevector(lo, hi, 0, 1, 2, 3, 4, 5, 6, 7));
            }
        }
    };
    auto pretile_mix = [&]() __attribute__((always_inline)) {
        if (pu < 400) {
#pragma unroll
            for (int i = 0; i < 4; ++i) {
                const unsigned r0 = ptf[i][0], r1 = ptf[i][1], i0 = ptf[i][2] ^ 0x80008000u, i1 = ptf[i][3] ^ 0x80008000u;
                ptf[i] = (u32x4){(r0 & 0xffffu) | (i0 << 16), (r0 >> 16) | (i0 & 0xffff0000u), (r1 & 0xffffu) | (i1 << 16), (r1 >> 16) | (i1 & 0xffff0000u)};
            }
        }
    };
    auto pretile_dst = [&](unsigned i) __attribute__((always_inline)) {
        const unsigned g = pu < 400 ? pu - 40 * pkg : 4 * ptg + i, KS = pu < 400 ? 4 * pkg + i : 40 + pv - 48 * ptg;
        const unsigned cB = (g * 13u) >> 6;
        unsigned o = (unsigned)WS_W2 + (cB * (5 * NKS - 5) + KS * 5 + g) * 1024u, j = (unsigned)WS_JUNK + (pu * 4 + i) * 1024u;
        asm("" : "+s"(o), "+s"(j));
        return pu < 880 ? o : j;
    };

    const int bco = wave == 6 ? 6 * 20480 + 4096 : wave * 20480;
    u32x4 Ab[4][4], Bb[4][5];
    f32x4 ac2[4][5];
    unsigned gl[4];
#define P2_L1(dst, P, OFF) asm volatile("global_load_dwordx4 %0, %1, off offset:" #OFF " sc1" : "=&v"(dst) : "v"(P) : "memory")
#define P2_LDA(slot, P)                                                                           \
    do { P2_L1(Ab[slot][0], P, 0); P2_L1(Ab[slot][1], P, 1024); P2_L1(Ab[slot][2], P, 2048); P2_L1(Ab[slot][3], P, 3072); } while (0)
    auto pt_check = [&]() __attribute__((always_inline)) {
        const u32x4 v0 = *(const u32x4*)(sm + K1_FM + lane * 16), v1 = *(const u32x4*)(sm + K1_FM + 1024 + lane * 16);
        const unsigned lo = (unsigned)tok, hi = (unsigned)(tok >> 32);
        if (!wave_all(v0[0] == lo && v0[1] == hi && v0[2] == lo && v0[3] == hi && v1[0] == lo && v1[1] == hi && v1[2] == lo && v1[3] == hi)) {
#pragma unroll 1
            for (int k = 0; k < 4; ++k) flag_wait(ws + WS_FPT + (size_t)(64 * k + lane) * 8, true, tok);
        }
    };
    const char* g3 = pb - 1024 + 4 * 5120;
#define P2_CACHE(i) glds16_c(g3 + (i) * 1024, smb + bco + (i) * 1024)
#define P2_M2(ua, ub, i)                                                                          \
    do {                                                                                          \
        ac2[(i) / 5][(i) % 5] = mfma16(__builtin_bit_cast(s16x8, Bb[ub][(i) % 5]), __builtin_bit_cast(s16x8, Ab[ua][(i) / 5]), ac2[(i) / 5][(i) % 5]); \
        ac2[((i) + 1) / 5][((i) + 1) % 5] = mfma16(__builtin_bit_cast(s16x8, Bb[ub][((i) + 1) % 5]), __builtin_bit_cast(s16x8, Ab[ua][((i) + 1) / 5]), ac2[((i) + 1) / 5][((i) + 1) % 5]); \
        SCHED_FENCE();                                                                            \
    } while (0)
#define P2_STEP(j, NS, WN, NL)                                                                    \
    do {                                                                                          \
        asm volatile("s_waitcnt vmcnt(%9)" : "+v"(Ab[(j) & 3][0]), "+v"(Ab[(j) & 3][1]), "+v"(Ab[(j) & 3][2]), "+v"(Ab[(j) & 3][3]), \
                     "+v"(Bb[(j) & 3][0]), "+v"(Bb[(j) & 3][1]), "+v"(Bb[(j) & 3][2]), "+v"(Bb[(j) & 3][3]), "+v"(Bb[(j) & 3][4]) : "n"(WN) : "memory"); \
        SCHED_FENCE();                                                                            \
        const char* pa_ = pa + ((j) + 3) * 8192;                                                  \
        const char* pb_ = pb + ((j) + 3) * 5120;                                                  \
        constexpr bool LA_ = (j) + 3 < (NS), LBG_ = (j) + 3 >= (NL) && (j) + 3 < (NS), LBL_ = (j) + 3 >= 4 && (j) + 3 < (NL); \
        P2_M2((j) & 3, (j) & 3, 0); if (LA_) { P2_L1(Ab[((j) + 3) & 3][0], pa_, 0); SCHED_FENCE(); }      \
        P2_M2((j) & 3, (j) & 3, 2); if (LA_) { P2_L1(Ab[((j) + 3) & 3][1], pa_, 1024); SCHED_FENCE(); }   \
        P2_M2((j) & 3, (j) & 3, 4); if (LA_) { P2_L1(Ab[((j) + 3) & 3][2], pa_, 2048); SCHED_FENCE(); }   \
        P2_M2((j) & 3, (j) & 3, 6); if (LA_) { P2_L1(Ab[((j) + 3) & 3][3], pa_, 3072); SCHED_FENCE(); }   \
        if (LBL_) { _Pragma("unroll") for (int n_ = 0; n_ < 5; ++n_) Bb[((j) + 3) & 3][n_] = *(const u32x4*)(sm + bco + (((j) + 3 - 4) * 5 + n_) * 1024 + lane * 16); SCHED_FENCE(); } \
        P2_M2((j) & 3, (j) & 3, 8); if (LBG_) { P2_L1(Bb[((j) + 3) & 3][0], pb_, -1024); SCHED_FENCE(); } \
        P2_M2((j) & 3, (j) & 3, 10); if (LBG_) { P2_L1(Bb[((j) + 3) & 3][1], pb_, 0); SCHED_FENCE(); }    \
        P2_M2((j) & 3, (j) & 3, 12); if (LBG_) { P2_L1(Bb[((j) + 3) & 3][2], pb_, 1024); SCHED_FENCE(); } \
        P2_M2((j) & 3, (j) & 3, 14); if (LBG_) { P2_L1(Bb[((j) + 3) & 3][3], pb_, 2048); SCHED_FENCE(); } \
        P2_M2((j) & 3, (j) & 3, 16); if (LBG_) { P2_L1(Bb[((j) + 3) & 3][4], pb_, 3072); SCHED_FENCE(); } \
        P2_M2((j) & 3, (j) & 3, 18);                                                              \
    } while (0)
    u64 pl[3];
#define POLL1(i, PSL) "s_waitcnt vmcnt(2)\n\tv_cmp_ne_u64 vcc, %" #i ", %5\n\ts_cmp_eq_u64 vcc, 0\n\ts_cbranch_scc1 2f\n\t" \
                 "global_load_dwordx2 %" #i ", %4, off sc1\n\ts_sleep " PSL "\n\t"
#define P2_POLLED() asm volatile("" : "+v"(xv[0]), "+v"(xv[1]), "+v"(xv[2]), "+v"(dv[0]), "+v"(dv[1]), "+v"(dv[2]) : "v"(pl[0]), "v"(pl[1]), "v"(pl[2]))
#define P2_BEGIN(role, PSL)                                                                        \
    do {                                                                                          \
        if ((role) == 1) { _Pragma("unroll") for (int i = 0; i < 15; ++i) P2_CACHE(i); WAIT_VM(15); } else WAIT_VM(0); \
        if (lane == 0) flag_set(ws + WS_FP1 + (size_t)(((((role) * 16 + rb) * 2 + (wc >> 1)) * 16 + cb) * 2 + (wc & 1)) * 8, tok); \
        if ((role) == 1) {         \
            lds_wait(sm + K1_CNT, 4u);                                                            \
            const char* gs_ = ws + WS_W2 + ((size_t)CB * NKS + 10 * wc + 4) * 5120 + lane * 16;   \
            _Pragma("unroll") for (int i = 0; i < 5; ++i) glds16x4_c(gs_ + i * 4096, __builtin_amdgcn_readfirstlane(smb + wc * 20480 + i * 4096)); \
        }                                                                                         \
        _Pragma("unroll") for (int m = 0; m < 3; ++m) {                                           \
            int tile = wave + 8 * m; tile = tile < 20 ? tile : wave;                              \
            const int r = tile / 5, n = tile - 5 * r;                                             \
            const int col = 80 * CB + 16 * n + 4 * q;                                             \
            oo[m] = (64 * RG + 32 * (r >> 1) + 8 * (t >> 2) + 4 * (r & 1) + (t & 3)) * DM + col;  \
            xv[m] = ld64_asm(x + oo[m]); dv[m] = ld64_asm(Dskip + col);                           \
        }                                                                                         \
        if ((role) == 0) lds_wait(sm + K1_CNT + 16 + 4 * wc, 1u);     \
        unsigned cnt_;                                         \
        asm volatile("global_load_dwordx2 %0, %4, off sc1\n\ts_sleep " PSL "\n\tglobal_load_dwordx2 %1, %4, off sc1\n\ts_sleep " PSL "\n\t" \
                     "global_load_dwordx2 %2, %4, off sc1\n\ts_mov_b32 %3, 0x80000\n"             \
                     "1:\n\t" POLL1(0, PSL) POLL1(1, PSL) POLL1(2, PSL)                                 \
                     "s_sub_u32 %3, %3, 1\n\ts_cbranch_scc0 1b\n"                                 \
                     "2:"                                                                         \
                     : "=&v"(pl[0]), "=&v"(pl[1]), "=&v"(pl[2]), "=&s"(cnt_)                      \
                     : "v"(ws + WS_FP1 + (size_t)(((role) * 32 + RG) * 32 + 8 * wc + (lane & 7)) * 8), "s"(tok) : "memory", "vcc", "scc"); \
        if ((role) == 0) P2_GATES();                                                              \
        P2_LDA(0, pa); P2_LDA(1, pa + 8192); P2_LDA(2, pa + 16384);                               \
        _Pragma("unroll") for (int r = 0; r < 4; ++r) _Pragma("unroll") for (int n = 0; n < 5; ++n) ac2[r][n] = (f32x4){0.f, 0.f, 0.f, 0.f}; \
    } while (0)
#define P2_GATES()                                                                                \
    _Pragma("unroll") for (int j_ = 0; j_ < 4; ++j_)                                              \
        asm volatile("global_load_dword %0, %1, off sc1" : "=&v"(gl[j_])                          \
                     : "v"(ws + WS_G + ((4 * wc + j_ < 10 ? 4 * wc + j_ : 9) * 2048 + 64 * RG + lane) * 4) : "memory")

    if constexpr (ROLE == 0) {
        f32x4 acc[6][2];
#pragma unroll
        for (int a = 0; a < 6; ++a) { acc[a][0] = (f32x4){0.f, 0.f, 0.f, 0.f}; acc[a][1] = (f32x4){0.f, 0.f, 0.f, 0.f}; }
        s16x8 fx[2][2], fw[2][5];
        const int woff = 16384 + t * 128;
        const int itg = cb >= 10 ? -1 : cb >= rot ? cb - rot : cb + 10 - rot;
#define S_RX(b, sb, co) do { fx[b][0] = *(const s16x8*)(sm + (sb) + xoff + (co)); fx[b][1] = *(const s16x8*)(sm + (sb) + xoff + 2048 + (co)); } while (0)
#define S_RW(b, sb, co, T) fw[b][T] = *(const s16x8*)(sm + (sb) + woff + (T) * 2048 + (co))
#define S_MM(b, T) do { acc[T][0] = mfma16(fx[b][0], fw[b][T], acc[T][0]); acc[T][1] = mfma16(fx[b][1], fw[b][T], acc[T][1]); SCHED_FENCE(); } while (0)
#define S_LB(slot, T) asm volatile("global_load_dwordx4 %0, %1, off sc1" : "=&v"(Bb[slot][T]) : "v"(pb + (slot) * 5120 + ((T) - 1) * 1024) : "memory")
#define S_PT(it, i) do { if ((it) == 1) asm volatile("global_store_dwordx4 %0, %1, %2 sc1\n\ts_nop 1" :: "v"(pto[i]), "v"(ptf[i]), "s"(ws) : "memory"); } while (0)
#define S_BODY(it, WN, XA0, XA1, XA2, XA3)                                                        \
    do {                                                                                          \
        constexpr bool RF_ = (it) < 7, RN_ = (it) < 9;                                            \
        constexpr int SB_ = (it) < 7 ? 0 : (it) - 7;                                              \
        S_MM(0, 0); S_RX(1, BUF(it), co1); S_RW(1, BUF(it), co1, 0); SCHED_FENCE();               \
        S_MM(0, 1); S_RW(1, BUF(it), co1, 1); S_RW(1, BUF(it), co1, 2); XA0; SCHED_FENCE();       \
        S_MM(0, 2); S_RW(1, BUF(it), co1, 3); S_RW(1, BUF(it), co1, 4); XA1; SCHED_FENCE();       \
        S_MM(0, 3); XA2; SCHED_FENCE();                                                           \
        S_MM(0, 4); XA3; SCHED_FENCE();                                                           \
        if ((it) == 0 && wave < 2) WAIT_VM(6); else if (RN_) WAIT_VM(WN);                         \
        WAIT_LGKM0();                                                                             \
        __builtin_amdgcn_s_barrier();                                                             \
        asm volatile("" ::: "memory");                                                            \
        SCHED_FENCE();                                                                            \
        S_MM(1, 0);                                                                               \
        if (__builtin_expect((it) == itg, 0)) {                                                   \
            const s16x8 g0_ = *(const s16x8*)(sm + K1_GW + (64 * cb + 8 * q) * 2), g1_ = *(const s16x8*)(sm + K1_GW + (64 * cb + 32 + 8 * q) * 2); \
            acc[5][0] = mfma16(fx[1][0], g1_, mfma16(fx[0][0], g0_, acc[5][0])); acc[5][1] = mfma16(fx[1][1], g1_, mfma16(fx[0][1], g0_, acc[5][1])); \
        }                                                                                         \
        if ((it) == 1) pretile_read();                                                            \
        if ((it) == 7) pt_check();                                                                \
        if (RF_) {                                                                                \
            W_NEXT((it) + 3);                                                                     \
            if (wave < 2) { W_M0((it) + 3, 2); G_NOP(); W_LD((it) + 3, 2); }                      \
            else if ((it) == 3) { if (lane == 0) flag_set(ws + WS_FPT + (size_t)bid * 8, tok); }    \
            else if ((it) == 6) glds16_c(ws + WS_FPT + (wave - 2) * 1024 + lane * 16, smb + K1_FM + (wave - 2) * 1024); \
            W_M0((it) + 3, 0);                                                                    \
        }                                                                                         \
        if (RN_) { S_RX(0, BUF((it) + 1), co0); S_RW(0, BUF((it) + 1), co0, 0); }                 \
        SCHED_FENCE();                                                                            \
        if (RF_) { W_LD((it) + 3, 0); W_M0((it) + 3, 1); } else S_LB(SB_, 0);                     \
        SCHED_FENCE();                                                                            \
        S_MM(1, 1); if (RN_) { S_RW(0, BUF((it) + 1), co0, 1); S_RW(0, BUF((it) + 1), co0, 2); }  \
        SCHED_FENCE();                                                                            \
        if (RF_) { W_LD((it) + 3, 1); M_M0((it) + 3, 0); } else S_LB(SB_, 1);                     \
        if ((it) == 1) pretile_mix();                                                             \
        S_PT(it, 0); SCHED_FENCE();                                                               \
        S_MM(1, 2); if (RN_) { S_RW(0, BUF((it) + 1), co0, 3); S_RW(0, BUF((it) + 1), co0, 4); }  \
        SCHED_FENCE();                                                                            \
        if (RF_) { M_LD(0); M_M0((it) + 3, 1); } else S_LB(SB_, 2);                               \
        S_PT(it, 1); SCHED_FENCE();                                                               \
        S_MM(1, 3);                                                                               \
        if (RF_) { M_LD(1); M_M0((it) + 3, 2); } else S_LB(SB_, 3);                               \
        S_PT(it, 2); SCHED_FENCE();                                                               \
        S_MM(1, 4);                                                                               \
        if (RF_) M_LD(2); else S_LB(SB_, 4);                                                      \
        S_PT(it, 3); SCHED_FENCE();                                                               \
    } while (0)
        if (wave == 0 && lane < 2) *(u32x4*)(sm + K1_CNT + lane * 16) = (u32x4){0u, 0u, 0u, 0u};
        unsigned pto[4];
#pragma unroll
        for (int i = 0; i < 4; ++i) pto[i] = pretile_dst(i) + lane * 16;
        asm volatile("" : "+v"(a2), "+v"(pa), "+v"(pb), "+v"(pto[0]), "+v"(pto[1]), "+v"(pto[2]), "+v"(pto[3]));
        asm volatile("" : "+v"(wsr[0]), "+v"(wsr[1]), "+v"(wsr[2]), "+v"(vox[0]), "+v"(vox[1]), "+v"(vox[2]));
        if (wave < 2) WAIT_VM(6); else WAIT_VM(5);
        WAIT_LGKM0();
        __builtin_amdgcn_s_barrier();
        asm volatile("" ::: "memory");

        S_RX(0, BUF(0), co0); S_RW(0, BUF(0), co0, 0); S_RW(0, BUF(0), co0, 1); S_RW(0, BUF(0), co0, 2); S_RW(0, BUF(0), co0, 3); S_RW(0, BUF(0), co0, 4);
        const int cl = lane < 40 ? lane : 39;
        float lr, li, za, zb;
        f32x2 pA, pB;
#define TB0() do { lr = bflo(*(const u16*)(sm + K1_PLR + cl * 2)); li = bflo(*(const u16*)(sm + K1_PLI + cl * 2)); za = bflo(*(const u16*)(sm + K1_PLD + cl * 2)); } while (0)
#define TB1() do { const float dt = __builtin_amdgcn_exp2f(za * LOG2E); za = dt * lr * LOG2E; zb = dt * li * INV2PI;                   \
                   const float nA = wc == 1 ? 8.f : wc == 2 ? 24.f : 1.f;                                                             \
                   const float mA = __builtin_amdgcn_exp2f(nA * za), rA = __builtin_amdgcn_fractf(nA * zb);                           \
                   pA = (f32x2){mA * __builtin_amdgcn_cosf(rA), mA * __builtin_amdgcn_sinf(rA)}; } while (0)
#define TB2() do { const float nB = wc == 1 ? 16.f : wc == 2 ? 32.f : 0.f;                                                            \
                   const float mB = __builtin_amdgcn_exp2f(nB * za), rB = __builtin_amdgcn_fractf(nB * zb);                           \
                   pB = (f32x2){mB * __builtin_amdgcn_cosf(rB), mB * __builtin_amdgcn_sinf(rB)}; } while (0)
#define TB3() do { const float nr = pA.x - 1.0f, ni = pA.y, rden = 1.0f / (lr * lr + li * li);                                        \
                   const float z8 = __builtin_amdgcn_fractf(8.f * zb);                                                                \
                   if (wc == 3) pA = (f32x2){(nr * lr + ni * li) * rden, (ni * lr - nr * li) * rden};                 \
                   f32x4* tb = (f32x4*)(sm + K1_TAB + cl * 16);                                              \
                   tb[wc == 0 ? 0 : wc == 1 ? 120 : wc == 2 ? 200 : 40] = (f32x4){pA.x, -pA.y, pA.x, pA.y};                           \
                   tb[wc == 0 ? 80 : wc == 1 ? 160 : wc == 2 ? 240 : 280] = wc == 3 ? (f32x4){8.f * za, z8, 8.f * za, z8} : (f32x4){pB.x, -pB.y, pB.x, pB.y}; } while (0)
        const f32x2* tab = (const f32x2*)(sm + K1_TAB) + t;
        f32x2 Wj[4];
        float Ma[5], Mb[5];
#define PRE_W(j0)                                                                                 \
    do {                                                                                          \
        const f32x2 zz = *(const f32x2*)(sm + K1_TAB + (7 * 80 + 2 * cl) * 8);                    \
        const float za = 16.f * zz.x, zb = __builtin_amdgcn_fractf(16.f * zz.y);                  \
        _Pragma("unroll") for (int j = (j0); j < (j0) + 2; ++j) {                                 \
            const int n = rb - 1 - 4 * wc - j;                                                    \
            const float fn = (float)(n < 0 ? 0 : n);                                              \
            const float mg = n < 0 ? 0.f : __builtin_amdgcn_exp2f(fn * za), rv = fn * zb;         \
            Wj[j] = (f32x2){mg * __builtin_amdgcn_cosf(rv), mg * __builtin_amdgcn_sinf(rv)};      \
            asm volatile("" : "+v"(Wj[j]));                                                       \
        }                                                                                         \
    } while (0)
#define PRE_M(T0, T1)                                                                             \
    do {                                                                                          \
        const float fs = (float)(4 * wc + q), qr = (t & 1) ? -0.25f : 0.f, sg = (t & 1) ? 1.f : -1.f; \
        _Pragma("unroll") for (int T = (T0); T < (T1); ++T) {                                     \
            const f32x2 cc = tab[80 + 16 * T], zz = tab[560 + 16 * T];                            \
            const float e = __builtin_amdgcn_exp2f(fs * zz.x) * __builtin_amdgcn_cosf(fmaf(fs, zz.y, qr)); \
            Ma[T] = fmaf(cc.y, swp(e), cc.x * e);                                                 \
            Mb[T] = sg * swp(Ma[T]);                                                              \
            asm volatile("" : "+v"(Ma[T]), "+v"(Mb[T]));                                          \
        }                                                                                         \
    } while (0)
#define SA0() do { W_NEXT(2); if (wave < 2) { W_M0(2, 2); G_NOP(); W_LD(2, 2); } W_M0(2, 0); G_NOP(); W_LD(2, 0); W_M0(2, 1); } while (0)
#define SA1() do { SCHED_FENCE(); W_LD(2, 1); M_M0(2, 0); G_NOP(); M_LD(0); M_M0(2, 1); } while (0)
#define SA2() do { M_LD(1); M_M0(2, 2); } while (0)
        S_BODY(0, 5, SA0(), SA1(), SA2(), M_LD(2)); S_BODY(1, 5, TB0(), , TB1(), ); S_BODY(2, 9, , TB2(), GET_TOK(), ); S_BODY(3, 5, , TB3(), , );
        S_BODY(4, 6, , , PRE_W(0), ); S_BODY(5, 5, , , PRE_W(2), ); S_BODY(6, 5, , , PRE_M(0, 2), );

        S_BODY(7, 5, , , PRE_M(2, 4), ); S_BODY(8, 5, , , PRE_M(4, 5), );
        f32x2 Lc[5], Sc[5];
#pragma unroll
        for (int T = 0; T < 5; ++T) { Lc[T] = tab[16 * T]; Sc[T] = tab[(5 - q) * 80 + 16 * T]; }
        const f32x2 L32 = *(const f32x2*)(sm + K1_TAB + (6 * 80 + 2 * cl + 1) * 8);
        S_BODY(9, 0, S_LB(3, 0); S_LB(3, 1), S_LB(3, 2), S_LB(3, 3), S_LB(3, 4));

        __builtin_amdgcn_s_setprio(3);
        float ag8[5];
#pragma unroll
        for (int T = 0; T < 5; ++T) ag8[T] = acc[T][0][0];
#pragma unroll
        for (int j = 1; j < 8; ++j)
#pragma unroll
            for (int T = 0; T < 5; ++T) ag8[T] = fmaf(Lc[T].y, swp(ag8[T]), fmaf(Lc[T].x, ag8[T], acc[T][j >> 2][j & 3]));
        {
            unsigned sv[5];
#pragma unroll
            for (int T = 0; T < 5; ++T) sv[T] = f2u(fmaf(Sc[T].y, swp(ag8[T]), Sc[T].x * ag8[T]));
            const auto r01 = __builtin_amdgcn_permlane32_swap(sv[0], sv[1], false, false);
            const auto r23 = __builtin_amdgcn_permlane32_swap(sv[2], sv[3], false, false);
            const auto r44 = __builtin_amdgcn_permlane32_swap(sv[4], sv[4], false, false);
            const unsigned p4 = f2u(u2f(r44[0]) + u2f(r44[1]));
            const auto ra = __builtin_amdgcn_permlane16_swap(f2u(u2f(r01[0]) + u2f(r01[1])), f2u(u2f(r23[0]) + u2f(r23[1])), false, false);
            const auto rb4 = __builtin_amdgcn_permlane16_swap(p4, p4, false, false);
            float* xa = (float*)(sm + K1_XCH) + wc * 80 + t;
            xa[16 * (((q & 1) << 1) | (q >> 1))] = u2f(ra[0]) + u2f(ra[1]);
            xa[64] = u2f(rb4[0]) + u2f(rb4[1]);
        }
        asm volatile("" ::: "memory");

        unsigned old = 0;
        if (lane == 0) old = __hip_atomic_fetch_add((LAS unsigned*)(sm + K1_CNT), 1u, __ATOMIC_RELAXED, __HIP_MEMORY_SCOPE_WORKGROUP);
        old = __builtin_amdgcn_readfirstlane(old);
        asm volatile("" ::: "memory");
        if (old == 3) {
            const f32x2* xc = (const f32x2*)(sm + K1_XCH) + cl;
            const f32x2 s = cfma(L32, cfma(L32, cfma(L32, xc[0], xc[40]), xc[80]), xc[120]);
            if (lane < 40) st128_wt(ws + WS_AGG + (size_t)(cb * 16 + rb) * 640 + lane * 16,
                                    (u32x4){f2u(s.x), f2u(s.y), (unsigned)tok, (unsigned)(tok >> 32)});
        }
        SCHED_FENCE();

        __builtin_amdgcn_s_setprio(PRIO_OFF);
        char* own = sm + (wc < 2 ? 5 * 20480 : 8 * 20480) - 5120 + (wc & 1) * 2560;
#pragma unroll
        for (int T = 0; T < 5; ++T) *(float*)(own + (16 * T + t) * 16 + q * 4) = ag8[T];
        if (cb < 10 && t < 2) {
            const f32x4 gv = t ? acc[5][1] : acc[5][0];
            st128_wt(ws + WS_G + (cb * 2048 + 128 * rb + 32 * wc + 8 * q + 4 * t) * 4, __builtin_bit_cast(u32x4, gv));
        }
        float pre[5];
        {
            f32x2 Cc[5], L8c[5];
#pragma unroll
            for (int T = 0; T < 5; ++T) {
                Cc[T] = tab[80 + 16 * T]; L8c[T] = tab[240 + 16 * T];
#pragma unroll
                for (int j = 0; j < 8; ++j) {
                    const float u = acc[T][j >> 2][j & 3];
                    acc[T][j >> 2][j & 3] = fmaf(Cc[T].y, swp(u), Cc[T].x * u);
                }
                pre[T] = 0.f;
            }
            {
                for (unsigned spins = 0; spins < (1u << 22); ++spins) {
                    if (__hip_atomic_load((LAS unsigned*)(sm + K1_CNT), __ATOMIC_RELAXED, __HIP_MEMORY_SCOPE_WORKGROUP) >= 4u) break;
                    __builtin_amdgcn_s_sleep(1);
                }
                asm volatile("" ::: "memory");
                const float* xa = (const float*)(sm + K1_XCH) + t;
#pragma unroll 1
                for (int w = 0; w < wc; ++w)
#pragma unroll
                    for (int T = 0; T < 5; ++T) {
                        const f32x2 z = tab[480 + 16 * T];
                        pre[T] = fmaf(z.y, swp(pre[T]), fmaf(z.x, pre[T], xa[w * 80 + 16 * T]));
                    }
            }
#pragma unroll
            for (int T = 0; T < 5; ++T) {
                const f32x4 xs = *(const f32x4*)(own + (16 * T + t) * 16);
#pragma unroll
                for (int k = 0; k < 3; ++k) {
                    const float nx = fmaf(L8c[T].y, swp(pre[T]), fmaf(L8c[T].x, pre[T], xs[k]));
                    pre[T] = k < q ? nx : pre[T];
                }
                pre[T] = fmaf(Cc[T].y, swp(pre[T]), Cc[T].x * pre[T]);
            }
        }
#pragma unroll
        for (int T = 0; T < 5; ++T) asm volatile("" : "+v"(acc[T][0]), "+v"(acc[T][1]), "+v"(pre[T]));
        SCHED_FENCE();
        __builtin_amdgcn_s_setprio(3);

        if (rb > 0) {
            f32x2 cy = {0.f, 0.f};
            if (4 * wc < rb) {
                const char* agp = ws + WS_AGG + (size_t)(cb * 16 + 4 * wc) * 640 + cl * 16;
                for (unsigned spins = 0; spins < (1u << 20); ++spins) {
                    u32x4 ag[4];
#pragma unroll
                    for (int j = 0; j < 4; ++j)
                        asm volatile("global_load_dwordx4 %0, %1, off sc1" : "=v"(ag[j]) : "v"(agp + (4 * wc + j < rb ? j : 0) * 640) : "memory");
                    asm volatile("s_waitcnt vmcnt(0)" : "+v"(ag[0]), "+v"(ag[1]), "+v"(ag[2]), "+v"(ag[3]) :: "memory");
                    bool ok = true;
#pragma unroll
                    for (int j = 0; j < 4; ++j) ok = ok && ag[j][2] == (unsigned)tok && ag[j][3] == (unsigned)(tok >> 32);
                    const f32x2 c0 = cfma(Wj[1], (f32x2){u2f(ag[1][0]), u2f(ag[1][1])}, cmul_pk(Wj[0], (f32x2){u2f(ag[0][0]), u2f(ag[0][1])}));
                    const f32x2 c1 = cfma(Wj[3], (f32x2){u2f(ag[3][0]), u2f(ag[3][1])}, cmul_pk(Wj[2], (f32x2){u2f(ag[2][0]), u2f(ag[2][1])}));
                    cy = (f32x2){c0.x + c1.x, c0.y + c1.y};
                    if (wave_all(ok)) break;
                    __builtin_amdgcn_s_sleep(4);
                }
            }
            if (lane < 40) *(f32x2*)(sm + K1_PRT + (lane * 4 + wc) * 8) = cy;
            asm volatile("" ::: "memory");
            if (lane == 0) __hip_atomic_fetch_add((LAS unsigned*)(sm + K1_CNT + 4), 1u, __ATOMIC_RELAXED, __HIP_MEMORY_SCOPE_WORKGROUP);
            for (unsigned spins = 0; spins < (1u << 22); ++spins) {
                if (__hip_atomic_load((LAS unsigned*)(sm + K1_CNT + 4), __ATOMIC_RELAXED, __HIP_MEMORY_SCOPE_WORKGROUP) >= 4u) break;
                __builtin_amdgcn_s_sleep(1);
            }
            asm volatile("" ::: "memory");
#pragma unroll
            for (int T = 0; T < 5; ++T) {
                const f32x4* pp = (const f32x4*)(sm + K1_PRT + (8 * T + (t >> 1)) * 32);
                const f32x4 p0 = pp[0], p1 = pp[1];
                pre[T] = fmaf(Mb[T], (p0[1] + p0[3]) + (p1[1] + p1[3]), fmaf(Ma[T], (p0[0] + p0[2]) + (p1[0] + p1[2]), pre[T]));
            }
        }

#pragma unroll
        for (int j = 0; j < 8; ++j)
#pragma unroll
            for (int T = 0; T < 5; ++T) {
                pre[T] = fmaf(Lc[T].y, swp(pre[T]), fmaf(Lc[T].x, pre[T], acc[T][j >> 2][j & 3]));
                acc[T][j >> 2][j & 3] = pre[T];
            }
        {
            const int od = cb & 1;
            char* dst = a2 + (size_t)((5 * cb) >> 1) * 8192;
            char* wp0 = own + (16 * od + t) * 80 + q * 16;
            char* wp1 = own + (16 * (od ^ 1) + t) * 80 + q * 16;
            const char* rp = own + (8 * q + (t >> 2)) * 80 + (t & 3) * 16;
            const char* rl = own + (16 * od + 8 * (q & 1) + (t >> 2)) * 80 + (t & 3) * 16 + (q >> 1) * 8;
#define H_WR(T) *(u32x4*)(((T) & 1) ? wp1 : wp0) = (u32x4){pack2(acc[T][0][0], acc[T][0][1]), pack2(acc[T][0][2], acc[T][0][3]), \
                                                           pack2(acc[T][1][0], acc[T][1][1]), pack2(acc[T][1][2], acc[T][1][3])}
#define H_RD(p) __builtin_bit_cast(u32x4, __builtin_shufflevector(__builtin_amdgcn_ds_read_tr16_b64_v4i16((LAS s16x4*)(p)), \
                                                                  __builtin_amdgcn_ds_read_tr16_b64_v4i16((LAS s16x4*)((p) + 320)), 0, 1, 2, 3, 4, 5, 6, 7))
#define H_PAIR(ks) do { asm volatile("" ::: "memory"); hf[2 * (ks)] = H_RD(rp); hf[2 * (ks) + 1] = H_RD(rp + 8); asm volatile("" ::: "memory"); } while (0)
#define H_LONE() do { asm volatile("" ::: "memory"); hf[4] = H_RD(rl); asm volatile("" ::: "memory"); } while (0)
            u32x4 hf[5];
            if (od) { H_WR(0); H_LONE(); H_WR(1); H_WR(2); H_PAIR(0); H_WR(3); H_WR(4); H_PAIR(1); }
            else { H_WR(0); H_WR(1); H_PAIR(0); H_WR(2); H_WR(3); H_PAIR(1); H_WR(4); H_LONE(); }
            st128_wt(dst + od * 8192, hf[0]); st128_wt(dst + od * 8192 + 1024, hf[1]);
            st128_wt(dst + od * 8192 + 8192, hf[2]); st128_wt(dst + od * 8192 + 9216, hf[3]);
            st128_wt(dst + (od ? 512 : 16384) + (q >> 1) * 512, hf[4]);
        }
        WAIT_LGKM0();
        if (lane == 0) __hip_atomic_fetch_add((LAS unsigned*)(sm + K1_CNT + 8), 1u, __ATOMIC_RELAXED, __HIP_MEMORY_SCOPE_WORKGROUP);

        __builtin_amdgcn_s_setprio(0);
        P2_BEGIN(0, PSL0);

        P2_STEP(0, 10, 8, 8); P2_POLLED();
        P2_STEP(1, 10, 8, 8); P2_STEP(2, 10, 8, 8); P2_STEP(3, 10, 8, 8); P2_STEP(4, 10, 8, 8);
        P2_STEP(5, 10, 8, 8); P2_STEP(6, 10, 13, 8); P2_STEP(7, 10, 18, 8); P2_STEP(8, 10, 9, 8); P2_STEP(9, 10, 0, 8);
        asm volatile("" : "+v"(gl[0]), "+v"(gl[1]), "+v"(gl[2]), "+v"(gl[3]) :: "memory");

        {
            const float gs = (u2f(gl[0]) + u2f(gl[1])) + (wave < 2 ? u2f(gl[2]) + u2f(gl[3]) : 0.f);
            keep = wave == 0 ? ac2[0][0][0] : wave == 1 ? ac2[0][1][0] : ac2[0][2][0];
#pragma unroll
            for (int n = 0; n < 3; ++n) ac2[0][n][0] = wave == n ? gs : ac2[0][n][0];
#pragma unroll
            for (int i = 0; i < 20; ++i) *(f32x4*)(sm + wave * 20480 + i * 1024 + lane * 16) = ac2[i / 5][i % 5];
        }
    } else {
        const int bb = q & 1;
        const int trb = 26624 + (8 * q + (t >> 2)) * 192 + (lane & 3) * 8;
        const int tra = trb + bb * 32;
        const int tr5 = trb + (bb ? 0 : 160);
        f32x4 acc[6][2];
        s16x8 fx[2][2], fw[2][6];
#define M_RW(b, sb, ks, j)                                                                        \
    do {                                                                                          \
        const char* pa_ = sm + (sb) + ((j) < 5 ? tra + (j) * 32 : tr5) + (ks) * 32 * 192;         \
        const s16x4 lo_ = __builtin_amdgcn_ds_read_tr16_b64_v4i16((LAS s16x4*)pa_);               \
        const s16x4 hi_ = __builtin_amdgcn_ds_read_tr16_b64_v4i16((LAS s16x4*)(pa_ + 4 * 192));   \
        fw[b][j] = __builtin_shufflevector(lo_, hi_, 0, 1, 2, 3, 4, 5, 6, 7);                     \
    } while (0)
#define M_BIAS(j)                                                                                 \
    do {                                                                                          \
        const u32x2 bp_ = *(const u32x2*)(sm + K1_PB1 + (16 * (j) + 4 * q) * 2);                  \
        const f32x4 bv_ = {bflo(bp_[0]), bfhi(bp_[0]), bflo(bp_[1]), bfhi(bp_[1])};               \
        acc[j][0] += bv_; acc[j][1] += bv_;                                                       \
    } while (0)
#define M_MM(b, j) do { acc[j][0] = mfma16(fw[b][j], fx[b][0], acc[j][0]); acc[j][1] = mfma16(fw[b][j], fx[b][1], acc[j][1]); SCHED_FENCE(); } while (0)
#define M_BODY(it, WN)                                                                            \
    do {                                                                                          \
        constexpr bool RF_ = (it) < 8, RN_ = (it) < 9;                                            \
        constexpr int SB_ = (it) < 8 ? 0 : (it) - 7;                                              \
        M_MM(0, 0); if (RF_) { X_NEXT((it) + 2); X_M0((it) + 2, 0); } S_RX(1, BUF(it), co1); M_RW(1, BUF(it), 1, 0); SCHED_FENCE(); \
        if (RF_) { X_LD((it) + 2, 0); X_M0((it) + 2, 1); } else S_LB(SB_, 0);                     \
        SCHED_FENCE();                                                                            \
        M_MM(0, 1); M_RW(1, BUF(it), 1, 1); M_RW(1, BUF(it), 1, 2); SCHED_FENCE();                \
        if (RF_) { X_LD((it) + 2, 1); X_M0((it) + 2, 2); } else S_LB(SB_, 1);                     \
        SCHED_FENCE();                                                                            \
        M_MM(0, 2); M_RW(1, BUF(it), 1, 3); M_RW(1, BUF(it), 1, 4); SCHED_FENCE();                \
        if (RF_) { X_LD((it) + 2, 2); X_M0((it) + 2, 3); } else S_LB(SB_, 2);                     \
        SCHED_FENCE();                                                                            \
        M_MM(0, 3); M_RW(1, BUF(it), 1, 5); SCHED_FENCE();                                        \
        if (RF_) X_LD((it) + 2, 3); else S_LB(SB_, 3);                                            \
        SCHED_FENCE();                                                                            \
        M_MM(0, 4); if (!RF_) S_LB(SB_, 4);                                                       \
        SCHED_FENCE();                                                                            \
        M_MM(0, 5);                                                                               \
        if (RN_) WAIT_VM(WN);                                                                     \
        WAIT_LGKM0();                                                                             \
        __builtin_amdgcn_s_barrier();                                                             \
        asm volatile("" ::: "memory");                                                            \
        SCHED_FENCE();                                                                            \
        M_MM(1, 0); if (RN_) { S_RX(0, BUF((it) + 1), co0); M_RW(0, BUF((it) + 1), 0, 0); }       \
        if ((it) == 7) { pt_check(); S_LB(0, 0); } if ((it) == 8) S_LB(3, 0);                                            \
        SCHED_FENCE();                                                                            \
        M_MM(1, 1); if (RN_) { M_RW(0, BUF((it) + 1), 0, 1); M_RW(0, BUF((it) + 1), 0, 2); } if ((it) == 7) S_LB(0, 1); if ((it) == 8) S_LB(3, 1); \
        if ((it) == 4) { M_BIAS(4); M_BIAS(5); }                                                  \
        SCHED_FENCE();                                                                            \
        M_MM(1, 2); if (RN_) { M_RW(0, BUF((it) + 1), 0, 3); M_RW(0, BUF((it) + 1), 0, 4); } if ((it) == 7) S_LB(0, 2); if ((it) == 8) S_LB(3, 2); SCHED_FENCE(); \
        M_MM(1, 3); if (RN_) M_RW(0, BUF((it) + 1), 0, 5); if ((it) == 7) S_LB(0, 3); if ((it) == 8) S_LB(3, 3); SCHED_FENCE(); \
        M_MM(1, 4); if ((it) == 7) S_LB(0, 4); if ((it) == 8) S_LB(3, 4);                                                    \
        if ((it) == 2) { M_BIAS(0); M_BIAS(1); } if ((it) == 3) { M_BIAS(2); M_BIAS(3); }               \
        if ((it) == 5) GET_TOK();                                                                 \
        SCHED_FENCE();                                                                            \
        M_MM(1, 5);                                                                               \
    } while (0)
        asm volatile("" : "+v"(a2), "+v"(pa), "+v"(pb), "+v"(vox[0]), "+v"(vox[1]), "+v"(vox[2]), "+v"(vox[3]));
        WAIT_VM(8);
        __builtin_amdgcn_s_barrier();
        asm volatile("" ::: "memory");

        S_RX(0, BUF(0), co0); M_RW(0, BUF(0), 0, 0); M_RW(0, BUF(0), 0, 1); M_RW(0, BUF(0), 0, 2); M_RW(0, BUF(0), 0, 3); M_RW(0, BUF(0), 0, 4); M_RW(0, BUF(0), 0, 5);
#pragma unroll
        for (int j = 0; j < 6; ++j) { acc[j][0] = (f32x4){0.f, 0.f, 0.f, 0.f}; acc[j][1] = (f32x4){0.f, 0.f, 0.f, 0.f}; }
        M_BODY(0, 8); M_BODY(1, 4); M_BODY(2, 4); M_BODY(3, 4); M_BODY(4, 4); M_BODY(5, 4); M_BODY(6, 4);

        M_BODY(7, 4); M_BODY(8, 10); M_BODY(9, 0);

        __builtin_amdgcn_s_setprio(2);
#pragma unroll
        for (int v = 0; v < 3; ++v) {
#pragma unroll
            for (int rt = 0; rt < 2; ++rt) {
                u32x4 o;
#pragma unroll
                for (int jj = 0; jj < 2; ++jj) {
                    float r[4];
#pragma unroll
                    for (int i = 0; i < 4; ++i) {
                        const float h = acc[2 * v + jj][rt][i];
                        const float e = __builtin_amdgcn_exp2f(h * LOG2E);
                        const float d = fmaf(e, fmaf(0.5f, e, 1.0f), 1.0f);
                        r[i] = fmaf(-h, __builtin_amdgcn_rcpf(d), h);
                    }
                    o[2 * jj] = pack2(r[0], r[1]);
                    o[2 * jj + 1] = pack2(r[2], r[3]);
                }
                st128_wt(a2 + (size_t)(40 + 3 * cb + v) * 8192 + rt * 1024, o);
            }
        }

        P2_BEGIN(1, PSL1);

        P2_STEP(0, 12, 8, 7); P2_POLLED();
        P2_STEP(1, 12, 8, 7); P2_STEP(2, 12, 8, 7); P2_STEP(3, 12, 8, 7); P2_STEP(4, 12, 8, 7); P2_STEP(5, 12, 13, 7); lds_arrive(sm + K1_CNT + 16 + 4 * wc, lane);
        P2_STEP(6, 12, 18, 7); P2_STEP(7, 12, 18, 7); P2_STEP(8, 12, 18, 7);
        lds_wait(sm + K1_CNT + 8, 4u);
        P2_STEP(9, 12, 18, 7); P2_STEP(10, 12, 9, 7); P2_STEP(11, 12, 0, 7);
        __builtin_amdgcn_s_setprio(0);
        {
            const int rr = (wc + 2) & 3;
            f32x4 o5[5];
#pragma unroll
            for (int r = 0; r < 4; ++r) {
                if (r == rr) {
#pragma unroll
                    for (int n = 0; n < 5; ++n) o5[n] = ac2[r][n];
                } else {
#pragma unroll
                    for (int n = 0; n < 5; ++n) *(f32x4*)(sm + wave * 20480 + (r * 5 + n) * 1024 + lane * 16) = ac2[r][n];
                }
            }
            lds_arrive(sm + K1_CNT + 12, lane);
            lds_wait(sm + K1_CNT + 12, 4u);
            char* rp = sm + 81920 + rr * 5120 + lane * 16;
            char* r1 = rp + ((wc + 1) & 3) * 20480;
            const char* r2 = rp + ((wc + 2) & 3) * 20480;
            const char* r3 = rp + ((wc + 3) & 3) * 20480;
#pragma unroll
            for (int n = 0; n < 5; ++n)
                *(f32x4*)(r1 + n * 1024) = (o5[n] + *(const f32x4*)(r1 + n * 1024)) + (*(const f32x4*)(r2 + n * 1024) + *(const f32x4*)(r3 + n * 1024));
        }

        if (bid == 255 && wave == 7 && lane == 0) flag_set(ws + WS_EP, tok);
    }
    };
    if (wave < 4) run(IC<0>{}, IC<0>{}); else run(IC<1>{}, IC<0>{});
    int role = wave >> 2;
    asm volatile("" : "+s"(role));
    if (role == 0) run(IC<0>{}, IC<1>{}); else run(IC<1>{}, IC<1>{});
    f32x4 xd[3];
    const char *rp[3], *rm[3], *gp[3];
#pragma unroll
    for (int m = 0; m < 3; ++m) {
        xd[m] = (f32x4){bflo(xv[m][0]) * bflo(dv[m][0]), bfhi(xv[m][0]) * bfhi(dv[m][0]), bflo(xv[m][1]) * bflo(dv[m][1]), bfhi(xv[m][1]) * bfhi(dv[m][1])};
        const int tile = wave + 8 * m < 20 ? wave + 8 * m : wave, r = tile / 5;
        rp[m] = sm + tile * 1024 + lane * 16;
        rm[m] = rp[m] + 81920 + ((r + 3) & 3) * 20480;
        gp[m] = sm + (32 * (r >> 1) + 8 * (t >> 2) + 4 * (r & 1) + (t & 3)) * 16;
        asm volatile("" : "+v"(xd[m]));
    }
    wg_sync();

#pragma unroll
    for (int m = 0; m < 3; ++m) {
        if (m < 2 || wave < 4) {
            const float lg = (*(const float*)gp[m] + *(const float*)(gp[m] + 21504)) + *(const float*)(gp[m] + 43008);
            f32x4 p0 = *(const f32x4*)rp[m], p1 = *(const f32x4*)(rp[m] + 20480), p2 = *(const f32x4*)(rp[m] + 40960);
            const f32x4 p3 = *(const f32x4*)(rp[m] + 61440), vm = *(const f32x4*)rm[m];
            if (m == 0) { p0[0] = wave == 0 ? keep : p0[0]; p1[0] = wave == 1 ? keep : p1[0]; p2[0] = wave == 2 ? keep : p2[0]; }
            const float g = __builtin_amdgcn_rcpf(1.0f + __builtin_amdgcn_exp2f(lg * -LOG2E));
            const f32x4 vs = ((p0 + p1) + (p2 + p3)) * 2.0f + xd[m];
            st128_wt(out + oo[m], __builtin_bit_cast(u32x4, (vs - vm) * g + vm));
        }
    }

}

extern "C" void kernel_launch(void* const* d_in, const int* in_sizes, int n_in, void* d_out, int out_size, void* d_ws,
                              size_t ws_size, hipStream_t stream) {
    kf<<<256, 512, 0, stream>>>((const u16*)d_in[0], (const u16*)d_in[1], (const u16*)d_in[2], (const u16*)d_in[3], (const u16*)d_in[4],
                                (const u16*)d_in[5], (const u16*)d_in[6], (const u16*)d_in[7], (const u16*)d_in[8], (const u16*)d_in[10],
                                (const u16*)d_in[11], (const u16*)d_in[12], (const u16*)d_in[13], (char*)d_ws, (float*)d_out);
}
```
